```python
import torch

def _softmax(x, axis=-1):
    return torch.softmax(x, dim=axis)

def _gelu(x):
    return torch.nn.functional.gelu(x, approximate='tanh')

def _silu(x):
    return torch.nn.functional.silu(x)

def _sigmoid(x):
    return torch.sigmoid(x)

def _layernorm(x, axis=-1, eps=1e-05):
    m = x.mean(dim=axis, keepdim=True)
    v = x.var(dim=axis, keepdim=True, unbiased=False)
    return (x - m) / torch.sqrt(v + eps)

def _scatter_add(tgt, idx, src, axis=0):
    return tgt.clone().index_add_(axis, idx, src)

def _segment_sum(src, seg, n):
    out = torch.zeros((n,) + src.shape[1:], dtype=src.dtype)
    return out.index_add_(0, seg, src)

def _pad(x, pw):
    flat = tuple((v for pair in reversed(pw) for v in pair))
    return torch.nn.functional.pad(x, flat)
kernel_name = 'recurrent_scan__mha_block_cached__mixer_gmlp__prompt'

def setup_inputs():

    _w = lambda a: (lambda t: t.to(torch.bfloat16) if t.is_floating_point() else t)(torch.as_tensor(a))

    def _A_setup():
        B, Sq, D = (1, 2048, 448)
        H, Sk, Dh = (16, 2048, 256)
        return {'x': _w((lambda *s: torch.randn(*s, dtype=torch.bfloat16) * 0.05)(B, Sq, D)), 'k_cache': _w((lambda *s: torch.randn(*s, dtype=torch.bfloat16) * 0.8)(B, H, Sk, Dh)), 'v_cache': _w((lambda *s: torch.randn(*s, dtype=torch.bfloat16) * 0.2)(B, H, Sk, Dh)), 'wq': _w((lambda *s: torch.randn(*s, dtype=torch.bfloat16) * 0.8)(D, H * Dh) * D ** (-0.5)), 'wo': _w((lambda *s: torch.randn(*s, dtype=torch.bfloat16) * 0.2)(H * Dh, D) * (H * Dh) ** (-0.5))}
    _A_in = _A_setup()

    def _B_setup():
        B, S, D, F = (1, 2048, 448, 1792)
        Fh = F // 2
        return {'x': _w((lambda *s: torch.randn(*s, dtype=torch.bfloat16) * 0.05)(B, S, D)), 'w_in': _w((lambda *s: torch.randn(*s, dtype=torch.bfloat16) * 0.4)(D, 2 * Fh)), 'w_sp': _w(0.01 * (lambda *s: torch.randn(*s, dtype=torch.bfloat16) * 0.4)(S, S)), 'b_sp': _w(1.0 + 0.01 * (lambda *s: torch.randn(*s, dtype=torch.bfloat16) * 0.1)(S)), 'w_out': _w((lambda *s: torch.randn(*s, dtype=torch.bfloat16) * 0.1)(Fh, D))}
    _B_in = _B_setup()
    return {'A_x': _A_in['x'], 'A_k_cache': _A_in['k_cache'], 'A_v_cache': _A_in['v_cache'], 'A_wq': _A_in['wq'], 'A_wo': _A_in['wo'], 'B_x': _B_in['x'], 'B_w_in': _B_in['w_in'], 'B_w_sp': _B_in['w_sp'], 'B_b_sp': _B_in['b_sp'], 'B_w_out': _B_in['w_out']}

def reference(A_x, A_k_cache, A_v_cache, A_wq, A_wo, B_x, B_w_in, B_w_sp, B_b_sp, B_w_out):

    def _A_ref(x, k_cache, v_cache, wq, wo):
        B, Sq, D = x.shape
        H, Dh = (16, 256)
        q = (lambda x, *d: x.permute(*d))((x @ wq).reshape(B, Sq, H, Dh), 0, 2, 1, 3)
        scores = torch.einsum('bhqd,bhkd->bhqk', q, k_cache) * Dh ** (-0.5)
        p = _softmax(scores, axis=-1)
        o = torch.einsum('bhqk,bhkd->bhqd', p, v_cache)
        return (lambda x, *d: x.permute(*d))(o, 0, 2, 1, 3).reshape(B, Sq, H * Dh) @ wo

    def _B_ref(x, w_in, w_sp, b_sp, w_out):
        B, S, D = x.shape
        F = w_in.shape[-1]
        Fh = F // 2
        z = _gelu(torch.einsum('bsd,df->bsf', x, w_in))
        z1 = z[..., :Fh].clone()
        z2 = _layernorm(z[..., Fh:], axis=-1)
        g = torch.einsum('ts,bsf->btf', w_sp, z2) + b_sp[None, :, None]
        return torch.einsum('bsf,fd->bsd', z1 * g, w_out)
    x = A_x
    xn = _layernorm(x, axis=-1)
    s = _A_ref(xn, A_k_cache, A_v_cache, A_wq, A_wo)
    g = _silu(_B_ref(xn, B_w_in, B_w_sp, B_b_sp, B_w_out))
    return x + s * g
```

```cpp
#include <hip/hip_runtime.h>
#include <hip/hip_bf16.h>
typedef float f32x16 __attribute__((ext_vector_type(16)));
typedef float f32x4 __attribute__((ext_vector_type(4)));
typedef float f32x2 __attribute__((ext_vector_type(2)));
typedef unsigned u32x16 __attribute__((ext_vector_type(16)));
typedef unsigned u32x4 __attribute__((ext_vector_type(4)));
typedef unsigned u32x2 __attribute__((ext_vector_type(2)));
typedef __bf16 bf16x2 __attribute__((ext_vector_type(2)));
typedef __bf16 bf16x8 __attribute__((ext_vector_type(8)));
typedef unsigned short u16;
#define DI __device__ __forceinline__
DI unsigned rfl(unsigned x) { return __builtin_amdgcn_readfirstlane(x); }
DI unsigned cvtpk(float lo, float hi) { f32x2 v = {lo, hi}; bf16x2 b = __builtin_convertvector(v, bf16x2); return __builtin_bit_cast(unsigned, b); }
DI float bflo(unsigned w) { return __uint_as_float(w << 16); }
DI float bfhi(unsigned w) { return __uint_as_float(w & 0xffff0000u); }
DI float gsum(float v) {
  auto a = __builtin_amdgcn_permlane32_swap(__float_as_uint(v), __float_as_uint(v), false, false);
  v = __uint_as_float(a[0]) + __uint_as_float(a[1]);
  auto b = __builtin_amdgcn_permlane16_swap(__float_as_uint(v), __float_as_uint(v), false, false);
  return __uint_as_float(b[0]) + __uint_as_float(b[1]);
}
DI float gmax(float v) {
  auto a = __builtin_amdgcn_permlane32_swap(__float_as_uint(v), __float_as_uint(v), false, false);
  v = fmaxf(__uint_as_float(a[0]), __uint_as_float(a[1]));
  auto b = __builtin_amdgcn_permlane16_swap(__float_as_uint(v), __float_as_uint(v), false, false);
  return fmaxf(__uint_as_float(b[0]), __uint_as_float(b[1]));
}
DI u32x4 regroup(unsigned a0, unsigned a1, unsigned b0, unsigned b1) {
  auto s0 = __builtin_amdgcn_permlane32_swap(a0, b0, false, false);
  auto s1 = __builtin_amdgcn_permlane32_swap(a1, b1, false, false);
  auto t0 = __builtin_amdgcn_permlane16_swap(s0[0], s0[1], false, false);
  auto t1 = __builtin_amdgcn_permlane16_swap(s1[0], s1[1], false, false);
  u32x4 r = {t0[0], t1[0], t0[1], t1[1]};
  return r;
}
DI float gelu_t(float x) {
  const float u = 0.7978845608028654f * (x + 0.044715f * x * x * x);
  return x * __builtin_amdgcn_rcpf(1.0f + __builtin_amdgcn_exp2f(-2.885390081777927f * u));
}
#define SRD3 0x00020000
#define WARM_KV 16384u
#define WARM_TLB 0
#define WARM_CODE 1
#define WARM_W 1
#define WARM_P 0
#define PART_AUX 16
#define TS(k, i)
constexpr size_t WS_CNT = 0;
constexpr size_t WS_P = 16384;
constexpr size_t WS_Z1 = WS_P + 29360128;
constexpr size_t WS_Z2R = WS_Z1 + 3670016;
constexpr size_t WS_Z2N = WS_Z2R + 3670016;
constexpr size_t WS_U = WS_Z2N + 3670016;
constexpr size_t WS_ST = WS_U + 3670016;
constexpr size_t WS_XN = WS_ST + 262144;
constexpr size_t WS_END = WS_XN + 1835008;
typedef short s16x2 __attribute__((ext_vector_type(2)));
DI float dot2(unsigned a, unsigned b, float c) {
  return __builtin_amdgcn_fdot2_f32_bf16(__builtin_bit_cast(bf16x2, a), __builtin_bit_cast(bf16x2, b), c, false);
}

__global__ __launch_bounds__(512) void k_pre(const u16* __restrict__ X, char* __restrict__ ws, const u16* __restrict__ WQ, const u16* __restrict__ WO,
                                             const u16* __restrict__ Kc, const u16* __restrict__ Vc, const u16* __restrict__ WI) {
  const unsigned tid = threadIdx.x, lane = tid & 63u, c = lane & 15u, g = lane >> 4;
  const unsigned b = blockIdx.x;
  TS(0, 0);
  if (tid >= 64u || b >= 128u) {
    const unsigned l = (255u - b) * 512u + tid;
    if (b >= 222u) { u32x4 z = {0u, 0u, 0u, 0u}; const unsigned i = (b - 222u) * 512u + tid;
                     if (i < 1024u) ((u32x4*)(ws + WS_CNT))[i] = z; else ((u32x4*)(ws + WS_ST))[i - 1024u] = z; }
    if (b == 128u || b == 129u) {
      unsigned dd, oo;
      asm volatile("s_getpc_b64 s[40:41]\n\t.Lkp_pc0:\n\tv_min_u32 %1, .Lkm_code_end-.Lkp_pc0-4, %2\n\tglobal_load_dword %0, %1, s[40:41]\n\ts_waitcnt vmcnt(0)"
                   : "=&v"(dd), "=&v"(oo) : "v"(((b - 128u) * 512u + tid) * 128u) : "s40", "s41", "memory");
    }
    if (l < 41216u + WARM_KV) {
      const unsigned m = l - 41216u;
      const char* warm = l < 28672u ? (const char*)WQ + (size_t)l * 128u : l < 41216u ? (const char*)WI + (size_t)(l - 28672u) * 128u
                       : (const char*)((m & 8192u) ? Vc : Kc) + (size_t)((m >> 9) & 15u) * 1048576u + (m & 511u) * 128u;
      unsigned a0;
      asm volatile("global_load_dword %0, %1, off\n\ts_waitcnt vmcnt(0)" : "=&v"(a0) : "v"(warm) : "memory");
    }
    return;
  }
  u32x4 xr[14];
  #pragma unroll
  for (int f = 0; f < 14; ++f) xr[f] = *(const u32x4*)(X + (size_t)(b * 16u + c) * 448 + f * 32 + g * 8);
  float s0 = 0.f, s1 = 0.f, q0 = 0.f, q1 = 0.f;
  #pragma unroll
  for (int k = 0; k < 14; ++k) {
    s0 = dot2(xr[k][0], 0x3f803f80u, s0); s1 = dot2(xr[k][1], 0x3f803f80u, s1);
    s0 = dot2(xr[k][2], 0x3f803f80u, s0); s1 = dot2(xr[k][3], 0x3f803f80u, s1);
  }
  const float mean = gsum(s0 + s1) * (1.0f / 448.0f);
  #pragma unroll
  for (int k = 0; k < 14; ++k)
    #pragma unroll
    for (int e = 0; e < 4; ++e) { const float a = bflo(xr[k][e]) - mean, bb = bfhi(xr[k][e]) - mean; q0 = fmaf(a, a, q0); q1 = fmaf(bb, bb, q1); }
  const float rstd = __builtin_amdgcn_rsqf(gsum(q0 + q1) * (1.0f / 448.0f) + 1e-5f), nm = -mean * rstd;
  #pragma unroll
  for (int k = 0; k < 14; ++k) {
    u32x4 w;
    #pragma unroll
    for (int e = 0; e < 4; ++e) w[e] = cvtpk(fmaf(bflo(xr[k][e]), rstd, nm), fmaf(bfhi(xr[k][e]), rstd, nm));
    *(u32x4*)(ws + WS_XN + ((size_t)(b * 14u + k) * 64u + lane) * 16u) = w;
  }
  TS(0, 1);
}

__global__ __launch_bounds__(256, 1) void k_main(const u16* __restrict__ K, const u16* __restrict__ V, const u16* __restrict__ WQ,
                                                 const u16* __restrict__ WO, const u16* __restrict__ WSP, const u16* __restrict__ BSP,
                                                 const u16* __restrict__ WI, const u16* __restrict__ WG, const u16* __restrict__ X,
                                                 char* __restrict__ ws, u16* __restrict__ out) {
  extern __shared__ __attribute__((aligned(16))) char lds[];
  const unsigned tid = threadIdx.x, lane = tid & 63u, wid = rfl(tid >> 6), c = lane & 15u, g = lane >> 4;
  const unsigned b = blockIdx.x, xcd = b & 7u, idx = b >> 3, head = 2u * xcd + (idx & 1u), qblk = idx >> 1;
  const unsigned ldsb = (unsigned)(size_t)lds;
  TS(1, 0);
  const bool has_s1 = head < 14u;
  const unsigned row0 = qblk * 128u + wid * 32u;
  const unsigned k7 = (lane >> 2) & 7u;
  const unsigned vchunk = (lane >> 5) * 64u + (((lane & 3u) ^ ((k7 >> 2) << 1)) * 16u);
  const unsigned prow = 8u * (k7 >> 2) + (k7 & 3u);
  unsigned wqs = prow * 8192u + head * 512u + wid * 128u + vchunk;
  unsigned wis = (prow + 4u * (wid >> 1)) * 3584u + head * 256u + (wid & 1u) * 128u + vchunk;
  const unsigned xs = (row0 >> 4) * 14336u + lane * 16u;
  __amdgpu_buffer_rsrc_t srdq = __builtin_amdgcn_make_buffer_rsrc((void*)WQ, (short)0, 448 * 4096 * 2, SRD3);
  __amdgpu_buffer_rsrc_t srdi = __builtin_amdgcn_make_buffer_rsrc((void*)WI, (short)0, 448 * 1792 * 2, SRD3);
  __amdgpu_buffer_rsrc_t srdx = __builtin_amdgcn_make_buffer_rsrc((void*)(ws + WS_XN), (short)0, 1835008, SRD3);
  __amdgpu_buffer_rsrc_t srdk = __builtin_amdgcn_make_buffer_rsrc((void*)K, (short)0, 16 * 2048 * 512, SRD3);
  __amdgpu_buffer_rsrc_t srdv = __builtin_amdgcn_make_buffer_rsrc((void*)V, (short)0, 16 * 2048 * 512, SRD3);
  __amdgpu_buffer_rsrc_t srdo = __builtin_amdgcn_make_buffer_rsrc((void*)WO, (short)0, 4096 * 448 * 2, SRD3);
  __amdgpu_buffer_rsrc_t srdz = __builtin_amdgcn_make_buffer_rsrc((void*)(ws + WS_Z2N), (short)0, 3670016, SRD3);
  const unsigned m0k = rfl(ldsb + wid * 4096u), m0v = rfl(ldsb + 65536u + wid * 4096u);
  const unsigned m0i = rfl(ldsb + (wid & 1u) * 4096u + (wid >> 1) * 2048u);
  const unsigned swz = (c >> 1) & 7u;
  const unsigned ka0 = ldsb + (c >> 3) * 1024u + (c & 7u) * 128u + ((g ^ swz) * 16u);
  const unsigned ka1 = ldsb + (c >> 3) * 1024u + (c & 7u) * 128u + (((4u + g) ^ swz) * 16u);
  const unsigned vbase = ldsb + (g >> 1) * 1024u + ((g & 1u) * 4u + (c >> 2)) * 64u + (c & 3u) * 8u;
  const unsigned vk0 = vbase + (g & 1u) * 32u, vk1 = vbase + ((g & 1u) ^ 1u) * 32u;
  const unsigned va0 = vk0 + 65536u, va1 = vk1 + 65536u;
  const unsigned hb = head * 2048u * 512u, r7 = lane >> 3;
  unsigned ks0 = hb + r7 * 512u + wid * 128u + (((lane & 7u) ^ (r7 >> 1)) * 16u);
  unsigned ks1 = hb + r7 * 512u + wid * 128u + (((lane & 7u) ^ (4u | (r7 >> 1))) * 16u);
  unsigned vs = hb + k7 * 512u + wid * 128u + vchunk;
  unsigned wos = (head * 256u + wid * 64u + k7) * 896u + vchunk;
  const unsigned m0o = rfl(ldsb + wid * 8192u);
  u32x16 q[4];
  u32x16 zp[2];
  const bool is2 = head >= 7u && has_s1;
  const unsigned fb1 = head >= 7u ? head - 7u : head;
  __amdgpu_buffer_rsrc_t srdp = __builtin_amdgcn_make_buffer_rsrc((void*)WSP, (short)0, 2048 * 2048 * 2, SRD3);
  __amdgpu_buffer_rsrc_t srdws = __builtin_amdgcn_make_buffer_rsrc((void*)ws, (short)0, (int)WS_END, SRD3);
  __amdgpu_buffer_rsrc_t srdg = __builtin_amdgcn_make_buffer_rsrc((void*)WG, (short)0, 896 * 448 * 2, SRD3);
  const bool has_s2 = idx < 28u;
  const unsigned hs2 = 1u - ((idx + 4u) >> 5);
  {
    unsigned dd, oo;
    asm volatile("s_getpc_b64 s[40:41]\n\t.Lkm_pc0:\n\tv_min_u32 %1, .Lkm_code_end-.Lkm_pc0-4, %2\n\ts_mov_b32 m0, %3\n\ts_nop 0\n\tglobal_load_lds_dword %1, s[40:41]"
                 : "=&v"(dd), "=&v"(oo) : "v"((((idx & 1u) * 256u) + tid) * 128u), "s"(rfl(ldsb + 147456u + wid * 256u)) : "s40", "s41", "memory");
  }
  TS(1, 1);
  {
    f32x16 acc[12];
    if (has_s1) { asm volatile(
  "s_mov_b32 s46, 130048\n\t"
  "s_mov_b32 s47, 30720\n\t"
  "s_mov_b32 s48, 160768\n\t"
  "s_mov_b32 s40, 3072\n\t"
  "s_mov_b32 s41, 6144\n\t"
  "s_mov_b32 s42, 9216\n\t"
  "s_mov_b32 s49, 56320\n\t"
  "s_mov_b32 s51, 4096\n\t"
  "s_mov_b32 s52, 8192\n\t"
  "s_mov_b32 s53, 12288\n\t"
  "s_mov_b32 s54, 16384\n\t"
  "s_mov_b32 s55, 20480\n\t"
  "s_mov_b32 s56, 24576\n\t"
  "buffer_load_dwordx4 v[64:67], %[xs], %[srdx], 0 offen offset:0\n\t"
  "buffer_load_dwordx4 v[120:123], %[xs], %[srdx], s53 offen offset:2048\n\t"
  "s_add_u32 m0, %[m0k], 98304\n\t"
  "s_nop 0\n\t"
  "buffer_load_dwordx4 %[wqs], %[srdq], 0 offen offset:0 lds\n\t"
  "buffer_load_dwordx4 %[wqs], %[srdq], s46 offen offset:1024 lds\n\t"
  "buffer_load_dwordx4 %[wqs], %[srdq], s47 offen offset:2048 lds\n\t"
  "buffer_load_dwordx4 %[wqs], %[srdq], s48 offen offset:3072 lds\n\t"
  "v_add_u32 %[wqs], 0x40000, %[wqs]\n\t"
  "s_add_u32 m0, %[m0i], 114688\n\t"
  "s_nop 0\n\t"
  "buffer_load_dwordx4 %[wis], %[srdi], 0 offen offset:0 lds\n\t"
  "buffer_load_dwordx4 %[wis], %[srdi], s49 offen offset:1024 lds\n\t"
  "v_add_u32 %[wis], 114688, %[wis]\n\t"
  "buffer_load_dwordx4 v[68:71], %[xs], %[srdx], 0 offen offset:1024\n\t"
  "buffer_load_dwordx4 v[124:127], %[xs], %[srdx], s53 offen offset:3072\n\t"
  "s_add_u32 m0, %[m0k], 122880\n\t"
  "s_nop 0\n\t"
  "buffer_load_dwordx4 %[wqs], %[srdq], 0 offen offset:0 lds\n\t"
  "buffer_load_dwordx4 %[wqs], %[srdq], s46 offen offset:1024 lds\n\t"
  "buffer_load_dwordx4 %[wqs], %[srdq], s47 offen offset:2048 lds\n\t"
  "buffer_load_dwordx4 %[wqs], %[srdq], s48 offen offset:3072 lds\n\t"
  "v_add_u32 %[wqs], 0x40000, %[wqs]\n\t"
  "s_add_u32 m0, %[m0i], 139264\n\t"
  "s_nop 0\n\t"
  "buffer_load_dwordx4 %[wis], %[srdi], 0 offen offset:0 lds\n\t"
  "buffer_load_dwordx4 %[wis], %[srdi], s49 offen offset:1024 lds\n\t"
  "v_add_u32 %[wis], 114688, %[wis]\n\t"
  "buffer_load_dwordx4 v[72:75], %[xs], %[srdx], 0 offen offset:2048\n\t"
  "buffer_load_dwordx4 v[128:131], %[xs], %[srdx], s54 offen offset:0\n\t"
  "s_add_u32 m0, %[m0k], 0\n\t"
  "s_nop 0\n\t"
  "buffer_load_dwordx4 %[wqs], %[srdq], 0 offen offset:0 lds\n\t"
  "buffer_load_dwordx4 %[wqs], %[srdq], s46 offen offset:1024 lds\n\t"
  "buffer_load_dwordx4 %[wqs], %[srdq], s47 offen offset:2048 lds\n\t"
  "buffer_load_dwordx4 %[wqs], %[srdq], s48 offen offset:3072 lds\n\t"
  "v_add_u32 %[wqs], 0x40000, %[wqs]\n\t"
  "s_add_u32 m0, %[m0i], 16384\n\t"
  "s_nop 0\n\t"
  "buffer_load_dwordx4 %[wis], %[srdi], 0 offen offset:0 lds\n\t"
  "buffer_load_dwordx4 %[wis], %[srdi], s49 offen offset:1024 lds\n\t"
  "v_add_u32 %[wis], 114688, %[wis]\n\t"
  "s_waitcnt vmcnt(16)\n\t"
  "s_barrier\n\t"
  "ds_read_b64_tr_b16 v[176:177], %[va0] offset:32768\n\t"
  "ds_read_b64_tr_b16 v[178:179], %[va0] offset:34816\n\t"
  "ds_read_b64_tr_b16 v[180:181], %[va1] offset:32768\n\t"
  "ds_read_b64_tr_b16 v[182:183], %[va1] offset:34816\n\t"
  "ds_read_b64_tr_b16 v[184:185], %[va0] offset:33280\n\t"
  "ds_read_b64_tr_b16 v[186:187], %[va0] offset:35328\n\t"
  "ds_read_b64_tr_b16 v[188:189], %[va1] offset:33280\n\t"
  "ds_read_b64_tr_b16 v[190:191], %[va1] offset:35328\n\t"
  "ds_read_b64_tr_b16 v[192:193], %[va0] offset:36864\n\t"
  "ds_read_b64_tr_b16 v[194:195], %[va0] offset:38912\n\t"
  "ds_read_b64_tr_b16 v[196:197], %[va1] offset:36864\n\t"
  "ds_read_b64_tr_b16 v[198:199], %[va1] offset:38912\n\t"
  "ds_read_b64_tr_b16 v[200:201], %[va0] offset:37376\n\t"
  "ds_read_b64_tr_b16 v[202:203], %[va0] offset:39424\n\t"
  "ds_read_b64_tr_b16 v[204:205], %[va1] offset:37376\n\t"
  "ds_read_b64_tr_b16 v[206:207], %[va1] offset:39424\n\t"
  "ds_read_b64_tr_b16 v[208:209], %[va0] offset:40960\n\t"
  "ds_read_b64_tr_b16 v[210:211], %[va0] offset:43008\n\t"
  "ds_read_b64_tr_b16 v[212:213], %[va1] offset:40960\n\t"
  "ds_read_b64_tr_b16 v[214:215], %[va1] offset:43008\n\t"
  "ds_read_b64_tr_b16 v[216:217], %[va0] offset:41472\n\t"
  "ds_read_b64_tr_b16 v[218:219], %[va0] offset:43520\n\t"
  "s_waitcnt lgkmcnt(14)\n\t"
  "v_mfma_f32_16x16x32_bf16 a[0:3], v[176:179], v[64:67], 0\n\t"
  "ds_read_b64_tr_b16 v[220:221], %[va1] offset:41472\n\t"
  "ds_read_b64_tr_b16 v[222:223], %[va1] offset:43520\n\t"
  "v_mfma_f32_16x16x32_bf16 a[4:7], v[176:179], v[120:123], 0\n\t"
  "buffer_load_dwordx4 v[76:79], %[xs], %[srdx], 0 offen offset:3072\n\t"
  "v_mfma_f32_16x16x32_bf16 a[8:11], v[180:183], v[64:67], 0\n\t"
  "ds_read_b64_tr_b16 v[224:225], %[va0] offset:45056\n\t"
  "ds_read_b64_tr_b16 v[226:227], %[va0] offset:47104\n\t"
  "v_mfma_f32_16x16x32_bf16 a[12:15], v[180:183], v[120:123], 0\n\t"
  "v_mfma_f32_16x16x32_bf16 a[16:19], v[184:187], v[64:67], 0\n\t"
  "ds_read_b64_tr_b16 v[228:229], %[va1] offset:45056\n\t"
  "ds_read_b64_tr_b16 v[230:231], %[va1] offset:47104\n\t"
  "v_mfma_f32_16x16x32_bf16 a[20:23], v[184:187], v[120:123], 0\n\t"
  "buffer_load_dwordx4 v[132:135], %[xs], %[srdx], s54 offen offset:1024\n\t"
  "s_add_u32 m0, %[m0k], 24576\n\t"
  "v_mfma_f32_16x16x32_bf16 a[24:27], v[188:191], v[64:67], 0\n\t"
  "ds_read_b64_tr_b16 v[232:233], %[va0] offset:45568\n\t"
  "ds_read_b64_tr_b16 v[234:235], %[va0] offset:47616\n\t"
  "v_mfma_f32_16x16x32_bf16 a[28:31], v[188:191], v[120:123], 0\n\t"
  "buffer_load_dwordx4 %[wqs], %[srdq], 0 offen offset:0 lds\n\t"
  "s_waitcnt lgkmcnt(14)\n\t"
  "v_mfma_f32_16x16x32_bf16 a[32:35], v[192:195], v[64:67], 0\n\t"
  "ds_read_b64_tr_b16 v[236:237], %[va1] offset:45568\n\t"
  "ds_read_b64_tr_b16 v[238:239], %[va1] offset:47616\n\t"
  "v_mfma_f32_16x16x32_bf16 a[36:39], v[192:195], v[120:123], 0\n\t"
  "v_mfma_f32_16x16x32_bf16 a[40:43], v[196:199], v[64:67], 0\n\t"
  "ds_read_b64_tr_b16 v[176:177], %[va0] offset:49152\n\t"
  "ds_read_b64_tr_b16 v[178:179], %[va0] offset:51200\n\t"
  "v_mfma_f32_16x16x32_bf16 a[44:47], v[196:199], v[120:123], 0\n\t"
  "buffer_load_dwordx4 %[wqs], %[srdq], s46 offen offset:1024 lds\n\t"
  "v_mfma_f32_16x16x32_bf16 a[48:51], v[200:203], v[64:67], 0\n\t"
  "ds_read_b64_tr_b16 v[180:181], %[va1] offset:49152\n\t"
  "ds_read_b64_tr_b16 v[182:183], %[va1] offset:51200\n\t"
  "v_mfma_f32_16x16x32_bf16 a[52:55], v[200:203], v[120:123], 0\n\t"
  "buffer_load_dwordx4 %[wqs], %[srdq], s47 offen offset:2048 lds\n\t"
  "v_mfma_f32_16x16x32_bf16 a[56:59], v[204:207], v[64:67], 0\n\t"
  "ds_read_b64_tr_b16 v[184:185], %[va0] offset:49664\n\t"
  "ds_read_b64_tr_b16 v[186:187], %[va0] offset:51712\n\t"
  "v_mfma_f32_16x16x32_bf16 a[60:63], v[204:207], v[120:123], 0\n\t"
  "buffer_load_dwordx4 %[wqs], %[srdq], s48 offen offset:3072 lds\n\t"
  "v_add_u32 %[wqs], 0x40000, %[wqs]\n\t"
  "s_add_u32 m0, %[m0i], 40960\n\t"
  "s_waitcnt lgkmcnt(14)\n\t"
  "v_mfma_f32_16x16x32_bf16 a[64:67], v[208:211], v[64:67], 0\n\t"
  "ds_read_b64_tr_b16 v[188:189], %[va1] offset:49664\n\t"
  "ds_read_b64_tr_b16 v[190:191], %[va1] offset:51712\n\t"
  "v_mfma_f32_16x16x32_bf16 a[68:71], v[208:211], v[120:123], 0\n\t"
  "v_mfma_f32_16x16x32_bf16 a[72:75], v[212:215], v[64:67], 0\n\t"
  "ds_read_b64_tr_b16 v[192:193], %[va0] offset:53248\n\t"
  "ds_read_b64_tr_b16 v[194:195], %[va0] offset:55296\n\t"
  "v_mfma_f32_16x16x32_bf16 a[76:79], v[212:215], v[120:123], 0\n\t"
  "buffer_load_dwordx4 %[wis], %[srdi], 0 offen offset:0 lds\n\t"
  "v_mfma_f32_16x16x32_bf16 a[80:83], v[216:219], v[64:67], 0\n\t"
  "ds_read_b64_tr_b16 v[196:197], %[va1] offset:53248\n\t"
  "ds_read_b64_tr_b16 v[198:199], %[va1] offset:55296\n\t"
  "v_mfma_f32_16x16x32_bf16 a[84:87], v[216:219], v[120:123], 0\n\t"
  "buffer_load_dwordx4 %[wis], %[srdi], s49 offen offset:1024 lds\n\t"
  "v_add_u32 %[wis], 114688, %[wis]\n\t"
  "v_mfma_f32_16x16x32_bf16 a[88:91], v[220:223], v[64:67], 0\n\t"
  "ds_read_b64_tr_b16 v[200:201], %[va0] offset:53760\n\t"
  "ds_read_b64_tr_b16 v[202:203], %[va0] offset:55808\n\t"
  "v_mfma_f32_16x16x32_bf16 a[92:95], v[220:223], v[120:123], 0\n\t"
  "s_waitcnt lgkmcnt(14)\n\t"
  "v_mfma_f32_16x16x32_bf16 a[96:99], v[224:227], v[64:67], 0\n\t"
  "ds_read_b64_tr_b16 v[204:205], %[va1] offset:53760\n\t"
  "ds_read_b64_tr_b16 v[206:207], %[va1] offset:55808\n\t"
  "v_mfma_f32_16x16x32_bf16 a[100:103], v[224:227], v[120:123], 0\n\t"
  "v_mfma_f32_16x16x32_bf16 a[104:107], v[228:231], v[64:67], 0\n\t"
  "s_waitcnt vmcnt(16)\n\t"
  "s_barrier\n\t"
  "ds_read_b64_tr_b16 v[208:209], %[va0] offset:57344\n\t"
  "ds_read_b64_tr_b16 v[210:211], %[va0] offset:59392\n\t"
  "v_mfma_f32_16x16x32_bf16 a[108:111], v[228:231], v[120:123], 0\n\t"
  "buffer_load_dwordx4 v[80:83], %[xs], %[srdx], s51 offen offset:0\n\t"
  "v_mfma_f32_16x16x32_bf16 a[112:115], v[232:235], v[64:67], 0\n\t"
  "ds_read_b64_tr_b16 v[212:213], %[va1] offset:57344\n\t"
  "ds_read_b64_tr_b16 v[214:215], %[va1] offset:59392\n\t"
  "v_mfma_f32_16x16x32_bf16 a[116:119], v[232:235], v[120:123], 0\n\t"
  "v_mfma_f32_16x16x32_bf16 a[120:123], v[236:239], v[64:67], 0\n\t"
  "ds_read_b64_tr_b16 v[216:217], %[va0] offset:57856\n\t"
  "ds_read_b64_tr_b16 v[218:219], %[va0] offset:59904\n\t"
  "v_mfma_f32_16x16x32_bf16 a[124:127], v[236:239], v[120:123], 0\n\t"
  "s_waitcnt lgkmcnt(14)\n\t"
  "v_mfma_f32_16x16x32_bf16 a[128:131], v[176:179], v[64:67], 0\n\t"
  "ds_read_b64_tr_b16 v[220:221], %[va1] offset:57856\n\t"
  "ds_read_b64_tr_b16 v[222:223], %[va1] offset:59904\n\t"
  "v_mfma_f32_16x16x32_bf16 a[132:135], v[176:179], v[120:123], 0\n\t"
  "buffer_load_dwordx4 v[136:139], %[xs], %[srdx], s54 offen offset:2048\n\t"
  "s_add_u32 m0, %[m0k], 49152\n\t"
  "v_mfma_f32_16x16x32_bf16 a[136:139], v[180:183], v[64:67], 0\n\t"
  "ds_read_b64_tr_b16 v[224:225], %[va0] offset:61440\n\t"
  "ds_read_b64_tr_b16 v[226:227], %[va0] offset:63488\n\t"
  "v_mfma_f32_16x16x32_bf16 a[140:143], v[180:183], v[120:123], 0\n\t"
  "v_mfma_f32_16x16x32_bf16 a[144:147], v[184:187], v[64:67], 0\n\t"
  "ds_read_b64_tr_b16 v[228:229], %[va1] offset:61440\n\t"
  "ds_read_b64_tr_b16 v[230:231], %[va1] offset:63488\n\t"
  "v_mfma_f32_16x16x32_bf16 a[148:151], v[184:187], v[120:123], 0\n\t"
  "v_mfma_f32_16x16x32_bf16 a[152:155], v[188:191], v[64:67], 0\n\t"
  "ds_read_b64_tr_b16 v[232:233], %[va0] offset:61952\n\t"
  "ds_read_b64_tr_b16 v[234:235], %[va0] offset:64000\n\t"
  "v_mfma_f32_16x16x32_bf16 a[156:159], v[188:191], v[120:123], 0\n\t"
  "buffer_load_dwordx4 %[wqs], %[srdq], 0 offen offset:0 lds\n\t"
  "s_waitcnt lgkmcnt(14)\n\t"
  "v_mfma_f32_16x16x32_bf16 a[160:163], v[192:195], v[64:67], 0\n\t"
  "ds_read_b64_tr_b16 v[236:237], %[va1] offset:61952\n\t"
  "ds_read_b64_tr_b16 v[238:239], %[va1] offset:64000\n\t"
  "v_mfma_f32_16x16x32_bf16 a[164:167], v[192:195], v[120:123], 0\n\t"
  "v_mfma_f32_16x16x32_bf16 a[168:171], v[196:199], v[64:67], 0\n\t"
  "ds_read_b64_tr_b16 v[176:177], %[vz0] offset:0\n\t"
  "ds_read_b64_tr_b16 v[178:179], %[vz0] offset:2048\n\t"
  "v_mfma_f32_16x16x32_bf16 a[172:175], v[196:199], v[120:123], 0\n\t"
  "buffer_load_dwordx4 %[wqs], %[srdq], s46 offen offset:1024 lds\n\t"
  "v_mfma_f32_16x16x32_bf16 a[176:179], v[200:203], v[64:67], 0\n\t"
  "ds_read_b64_tr_b16 v[180:181], %[vz1] offset:0\n\t"
  "ds_read_b64_tr_b16 v[182:183], %[vz1] offset:2048\n\t"
  "v_mfma_f32_16x16x32_bf16 a[180:183], v[200:203], v[120:123], 0\n\t"
  "v_mfma_f32_16x16x32_bf16 a[184:187], v[204:207], v[64:67], 0\n\t"
  "ds_read_b64_tr_b16 v[184:185], %[vz0] offset:512\n\t"
  "ds_read_b64_tr_b16 v[186:187], %[vz0] offset:2560\n\t"
  "v_mfma_f32_16x16x32_bf16 a[188:191], v[204:207], v[120:123], 0\n\t"
  "s_waitcnt lgkmcnt(14)\n\t"
  "v_mfma_f32_16x16x32_bf16 a[0:3], v[208:211], v[68:71], a[0:3]\n\t"
  "ds_read_b64_tr_b16 v[188:189], %[vz1] offset:512\n\t"
  "ds_read_b64_tr_b16 v[190:191], %[vz1] offset:2560\n\t"
  "v_mfma_f32_16x16x32_bf16 a[4:7], v[208:211], v[124:127], a[4:7]\n\t"
  "buffer_load_dwordx4 %[wqs], %[srdq], s47 offen offset:2048 lds\n\t"
  "v_mfma_f32_16x16x32_bf16 a[8:11], v[212:215], v[68:71], a[8:11]\n\t"
  "ds_read_b64_tr_b16 v[192:193], %[vz0] offset:4096\n\t"
  "ds_read_b64_tr_b16 v[194:195], %[vz0] offset:6144\n\t"
  "v_mfma_f32_16x16x32_bf16 a[12:15], v[212:215], v[124:127], a[12:15]\n\t"
  "v_mfma_f32_16x16x32_bf16 a[16:19], v[216:219], v[68:71], a[16:19]\n\t"
  "ds_read_b64_tr_b16 v[196:197], %[vz1] offset:4096\n\t"
  "ds_read_b64_tr_b16 v[198:199], %[vz1] offset:6144\n\t"
  "v_mfma_f32_16x16x32_bf16 a[20:23], v[216:219], v[124:127], a[20:23]\n\t"
  "buffer_load_dwordx4 %[wqs], %[srdq], s48 offen offset:3072 lds\n\t"
  "v_add_u32 %[wqs], 0x40000, %[wqs]\n\t"
  "s_add_u32 m0, %[m0i], 65536\n\t"
  "v_mfma_f32_16x16x32_bf16 a[24:27], v[220:223], v[68:71], a[24:27]\n\t"
  "ds_read_b64_tr_b16 v[200:201], %[vz0] offset:4608\n\t"
  "ds_read_b64_tr_b16 v[202:203], %[vz0] offset:6656\n\t"
  "v_mfma_f32_16x16x32_bf16 a[28:31], v[220:223], v[124:127], a[28:31]\n\t"
  "s_waitcnt lgkmcnt(14)\n\t"
  "v_mfma_f32_16x16x32_bf16 a[32:35], v[224:227], v[68:71], a[32:35]\n\t"
  "ds_read_b64_tr_b16 v[204:205], %[vz1] offset:4608\n\t"
  "ds_read_b64_tr_b16 v[206:207], %[vz1] offset:6656\n\t"
  "v_mfma_f32_16x16x32_bf16 a[36:39], v[224:227], v[124:127], a[36:39]\n\t"
  "v_mfma_f32_16x16x32_bf16 a[40:43], v[228:231], v[68:71], a[40:43]\n\t"
  "ds_read_b64_tr_b16 v[208:209], %[vz0] offset:8192\n\t"
  "ds_read_b64_tr_b16 v[210:211], %[vz0] offset:10240\n\t"
  "v_mfma_f32_16x16x32_bf16 a[44:47], v[228:231], v[124:127], a[44:47]\n\t"
  "buffer_load_dwordx4 %[wis], %[srdi], 0 offen offset:0 lds\n\t"
  "v_mfma_f32_16x16x32_bf16 a[48:51], v[232:235], v[68:71], a[48:51]\n\t"
  "ds_read_b64_tr_b16 v[212:213], %[vz1] offset:8192\n\t"
  "ds_read_b64_tr_b16 v[214:215], %[vz1] offset:10240\n\t"
  "v_mfma_f32_16x16x32_bf16 a[52:55], v[232:235], v[124:127], a[52:55]\n\t"
  "v_mfma_f32_16x16x32_bf16 a[56:59], v[236:239], v[68:71], a[56:59]\n\t"
  "ds_read_b64_tr_b16 v[216:217], %[vz0] offset:8704\n\t"
  "ds_read_b64_tr_b16 v[218:219], %[vz0] offset:10752\n\t"
  "v_mfma_f32_16x16x32_bf16 a[60:63], v[236:239], v[124:127], a[60:63]\n\t"
  "s_waitcnt lgkmcnt(14)\n\t"
  "v_mfma_f32_16x16x32_bf16 a[64:67], v[176:179], v[68:71], a[64:67]\n\t"
  "ds_read_b64_tr_b16 v[220:221], %[vz1] offset:8704\n\t"
  "ds_read_b64_tr_b16 v[222:223], %[vz1] offset:10752\n\t"
  "v_mfma_f32_16x16x32_bf16 a[68:71], v[176:179], v[124:127], a[68:71]\n\t"
  "buffer_load_dwordx4 %[wis], %[srdi], s49 offen offset:1024 lds\n\t"
  "v_add_u32 %[wis], 114688, %[wis]\n\t"
  "v_mfma_f32_16x16x32_bf16 a[72:75], v[180:183], v[68:71], a[72:75]\n\t"
  "ds_read_b64_tr_b16 v[224:225], %[vz0] offset:12288\n\t"
  "ds_read_b64_tr_b16 v[226:227], %[vz0] offset:14336\n\t"
  "v_mfma_f32_16x16x32_bf16 a[76:79], v[180:183], v[124:127], a[76:79]\n\t"
  "v_mfma_f32_16x16x32_bf16 a[80:83], v[184:187], v[68:71], a[80:83]\n\t"
  "ds_read_b64_tr_b16 v[228:229], %[vz1] offset:12288\n\t"
  "ds_read_b64_tr_b16 v[230:231], %[vz1] offset:14336\n\t"
  "v_mfma_f32_16x16x32_bf16 a[84:87], v[184:187], v[124:127], a[84:87]\n\t"
  "v_mfma_f32_16x16x32_bf16 a[88:91], v[188:191], v[68:71], a[88:91]\n\t"
  "ds_read_b64_tr_b16 v[232:233], %[vz0] offset:12800\n\t"
  "ds_read_b64_tr_b16 v[234:235], %[vz0] offset:14848\n\t"
  "v_mfma_f32_16x16x32_bf16 a[92:95], v[188:191], v[124:127], a[92:95]\n\t"
  "s_waitcnt lgkmcnt(14)\n\t"
  "v_mfma_f32_16x16x32_bf16 a[96:99], v[192:195], v[68:71], a[96:99]\n\t"
  "ds_read_b64_tr_b16 v[236:237], %[vz1] offset:12800\n\t"
  "ds_read_b64_tr_b16 v[238:239], %[vz1] offset:14848\n\t"
  "v_mfma_f32_16x16x32_bf16 a[100:103], v[192:195], v[124:127], a[100:103]\n\t"
  "v_mfma_f32_16x16x32_bf16 a[104:107], v[196:199], v[68:71], a[104:107]\n\t"
  "s_waitcnt vmcnt(16)\n\t"
  "s_barrier\n\t"
  "ds_read_b64_tr_b16 v[176:177], %[vk0] offset:0\n\t"
  "ds_read_b64_tr_b16 v[178:179], %[vk0] offset:2048\n\t"
  "v_mfma_f32_16x16x32_bf16 a[108:111], v[196:199], v[124:127], a[108:111]\n\t"
  "buffer_load_dwordx4 v[84:87], %[xs], %[srdx], s51 offen offset:1024\n\t"
  "v_mfma_f32_16x16x32_bf16 a[112:115], v[200:203], v[68:71], a[112:115]\n\t"
  "ds_read_b64_tr_b16 v[180:181], %[vk1] offset:0\n\t"
  "ds_read_b64_tr_b16 v[182:183], %[vk1] offset:2048\n\t"
  "v_mfma_f32_16x16x32_bf16 a[116:119], v[200:203], v[124:127], a[116:119]\n\t"
  "v_mfma_f32_16x16x32_bf16 a[120:123], v[204:207], v[68:71], a[120:123]\n\t"
  "ds_read_b64_tr_b16 v[184:185], %[vk0] offset:512\n\t"
  "ds_read_b64_tr_b16 v[186:187], %[vk0] offset:2560\n\t"
  "v_mfma_f32_16x16x32_bf16 a[124:127], v[204:207], v[124:127], a[124:127]\n\t"
  "s_waitcnt lgkmcnt(14)\n\t"
  "v_mfma_f32_16x16x32_bf16 a[128:131], v[208:211], v[68:71], a[128:131]\n\t"
  "ds_read_b64_tr_b16 v[188:189], %[vk1] offset:512\n\t"
  "ds_read_b64_tr_b16 v[190:191], %[vk1] offset:2560\n\t"
  "v_mfma_f32_16x16x32_bf16 a[132:135], v[208:211], v[124:127], a[132:135]\n\t"
  "buffer_load_dwordx4 v[140:143], %[xs], %[srdx], s54 offen offset:3072\n\t"
  "s_add_u32 m0, %[m0k], 73728\n\t"
  "v_mfma_f32_16x16x32_bf16 a[136:139], v[212:215], v[68:71], a[136:139]\n\t"
  "ds_read_b64_tr_b16 v[192:193], %[vk0] offset:4096\n\t"
  "ds_read_b64_tr_b16 v[194:195], %[vk0] offset:6144\n\t"
  "v_mfma_f32_16x16x32_bf16 a[140:143], v[212:215], v[124:127], a[140:143]\n\t"
  "v_mfma_f32_16x16x32_bf16 a[144:147], v[216:219], v[68:71], a[144:147]\n\t"
  "ds_read_b64_tr_b16 v[196:197], %[vk1] offset:4096\n\t"
  "ds_read_b64_tr_b16 v[198:199], %[vk1] offset:6144\n\t"
  "v_mfma_f32_16x16x32_bf16 a[148:151], v[216:219], v[124:127], a[148:151]\n\t"
  "v_mfma_f32_16x16x32_bf16 a[152:155], v[220:223], v[68:71], a[152:155]\n\t"
  "ds_read_b64_tr_b16 v[200:201], %[vk0] offset:4608\n\t"
  "ds_read_b64_tr_b16 v[202:203], %[vk0] offset:6656\n\t"
  "v_mfma_f32_16x16x32_bf16 a[156:159], v[220:223], v[124:127], a[156:159]\n\t"
  "buffer_load_dwordx4 %[wqs], %[srdq], 0 offen offset:0 lds\n\t"
  "s_waitcnt lgkmcnt(14)\n\t"
  "v_mfma_f32_16x16x32_bf16 a[160:163], v[224:227], v[68:71], a[160:163]\n\t"
  "ds_read_b64_tr_b16 v[204:205], %[vk1] offset:4608\n\t"
  "ds_read_b64_tr_b16 v[206:207], %[vk1] offset:6656\n\t"
  "v_mfma_f32_16x16x32_bf16 a[164:167], v[224:227], v[124:127], a[164:167]\n\t"
  "v_mfma_f32_16x16x32_bf16 a[168:171], v[228:231], v[68:71], a[168:171]\n\t"
  "ds_read_b64_tr_b16 v[208:209], %[vk0] offset:8192\n\t"
  "ds_read_b64_tr_b16 v[210:211], %[vk0] offset:10240\n\t"
  "v_mfma_f32_16x16x32_bf16 a[172:175], v[228:231], v[124:127], a[172:175]\n\t"
  "buffer_load_dwordx4 %[wqs], %[srdq], s46 offen offset:1024 lds\n\t"
  "v_mfma_f32_16x16x32_bf16 a[176:179], v[232:235], v[68:71], a[176:179]\n\t"
  "ds_read_b64_tr_b16 v[212:213], %[vk1] offset:8192\n\t"
  "ds_read_b64_tr_b16 v[214:215], %[vk1] offset:10240\n\t"
  "v_mfma_f32_16x16x32_bf16 a[180:183], v[232:235], v[124:127], a[180:183]\n\t"
  "v_mfma_f32_16x16x32_bf16 a[184:187], v[236:239], v[68:71], a[184:187]\n\t"
  "ds_read_b64_tr_b16 v[216:217], %[vk0] offset:8704\n\t"
  "ds_read_b64_tr_b16 v[218:219], %[vk0] offset:10752\n\t"
  "v_mfma_f32_16x16x32_bf16 a[188:191], v[236:239], v[124:127], a[188:191]\n\t"
  "s_waitcnt lgkmcnt(14)\n\t"
  "v_mfma_f32_16x16x32_bf16 a[0:3], v[176:179], v[72:75], a[0:3]\n\t"
  "ds_read_b64_tr_b16 v[220:221], %[vk1] offset:8704\n\t"
  "ds_read_b64_tr_b16 v[222:223], %[vk1] offset:10752\n\t"
  "v_mfma_f32_16x16x32_bf16 a[4:7], v[176:179], v[128:131], a[4:7]\n\t"
  "buffer_load_dwordx4 %[wqs], %[srdq], s47 offen offset:2048 lds\n\t"
  "v_mfma_f32_16x16x32_bf16 a[8:11], v[180:183], v[72:75], a[8:11]\n\t"
  "ds_read_b64_tr_b16 v[224:225], %[vk0] offset:12288\n\t"
  "ds_read_b64_tr_b16 v[226:227], %[vk0] offset:14336\n\t"
  "v_mfma_f32_16x16x32_bf16 a[12:15], v[180:183], v[128:131], a[12:15]\n\t"
  "v_mfma_f32_16x16x32_bf16 a[16:19], v[184:187], v[72:75], a[16:19]\n\t"
  "ds_read_b64_tr_b16 v[228:229], %[vk1] offset:12288\n\t"
  "ds_read_b64_tr_b16 v[230:231], %[vk1] offset:14336\n\t"
  "v_mfma_f32_16x16x32_bf16 a[20:23], v[184:187], v[128:131], a[20:23]\n\t"
  "buffer_load_dwordx4 %[wqs], %[srdq], s48 offen offset:3072 lds\n\t"
  "v_add_u32 %[wqs], 0x40000, %[wqs]\n\t"
  "s_add_u32 m0, %[m0i], 90112\n\t"
  "v_mfma_f32_16x16x32_bf16 a[24:27], v[188:191], v[72:75], a[24:27]\n\t"
  "ds_read_b64_tr_b16 v[232:233], %[vk0] offset:12800\n\t"
  "ds_read_b64_tr_b16 v[234:235], %[vk0] offset:14848\n\t"
  "v_mfma_f32_16x16x32_bf16 a[28:31], v[188:191], v[128:131], a[28:31]\n\t"
  "s_waitcnt lgkmcnt(14)\n\t"
  "v_mfma_f32_16x16x32_bf16 a[32:35], v[192:195], v[72:75], a[32:35]\n\t"
  "ds_read_b64_tr_b16 v[236:237], %[vk1] offset:12800\n\t"
  "ds_read_b64_tr_b16 v[238:239], %[vk1] offset:14848\n\t"
  "v_mfma_f32_16x16x32_bf16 a[36:39], v[192:195], v[128:131], a[36:39]\n\t"
  "v_mfma_f32_16x16x32_bf16 a[40:43], v[196:199], v[72:75], a[40:43]\n\t"
  "ds_read_b64_tr_b16 v[176:177], %[vk0] offset:16384\n\t"
  "ds_read_b64_tr_b16 v[178:179], %[vk0] offset:18432\n\t"
  "v_mfma_f32_16x16x32_bf16 a[44:47], v[196:199], v[128:131], a[44:47]\n\t"
  "buffer_load_dwordx4 %[wis], %[srdi], 0 offen offset:0 lds\n\t"
  "v_mfma_f32_16x16x32_bf16 a[48:51], v[200:203], v[72:75], a[48:51]\n\t"
  "ds_read_b64_tr_b16 v[180:181], %[vk1] offset:16384\n\t"
  "ds_read_b64_tr_b16 v[182:183], %[vk1] offset:18432\n\t"
  "v_mfma_f32_16x16x32_bf16 a[52:55], v[200:203], v[128:131], a[52:55]\n\t"
  "v_mfma_f32_16x16x32_bf16 a[56:59], v[204:207], v[72:75], a[56:59]\n\t"
  "ds_read_b64_tr_b16 v[184:185], %[vk0] offset:16896\n\t"
  "ds_read_b64_tr_b16 v[186:187], %[vk0] offset:18944\n\t"
  "v_mfma_f32_16x16x32_bf16 a[60:63], v[204:207], v[128:131], a[60:63]\n\t"
  "s_waitcnt lgkmcnt(14)\n\t"
  "v_mfma_f32_16x16x32_bf16 a[64:67], v[208:211], v[72:75], a[64:67]\n\t"
  "ds_read_b64_tr_b16 v[188:189], %[vk1] offset:16896\n\t"
  "ds_read_b64_tr_b16 v[190:191], %[vk1] offset:18944\n\t"
  "v_mfma_f32_16x16x32_bf16 a[68:71], v[208:211], v[128:131], a[68:71]\n\t"
  "buffer_load_dwordx4 %[wis], %[srdi], s49 offen offset:1024 lds\n\t"
  "v_add_u32 %[wis], 114688, %[wis]\n\t"
  "v_mfma_f32_16x16x32_bf16 a[72:75], v[212:215], v[72:75], a[72:75]\n\t"
  "ds_read_b64_tr_b16 v[192:193], %[vk0] offset:20480\n\t"
  "ds_read_b64_tr_b16 v[194:195], %[vk0] offset:22528\n\t"
  "v_mfma_f32_16x16x32_bf16 a[76:79], v[212:215], v[128:131], a[76:79]\n\t"
  "v_mfma_f32_16x16x32_bf16 a[80:83], v[216:219], v[72:75], a[80:83]\n\t"
  "ds_read_b64_tr_b16 v[196:197], %[vk1] offset:20480\n\t"
  "ds_read_b64_tr_b16 v[198:199], %[vk1] offset:22528\n\t"
  "v_mfma_f32_16x16x32_bf16 a[84:87], v[216:219], v[128:131], a[84:87]\n\t"
  "v_mfma_f32_16x16x32_bf16 a[88:91], v[220:223], v[72:75], a[88:91]\n\t"
  "ds_read_b64_tr_b16 v[200:201], %[vk0] offset:20992\n\t"
  "ds_read_b64_tr_b16 v[202:203], %[vk0] offset:23040\n\t"
  "v_mfma_f32_16x16x32_bf16 a[92:95], v[220:223], v[128:131], a[92:95]\n\t"
  "s_waitcnt lgkmcnt(14)\n\t"
  "v_mfma_f32_16x16x32_bf16 a[96:99], v[224:227], v[72:75], a[96:99]\n\t"
  "ds_read_b64_tr_b16 v[204:205], %[vk1] offset:20992\n\t"
  "ds_read_b64_tr_b16 v[206:207], %[vk1] offset:23040\n\t"
  "v_mfma_f32_16x16x32_bf16 a[100:103], v[224:227], v[128:131], a[100:103]\n\t"
  "v_mfma_f32_16x16x32_bf16 a[104:107], v[228:231], v[72:75], a[104:107]\n\t"
  "s_waitcnt vmcnt(16)\n\t"
  "s_barrier\n\t"
  "ds_read_b64_tr_b16 v[208:209], %[vk0] offset:24576\n\t"
  "ds_read_b64_tr_b16 v[210:211], %[vk0] offset:26624\n\t"
  "v_mfma_f32_16x16x32_bf16 a[108:111], v[228:231], v[128:131], a[108:111]\n\t"
  "buffer_load_dwordx4 v[88:91], %[xs], %[srdx], s51 offen offset:2048\n\t"
  "v_mfma_f32_16x16x32_bf16 a[112:115], v[232:235], v[72:75], a[112:115]\n\t"
  "ds_read_b64_tr_b16 v[212:213], %[vk1] offset:24576\n\t"
  "ds_read_b64_tr_b16 v[214:215], %[vk1] offset:26624\n\t"
  "v_mfma_f32_16x16x32_bf16 a[116:119], v[232:235], v[128:131], a[116:119]\n\t"
  "v_mfma_f32_16x16x32_bf16 a[120:123], v[236:239], v[72:75], a[120:123]\n\t"
  "ds_read_b64_tr_b16 v[216:217], %[vk0] offset:25088\n\t"
  "ds_read_b64_tr_b16 v[218:219], %[vk0] offset:27136\n\t"
  "v_mfma_f32_16x16x32_bf16 a[124:127], v[236:239], v[128:131], a[124:127]\n\t"
  "s_waitcnt lgkmcnt(14)\n\t"
  "v_mfma_f32_16x16x32_bf16 a[128:131], v[176:179], v[72:75], a[128:131]\n\t"
  "ds_read_b64_tr_b16 v[220:221], %[vk1] offset:25088\n\t"
  "ds_read_b64_tr_b16 v[222:223], %[vk1] offset:27136\n\t"
  "v_mfma_f32_16x16x32_bf16 a[132:135], v[176:179], v[128:131], a[132:135]\n\t"
  "buffer_load_dwordx4 v[144:147], %[xs], %[srdx], s55 offen offset:0\n\t"
  "s_add_u32 m0, %[m0k], 98304\n\t"
  "v_mfma_f32_16x16x32_bf16 a[136:139], v[180:183], v[72:75], a[136:139]\n\t"
  "ds_read_b64_tr_b16 v[224:225], %[vk0] offset:28672\n\t"
  "ds_read_b64_tr_b16 v[226:227], %[vk0] offset:30720\n\t"
  "v_mfma_f32_16x16x32_bf16 a[140:143], v[180:183], v[128:131], a[140:143]\n\t"
  "v_mfma_f32_16x16x32_bf16 a[144:147], v[184:187], v[72:75], a[144:147]\n\t"
  "ds_read_b64_tr_b16 v[228:229], %[vk1] offset:28672\n\t"
  "ds_read_b64_tr_b16 v[230:231], %[vk1] offset:30720\n\t"
  "v_mfma_f32_16x16x32_bf16 a[148:151], v[184:187], v[128:131], a[148:151]\n\t"
  "v_mfma_f32_16x16x32_bf16 a[152:155], v[188:191], v[72:75], a[152:155]\n\t"
  "ds_read_b64_tr_b16 v[232:233], %[vk0] offset:29184\n\t"
  "ds_read_b64_tr_b16 v[234:235], %[vk0] offset:31232\n\t"
  "v_mfma_f32_16x16x32_bf16 a[156:159], v[188:191], v[128:131], a[156:159]\n\t"
  "buffer_load_dwordx4 %[wqs], %[srdq], 0 offen offset:0 lds\n\t"
  "s_waitcnt lgkmcnt(14)\n\t"
  "v_mfma_f32_16x16x32_bf16 a[160:163], v[192:195], v[72:75], a[160:163]\n\t"
  "ds_read_b64_tr_b16 v[236:237], %[vk1] offset:29184\n\t"
  "ds_read_b64_tr_b16 v[238:239], %[vk1] offset:31232\n\t"
  "v_mfma_f32_16x16x32_bf16 a[164:167], v[192:195], v[128:131], a[164:167]\n\t"
  "v_mfma_f32_16x16x32_bf16 a[168:171], v[196:199], v[72:75], a[168:171]\n\t"
  "ds_read_b64_tr_b16 v[176:177], %[vk0] offset:32768\n\t"
  "ds_read_b64_tr_b16 v[178:179], %[vk0] offset:34816\n\t"
  "v_mfma_f32_16x16x32_bf16 a[172:175], v[196:199], v[128:131], a[172:175]\n\t"
  "buffer_load_dwordx4 %[wqs], %[srdq], s46 offen offset:1024 lds\n\t"
  "v_mfma_f32_16x16x32_bf16 a[176:179], v[200:203], v[72:75], a[176:179]\n\t"
  "ds_read_b64_tr_b16 v[180:181], %[vk1] offset:32768\n\t"
  "ds_read_b64_tr_b16 v[182:183], %[vk1] offset:34816\n\t"
  "v_mfma_f32_16x16x32_bf16 a[180:183], v[200:203], v[128:131], a[180:183]\n\t"
  "v_mfma_f32_16x16x32_bf16 a[184:187], v[204:207], v[72:75], a[184:187]\n\t"
  "ds_read_b64_tr_b16 v[184:185], %[vk0] offset:33280\n\t"
  "ds_read_b64_tr_b16 v[186:187], %[vk0] offset:35328\n\t"
  "v_mfma_f32_16x16x32_bf16 a[188:191], v[204:207], v[128:131], a[188:191]\n\t"
  "s_waitcnt lgkmcnt(14)\n\t"
  "v_mfma_f32_16x16x32_bf16 a[0:3], v[208:211], v[76:79], a[0:3]\n\t"
  "ds_read_b64_tr_b16 v[188:189], %[vk1] offset:33280\n\t"
  "ds_read_b64_tr_b16 v[190:191], %[vk1] offset:35328\n\t"
  "v_mfma_f32_16x16x32_bf16 a[4:7], v[208:211], v[132:135], a[4:7]\n\t"
  "buffer_load_dwordx4 %[wqs], %[srdq], s47 offen offset:2048 lds\n\t"
  "v_mfma_f32_16x16x32_bf16 a[8:11], v[212:215], v[76:79], a[8:11]\n\t"
  "ds_read_b64_tr_b16 v[192:193], %[vk0] offset:36864\n\t"
  "ds_read_b64_tr_b16 v[194:195], %[vk0] offset:38912\n\t"
  "v_mfma_f32_16x16x32_bf16 a[12:15], v[212:215], v[132:135], a[12:15]\n\t"
  "v_mfma_f32_16x16x32_bf16 a[16:19], v[216:219], v[76:79], a[16:19]\n\t"
  "ds_read_b64_tr_b16 v[196:197], %[vk1] offset:36864\n\t"
  "ds_read_b64_tr_b16 v[198:199], %[vk1] offset:38912\n\t"
  "v_mfma_f32_16x16x32_bf16 a[20:23], v[216:219], v[132:135], a[20:23]\n\t"
  "buffer_load_dwordx4 %[wqs], %[srdq], s48 offen offset:3072 lds\n\t"
  "v_add_u32 %[wqs], 0x40000, %[wqs]\n\t"
  "s_add_u32 m0, %[m0i], 114688\n\t"
  "v_mfma_f32_16x16x32_bf16 a[24:27], v[220:223], v[76:79], a[24:27]\n\t"
  "ds_read_b64_tr_b16 v[200:201], %[vk0] offset:37376\n\t"
  "ds_read_b64_tr_b16 v[202:203], %[vk0] offset:39424\n\t"
  "v_mfma_f32_16x16x32_bf16 a[28:31], v[220:223], v[132:135], a[28:31]\n\t"
  "s_waitcnt lgkmcnt(14)\n\t"
  "v_mfma_f32_16x16x32_bf16 a[32:35], v[224:227], v[76:79], a[32:35]\n\t"
  "ds_read_b64_tr_b16 v[204:205], %[vk1] offset:37376\n\t"
  "ds_read_b64_tr_b16 v[206:207], %[vk1] offset:39424\n\t"
  "v_mfma_f32_16x16x32_bf16 a[36:39], v[224:227], v[132:135], a[36:39]\n\t"
  "v_mfma_f32_16x16x32_bf16 a[40:43], v[228:231], v[76:79], a[40:43]\n\t"
  "ds_read_b64_tr_b16 v[208:209], %[vk0] offset:40960\n\t"
  "ds_read_b64_tr_b16 v[210:211], %[vk0] offset:43008\n\t"
  "v_mfma_f32_16x16x32_bf16 a[44:47], v[228:231], v[132:135], a[44:47]\n\t"
  "buffer_load_dwordx4 %[wis], %[srdi], 0 offen offset:0 lds\n\t"
  "v_mfma_f32_16x16x32_bf16 a[48:51], v[232:235], v[76:79], a[48:51]\n\t"
  "ds_read_b64_tr_b16 v[212:213], %[vk1] offset:40960\n\t"
  "ds_read_b64_tr_b16 v[214:215], %[vk1] offset:43008\n\t"
  "v_mfma_f32_16x16x32_bf16 a[52:55], v[232:235], v[132:135], a[52:55]\n\t"
  "v_mfma_f32_16x16x32_bf16 a[56:59], v[236:239], v[76:79], a[56:59]\n\t"
  "ds_read_b64_tr_b16 v[216:217], %[vk0] offset:41472\n\t"
  "ds_read_b64_tr_b16 v[218:219], %[vk0] offset:43520\n\t"
  "v_mfma_f32_16x16x32_bf16 a[60:63], v[236:239], v[132:135], a[60:63]\n\t"
  "s_waitcnt lgkmcnt(14)\n\t"
  "v_mfma_f32_16x16x32_bf16 a[64:67], v[176:179], v[76:79], a[64:67]\n\t"
  "ds_read_b64_tr_b16 v[220:221], %[vk1] offset:41472\n\t"
  "ds_read_b64_tr_b16 v[222:223], %[vk1] offset:43520\n\t"
  "v_mfma_f32_16x16x32_bf16 a[68:71], v[176:179], v[132:135], a[68:71]\n\t"
  "buffer_load_dwordx4 %[wis], %[srdi], s49 offen offset:1024 lds\n\t"
  "v_add_u32 %[wis], 114688, %[wis]\n\t"
  "v_mfma_f32_16x16x32_bf16 a[72:75], v[180:183], v[76:79], a[72:75]\n\t"
  "ds_read_b64_tr_b16 v[224:225], %[vk0] offset:45056\n\t"
  "ds_read_b64_tr_b16 v[226:227], %[vk0] offset:47104\n\t"
  "v_mfma_f32_16x16x32_bf16 a[76:79], v[180:183], v[132:135], a[76:79]\n\t"
  "v_mfma_f32_16x16x32_bf16 a[80:83], v[184:187], v[76:79], a[80:83]\n\t"
  "ds_read_b64_tr_b16 v[228:229], %[vk1] offset:45056\n\t"
  "ds_read_b64_tr_b16 v[230:231], %[vk1] offset:47104\n\t"
  "v_mfma_f32_16x16x32_bf16 a[84:87], v[184:187], v[132:135], a[84:87]\n\t"
  "v_mfma_f32_16x16x32_bf16 a[88:91], v[188:191], v[76:79], a[88:91]\n\t"
  "ds_read_b64_tr_b16 v[232:233], %[vk0] offset:45568\n\t"
  "ds_read_b64_tr_b16 v[234:235], %[vk0] offset:47616\n\t"
  "v_mfma_f32_16x16x32_bf16 a[92:95], v[188:191], v[132:135], a[92:95]\n\t"
  "s_waitcnt lgkmcnt(14)\n\t"
  "v_mfma_f32_16x16x32_bf16 a[96:99], v[192:195], v[76:79], a[96:99]\n\t"
  "ds_read_b64_tr_b16 v[236:237], %[vk1] offset:45568\n\t"
  "ds_read_b64_tr_b16 v[238:239], %[vk1] offset:47616\n\t"
  "v_mfma_f32_16x16x32_bf16 a[100:103], v[192:195], v[132:135], a[100:103]\n\t"
  "v_mfma_f32_16x16x32_bf16 a[104:107], v[196:199], v[76:79], a[104:107]\n\t"
  "s_waitcnt vmcnt(16)\n\t"
  "s_barrier\n\t"
  "ds_read_b64_tr_b16 v[176:177], %[vk0] offset:49152\n\t"
  "ds_read_b64_tr_b16 v[178:179], %[vk0] offset:51200\n\t"
  "v_mfma_f32_16x16x32_bf16 a[108:111], v[196:199], v[132:135], a[108:111]\n\t"
  "buffer_load_dwordx4 v[92:95], %[xs], %[srdx], s51 offen offset:3072\n\t"
  "v_mfma_f32_16x16x32_bf16 a[112:115], v[200:203], v[76:79], a[112:115]\n\t"
  "ds_read_b64_tr_b16 v[180:181], %[vk1] offset:49152\n\t"
  "ds_read_b64_tr_b16 v[182:183], %[vk1] offset:51200\n\t"
  "v_mfma_f32_16x16x32_bf16 a[116:119], v[200:203], v[132:135], a[116:119]\n\t"
  "v_mfma_f32_16x16x32_bf16 a[120:123], v[204:207], v[76:79], a[120:123]\n\t"
  "ds_read_b64_tr_b16 v[184:185], %[vk0] offset:49664\n\t"
  "ds_read_b64_tr_b16 v[186:187], %[vk0] offset:51712\n\t"
  "v_mfma_f32_16x16x32_bf16 a[124:127], v[204:207], v[132:135], a[124:127]\n\t"
  "s_waitcnt lgkmcnt(14)\n\t"
  "v_mfma_f32_16x16x32_bf16 a[128:131], v[208:211], v[76:79], a[128:131]\n\t"
  "ds_read_b64_tr_b16 v[188:189], %[vk1] offset:49664\n\t"
  "ds_read_b64_tr_b16 v[190:191], %[vk1] offset:51712\n\t"
  "v_mfma_f32_16x16x32_bf16 a[132:135], v[208:211], v[132:135], a[132:135]\n\t"
  "buffer_load_dwordx4 v[148:151], %[xs], %[srdx], s55 offen offset:1024\n\t"
  "s_add_u32 m0, %[m0k], 122880\n\t"
  "v_mfma_f32_16x16x32_bf16 a[136:139], v[212:215], v[76:79], a[136:139]\n\t"
  "ds_read_b64_tr_b16 v[192:193], %[vk0] offset:53248\n\t"
  "ds_read_b64_tr_b16 v[194:195], %[vk0] offset:55296\n\t"
  "v_mfma_f32_16x16x32_bf16 a[140:143], v[212:215], v[132:135], a[140:143]\n\t"
  "v_mfma_f32_16x16x32_bf16 a[144:147], v[216:219], v[76:79], a[144:147]\n\t"
  "ds_read_b64_tr_b16 v[196:197], %[vk1] offset:53248\n\t"
  "ds_read_b64_tr_b16 v[198:199], %[vk1] offset:55296\n\t"
  "v_mfma_f32_16x16x32_bf16 a[148:151], v[216:219], v[132:135], a[148:151]\n\t"
  "v_mfma_f32_16x16x32_bf16 a[152:155], v[220:223], v[76:79], a[152:155]\n\t"
  "ds_read_b64_tr_b16 v[200:201], %[vk0] offset:53760\n\t"
  "ds_read_b64_tr_b16 v[202:203], %[vk0] offset:55808\n\t"
  "v_mfma_f32_16x16x32_bf16 a[156:159], v[220:223], v[132:135], a[156:159]\n\t"
  "buffer_load_dwordx4 %[wqs], %[srdq], 0 offen offset:0 lds\n\t"
  "s_waitcnt lgkmcnt(14)\n\t"
  "v_mfma_f32_16x16x32_bf16 a[160:163], v[224:227], v[76:79], a[160:163]\n\t"
  "ds_read_b64_tr_b16 v[204:205], %[vk1] offset:53760\n\t"
  "ds_read_b64_tr_b16 v[206:207], %[vk1] offset:55808\n\t"
  "v_mfma_f32_16x16x32_bf16 a[164:167], v[224:227], v[132:135], a[164:167]\n\t"
  "v_mfma_f32_16x16x32_bf16 a[168:171], v[228:231], v[76:79], a[168:171]\n\t"
  "ds_read_b64_tr_b16 v[208:209], %[vk0] offset:57344\n\t"
  "ds_read_b64_tr_b16 v[210:211], %[vk0] offset:59392\n\t"
  "v_mfma_f32_16x16x32_bf16 a[172:175], v[228:231], v[132:135], a[172:175]\n\t"
  "buffer_load_dwordx4 %[wqs], %[srdq], s46 offen offset:1024 lds\n\t"
  "v_mfma_f32_16x16x32_bf16 a[176:179], v[232:235], v[76:79], a[176:179]\n\t"
  "ds_read_b64_tr_b16 v[212:213], %[vk1] offset:57344\n\t"
  "ds_read_b64_tr_b16 v[214:215], %[vk1] offset:59392\n\t"
  "v_mfma_f32_16x16x32_bf16 a[180:183], v[232:235], v[132:135], a[180:183]\n\t"
  "v_mfma_f32_16x16x32_bf16 a[184:187], v[236:239], v[76:79], a[184:187]\n\t"
  "ds_read_b64_tr_b16 v[216:217], %[vk0] offset:57856\n\t"
  "ds_read_b64_tr_b16 v[218:219], %[vk0] offset:59904\n\t"
  "v_mfma_f32_16x16x32_bf16 a[188:191], v[236:239], v[132:135], a[188:191]\n\t"
  "s_waitcnt lgkmcnt(14)\n\t"
  "v_mfma_f32_16x16x32_bf16 a[0:3], v[176:179], v[80:83], a[0:3]\n\t"
  "ds_read_b64_tr_b16 v[220:221], %[vk1] offset:57856\n\t"
  "ds_read_b64_tr_b16 v[222:223], %[vk1] offset:59904\n\t"
  "v_mfma_f32_16x16x32_bf16 a[4:7], v[176:179], v[136:139], a[4:7]\n\t"
  "buffer_load_dwordx4 %[wqs], %[srdq], s47 offen offset:2048 lds\n\t"
  "v_mfma_f32_16x16x32_bf16 a[8:11], v[180:183], v[80:83], a[8:11]\n\t"
  "ds_read_b64_tr_b16 v[224:225], %[vk0] offset:61440\n\t"
  "ds_read_b64_tr_b16 v[226:227], %[vk0] offset:63488\n\t"
  "v_mfma_f32_16x16x32_bf16 a[12:15], v[180:183], v[136:139], a[12:15]\n\t"
  "v_mfma_f32_16x16x32_bf16 a[16:19], v[184:187], v[80:83], a[16:19]\n\t"
  "ds_read_b64_tr_b16 v[228:229], %[vk1] offset:61440\n\t"
  "ds_read_b64_tr_b16 v[230:231], %[vk1] offset:63488\n\t"
  "v_mfma_f32_16x16x32_bf16 a[20:23], v[184:187], v[136:139], a[20:23]\n\t"
  "buffer_load_dwordx4 %[wqs], %[srdq], s48 offen offset:3072 lds\n\t"
  "v_add_u32 %[wqs], 0x40000, %[wqs]\n\t"
  "s_add_u32 m0, %[m0i], 139264\n\t"
  "v_mfma_f32_16x16x32_bf16 a[24:27], v[188:191], v[80:83], a[24:27]\n\t"
  "ds_read_b64_tr_b16 v[232:233], %[vk0] offset:61952\n\t"
  "ds_read_b64_tr_b16 v[234:235], %[vk0] offset:64000\n\t"
  "v_mfma_f32_16x16x32_bf16 a[28:31], v[188:191], v[136:139], a[28:31]\n\t"
  "s_waitcnt lgkmcnt(14)\n\t"
  "v_mfma_f32_16x16x32_bf16 a[32:35], v[192:195], v[80:83], a[32:35]\n\t"
  "ds_read_b64_tr_b16 v[236:237], %[vk1] offset:61952\n\t"
  "ds_read_b64_tr_b16 v[238:239], %[vk1] offset:64000\n\t"
  "v_mfma_f32_16x16x32_bf16 a[36:39], v[192:195], v[136:139], a[36:39]\n\t"
  "v_mfma_f32_16x16x32_bf16 a[40:43], v[196:199], v[80:83], a[40:43]\n\t"
  "ds_read_b64_tr_b16 v[176:177], %[va0] offset:0\n\t"
  "ds_read_b64_tr_b16 v[178:179], %[va0] offset:2048\n\t"
  "v_mfma_f32_16x16x32_bf16 a[44:47], v[196:199], v[136:139], a[44:47]\n\t"
  "buffer_load_dwordx4 %[wis], %[srdi], 0 offen offset:0 lds\n\t"
  "v_mfma_f32_16x16x32_bf16 a[48:51], v[200:203], v[80:83], a[48:51]\n\t"
  "ds_read_b64_tr_b16 v[180:181], %[va1] offset:0\n\t"
  "ds_read_b64_tr_b16 v[182:183], %[va1] offset:2048\n\t"
  "v_mfma_f32_16x16x32_bf16 a[52:55], v[200:203], v[136:139], a[52:55]\n\t"
  "v_mfma_f32_16x16x32_bf16 a[56:59], v[204:207], v[80:83], a[56:59]\n\t"
  "ds_read_b64_tr_b16 v[184:185], %[va0] offset:512\n\t"
  "ds_read_b64_tr_b16 v[186:187], %[va0] offset:2560\n\t"
  "v_mfma_f32_16x16x32_bf16 a[60:63], v[204:207], v[136:139], a[60:63]\n\t"
  "s_waitcnt lgkmcnt(14)\n\t"
  "v_mfma_f32_16x16x32_bf16 a[64:67], v[208:211], v[80:83], a[64:67]\n\t"
  "ds_read_b64_tr_b16 v[188:189], %[va1] offset:512\n\t"
  "ds_read_b64_tr_b16 v[190:191], %[va1] offset:2560\n\t"
  "v_mfma_f32_16x16x32_bf16 a[68:71], v[208:211], v[136:139], a[68:71]\n\t"
  "buffer_load_dwordx4 %[wis], %[srdi], s49 offen offset:1024 lds\n\t"
  "v_add_u32 %[wis], 114688, %[wis]\n\t"
  "v_mfma_f32_16x16x32_bf16 a[72:75], v[212:215], v[80:83], a[72:75]\n\t"
  "ds_read_b64_tr_b16 v[192:193], %[va0] offset:4096\n\t"
  "ds_read_b64_tr_b16 v[194:195], %[va0] offset:6144\n\t"
  "v_mfma_f32_16x16x32_bf16 a[76:79], v[212:215], v[136:139], a[76:79]\n\t"
  "v_mfma_f32_16x16x32_bf16 a[80:83], v[216:219], v[80:83], a[80:83]\n\t"
  "ds_read_b64_tr_b16 v[196:197], %[va1] offset:4096\n\t"
  "ds_read_b64_tr_b16 v[198:199], %[va1] offset:6144\n\t"
  "v_mfma_f32_16x16x32_bf16 a[84:87], v[216:219], v[136:139], a[84:87]\n\t"
  "v_mfma_f32_16x16x32_bf16 a[88:91], v[220:223], v[80:83], a[88:91]\n\t"
  "ds_read_b64_tr_b16 v[200:201], %[va0] offset:4608\n\t"
  "ds_read_b64_tr_b16 v[202:203], %[va0] offset:6656\n\t"
  "v_mfma_f32_16x16x32_bf16 a[92:95], v[220:223], v[136:139], a[92:95]\n\t"
  "s_waitcnt lgkmcnt(14)\n\t"
  "v_mfma_f32_16x16x32_bf16 a[96:99], v[224:227], v[80:83], a[96:99]\n\t"
  "ds_read_b64_tr_b16 v[204:205], %[va1] offset:4608\n\t"
  "ds_read_b64_tr_b16 v[206:207], %[va1] offset:6656\n\t"
  "v_mfma_f32_16x16x32_bf16 a[100:103], v[224:227], v[136:139], a[100:103]\n\t"
  "v_mfma_f32_16x16x32_bf16 a[104:107], v[228:231], v[80:83], a[104:107]\n\t"
  "s_waitcnt vmcnt(16)\n\t"
  "s_barrier\n\t"
  "ds_read_b64_tr_b16 v[208:209], %[va0] offset:8192\n\t"
  "ds_read_b64_tr_b16 v[210:211], %[va0] offset:10240\n\t"
  "v_mfma_f32_16x16x32_bf16 a[108:111], v[228:231], v[136:139], a[108:111]\n\t"
  "buffer_load_dwordx4 v[96:99], %[xs], %[srdx], s52 offen offset:0\n\t"
  "v_mfma_f32_16x16x32_bf16 a[112:115], v[232:235], v[80:83], a[112:115]\n\t"
  "ds_read_b64_tr_b16 v[212:213], %[va1] offset:8192\n\t"
  "ds_read_b64_tr_b16 v[214:215], %[va1] offset:10240\n\t"
  "v_mfma_f32_16x16x32_bf16 a[116:119], v[232:235], v[136:139], a[116:119]\n\t"
  "v_mfma_f32_16x16x32_bf16 a[120:123], v[236:239], v[80:83], a[120:123]\n\t"
  "ds_read_b64_tr_b16 v[216:217], %[va0] offset:8704\n\t"
  "ds_read_b64_tr_b16 v[218:219], %[va0] offset:10752\n\t"
  "v_mfma_f32_16x16x32_bf16 a[124:127], v[236:239], v[136:139], a[124:127]\n\t"
  "s_waitcnt lgkmcnt(14)\n\t"
  "v_mfma_f32_16x16x32_bf16 a[128:131], v[176:179], v[80:83], a[128:131]\n\t"
  "ds_read_b64_tr_b16 v[220:221], %[va1] offset:8704\n\t"
  "ds_read_b64_tr_b16 v[222:223], %[va1] offset:10752\n\t"
  "v_mfma_f32_16x16x32_bf16 a[132:135], v[176:179], v[136:139], a[132:135]\n\t"
  "buffer_load_dwordx4 v[152:155], %[xs], %[srdx], s55 offen offset:2048\n\t"
  "s_add_u32 m0, %[m0k], 0\n\t"
  "v_mfma_f32_16x16x32_bf16 a[136:139], v[180:183], v[80:83], a[136:139]\n\t"
  "ds_read_b64_tr_b16 v[224:225], %[va0] offset:12288\n\t"
  "ds_read_b64_tr_b16 v[226:227], %[va0] offset:14336\n\t"
  "v_mfma_f32_16x16x32_bf16 a[140:143], v[180:183], v[136:139], a[140:143]\n\t"
  "v_mfma_f32_16x16x32_bf16 a[144:147], v[184:187], v[80:83], a[144:147]\n\t"
  "ds_read_b64_tr_b16 v[228:229], %[va1] offset:12288\n\t"
  "ds_read_b64_tr_b16 v[230:231], %[va1] offset:14336\n\t"
  "v_mfma_f32_16x16x32_bf16 a[148:151], v[184:187], v[136:139], a[148:151]\n\t"
  "v_mfma_f32_16x16x32_bf16 a[152:155], v[188:191], v[80:83], a[152:155]\n\t"
  "ds_read_b64_tr_b16 v[232:233], %[va0] offset:12800\n\t"
  "ds_read_b64_tr_b16 v[234:235], %[va0] offset:14848\n\t"
  "v_mfma_f32_16x16x32_bf16 a[156:159], v[188:191], v[136:139], a[156:159]\n\t"
  "buffer_load_dwordx4 %[wqs], %[srdq], 0 offen offset:0 lds\n\t"
  "s_waitcnt lgkmcnt(14)\n\t"
  "v_mfma_f32_16x16x32_bf16 a[160:163], v[192:195], v[80:83], a[160:163]\n\t"
  "ds_read_b64_tr_b16 v[236:237], %[va1] offset:12800\n\t"
  "ds_read_b64_tr_b16 v[238:239], %[va1] offset:14848\n\t"
  "v_mfma_f32_16x16x32_bf16 a[164:167], v[192:195], v[136:139], a[164:167]\n\t"
  "v_mfma_f32_16x16x32_bf16 a[168:171], v[196:199], v[80:83], a[168:171]\n\t"
  "ds_read_b64_tr_b16 v[176:177], %[va0] offset:16384\n\t"
  "ds_read_b64_tr_b16 v[178:179], %[va0] offset:18432\n\t"
  "v_mfma_f32_16x16x32_bf16 a[172:175], v[196:199], v[136:139], a[172:175]\n\t"
  "buffer_load_dwordx4 %[wqs], %[srdq], s46 offen offset:1024 lds\n\t"
  "v_mfma_f32_16x16x32_bf16 a[176:179], v[200:203], v[80:83], a[176:179]\n\t"
  "ds_read_b64_tr_b16 v[180:181], %[va1] offset:16384\n\t"
  "ds_read_b64_tr_b16 v[182:183], %[va1] offset:18432\n\t"
  "v_mfma_f32_16x16x32_bf16 a[180:183], v[200:203], v[136:139], a[180:183]\n\t"
  "v_mfma_f32_16x16x32_bf16 a[184:187], v[204:207], v[80:83], a[184:187]\n\t"
  "ds_read_b64_tr_b16 v[184:185], %[va0] offset:16896\n\t"
  "ds_read_b64_tr_b16 v[186:187], %[va0] offset:18944\n\t"
  "v_mfma_f32_16x16x32_bf16 a[188:191], v[204:207], v[136:139], a[188:191]\n\t"
  "s_waitcnt lgkmcnt(14)\n\t"
  "v_mfma_f32_16x16x32_bf16 a[0:3], v[208:211], v[84:87], a[0:3]\n\t"
  "ds_read_b64_tr_b16 v[188:189], %[va1] offset:16896\n\t"
  "ds_read_b64_tr_b16 v[190:191], %[va1] offset:18944\n\t"
  "v_mfma_f32_16x16x32_bf16 a[4:7], v[208:211], v[140:143], a[4:7]\n\t"
  "buffer_load_dwordx4 %[wqs], %[srdq], s47 offen offset:2048 lds\n\t"
  "v_mfma_f32_16x16x32_bf16 a[8:11], v[212:215], v[84:87], a[8:11]\n\t"
  "ds_read_b64_tr_b16 v[192:193], %[va0] offset:20480\n\t"
  "ds_read_b64_tr_b16 v[194:195], %[va0] offset:22528\n\t"
  "v_mfma_f32_16x16x32_bf16 a[12:15], v[212:215], v[140:143], a[12:15]\n\t"
  "v_mfma_f32_16x16x32_bf16 a[16:19], v[216:219], v[84:87], a[16:19]\n\t"
  "ds_read_b64_tr_b16 v[196:197], %[va1] offset:20480\n\t"
  "ds_read_b64_tr_b16 v[198:199], %[va1] offset:22528\n\t"
  "v_mfma_f32_16x16x32_bf16 a[20:23], v[216:219], v[140:143], a[20:23]\n\t"
  "buffer_load_dwordx4 %[wqs], %[srdq], s48 offen offset:3072 lds\n\t"
  "v_add_u32 %[wqs], 0x40000, %[wqs]\n\t"
  "s_add_u32 m0, %[m0i], 16384\n\t"
  "v_mfma_f32_16x16x32_bf16 a[24:27], v[220:223], v[84:87], a[24:27]\n\t"
  "ds_read_b64_tr_b16 v[200:201], %[va0] offset:20992\n\t"
  "ds_read_b64_tr_b16 v[202:203], %[va0] offset:23040\n\t"
  "v_mfma_f32_16x16x32_bf16 a[28:31], v[220:223], v[140:143], a[28:31]\n\t"
  "s_waitcnt lgkmcnt(14)\n\t"
  "v_mfma_f32_16x16x32_bf16 a[32:35], v[224:227], v[84:87], a[32:35]\n\t"
  "ds_read_b64_tr_b16 v[204:205], %[va1] offset:20992\n\t"
  "ds_read_b64_tr_b16 v[206:207], %[va1] offset:23040\n\t"
  "v_mfma_f32_16x16x32_bf16 a[36:39], v[224:227], v[140:143], a[36:39]\n\t"
  "v_mfma_f32_16x16x32_bf16 a[40:43], v[228:231], v[84:87], a[40:43]\n\t"
  "ds_read_b64_tr_b16 v[208:209], %[va0] offset:24576\n\t"
  "ds_read_b64_tr_b16 v[210:211], %[va0] offset:26624\n\t"
  "v_mfma_f32_16x16x32_bf16 a[44:47], v[228:231], v[140:143], a[44:47]\n\t"
  "buffer_load_dwordx4 %[wis], %[srdi], 0 offen offset:0 lds\n\t"
  "v_mfma_f32_16x16x32_bf16 a[48:51], v[232:235], v[84:87], a[48:51]\n\t"
  "ds_read_b64_tr_b16 v[212:213], %[va1] offset:24576\n\t"
  "ds_read_b64_tr_b16 v[214:215], %[va1] offset:26624\n\t"
  "v_mfma_f32_16x16x32_bf16 a[52:55], v[232:235], v[140:143], a[52:55]\n\t"
  "v_mfma_f32_16x16x32_bf16 a[56:59], v[236:239], v[84:87], a[56:59]\n\t"
  "ds_read_b64_tr_b16 v[216:217], %[va0] offset:25088\n\t"
  "ds_read_b64_tr_b16 v[218:219], %[va0] offset:27136\n\t"
  "v_mfma_f32_16x16x32_bf16 a[60:63], v[236:239], v[140:143], a[60:63]\n\t"
  "s_waitcnt lgkmcnt(14)\n\t"
  "v_mfma_f32_16x16x32_bf16 a[64:67], v[176:179], v[84:87], a[64:67]\n\t"
  "ds_read_b64_tr_b16 v[220:221], %[va1] offset:25088\n\t"
  "ds_read_b64_tr_b16 v[222:223], %[va1] offset:27136\n\t"
  "v_mfma_f32_16x16x32_bf16 a[68:71], v[176:179], v[140:143], a[68:71]\n\t"
  "buffer_load_dwordx4 %[wis], %[srdi], s49 offen offset:1024 lds\n\t"
  "v_add_u32 %[wis], 114688, %[wis]\n\t"
  "v_mfma_f32_16x16x32_bf16 a[72:75], v[180:183], v[84:87], a[72:75]\n\t"
  "ds_read_b64_tr_b16 v[224:225], %[va0] offset:28672\n\t"
  "ds_read_b64_tr_b16 v[226:227], %[va0] offset:30720\n\t"
  "v_mfma_f32_16x16x32_bf16 a[76:79], v[180:183], v[140:143], a[76:79]\n\t"
  "v_mfma_f32_16x16x32_bf16 a[80:83], v[184:187], v[84:87], a[80:83]\n\t"
  "ds_read_b64_tr_b16 v[228:229], %[va1] offset:28672\n\t"
  "ds_read_b64_tr_b16 v[230:231], %[va1] offset:30720\n\t"
  "v_mfma_f32_16x16x32_bf16 a[84:87], v[184:187], v[140:143], a[84:87]\n\t"
  "v_mfma_f32_16x16x32_bf16 a[88:91], v[188:191], v[84:87], a[88:91]\n\t"
  "ds_read_b64_tr_b16 v[232:233], %[va0] offset:29184\n\t"
  "ds_read_b64_tr_b16 v[234:235], %[va0] offset:31232\n\t"
  "v_mfma_f32_16x16x32_bf16 a[92:95], v[188:191], v[140:143], a[92:95]\n\t"
  "s_waitcnt lgkmcnt(14)\n\t"
  "v_mfma_f32_16x16x32_bf16 a[96:99], v[192:195], v[84:87], a[96:99]\n\t"
  "ds_read_b64_tr_b16 v[236:237], %[va1] offset:29184\n\t"
  "ds_read_b64_tr_b16 v[238:239], %[va1] offset:31232\n\t"
  "v_mfma_f32_16x16x32_bf16 a[100:103], v[192:195], v[140:143], a[100:103]\n\t"
  "v_mfma_f32_16x16x32_bf16 a[104:107], v[196:199], v[84:87], a[104:107]\n\t"
  "s_waitcnt vmcnt(16)\n\t"
  "s_barrier\n\t"
  "ds_read_b64_tr_b16 v[176:177], %[va0] offset:32768\n\t"
  "ds_read_b64_tr_b16 v[178:179], %[va0] offset:34816\n\t"
  "v_mfma_f32_16x16x32_bf16 a[108:111], v[196:199], v[140:143], a[108:111]\n\t"
  "buffer_load_dwordx4 v[100:103], %[xs], %[srdx], s52 offen offset:1024\n\t"
  "v_mfma_f32_16x16x32_bf16 a[112:115], v[200:203], v[84:87], a[112:115]\n\t"
  "ds_read_b64_tr_b16 v[180:181], %[va1] offset:32768\n\t"
  "ds_read_b64_tr_b16 v[182:183], %[va1] offset:34816\n\t"
  "v_mfma_f32_16x16x32_bf16 a[116:119], v[200:203], v[140:143], a[116:119]\n\t"
  "v_mfma_f32_16x16x32_bf16 a[120:123], v[204:207], v[84:87], a[120:123]\n\t"
  "ds_read_b64_tr_b16 v[184:185], %[va0] offset:33280\n\t"
  "ds_read_b64_tr_b16 v[186:187], %[va0] offset:35328\n\t"
  "v_mfma_f32_16x16x32_bf16 a[124:127], v[204:207], v[140:143], a[124:127]\n\t"
  "s_waitcnt lgkmcnt(14)\n\t"
  "v_mfma_f32_16x16x32_bf16 a[128:131], v[208:211], v[84:87], a[128:131]\n\t"
  "ds_read_b64_tr_b16 v[188:189], %[va1] offset:33280\n\t"
  "ds_read_b64_tr_b16 v[190:191], %[va1] offset:35328\n\t"
  "v_mfma_f32_16x16x32_bf16 a[132:135], v[208:211], v[140:143], a[132:135]\n\t"
  "buffer_load_dwordx4 v[156:159], %[xs], %[srdx], s55 offen offset:3072\n\t"
  "s_add_u32 m0, %[m0k], 24576\n\t"
  "v_mfma_f32_16x16x32_bf16 a[136:139], v[212:215], v[84:87], a[136:139]\n\t"
  "ds_read_b64_tr_b16 v[192:193], %[va0] offset:36864\n\t"
  "ds_read_b64_tr_b16 v[194:195], %[va0] offset:38912\n\t"
  "v_mfma_f32_16x16x32_bf16 a[140:143], v[212:215], v[140:143], a[140:143]\n\t"
  "v_mfma_f32_16x16x32_bf16 a[144:147], v[216:219], v[84:87], a[144:147]\n\t"
  "ds_read_b64_tr_b16 v[196:197], %[va1] offset:36864\n\t"
  "ds_read_b64_tr_b16 v[198:199], %[va1] offset:38912\n\t"
  "v_mfma_f32_16x16x32_bf16 a[148:151], v[216:219], v[140:143], a[148:151]\n\t"
  "v_mfma_f32_16x16x32_bf16 a[152:155], v[220:223], v[84:87], a[152:155]\n\t"
  "ds_read_b64_tr_b16 v[200:201], %[va0] offset:37376\n\t"
  "ds_read_b64_tr_b16 v[202:203], %[va0] offset:39424\n\t"
  "v_mfma_f32_16x16x32_bf16 a[156:159], v[220:223], v[140:143], a[156:159]\n\t"
  "buffer_load_dwordx4 %[wqs], %[srdq], 0 offen offset:0 lds\n\t"
  "s_waitcnt lgkmcnt(14)\n\t"
  "v_mfma_f32_16x16x32_bf16 a[160:163], v[224:227], v[84:87], a[160:163]\n\t"
  "ds_read_b64_tr_b16 v[204:205], %[va1] offset:37376\n\t"
  "ds_read_b64_tr_b16 v[206:207], %[va1] offset:39424\n\t"
  "v_mfma_f32_16x16x32_bf16 a[164:167], v[224:227], v[140:143], a[164:167]\n\t"
  "v_mfma_f32_16x16x32_bf16 a[168:171], v[228:231], v[84:87], a[168:171]\n\t"
  "ds_read_b64_tr_b16 v[208:209], %[va0] offset:40960\n\t"
  "ds_read_b64_tr_b16 v[210:211], %[va0] offset:43008\n\t"
  "v_mfma_f32_16x16x32_bf16 a[172:175], v[228:231], v[140:143], a[172:175]\n\t"
  "buffer_load_dwordx4 %[wqs], %[srdq], s46 offen offset:1024 lds\n\t"
  "v_mfma_f32_16x16x32_bf16 a[176:179], v[232:235], v[84:87], a[176:179]\n\t"
  "ds_read_b64_tr_b16 v[212:213], %[va1] offset:40960\n\t"
  "ds_read_b64_tr_b16 v[214:215], %[va1] offset:43008\n\t"
  "v_mfma_f32_16x16x32_bf16 a[180:183], v[232:235], v[140:143], a[180:183]\n\t"
  "v_mfma_f32_16x16x32_bf16 a[184:187], v[236:239], v[84:87], a[184:187]\n\t"
  "ds_read_b64_tr_b16 v[216:217], %[va0] offset:41472\n\t"
  "ds_read_b64_tr_b16 v[218:219], %[va0] offset:43520\n\t"
  "v_mfma_f32_16x16x32_bf16 a[188:191], v[236:239], v[140:143], a[188:191]\n\t"
  "s_waitcnt lgkmcnt(14)\n\t"
  "v_mfma_f32_16x16x32_bf16 a[0:3], v[176:179], v[88:91], a[0:3]\n\t"
  "ds_read_b64_tr_b16 v[220:221], %[va1] offset:41472\n\t"
  "ds_read_b64_tr_b16 v[222:223], %[va1] offset:43520\n\t"
  "v_mfma_f32_16x16x32_bf16 a[4:7], v[176:179], v[144:147], a[4:7]\n\t"
  "buffer_load_dwordx4 %[wqs], %[srdq], s47 offen offset:2048 lds\n\t"
  "v_mfma_f32_16x16x32_bf16 a[8:11], v[180:183], v[88:91], a[8:11]\n\t"
  "ds_read_b64_tr_b16 v[224:225], %[va0] offset:45056\n\t"
  "ds_read_b64_tr_b16 v[226:227], %[va0] offset:47104\n\t"
  "v_mfma_f32_16x16x32_bf16 a[12:15], v[180:183], v[144:147], a[12:15]\n\t"
  "v_mfma_f32_16x16x32_bf16 a[16:19], v[184:187], v[88:91], a[16:19]\n\t"
  "ds_read_b64_tr_b16 v[228:229], %[va1] offset:45056\n\t"
  "ds_read_b64_tr_b16 v[230:231], %[va1] offset:47104\n\t"
  "v_mfma_f32_16x16x32_bf16 a[20:23], v[184:187], v[144:147], a[20:23]\n\t"
  "buffer_load_dwordx4 %[wqs], %[srdq], s48 offen offset:3072 lds\n\t"
  "v_add_u32 %[wqs], 0x40000, %[wqs]\n\t"
  "s_add_u32 m0, %[m0i], 40960\n\t"
  "v_mfma_f32_16x16x32_bf16 a[24:27], v[188:191], v[88:91], a[24:27]\n\t"
  "ds_read_b64_tr_b16 v[232:233], %[va0] offset:45568\n\t"
  "ds_read_b64_tr_b16 v[234:235], %[va0] offset:47616\n\t"
  "v_mfma_f32_16x16x32_bf16 a[28:31], v[188:191], v[144:147], a[28:31]\n\t"
  "s_waitcnt lgkmcnt(14)\n\t"
  "v_mfma_f32_16x16x32_bf16 a[32:35], v[192:195], v[88:91], a[32:35]\n\t"
  "ds_read_b64_tr_b16 v[236:237], %[va1] offset:45568\n\t"
  "ds_read_b64_tr_b16 v[238:239], %[va1] offset:47616\n\t"
  "v_mfma_f32_16x16x32_bf16 a[36:39], v[192:195], v[144:147], a[36:39]\n\t"
  "v_mfma_f32_16x16x32_bf16 a[40:43], v[196:199], v[88:91], a[40:43]\n\t"
  "ds_read_b64_tr_b16 v[176:177], %[va0] offset:49152\n\t"
  "ds_read_b64_tr_b16 v[178:179], %[va0] offset:51200\n\t"
  "v_mfma_f32_16x16x32_bf16 a[44:47], v[196:199], v[144:147], a[44:47]\n\t"
  "buffer_load_dwordx4 %[wis], %[srdi], 0 offen offset:0 lds\n\t"
  "v_mfma_f32_16x16x32_bf16 a[48:51], v[200:203], v[88:91], a[48:51]\n\t"
  "ds_read_b64_tr_b16 v[180:181], %[va1] offset:49152\n\t"
  "ds_read_b64_tr_b16 v[182:183], %[va1] offset:51200\n\t"
  "v_mfma_f32_16x16x32_bf16 a[52:55], v[200:203], v[144:147], a[52:55]\n\t"
  "v_mfma_f32_16x16x32_bf16 a[56:59], v[204:207], v[88:91], a[56:59]\n\t"
  "ds_read_b64_tr_b16 v[184:185], %[va0] offset:49664\n\t"
  "ds_read_b64_tr_b16 v[186:187], %[va0] offset:51712\n\t"
  "v_mfma_f32_16x16x32_bf16 a[60:63], v[204:207], v[144:147], a[60:63]\n\t"
  "s_waitcnt lgkmcnt(14)\n\t"
  "v_mfma_f32_16x16x32_bf16 a[64:67], v[208:211], v[88:91], a[64:67]\n\t"
  "ds_read_b64_tr_b16 v[188:189], %[va1] offset:49664\n\t"
  "ds_read_b64_tr_b16 v[190:191], %[va1] offset:51712\n\t"
  "v_mfma_f32_16x16x32_bf16 a[68:71], v[208:211], v[144:147], a[68:71]\n\t"
  "buffer_load_dwordx4 %[wis], %[srdi], s49 offen offset:1024 lds\n\t"
  "v_add_u32 %[wis], 114688, %[wis]\n\t"
  "v_mfma_f32_16x16x32_bf16 a[72:75], v[212:215], v[88:91], a[72:75]\n\t"
  "ds_read_b64_tr_b16 v[192:193], %[va0] offset:53248\n\t"
  "ds_read_b64_tr_b16 v[194:195], %[va0] offset:55296\n\t"
  "v_mfma_f32_16x16x32_bf16 a[76:79], v[212:215], v[144:147], a[76:79]\n\t"
  "v_mfma_f32_16x16x32_bf16 a[80:83], v[216:219], v[88:91], a[80:83]\n\t"
  "ds_read_b64_tr_b16 v[196:197], %[va1] offset:53248\n\t"
  "ds_read_b64_tr_b16 v[198:199], %[va1] offset:55296\n\t"
  "v_mfma_f32_16x16x32_bf16 a[84:87], v[216:219], v[144:147], a[84:87]\n\t"
  "v_mfma_f32_16x16x32_bf16 a[88:91], v[220:223], v[88:91], a[88:91]\n\t"
  "ds_read_b64_tr_b16 v[200:201], %[va0] offset:53760\n\t"
  "ds_read_b64_tr_b16 v[202:203], %[va0] offset:55808\n\t"
  "v_mfma_f32_16x16x32_bf16 a[92:95], v[220:223], v[144:147], a[92:95]\n\t"
  "s_waitcnt lgkmcnt(14)\n\t"
  "v_mfma_f32_16x16x32_bf16 a[96:99], v[224:227], v[88:91], a[96:99]\n\t"
  "ds_read_b64_tr_b16 v[204:205], %[va1] offset:53760\n\t"
  "ds_read_b64_tr_b16 v[206:207], %[va1] offset:55808\n\t"
  "v_mfma_f32_16x16x32_bf16 a[100:103], v[224:227], v[144:147], a[100:103]\n\t"
  "v_mfma_f32_16x16x32_bf16 a[104:107], v[228:231], v[88:91], a[104:107]\n\t"
  "s_waitcnt vmcnt(16)\n\t"
  "s_barrier\n\t"
  "ds_read_b64_tr_b16 v[208:209], %[va0] offset:57344\n\t"
  "ds_read_b64_tr_b16 v[210:211], %[va0] offset:59392\n\t"
  "v_mfma_f32_16x16x32_bf16 a[108:111], v[228:231], v[144:147], a[108:111]\n\t"
  "buffer_load_dwordx4 v[104:107], %[xs], %[srdx], s52 offen offset:2048\n\t"
  "v_mfma_f32_16x16x32_bf16 a[112:115], v[232:235], v[88:91], a[112:115]\n\t"
  "ds_read_b64_tr_b16 v[212:213], %[va1] offset:57344\n\t"
  "ds_read_b64_tr_b16 v[214:215], %[va1] offset:59392\n\t"
  "v_mfma_f32_16x16x32_bf16 a[116:119], v[232:235], v[144:147], a[116:119]\n\t"
  "v_mfma_f32_16x16x32_bf16 a[120:123], v[236:239], v[88:91], a[120:123]\n\t"
  "ds_read_b64_tr_b16 v[216:217], %[va0] offset:57856\n\t"
  "ds_read_b64_tr_b16 v[218:219], %[va0] offset:59904\n\t"
  "v_mfma_f32_16x16x32_bf16 a[124:127], v[236:239], v[144:147], a[124:127]\n\t"
  "s_waitcnt lgkmcnt(14)\n\t"
  "v_mfma_f32_16x16x32_bf16 a[128:131], v[176:179], v[88:91], a[128:131]\n\t"
  "ds_read_b64_tr_b16 v[220:221], %[va1] offset:57856\n\t"
  "ds_read_b64_tr_b16 v[222:223], %[va1] offset:59904\n\t"
  "v_mfma_f32_16x16x32_bf16 a[132:135], v[176:179], v[144:147], a[132:135]\n\t"
  "buffer_load_dwordx4 v[160:163], %[xs], %[srdx], s56 offen offset:0\n\t"
  "s_add_u32 m0, %[m0k], 49152\n\t"
  "v_mfma_f32_16x16x32_bf16 a[136:139], v[180:183], v[88:91], a[136:139]\n\t"
  "ds_read_b64_tr_b16 v[224:225], %[va0] offset:61440\n\t"
  "ds_read_b64_tr_b16 v[226:227], %[va0] offset:63488\n\t"
  "v_mfma_f32_16x16x32_bf16 a[140:143], v[180:183], v[144:147], a[140:143]\n\t"
  "v_mfma_f32_16x16x32_bf16 a[144:147], v[184:187], v[88:91], a[144:147]\n\t"
  "ds_read_b64_tr_b16 v[228:229], %[va1] offset:61440\n\t"
  "ds_read_b64_tr_b16 v[230:231], %[va1] offset:63488\n\t"
  "v_mfma_f32_16x16x32_bf16 a[148:151], v[184:187], v[144:147], a[148:151]\n\t"
  "v_mfma_f32_16x16x32_bf16 a[152:155], v[188:191], v[88:91], a[152:155]\n\t"
  "ds_read_b64_tr_b16 v[232:233], %[va0] offset:61952\n\t"
  "ds_read_b64_tr_b16 v[234:235], %[va0] offset:64000\n\t"
  "v_mfma_f32_16x16x32_bf16 a[156:159], v[188:191], v[144:147], a[156:159]\n\t"
  "buffer_load_dwordx4 %[wqs], %[srdq], 0 offen offset:0 lds\n\t"
  "s_waitcnt lgkmcnt(14)\n\t"
  "v_mfma_f32_16x16x32_bf16 a[160:163], v[192:195], v[88:91], a[160:163]\n\t"
  "ds_read_b64_tr_b16 v[236:237], %[va1] offset:61952\n\t"
  "ds_read_b64_tr_b16 v[238:239], %[va1] offset:64000\n\t"
  "v_mfma_f32_16x16x32_bf16 a[164:167], v[192:195], v[144:147], a[164:167]\n\t"
  "v_mfma_f32_16x16x32_bf16 a[168:171], v[196:199], v[88:91], a[168:171]\n\t"
  "ds_read_b64_tr_b16 v[176:177], %[vz0] offset:0\n\t"
  "ds_read_b64_tr_b16 v[178:179], %[vz0] offset:2048\n\t"
  "v_mfma_f32_16x16x32_bf16 a[172:175], v[196:199], v[144:147], a[172:175]\n\t"
  "buffer_load_dwordx4 %[wqs], %[srdq], s46 offen offset:1024 lds\n\t"
  "v_mfma_f32_16x16x32_bf16 a[176:179], v[200:203], v[88:91], a[176:179]\n\t"
  "ds_read_b64_tr_b16 v[180:181], %[vz1] offset:0\n\t"
  "ds_read_b64_tr_b16 v[182:183], %[vz1] offset:2048\n\t"
  "v_mfma_f32_16x16x32_bf16 a[180:183], v[200:203], v[144:147], a[180:183]\n\t"
  "v_mfma_f32_16x16x32_bf16 a[184:187], v[204:207], v[88:91], a[184:187]\n\t"
  "ds_read_b64_tr_b16 v[184:185], %[vz0] offset:512\n\t"
  "ds_read_b64_tr_b16 v[186:187], %[vz0] offset:2560\n\t"
  "v_mfma_f32_16x16x32_bf16 a[188:191], v[204:207], v[144:147], a[188:191]\n\t"
  "s_waitcnt lgkmcnt(14)\n\t"
  "v_mfma_f32_16x16x32_bf16 a[0:3], v[208:211], v[92:95], a[0:3]\n\t"
  "ds_read_b64_tr_b16 v[188:189], %[vz1] offset:512\n\t"
  "ds_read_b64_tr_b16 v[190:191], %[vz1] offset:2560\n\t"
  "v_mfma_f32_16x16x32_bf16 a[4:7], v[208:211], v[148:151], a[4:7]\n\t"
  "buffer_load_dwordx4 %[wqs], %[srdq], s47 offen offset:2048 lds\n\t"
  "v_mfma_f32_16x16x32_bf16 a[8:11], v[212:215], v[92:95], a[8:11]\n\t"
  "ds_read_b64_tr_b16 v[192:193], %[vz0] offset:4096\n\t"
  "ds_read_b64_tr_b16 v[194:195], %[vz0] offset:6144\n\t"
  "v_mfma_f32_16x16x32_bf16 a[12:15], v[212:215], v[148:151], a[12:15]\n\t"
  "v_mfma_f32_16x16x32_bf16 a[16:19], v[216:219], v[92:95], a[16:19]\n\t"
  "ds_read_b64_tr_b16 v[196:197], %[vz1] offset:4096\n\t"
  "ds_read_b64_tr_b16 v[198:199], %[vz1] offset:6144\n\t"
  "v_mfma_f32_16x16x32_bf16 a[20:23], v[216:219], v[148:151], a[20:23]\n\t"
  "buffer_load_dwordx4 %[wqs], %[srdq], s48 offen offset:3072 lds\n\t"
  "v_add_u32 %[wqs], 0x40000, %[wqs]\n\t"
  "s_add_u32 m0, %[m0i], 65536\n\t"
  "v_mfma_f32_16x16x32_bf16 a[24:27], v[220:223], v[92:95], a[24:27]\n\t"
  "ds_read_b64_tr_b16 v[200:201], %[vz0] offset:4608\n\t"
  "ds_read_b64_tr_b16 v[202:203], %[vz0] offset:6656\n\t"
  "v_mfma_f32_16x16x32_bf16 a[28:31], v[220:223], v[148:151], a[28:31]\n\t"
  "s_waitcnt lgkmcnt(14)\n\t"
  "v_mfma_f32_16x16x32_bf16 a[32:35], v[224:227], v[92:95], a[32:35]\n\t"
  "ds_read_b64_tr_b16 v[204:205], %[vz1] offset:4608\n\t"
  "ds_read_b64_tr_b16 v[206:207], %[vz1] offset:6656\n\t"
  "v_mfma_f32_16x16x32_bf16 a[36:39], v[224:227], v[148:151], a[36:39]\n\t"
  "v_mfma_f32_16x16x32_bf16 a[40:43], v[228:231], v[92:95], a[40:43]\n\t"
  "ds_read_b64_tr_b16 v[208:209], %[vz0] offset:8192\n\t"
  "ds_read_b64_tr_b16 v[210:211], %[vz0] offset:10240\n\t"
  "v_mfma_f32_16x16x32_bf16 a[44:47], v[228:231], v[148:151], a[44:47]\n\t"
  "buffer_load_dwordx4 %[wis], %[srdi], 0 offen offset:0 lds\n\t"
  "v_mfma_f32_16x16x32_bf16 a[48:51], v[232:235], v[92:95], a[48:51]\n\t"
  "ds_read_b64_tr_b16 v[212:213], %[vz1] offset:8192\n\t"
  "ds_read_b64_tr_b16 v[214:215], %[vz1] offset:10240\n\t"
  "v_mfma_f32_16x16x32_bf16 a[52:55], v[232:235], v[148:151], a[52:55]\n\t"
  "v_mfma_f32_16x16x32_bf16 a[56:59], v[236:239], v[92:95], a[56:59]\n\t"
  "ds_read_b64_tr_b16 v[216:217], %[vz0] offset:8704\n\t"
  "ds_read_b64_tr_b16 v[218:219], %[vz0] offset:10752\n\t"
  "v_mfma_f32_16x16x32_bf16 a[60:63], v[236:239], v[148:151], a[60:63]\n\t"
  "s_waitcnt lgkmcnt(14)\n\t"
  "v_mfma_f32_16x16x32_bf16 a[64:67], v[176:179], v[92:95], a[64:67]\n\t"
  "ds_read_b64_tr_b16 v[220:221], %[vz1] offset:8704\n\t"
  "ds_read_b64_tr_b16 v[222:223], %[vz1] offset:10752\n\t"
  "v_mfma_f32_16x16x32_bf16 a[68:71], v[176:179], v[148:151], a[68:71]\n\t"
  "buffer_load_dwordx4 %[wis], %[srdi], s49 offen offset:1024 lds\n\t"
  "v_add_u32 %[wis], 114688, %[wis]\n\t"
  "v_mfma_f32_16x16x32_bf16 a[72:75], v[180:183], v[92:95], a[72:75]\n\t"
  "ds_read_b64_tr_b16 v[224:225], %[vz0] offset:12288\n\t"
  "ds_read_b64_tr_b16 v[226:227], %[vz0] offset:14336\n\t"
  "v_mfma_f32_16x16x32_bf16 a[76:79], v[180:183], v[148:151], a[76:79]\n\t"
  "v_mfma_f32_16x16x32_bf16 a[80:83], v[184:187], v[92:95], a[80:83]\n\t"
  "ds_read_b64_tr_b16 v[228:229], %[vz1] offset:12288\n\t"
  "ds_read_b64_tr_b16 v[230:231], %[vz1] offset:14336\n\t"
  "v_mfma_f32_16x16x32_bf16 a[84:87], v[184:187], v[148:151], a[84:87]\n\t"
  "v_mfma_f32_16x16x32_bf16 a[88:91], v[188:191], v[92:95], a[88:91]\n\t"
  "ds_read_b64_tr_b16 v[232:233], %[vz0] offset:12800\n\t"
  "ds_read_b64_tr_b16 v[234:235], %[vz0] offset:14848\n\t"
  "v_mfma_f32_16x16x32_bf16 a[92:95], v[188:191], v[148:151], a[92:95]\n\t"
  "s_waitcnt lgkmcnt(14)\n\t"
  "v_mfma_f32_16x16x32_bf16 a[96:99], v[192:195], v[92:95], a[96:99]\n\t"
  "ds_read_b64_tr_b16 v[236:237], %[vz1] offset:12800\n\t"
  "ds_read_b64_tr_b16 v[238:239], %[vz1] offset:14848\n\t"
  "v_mfma_f32_16x16x32_bf16 a[100:103], v[192:195], v[148:151], a[100:103]\n\t"
  "v_mfma_f32_16x16x32_bf16 a[104:107], v[196:199], v[92:95], a[104:107]\n\t"
  "s_waitcnt vmcnt(16)\n\t"
  "s_barrier\n\t"
  "ds_read_b64_tr_b16 v[176:177], %[vk0] offset:0\n\t"
  "ds_read_b64_tr_b16 v[178:179], %[vk0] offset:2048\n\t"
  "v_mfma_f32_16x16x32_bf16 a[108:111], v[196:199], v[148:151], a[108:111]\n\t"
  "buffer_load_dwordx4 v[108:111], %[xs], %[srdx], s52 offen offset:3072\n\t"
  "v_mfma_f32_16x16x32_bf16 a[112:115], v[200:203], v[92:95], a[112:115]\n\t"
  "ds_read_b64_tr_b16 v[180:181], %[vk1] offset:0\n\t"
  "ds_read_b64_tr_b16 v[182:183], %[vk1] offset:2048\n\t"
  "v_mfma_f32_16x16x32_bf16 a[116:119], v[200:203], v[148:151], a[116:119]\n\t"
  "v_mfma_f32_16x16x32_bf16 a[120:123], v[204:207], v[92:95], a[120:123]\n\t"
  "ds_read_b64_tr_b16 v[184:185], %[vk0] offset:512\n\t"
  "ds_read_b64_tr_b16 v[186:187], %[vk0] offset:2560\n\t"
  "v_mfma_f32_16x16x32_bf16 a[124:127], v[204:207], v[148:151], a[124:127]\n\t"
  "s_waitcnt lgkmcnt(14)\n\t"
  "v_mfma_f32_16x16x32_bf16 a[128:131], v[208:211], v[92:95], a[128:131]\n\t"
  "ds_read_b64_tr_b16 v[188:189], %[vk1] offset:512\n\t"
  "ds_read_b64_tr_b16 v[190:191], %[vk1] offset:2560\n\t"
  "v_mfma_f32_16x16x32_bf16 a[132:135], v[208:211], v[148:151], a[132:135]\n\t"
  "buffer_load_dwordx4 v[164:167], %[xs], %[srdx], s56 offen offset:1024\n\t"
  "s_add_u32 m0, %[m0k], 73728\n\t"
  "v_mfma_f32_16x16x32_bf16 a[136:139], v[212:215], v[92:95], a[136:139]\n\t"
  "ds_read_b64_tr_b16 v[192:193], %[vk0] offset:4096\n\t"
  "ds_read_b64_tr_b16 v[194:195], %[vk0] offset:6144\n\t"
  "v_mfma_f32_16x16x32_bf16 a[140:143], v[212:215], v[148:151], a[140:143]\n\t"
  "v_mfma_f32_16x16x32_bf16 a[144:147], v[216:219], v[92:95], a[144:147]\n\t"
  "ds_read_b64_tr_b16 v[196:197], %[vk1] offset:4096\n\t"
  "ds_read_b64_tr_b16 v[198:199], %[vk1] offset:6144\n\t"
  "v_mfma_f32_16x16x32_bf16 a[148:151], v[216:219], v[148:151], a[148:151]\n\t"
  "v_mfma_f32_16x16x32_bf16 a[152:155], v[220:223], v[92:95], a[152:155]\n\t"
  "ds_read_b64_tr_b16 v[200:201], %[vk0] offset:4608\n\t"
  "ds_read_b64_tr_b16 v[202:203], %[vk0] offset:6656\n\t"
  "v_mfma_f32_16x16x32_bf16 a[156:159], v[220:223], v[148:151], a[156:159]\n\t"
  "buffer_load_dwordx4 %[wqs], %[srdq], 0 offen offset:0 lds\n\t"
  "s_waitcnt lgkmcnt(14)\n\t"
  "v_mfma_f32_16x16x32_bf16 a[160:163], v[224:227], v[92:95], a[160:163]\n\t"
  "ds_read_b64_tr_b16 v[204:205], %[vk1] offset:4608\n\t"
  "ds_read_b64_tr_b16 v[206:207], %[vk1] offset:6656\n\t"
  "v_mfma_f32_16x16x32_bf16 a[164:167], v[224:227], v[148:151], a[164:167]\n\t"
  "v_mfma_f32_16x16x32_bf16 a[168:171], v[228:231], v[92:95], a[168:171]\n\t"
  "ds_read_b64_tr_b16 v[208:209], %[vk0] offset:8192\n\t"
  "ds_read_b64_tr_b16 v[210:211], %[vk0] offset:10240\n\t"
  "v_mfma_f32_16x16x32_bf16 a[172:175], v[228:231], v[148:151], a[172:175]\n\t"
  "buffer_load_dwordx4 %[wqs], %[srdq], s46 offen offset:1024 lds\n\t"
  "v_mfma_f32_16x16x32_bf16 a[176:179], v[232:235], v[92:95], a[176:179]\n\t"
  "ds_read_b64_tr_b16 v[212:213], %[vk1] offset:8192\n\t"
  "ds_read_b64_tr_b16 v[214:215], %[vk1] offset:10240\n\t"
  "v_mfma_f32_16x16x32_bf16 a[180:183], v[232:235], v[148:151], a[180:183]\n\t"
  "v_mfma_f32_16x16x32_bf16 a[184:187], v[236:239], v[92:95], a[184:187]\n\t"
  "ds_read_b64_tr_b16 v[216:217], %[vk0] offset:8704\n\t"
  "ds_read_b64_tr_b16 v[218:219], %[vk0] offset:10752\n\t"
  "v_mfma_f32_16x16x32_bf16 a[188:191], v[236:239], v[148:151], a[188:191]\n\t"
  "s_waitcnt lgkmcnt(14)\n\t"
  "v_mfma_f32_16x16x32_bf16 a[0:3], v[176:179], v[96:99], a[0:3]\n\t"
  "ds_read_b64_tr_b16 v[220:221], %[vk1] offset:8704\n\t"
  "ds_read_b64_tr_b16 v[222:223], %[vk1] offset:10752\n\t"
  "v_mfma_f32_16x16x32_bf16 a[4:7], v[176:179], v[152:155], a[4:7]\n\t"
  "buffer_load_dwordx4 %[wqs], %[srdq], s47 offen offset:2048 lds\n\t"
  "v_mfma_f32_16x16x32_bf16 a[8:11], v[180:183], v[96:99], a[8:11]\n\t"
  "ds_read_b64_tr_b16 v[224:225], %[vk0] offset:12288\n\t"
  "ds_read_b64_tr_b16 v[226:227], %[vk0] offset:14336\n\t"
  "v_mfma_f32_16x16x32_bf16 a[12:15], v[180:183], v[152:155], a[12:15]\n\t"
  "v_mfma_f32_16x16x32_bf16 a[16:19], v[184:187], v[96:99], a[16:19]\n\t"
  "ds_read_b64_tr_b16 v[228:229], %[vk1] offset:12288\n\t"
  "ds_read_b64_tr_b16 v[230:231], %[vk1] offset:14336\n\t"
  "v_mfma_f32_16x16x32_bf16 a[20:23], v[184:187], v[152:155], a[20:23]\n\t"
  "buffer_load_dwordx4 %[wqs], %[srdq], s48 offen offset:3072 lds\n\t"
  "v_add_u32 %[wqs], 0x40000, %[wqs]\n\t"
  "s_add_u32 m0, %[m0i], 90112\n\t"
  "v_mfma_f32_16x16x32_bf16 a[24:27], v[188:191], v[96:99], a[24:27]\n\t"
  "ds_read_b64_tr_b16 v[232:233], %[vk0] offset:12800\n\t"
  "ds_read_b64_tr_b16 v[234:235], %[vk0] offset:14848\n\t"
  "v_mfma_f32_16x16x32_bf16 a[28:31], v[188:191], v[152:155], a[28:31]\n\t"
  "s_waitcnt lgkmcnt(14)\n\t"
  "v_mfma_f32_16x16x32_bf16 a[32:35], v[192:195], v[96:99], a[32:35]\n\t"
  "ds_read_b64_tr_b16 v[236:237], %[vk1] offset:12800\n\t"
  "ds_read_b64_tr_b16 v[238:239], %[vk1] offset:14848\n\t"
  "v_mfma_f32_16x16x32_bf16 a[36:39], v[192:195], v[152:155], a[36:39]\n\t"
  "v_mfma_f32_16x16x32_bf16 a[40:43], v[196:199], v[96:99], a[40:43]\n\t"
  "ds_read_b64_tr_b16 v[176:177], %[vk0] offset:16384\n\t"
  "ds_read_b64_tr_b16 v[178:179], %[vk0] offset:18432\n\t"
  "v_mfma_f32_16x16x32_bf16 a[44:47], v[196:199], v[152:155], a[44:47]\n\t"
  "buffer_load_dwordx4 %[wis], %[srdi], 0 offen offset:0 lds\n\t"
  "v_mfma_f32_16x16x32_bf16 a[48:51], v[200:203], v[96:99], a[48:51]\n\t"
  "ds_read_b64_tr_b16 v[180:181], %[vk1] offset:16384\n\t"
  "ds_read_b64_tr_b16 v[182:183], %[vk1] offset:18432\n\t"
  "v_mfma_f32_16x16x32_bf16 a[52:55], v[200:203], v[152:155], a[52:55]\n\t"
  "v_mfma_f32_16x16x32_bf16 a[56:59], v[204:207], v[96:99], a[56:59]\n\t"
  "ds_read_b64_tr_b16 v[184:185], %[vk0] offset:16896\n\t"
  "ds_read_b64_tr_b16 v[186:187], %[vk0] offset:18944\n\t"
  "v_mfma_f32_16x16x32_bf16 a[60:63], v[204:207], v[152:155], a[60:63]\n\t"
  "s_waitcnt lgkmcnt(14)\n\t"
  "v_mfma_f32_16x16x32_bf16 a[64:67], v[208:211], v[96:99], a[64:67]\n\t"
  "ds_read_b64_tr_b16 v[188:189], %[vk1] offset:16896\n\t"
  "ds_read_b64_tr_b16 v[190:191], %[vk1] offset:18944\n\t"
  "v_mfma_f32_16x16x32_bf16 a[68:71], v[208:211], v[152:155], a[68:71]\n\t"
  "buffer_load_dwordx4 %[wis], %[srdi], s49 offen offset:1024 lds\n\t"
  "v_add_u32 %[wis], 114688, %[wis]\n\t"
  "v_mfma_f32_16x16x32_bf16 a[72:75], v[212:215], v[96:99], a[72:75]\n\t"
  "ds_read_b64_tr_b16 v[192:193], %[vk0] offset:20480\n\t"
  "ds_read_b64_tr_b16 v[194:195], %[vk0] offset:22528\n\t"
  "v_mfma_f32_16x16x32_bf16 a[76:79], v[212:215], v[152:155], a[76:79]\n\t"
  "v_mfma_f32_16x16x32_bf16 a[80:83], v[216:219], v[96:99], a[80:83]\n\t"
  "ds_read_b64_tr_b16 v[196:197], %[vk1] offset:20480\n\t"
  "ds_read_b64_tr_b16 v[198:199], %[vk1] offset:22528\n\t"
  "v_mfma_f32_16x16x32_bf16 a[84:87], v[216:219], v[152:155], a[84:87]\n\t"
  "v_mfma_f32_16x16x32_bf16 a[88:91], v[220:223], v[96:99], a[88:91]\n\t"
  "ds_read_b64_tr_b16 v[200:201], %[vk0] offset:20992\n\t"
  "ds_read_b64_tr_b16 v[202:203], %[vk0] offset:23040\n\t"
  "v_mfma_f32_16x16x32_bf16 a[92:95], v[220:223], v[152:155], a[92:95]\n\t"
  "s_waitcnt lgkmcnt(14)\n\t"
  "v_mfma_f32_16x16x32_bf16 a[96:99], v[224:227], v[96:99], a[96:99]\n\t"
  "ds_read_b64_tr_b16 v[204:205], %[vk1] offset:20992\n\t"
  "ds_read_b64_tr_b16 v[206:207], %[vk1] offset:23040\n\t"
  "v_mfma_f32_16x16x32_bf16 a[100:103], v[224:227], v[152:155], a[100:103]\n\t"
  "v_mfma_f32_16x16x32_bf16 a[104:107], v[228:231], v[96:99], a[104:107]\n\t"
  "s_waitcnt vmcnt(16)\n\t"
  "s_barrier\n\t"
  "ds_read_b64_tr_b16 v[208:209], %[vk0] offset:24576\n\t"
  "ds_read_b64_tr_b16 v[210:211], %[vk0] offset:26624\n\t"
  "v_mfma_f32_16x16x32_bf16 a[108:111], v[228:231], v[152:155], a[108:111]\n\t"
  "buffer_load_dwordx4 v[112:115], %[xs], %[srdx], s53 offen offset:0\n\t"
  "v_mfma_f32_16x16x32_bf16 a[112:115], v[232:235], v[96:99], a[112:115]\n\t"
  "ds_read_b64_tr_b16 v[212:213], %[vk1] offset:24576\n\t"
  "ds_read_b64_tr_b16 v[214:215], %[vk1] offset:26624\n\t"
  "v_mfma_f32_16x16x32_bf16 a[116:119], v[232:235], v[152:155], a[116:119]\n\t"
  "v_mfma_f32_16x16x32_bf16 a[120:123], v[236:239], v[96:99], a[120:123]\n\t"
  "ds_read_b64_tr_b16 v[216:217], %[vk0] offset:25088\n\t"
  "ds_read_b64_tr_b16 v[218:219], %[vk0] offset:27136\n\t"
  "v_mfma_f32_16x16x32_bf16 a[124:127], v[236:239], v[152:155], a[124:127]\n\t"
  "s_waitcnt lgkmcnt(14)\n\t"
  "v_mfma_f32_16x16x32_bf16 a[128:131], v[176:179], v[96:99], a[128:131]\n\t"
  "ds_read_b64_tr_b16 v[220:221], %[vk1] offset:25088\n\t"
  "ds_read_b64_tr_b16 v[222:223], %[vk1] offset:27136\n\t"
  "v_mfma_f32_16x16x32_bf16 a[132:135], v[176:179], v[152:155], a[132:135]\n\t"
  "buffer_load_dwordx4 v[168:171], %[xs], %[srdx], s56 offen offset:2048\n\t"
  "s_add_u32 m0, %[m0k], 98304\n\t"
  "v_mfma_f32_16x16x32_bf16 a[136:139], v[180:183], v[96:99], a[136:139]\n\t"
  "ds_read_b64_tr_b16 v[224:225], %[vk0] offset:28672\n\t"
  "ds_read_b64_tr_b16 v[226:227], %[vk0] offset:30720\n\t"
  "v_mfma_f32_16x16x32_bf16 a[140:143], v[180:183], v[152:155], a[140:143]\n\t"
  "v_mfma_f32_16x16x32_bf16 a[144:147], v[184:187], v[96:99], a[144:147]\n\t"
  "ds_read_b64_tr_b16 v[228:229], %[vk1] offset:28672\n\t"
  "ds_read_b64_tr_b16 v[230:231], %[vk1] offset:30720\n\t"
  "v_mfma_f32_16x16x32_bf16 a[148:151], v[184:187], v[152:155], a[148:151]\n\t"
  "v_mfma_f32_16x16x32_bf16 a[152:155], v[188:191], v[96:99], a[152:155]\n\t"
  "ds_read_b64_tr_b16 v[232:233], %[vk0] offset:29184\n\t"
  "ds_read_b64_tr_b16 v[234:235], %[vk0] offset:31232\n\t"
  "v_mfma_f32_16x16x32_bf16 a[156:159], v[188:191], v[152:155], a[156:159]\n\t"
  "buffer_load_dwordx4 %[wqs], %[srdq], 0 offen offset:0 lds\n\t"
  "s_waitcnt lgkmcnt(14)\n\t"
  "v_mfma_f32_16x16x32_bf16 a[160:163], v[192:195], v[96:99], a[160:163]\n\t"
  "ds_read_b64_tr_b16 v[236:237], %[vk1] offset:29184\n\t"
  "ds_read_b64_tr_b16 v[238:239], %[vk1] offset:31232\n\t"
  "v_mfma_f32_16x16x32_bf16 a[164:167], v[192:195], v[152:155], a[164:167]\n\t"
  "v_mfma_f32_16x16x32_bf16 a[168:171], v[196:199], v[96:99], a[168:171]\n\t"
  "ds_read_b64_tr_b16 v[176:177], %[vk0] offset:32768\n\t"
  "ds_read_b64_tr_b16 v[178:179], %[vk0] offset:34816\n\t"
  "v_mfma_f32_16x16x32_bf16 a[172:175], v[196:199], v[152:155], a[172:175]\n\t"
  "buffer_load_dwordx4 %[wqs], %[srdq], s46 offen offset:1024 lds\n\t"
  "v_mfma_f32_16x16x32_bf16 a[176:179], v[200:203], v[96:99], a[176:179]\n\t"
  "ds_read_b64_tr_b16 v[180:181], %[vk1] offset:32768\n\t"
  "ds_read_b64_tr_b16 v[182:183], %[vk1] offset:34816\n\t"
  "v_mfma_f32_16x16x32_bf16 a[180:183], v[200:203], v[152:155], a[180:183]\n\t"
  "v_mfma_f32_16x16x32_bf16 a[184:187], v[204:207], v[96:99], a[184:187]\n\t"
  "ds_read_b64_tr_b16 v[184:185], %[vk0] offset:33280\n\t"
  "ds_read_b64_tr_b16 v[186:187], %[vk0] offset:35328\n\t"
  "v_mfma_f32_16x16x32_bf16 a[188:191], v[204:207], v[152:155], a[188:191]\n\t"
  "s_waitcnt lgkmcnt(14)\n\t"
  "v_mfma_f32_16x16x32_bf16 a[0:3], v[208:211], v[100:103], a[0:3]\n\t"
  "ds_read_b64_tr_b16 v[188:189], %[vk1] offset:33280\n\t"
  "ds_read_b64_tr_b16 v[190:191], %[vk1] offset:35328\n\t"
  "v_mfma_f32_16x16x32_bf16 a[4:7], v[208:211], v[156:159], a[4:7]\n\t"
  "buffer_load_dwordx4 %[wqs], %[srdq], s47 offen offset:2048 lds\n\t"
  "v_mfma_f32_16x16x32_bf16 a[8:11], v[212:215], v[100:103], a[8:11]\n\t"
  "ds_read_b64_tr_b16 v[192:193], %[vk0] offset:36864\n\t"
  "ds_read_b64_tr_b16 v[194:195], %[vk0] offset:38912\n\t"
  "v_mfma_f32_16x16x32_bf16 a[12:15], v[212:215], v[156:159], a[12:15]\n\t"
  "v_mfma_f32_16x16x32_bf16 a[16:19], v[216:219], v[100:103], a[16:19]\n\t"
  "ds_read_b64_tr_b16 v[196:197], %[vk1] offset:36864\n\t"
  "ds_read_b64_tr_b16 v[198:199], %[vk1] offset:38912\n\t"
  "v_mfma_f32_16x16x32_bf16 a[20:23], v[216:219], v[156:159], a[20:23]\n\t"
  "buffer_load_dwordx4 %[wqs], %[srdq], s48 offen offset:3072 lds\n\t"
  "v_add_u32 %[wqs], 0x40000, %[wqs]\n\t"
  "s_add_u32 m0, %[m0i], 114688\n\t"
  "v_mfma_f32_16x16x32_bf16 a[24:27], v[220:223], v[100:103], a[24:27]\n\t"
  "ds_read_b64_tr_b16 v[200:201], %[vk0] offset:37376\n\t"
  "ds_read_b64_tr_b16 v[202:203], %[vk0] offset:39424\n\t"
  "v_mfma_f32_16x16x32_bf16 a[28:31], v[220:223], v[156:159], a[28:31]\n\t"
  "s_waitcnt lgkmcnt(14)\n\t"
  "v_mfma_f32_16x16x32_bf16 a[32:35], v[224:227], v[100:103], a[32:35]\n\t"
  "ds_read_b64_tr_b16 v[204:205], %[vk1] offset:37376\n\t"
  "ds_read_b64_tr_b16 v[206:207], %[vk1] offset:39424\n\t"
  "v_mfma_f32_16x16x32_bf16 a[36:39], v[224:227], v[156:159], a[36:39]\n\t"
  "v_mfma_f32_16x16x32_bf16 a[40:43], v[228:231], v[100:103], a[40:43]\n\t"
  "ds_read_b64_tr_b16 v[208:209], %[vk0] offset:40960\n\t"
  "ds_read_b64_tr_b16 v[210:211], %[vk0] offset:43008\n\t"
  "v_mfma_f32_16x16x32_bf16 a[44:47], v[228:231], v[156:159], a[44:47]\n\t"
  "buffer_load_dwordx4 %[wis], %[srdi], 0 offen offset:0 lds\n\t"
  "v_mfma_f32_16x16x32_bf16 a[48:51], v[232:235], v[100:103], a[48:51]\n\t"
  "ds_read_b64_tr_b16 v[212:213], %[vk1] offset:40960\n\t"
  "ds_read_b64_tr_b16 v[214:215], %[vk1] offset:43008\n\t"
  "v_mfma_f32_16x16x32_bf16 a[52:55], v[232:235], v[156:159], a[52:55]\n\t"
  "v_mfma_f32_16x16x32_bf16 a[56:59], v[236:239], v[100:103], a[56:59]\n\t"
  "ds_read_b64_tr_b16 v[216:217], %[vk0] offset:41472\n\t"
  "ds_read_b64_tr_b16 v[218:219], %[vk0] offset:43520\n\t"
  "v_mfma_f32_16x16x32_bf16 a[60:63], v[236:239], v[156:159], a[60:63]\n\t"
  "s_waitcnt lgkmcnt(14)\n\t"
  "v_mfma_f32_16x16x32_bf16 a[64:67], v[176:179], v[100:103], a[64:67]\n\t"
  "ds_read_b64_tr_b16 v[220:221], %[vk1] offset:41472\n\t"
  "ds_read_b64_tr_b16 v[222:223], %[vk1] offset:43520\n\t"
  "v_mfma_f32_16x16x32_bf16 a[68:71], v[176:179], v[156:159], a[68:71]\n\t"
  "buffer_load_dwordx4 %[wis], %[srdi], s49 offen offset:1024 lds\n\t"
  "v_add_u32 %[wis], 114688, %[wis]\n\t"
  "v_mfma_f32_16x16x32_bf16 a[72:75], v[180:183], v[100:103], a[72:75]\n\t"
  "ds_read_b64_tr_b16 v[224:225], %[vk0] offset:45056\n\t"
  "ds_read_b64_tr_b16 v[226:227], %[vk0] offset:47104\n\t"
  "v_mfma_f32_16x16x32_bf16 a[76:79], v[180:183], v[156:159], a[76:79]\n\t"
  "v_mfma_f32_16x16x32_bf16 a[80:83], v[184:187], v[100:103], a[80:83]\n\t"
  "ds_read_b64_tr_b16 v[228:229], %[vk1] offset:45056\n\t"
  "ds_read_b64_tr_b16 v[230:231], %[vk1] offset:47104\n\t"
  "v_mfma_f32_16x16x32_bf16 a[84:87], v[184:187], v[156:159], a[84:87]\n\t"
  "v_mfma_f32_16x16x32_bf16 a[88:91], v[188:191], v[100:103], a[88:91]\n\t"
  "ds_read_b64_tr_b16 v[232:233], %[vk0] offset:45568\n\t"
  "ds_read_b64_tr_b16 v[234:235], %[vk0] offset:47616\n\t"
  "v_mfma_f32_16x16x32_bf16 a[92:95], v[188:191], v[156:159], a[92:95]\n\t"
  "s_waitcnt lgkmcnt(14)\n\t"
  "v_mfma_f32_16x16x32_bf16 a[96:99], v[192:195], v[100:103], a[96:99]\n\t"
  "ds_read_b64_tr_b16 v[236:237], %[vk1] offset:45568\n\t"
  "ds_read_b64_tr_b16 v[238:239], %[vk1] offset:47616\n\t"
  "v_mfma_f32_16x16x32_bf16 a[100:103], v[192:195], v[156:159], a[100:103]\n\t"
  "v_mfma_f32_16x16x32_bf16 a[104:107], v[196:199], v[100:103], a[104:107]\n\t"
  "s_waitcnt vmcnt(16)\n\t"
  "s_barrier\n\t"
  "ds_read_b64_tr_b16 v[176:177], %[vk0] offset:49152\n\t"
  "ds_read_b64_tr_b16 v[178:179], %[vk0] offset:51200\n\t"
  "v_mfma_f32_16x16x32_bf16 a[108:111], v[196:199], v[156:159], a[108:111]\n\t"
  "buffer_load_dwordx4 v[116:119], %[xs], %[srdx], s53 offen offset:1024\n\t"
  "v_mfma_f32_16x16x32_bf16 a[112:115], v[200:203], v[100:103], a[112:115]\n\t"
  "ds_read_b64_tr_b16 v[180:181], %[vk1] offset:49152\n\t"
  "ds_read_b64_tr_b16 v[182:183], %[vk1] offset:51200\n\t"
  "v_mfma_f32_16x16x32_bf16 a[116:119], v[200:203], v[156:159], a[116:119]\n\t"
  "v_mfma_f32_16x16x32_bf16 a[120:123], v[204:207], v[100:103], a[120:123]\n\t"
  "ds_read_b64_tr_b16 v[184:185], %[vk0] offset:49664\n\t"
  "ds_read_b64_tr_b16 v[186:187], %[vk0] offset:51712\n\t"
  "v_mfma_f32_16x16x32_bf16 a[124:127], v[204:207], v[156:159], a[124:127]\n\t"
  "buffer_load_dwordx4 v[172:175], %[xs], %[srdx], s56 offen offset:3072\n\t"
  "s_add_u32 m0, %[m0k], 122880\n\t"
  "s_waitcnt lgkmcnt(14)\n\t"
  "v_mfma_f32_16x16x32_bf16 a[128:131], v[208:211], v[100:103], a[128:131]\n\t"
  "ds_read_b64_tr_b16 v[188:189], %[vk1] offset:49664\n\t"
  "ds_read_b64_tr_b16 v[190:191], %[vk1] offset:51712\n\t"
  "v_mfma_f32_16x16x32_bf16 a[132:135], v[208:211], v[156:159], a[132:135]\n\t"
  "v_mfma_f32_16x16x32_bf16 a[136:139], v[212:215], v[100:103], a[136:139]\n\t"
  "ds_read_b64_tr_b16 v[192:193], %[vk0] offset:53248\n\t"
  "ds_read_b64_tr_b16 v[194:195], %[vk0] offset:55296\n\t"
  "v_mfma_f32_16x16x32_bf16 a[140:143], v[212:215], v[156:159], a[140:143]\n\t"
  "buffer_load_dwordx4 %[wqs], %[srdq], 0 offen offset:0 lds\n\t"
  "v_mfma_f32_16x16x32_bf16 a[144:147], v[216:219], v[100:103], a[144:147]\n\t"
  "ds_read_b64_tr_b16 v[196:197], %[vk1] offset:53248\n\t"
  "ds_read_b64_tr_b16 v[198:199], %[vk1] offset:55296\n\t"
  "v_mfma_f32_16x16x32_bf16 a[148:151], v[216:219], v[156:159], a[148:151]\n\t"
  "v_mfma_f32_16x16x32_bf16 a[152:155], v[220:223], v[100:103], a[152:155]\n\t"
  "ds_read_b64_tr_b16 v[200:201], %[vk0] offset:53760\n\t"
  "ds_read_b64_tr_b16 v[202:203], %[vk0] offset:55808\n\t"
  "v_mfma_f32_16x16x32_bf16 a[156:159], v[220:223], v[156:159], a[156:159]\n\t"
  "buffer_load_dwordx4 %[wqs], %[srdq], s46 offen offset:1024 lds\n\t"
  "s_waitcnt lgkmcnt(14)\n\t"
  "v_mfma_f32_16x16x32_bf16 a[160:163], v[224:227], v[100:103], a[160:163]\n\t"
  "ds_read_b64_tr_b16 v[204:205], %[vk1] offset:53760\n\t"
  "ds_read_b64_tr_b16 v[206:207], %[vk1] offset:55808\n\t"
  "v_mfma_f32_16x16x32_bf16 a[164:167], v[224:227], v[156:159], a[164:167]\n\t"
  "buffer_load_dwordx4 %[wqs], %[srdq], s47 offen offset:2048 lds\n\t"
  "v_mfma_f32_16x16x32_bf16 a[168:171], v[228:231], v[100:103], a[168:171]\n\t"
  "ds_read_b64_tr_b16 v[208:209], %[vk0] offset:57344\n\t"
  "ds_read_b64_tr_b16 v[210:211], %[vk0] offset:59392\n\t"
  "v_mfma_f32_16x16x32_bf16 a[172:175], v[228:231], v[156:159], a[172:175]\n\t"
  "v_mfma_f32_16x16x32_bf16 a[176:179], v[232:235], v[100:103], a[176:179]\n\t"
  "ds_read_b64_tr_b16 v[212:213], %[vk1] offset:57344\n\t"
  "ds_read_b64_tr_b16 v[214:215], %[vk1] offset:59392\n\t"
  "v_mfma_f32_16x16x32_bf16 a[180:183], v[232:235], v[156:159], a[180:183]\n\t"
  "buffer_load_dwordx4 %[wqs], %[srdq], s48 offen offset:3072 lds\n\t"
  "v_add_u32 %[wqs], 0x40000, %[wqs]\n\t"
  "s_add_u32 m0, %[m0i], 139264\n\t"
  "v_mfma_f32_16x16x32_bf16 a[184:187], v[236:239], v[100:103], a[184:187]\n\t"
  "ds_read_b64_tr_b16 v[216:217], %[vk0] offset:57856\n\t"
  "ds_read_b64_tr_b16 v[218:219], %[vk0] offset:59904\n\t"
  "v_mfma_f32_16x16x32_bf16 a[188:191], v[236:239], v[156:159], a[188:191]\n\t"
  "s_waitcnt lgkmcnt(14)\n\t"
  "v_mfma_f32_16x16x32_bf16 a[0:3], v[176:179], v[104:107], a[0:3]\n\t"
  "ds_read_b64_tr_b16 v[220:221], %[vk1] offset:57856\n\t"
  "ds_read_b64_tr_b16 v[222:223], %[vk1] offset:59904\n\t"
  "v_mfma_f32_16x16x32_bf16 a[4:7], v[176:179], v[160:163], a[4:7]\n\t"
  "buffer_load_dwordx4 %[wis], %[srdi], 0 offen offset:0 lds\n\t"
  "v_mfma_f32_16x16x32_bf16 a[8:11], v[180:183], v[104:107], a[8:11]\n\t"
  "ds_read_b64_tr_b16 v[224:225], %[vk0] offset:61440\n\t"
  "ds_read_b64_tr_b16 v[226:227], %[vk0] offset:63488\n\t"
  "v_mfma_f32_16x16x32_bf16 a[12:15], v[180:183], v[160:163], a[12:15]\n\t"
  "v_mfma_f32_16x16x32_bf16 a[16:19], v[184:187], v[104:107], a[16:19]\n\t"
  "ds_read_b64_tr_b16 v[228:229], %[vk1] offset:61440\n\t"
  "ds_read_b64_tr_b16 v[230:231], %[vk1] offset:63488\n\t"
  "v_mfma_f32_16x16x32_bf16 a[20:23], v[184:187], v[160:163], a[20:23]\n\t"
  "buffer_load_dwordx4 %[wis], %[srdi], s49 offen offset:1024 lds\n\t"
  "v_add_u32 %[wis], 114688, %[wis]\n\t"
  "s_add_u32 m0, %[m0k], 0\n\t"
  "v_mfma_f32_16x16x32_bf16 a[24:27], v[188:191], v[104:107], a[24:27]\n\t"
  "ds_read_b64_tr_b16 v[232:233], %[vk0] offset:61952\n\t"
  "ds_read_b64_tr_b16 v[234:235], %[vk0] offset:64000\n\t"
  "v_mfma_f32_16x16x32_bf16 a[28:31], v[188:191], v[160:163], a[28:31]\n\t"
  "buffer_load_dwordx4 %[ks0], %[srdk], 0 offen offset:0 lds\n\t"
  "s_waitcnt lgkmcnt(14)\n\t"
  "v_mfma_f32_16x16x32_bf16 a[32:35], v[192:195], v[104:107], a[32:35]\n\t"
  "ds_read_b64_tr_b16 v[236:237], %[vk1] offset:61952\n\t"
  "ds_read_b64_tr_b16 v[238:239], %[vk1] offset:64000\n\t"
  "v_mfma_f32_16x16x32_bf16 a[36:39], v[192:195], v[160:163], a[36:39]\n\t"
  "v_mfma_f32_16x16x32_bf16 a[40:43], v[196:199], v[104:107], a[40:43]\n\t"
  "ds_read_b64_tr_b16 v[176:177], %[va0] offset:0\n\t"
  "ds_read_b64_tr_b16 v[178:179], %[va0] offset:2048\n\t"
  "v_mfma_f32_16x16x32_bf16 a[44:47], v[196:199], v[160:163], a[44:47]\n\t"
  "buffer_load_dwordx4 %[ks1], %[srdk], s40 offen offset:1024 lds\n\t"
  "v_mfma_f32_16x16x32_bf16 a[48:51], v[200:203], v[104:107], a[48:51]\n\t"
  "ds_read_b64_tr_b16 v[180:181], %[va1] offset:0\n\t"
  "ds_read_b64_tr_b16 v[182:183], %[va1] offset:2048\n\t"
  "v_mfma_f32_16x16x32_bf16 a[52:55], v[200:203], v[160:163], a[52:55]\n\t"
  "v_mfma_f32_16x16x32_bf16 a[56:59], v[204:207], v[104:107], a[56:59]\n\t"
  "ds_read_b64_tr_b16 v[184:185], %[va0] offset:512\n\t"
  "ds_read_b64_tr_b16 v[186:187], %[va0] offset:2560\n\t"
  "v_mfma_f32_16x16x32_bf16 a[60:63], v[204:207], v[160:163], a[60:63]\n\t"
  "buffer_load_dwordx4 %[ks0], %[srdk], s41 offen offset:2048 lds\n\t"
  "s_waitcnt lgkmcnt(14)\n\t"
  "v_mfma_f32_16x16x32_bf16 a[64:67], v[208:211], v[104:107], a[64:67]\n\t"
  "ds_read_b64_tr_b16 v[188:189], %[va1] offset:512\n\t"
  "ds_read_b64_tr_b16 v[190:191], %[va1] offset:2560\n\t"
  "v_mfma_f32_16x16x32_bf16 a[68:71], v[208:211], v[160:163], a[68:71]\n\t"
  "buffer_load_dwordx4 %[ks1], %[srdk], s42 offen offset:3072 lds\n\t"
  "v_add_u32 %[ks0], 0x4000, %[ks0]\n\t"
  "v_add_u32 %[ks1], 0x4000, %[ks1]\n\t"
  "v_mfma_f32_16x16x32_bf16 a[72:75], v[212:215], v[104:107], a[72:75]\n\t"
  "ds_read_b64_tr_b16 v[192:193], %[va0] offset:4096\n\t"
  "ds_read_b64_tr_b16 v[194:195], %[va0] offset:6144\n\t"
  "v_mfma_f32_16x16x32_bf16 a[76:79], v[212:215], v[160:163], a[76:79]\n\t"
  "v_mfma_f32_16x16x32_bf16 a[80:83], v[216:219], v[104:107], a[80:83]\n\t"
  "ds_read_b64_tr_b16 v[196:197], %[va1] offset:4096\n\t"
  "ds_read_b64_tr_b16 v[198:199], %[va1] offset:6144\n\t"
  "v_mfma_f32_16x16x32_bf16 a[84:87], v[216:219], v[160:163], a[84:87]\n\t"
  "v_mfma_f32_16x16x32_bf16 a[88:91], v[220:223], v[104:107], a[88:91]\n\t"
  "ds_read_b64_tr_b16 v[200:201], %[va0] offset:4608\n\t"
  "ds_read_b64_tr_b16 v[202:203], %[va0] offset:6656\n\t"
  "v_mfma_f32_16x16x32_bf16 a[92:95], v[220:223], v[160:163], a[92:95]\n\t"
  "s_waitcnt lgkmcnt(14)\n\t"
  "v_mfma_f32_16x16x32_bf16 a[96:99], v[224:227], v[104:107], a[96:99]\n\t"
  "ds_read_b64_tr_b16 v[204:205], %[va1] offset:4608\n\t"
  "ds_read_b64_tr_b16 v[206:207], %[va1] offset:6656\n\t"
  "v_mfma_f32_16x16x32_bf16 a[100:103], v[224:227], v[160:163], a[100:103]\n\t"
  "v_mfma_f32_16x16x32_bf16 a[104:107], v[228:231], v[104:107], a[104:107]\n\t"
  "s_waitcnt vmcnt(20)\n\t"
  "s_barrier\n\t"
  "ds_read_b64_tr_b16 v[208:209], %[va0] offset:8192\n\t"
  "ds_read_b64_tr_b16 v[210:211], %[va0] offset:10240\n\t"
  "v_mfma_f32_16x16x32_bf16 a[108:111], v[228:231], v[160:163], a[108:111]\n\t"
  "s_add_u32 m0, %[m0k], 16384\n\t"
  "s_nop 0\n\t"
  "buffer_load_dwordx4 %[ks0], %[srdk], 0 offen offset:0 lds\n\t"
  "v_mfma_f32_16x16x32_bf16 a[112:115], v[232:235], v[104:107], a[112:115]\n\t"
  "ds_read_b64_tr_b16 v[212:213], %[va1] offset:8192\n\t"
  "ds_read_b64_tr_b16 v[214:215], %[va1] offset:10240\n\t"
  "v_mfma_f32_16x16x32_bf16 a[116:119], v[232:235], v[160:163], a[116:119]\n\t"
  "v_mfma_f32_16x16x32_bf16 a[120:123], v[236:239], v[104:107], a[120:123]\n\t"
  "ds_read_b64_tr_b16 v[216:217], %[va0] offset:8704\n\t"
  "ds_read_b64_tr_b16 v[218:219], %[va0] offset:10752\n\t"
  "v_mfma_f32_16x16x32_bf16 a[124:127], v[236:239], v[160:163], a[124:127]\n\t"
  "s_waitcnt lgkmcnt(14)\n\t"
  "v_mfma_f32_16x16x32_bf16 a[128:131], v[176:179], v[104:107], a[128:131]\n\t"
  "ds_read_b64_tr_b16 v[220:221], %[va1] offset:8704\n\t"
  "ds_read_b64_tr_b16 v[222:223], %[va1] offset:10752\n\t"
  "v_mfma_f32_16x16x32_bf16 a[132:135], v[176:179], v[160:163], a[132:135]\n\t"
  "buffer_load_dwordx4 %[ks1], %[srdk], s40 offen offset:1024 lds\n\t"
  "v_mfma_f32_16x16x32_bf16 a[136:139], v[180:183], v[104:107], a[136:139]\n\t"
  "ds_read_b64_tr_b16 v[224:225], %[va0] offset:12288\n\t"
  "ds_read_b64_tr_b16 v[226:227], %[va0] offset:14336\n\t"
  "v_mfma_f32_16x16x32_bf16 a[140:143], v[180:183], v[160:163], a[140:143]\n\t"
  "v_mfma_f32_16x16x32_bf16 a[144:147], v[184:187], v[104:107], a[144:147]\n\t"
  "ds_read_b64_tr_b16 v[228:229], %[va1] offset:12288\n\t"
  "ds_read_b64_tr_b16 v[230:231], %[va1] offset:14336\n\t"
  "v_mfma_f32_16x16x32_bf16 a[148:151], v[184:187], v[160:163], a[148:151]\n\t"
  "v_mfma_f32_16x16x32_bf16 a[152:155], v[188:191], v[104:107], a[152:155]\n\t"
  "ds_read_b64_tr_b16 v[232:233], %[va0] offset:12800\n\t"
  "ds_read_b64_tr_b16 v[234:235], %[va0] offset:14848\n\t"
  "v_mfma_f32_16x16x32_bf16 a[156:159], v[188:191], v[160:163], a[156:159]\n\t"
  "buffer_load_dwordx4 %[ks0], %[srdk], s41 offen offset:2048 lds\n\t"
  "s_waitcnt lgkmcnt(14)\n\t"
  "v_mfma_f32_16x16x32_bf16 a[160:163], v[192:195], v[104:107], a[160:163]\n\t"
  "ds_read_b64_tr_b16 v[236:237], %[va1] offset:12800\n\t"
  "ds_read_b64_tr_b16 v[238:239], %[va1] offset:14848\n\t"
  "v_mfma_f32_16x16x32_bf16 a[164:167], v[192:195], v[160:163], a[164:167]\n\t"
  "v_mfma_f32_16x16x32_bf16 a[168:171], v[196:199], v[104:107], a[168:171]\n\t"
  "ds_read_b64_tr_b16 v[176:177], %[va0] offset:16384\n\t"
  "ds_read_b64_tr_b16 v[178:179], %[va0] offset:18432\n\t"
  "v_mfma_f32_16x16x32_bf16 a[172:175], v[196:199], v[160:163], a[172:175]\n\t"
  "buffer_load_dwordx4 %[ks1], %[srdk], s42 offen offset:3072 lds\n\t"
  "v_add_u32 %[ks0], 0x4000, %[ks0]\n\t"
  "v_add_u32 %[ks1], 0x4000, %[ks1]\n\t"
  "s_add_u32 m0, %[m0k], 32768\n\t"
  "v_mfma_f32_16x16x32_bf16 a[176:179], v[200:203], v[104:107], a[176:179]\n\t"
  "ds_read_b64_tr_b16 v[180:181], %[va1] offset:16384\n\t"
  "ds_read_b64_tr_b16 v[182:183], %[va1] offset:18432\n\t"
  "v_mfma_f32_16x16x32_bf16 a[180:183], v[200:203], v[160:163], a[180:183]\n\t"
  "v_mfma_f32_16x16x32_bf16 a[184:187], v[204:207], v[104:107], a[184:187]\n\t"
  "ds_read_b64_tr_b16 v[184:185], %[va0] offset:16896\n\t"
  "ds_read_b64_tr_b16 v[186:187], %[va0] offset:18944\n\t"
  "v_mfma_f32_16x16x32_bf16 a[188:191], v[204:207], v[160:163], a[188:191]\n\t"
  "s_waitcnt lgkmcnt(14)\n\t"
  "v_mfma_f32_16x16x32_bf16 a[0:3], v[208:211], v[108:111], a[0:3]\n\t"
  "ds_read_b64_tr_b16 v[188:189], %[va1] offset:16896\n\t"
  "ds_read_b64_tr_b16 v[190:191], %[va1] offset:18944\n\t"
  "v_mfma_f32_16x16x32_bf16 a[4:7], v[208:211], v[164:167], a[4:7]\n\t"
  "buffer_load_dwordx4 %[ks0], %[srdk], 0 offen offset:0 lds\n\t"
  "v_mfma_f32_16x16x32_bf16 a[8:11], v[212:215], v[108:111], a[8:11]\n\t"
  "ds_read_b64_tr_b16 v[192:193], %[va0] offset:20480\n\t"
  "ds_read_b64_tr_b16 v[194:195], %[va0] offset:22528\n\t"
  "v_mfma_f32_16x16x32_bf16 a[12:15], v[212:215], v[164:167], a[12:15]\n\t"
  "v_mfma_f32_16x16x32_bf16 a[16:19], v[216:219], v[108:111], a[16:19]\n\t"
  "ds_read_b64_tr_b16 v[196:197], %[va1] offset:20480\n\t"
  "ds_read_b64_tr_b16 v[198:199], %[va1] offset:22528\n\t"
  "v_mfma_f32_16x16x32_bf16 a[20:23], v[216:219], v[164:167], a[20:23]\n\t"
  "buffer_load_dwordx4 %[ks1], %[srdk], s40 offen offset:1024 lds\n\t"
  "v_mfma_f32_16x16x32_bf16 a[24:27], v[220:223], v[108:111], a[24:27]\n\t"
  "ds_read_b64_tr_b16 v[200:201], %[va0] offset:20992\n\t"
  "ds_read_b64_tr_b16 v[202:203], %[va0] offset:23040\n\t"
  "v_mfma_f32_16x16x32_bf16 a[28:31], v[220:223], v[164:167], a[28:31]\n\t"
  "s_waitcnt lgkmcnt(14)\n\t"
  "v_mfma_f32_16x16x32_bf16 a[32:35], v[224:227], v[108:111], a[32:35]\n\t"
  "ds_read_b64_tr_b16 v[204:205], %[va1] offset:20992\n\t"
  "ds_read_b64_tr_b16 v[206:207], %[va1] offset:23040\n\t"
  "v_mfma_f32_16x16x32_bf16 a[36:39], v[224:227], v[164:167], a[36:39]\n\t"
  "v_mfma_f32_16x16x32_bf16 a[40:43], v[228:231], v[108:111], a[40:43]\n\t"
  "ds_read_b64_tr_b16 v[208:209], %[va0] offset:24576\n\t"
  "ds_read_b64_tr_b16 v[210:211], %[va0] offset:26624\n\t"
  "v_mfma_f32_16x16x32_bf16 a[44:47], v[228:231], v[164:167], a[44:47]\n\t"
  "buffer_load_dwordx4 %[ks0], %[srdk], s41 offen offset:2048 lds\n\t"
  "v_mfma_f32_16x16x32_bf16 a[48:51], v[232:235], v[108:111], a[48:51]\n\t"
  "ds_read_b64_tr_b16 v[212:213], %[va1] offset:24576\n\t"
  "ds_read_b64_tr_b16 v[214:215], %[va1] offset:26624\n\t"
  "v_mfma_f32_16x16x32_bf16 a[52:55], v[232:235], v[164:167], a[52:55]\n\t"
  "v_mfma_f32_16x16x32_bf16 a[56:59], v[236:239], v[108:111], a[56:59]\n\t"
  "ds_read_b64_tr_b16 v[216:217], %[va0] offset:25088\n\t"
  "ds_read_b64_tr_b16 v[218:219], %[va0] offset:27136\n\t"
  "v_mfma_f32_16x16x32_bf16 a[60:63], v[236:239], v[164:167], a[60:63]\n\t"
  "s_waitcnt lgkmcnt(14)\n\t"
  "v_mfma_f32_16x16x32_bf16 a[64:67], v[176:179], v[108:111], a[64:67]\n\t"
  "ds_read_b64_tr_b16 v[220:221], %[va1] offset:25088\n\t"
  "ds_read_b64_tr_b16 v[222:223], %[va1] offset:27136\n\t"
  "v_mfma_f32_16x16x32_bf16 a[68:71], v[176:179], v[164:167], a[68:71]\n\t"
  "buffer_load_dwordx4 %[ks1], %[srdk], s42 offen offset:3072 lds\n\t"
  "v_add_u32 %[ks0], 0x4000, %[ks0]\n\t"
  "v_add_u32 %[ks1], 0x4000, %[ks1]\n\t"
  "v_mfma_f32_16x16x32_bf16 a[72:75], v[180:183], v[108:111], a[72:75]\n\t"
  "ds_read_b64_tr_b16 v[224:225], %[va0] offset:28672\n\t"
  "ds_read_b64_tr_b16 v[226:227], %[va0] offset:30720\n\t"
  "v_mfma_f32_16x16x32_bf16 a[76:79], v[180:183], v[164:167], a[76:79]\n\t"
  "v_mfma_f32_16x16x32_bf16 a[80:83], v[184:187], v[108:111], a[80:83]\n\t"
  "ds_read_b64_tr_b16 v[228:229], %[va1] offset:28672\n\t"
  "ds_read_b64_tr_b16 v[230:231], %[va1] offset:30720\n\t"
  "v_mfma_f32_16x16x32_bf16 a[84:87], v[184:187], v[164:167], a[84:87]\n\t"
  "v_mfma_f32_16x16x32_bf16 a[88:91], v[188:191], v[108:111], a[88:91]\n\t"
  "ds_read_b64_tr_b16 v[232:233], %[va0] offset:29184\n\t"
  "ds_read_b64_tr_b16 v[234:235], %[va0] offset:31232\n\t"
  "v_mfma_f32_16x16x32_bf16 a[92:95], v[188:191], v[164:167], a[92:95]\n\t"
  "s_waitcnt lgkmcnt(14)\n\t"
  "v_mfma_f32_16x16x32_bf16 a[96:99], v[192:195], v[108:111], a[96:99]\n\t"
  "ds_read_b64_tr_b16 v[236:237], %[va1] offset:29184\n\t"
  "ds_read_b64_tr_b16 v[238:239], %[va1] offset:31232\n\t"
  "v_mfma_f32_16x16x32_bf16 a[100:103], v[192:195], v[164:167], a[100:103]\n\t"
  "v_mfma_f32_16x16x32_bf16 a[104:107], v[196:199], v[108:111], a[104:107]\n\t"
  "s_waitcnt vmcnt(20)\n\t"
  "s_barrier\n\t"
  "ds_read_b64_tr_b16 v[176:177], %[va0] offset:32768\n\t"
  "ds_read_b64_tr_b16 v[178:179], %[va0] offset:34816\n\t"
  "v_mfma_f32_16x16x32_bf16 a[108:111], v[196:199], v[164:167], a[108:111]\n\t"
  "s_add_u32 m0, %[m0k], 49152\n\t"
  "s_nop 0\n\t"
  "buffer_load_dwordx4 %[ks0], %[srdk], 0 offen offset:0 lds\n\t"
  "v_mfma_f32_16x16x32_bf16 a[112:115], v[200:203], v[108:111], a[112:115]\n\t"
  "ds_read_b64_tr_b16 v[180:181], %[va1] offset:32768\n\t"
  "ds_read_b64_tr_b16 v[182:183], %[va1] offset:34816\n\t"
  "v_mfma_f32_16x16x32_bf16 a[116:119], v[200:203], v[164:167], a[116:119]\n\t"
  "v_mfma_f32_16x16x32_bf16 a[120:123], v[204:207], v[108:111], a[120:123]\n\t"
  "ds_read_b64_tr_b16 v[184:185], %[va0] offset:33280\n\t"
  "ds_read_b64_tr_b16 v[186:187], %[va0] offset:35328\n\t"
  "v_mfma_f32_16x16x32_bf16 a[124:127], v[204:207], v[164:167], a[124:127]\n\t"
  "s_waitcnt lgkmcnt(14)\n\t"
  "v_mfma_f32_16x16x32_bf16 a[128:131], v[208:211], v[108:111], a[128:131]\n\t"
  "ds_read_b64_tr_b16 v[188:189], %[va1] offset:33280\n\t"
  "ds_read_b64_tr_b16 v[190:191], %[va1] offset:35328\n\t"
  "v_mfma_f32_16x16x32_bf16 a[132:135], v[208:211], v[164:167], a[132:135]\n\t"
  "v_mfma_f32_16x16x32_bf16 a[136:139], v[212:215], v[108:111], a[136:139]\n\t"
  "ds_read_b64_tr_b16 v[192:193], %[va0] offset:36864\n\t"
  "ds_read_b64_tr_b16 v[194:195], %[va0] offset:38912\n\t"
  "v_mfma_f32_16x16x32_bf16 a[140:143], v[212:215], v[164:167], a[140:143]\n\t"
  "v_mfma_f32_16x16x32_bf16 a[144:147], v[216:219], v[108:111], a[144:147]\n\t"
  "ds_read_b64_tr_b16 v[196:197], %[va1] offset:36864\n\t"
  "ds_read_b64_tr_b16 v[198:199], %[va1] offset:38912\n\t"
  "v_mfma_f32_16x16x32_bf16 a[148:151], v[216:219], v[164:167], a[148:151]\n\t"
  "v_mfma_f32_16x16x32_bf16 a[152:155], v[220:223], v[108:111], a[152:155]\n\t"
  "ds_read_b64_tr_b16 v[200:201], %[va0] offset:37376\n\t"
  "ds_read_b64_tr_b16 v[202:203], %[va0] offset:39424\n\t"
  "v_mfma_f32_16x16x32_bf16 a[156:159], v[220:223], v[164:167], a[156:159]\n\t"
  "buffer_load_dwordx4 %[ks1], %[srdk], s40 offen offset:1024 lds\n\t"
  "s_waitcnt lgkmcnt(14)\n\t"
  "v_mfma_f32_16x16x32_bf16 a[160:163], v[224:227], v[108:111], a[160:163]\n\t"
  "ds_read_b64_tr_b16 v[204:205], %[va1] offset:37376\n\t"
  "ds_read_b64_tr_b16 v[206:207], %[va1] offset:39424\n\t"
  "v_mfma_f32_16x16x32_bf16 a[164:167], v[224:227], v[164:167], a[164:167]\n\t"
  "v_mfma_f32_16x16x32_bf16 a[168:171], v[228:231], v[108:111], a[168:171]\n\t"
  "ds_read_b64_tr_b16 v[208:209], %[va0] offset:40960\n\t"
  "ds_read_b64_tr_b16 v[210:211], %[va0] offset:43008\n\t"
  "v_mfma_f32_16x16x32_bf16 a[172:175], v[228:231], v[164:167], a[172:175]\n\t"
  "v_mfma_f32_16x16x32_bf16 a[176:179], v[232:235], v[108:111], a[176:179]\n\t"
  "ds_read_b64_tr_b16 v[212:213], %[va1] offset:40960\n\t"
  "ds_read_b64_tr_b16 v[214:215], %[va1] offset:43008\n\t"
  "v_mfma_f32_16x16x32_bf16 a[180:183], v[232:235], v[164:167], a[180:183]\n\t"
  "v_mfma_f32_16x16x32_bf16 a[184:187], v[236:239], v[108:111], a[184:187]\n\t"
  "ds_read_b64_tr_b16 v[216:217], %[va0] offset:41472\n\t"
  "ds_read_b64_tr_b16 v[218:219], %[va0] offset:43520\n\t"
  "v_mfma_f32_16x16x32_bf16 a[188:191], v[236:239], v[164:167], a[188:191]\n\t"
  "s_waitcnt lgkmcnt(14)\n\t"
  "v_mfma_f32_16x16x32_bf16 a[0:3], v[176:179], v[112:115], a[0:3]\n\t"
  "ds_read_b64_tr_b16 v[220:221], %[va1] offset:41472\n\t"
  "ds_read_b64_tr_b16 v[222:223], %[va1] offset:43520\n\t"
  "v_mfma_f32_16x16x32_bf16 a[4:7], v[176:179], v[168:171], a[4:7]\n\t"
  "buffer_load_dwordx4 %[ks0], %[srdk], s41 offen offset:2048 lds\n\t"
  "v_mfma_f32_16x16x32_bf16 a[8:11], v[180:183], v[112:115], a[8:11]\n\t"
  "ds_read_b64_tr_b16 v[224:225], %[va0] offset:45056\n\t"
  "ds_read_b64_tr_b16 v[226:227], %[va0] offset:47104\n\t"
  "v_mfma_f32_16x16x32_bf16 a[12:15], v[180:183], v[168:171], a[12:15]\n\t"
  "v_mfma_f32_16x16x32_bf16 a[16:19], v[184:187], v[112:115], a[16:19]\n\t"
  "ds_read_b64_tr_b16 v[228:229], %[va1] offset:45056\n\t"
  "ds_read_b64_tr_b16 v[230:231], %[va1] offset:47104\n\t"
  "v_mfma_f32_16x16x32_bf16 a[20:23], v[184:187], v[168:171], a[20:23]\n\t"
  "v_mfma_f32_16x16x32_bf16 a[24:27], v[188:191], v[112:115], a[24:27]\n\t"
  "ds_read_b64_tr_b16 v[232:233], %[va0] offset:45568\n\t"
  "ds_read_b64_tr_b16 v[234:235], %[va0] offset:47616\n\t"
  "v_mfma_f32_16x16x32_bf16 a[28:31], v[188:191], v[168:171], a[28:31]\n\t"
  "s_waitcnt lgkmcnt(14)\n\t"
  "v_mfma_f32_16x16x32_bf16 a[32:35], v[192:195], v[112:115], a[32:35]\n\t"
  "ds_read_b64_tr_b16 v[236:237], %[va1] offset:45568\n\t"
  "ds_read_b64_tr_b16 v[238:239], %[va1] offset:47616\n\t"
  "v_mfma_f32_16x16x32_bf16 a[36:39], v[192:195], v[168:171], a[36:39]\n\t"
  "v_mfma_f32_16x16x32_bf16 a[40:43], v[196:199], v[112:115], a[40:43]\n\t"
  "ds_read_b64_tr_b16 v[176:177], %[va0] offset:49152\n\t"
  "ds_read_b64_tr_b16 v[178:179], %[va0] offset:51200\n\t"
  "v_mfma_f32_16x16x32_bf16 a[44:47], v[196:199], v[168:171], a[44:47]\n\t"
  "buffer_load_dwordx4 %[ks1], %[srdk], s42 offen offset:3072 lds\n\t"
  "v_add_u32 %[ks0], 0x4000, %[ks0]\n\t"
  "v_add_u32 %[ks1], 0x4000, %[ks1]\n\t"
  "v_mfma_f32_16x16x32_bf16 a[48:51], v[200:203], v[112:115], a[48:51]\n\t"
  "ds_read_b64_tr_b16 v[180:181], %[va1] offset:49152\n\t"
  "ds_read_b64_tr_b16 v[182:183], %[va1] offset:51200\n\t"
  "v_mfma_f32_16x16x32_bf16 a[52:55], v[200:203], v[168:171], a[52:55]\n\t"
  "v_mfma_f32_16x16x32_bf16 a[56:59], v[204:207], v[112:115], a[56:59]\n\t"
  "ds_read_b64_tr_b16 v[184:185], %[va0] offset:49664\n\t"
  "ds_read_b64_tr_b16 v[186:187], %[va0] offset:51712\n\t"
  "v_mfma_f32_16x16x32_bf16 a[60:63], v[204:207], v[168:171], a[60:63]\n\t"
  "s_waitcnt lgkmcnt(14)\n\t"
  "v_mfma_f32_16x16x32_bf16 a[64:67], v[208:211], v[112:115], a[64:67]\n\t"
  "ds_read_b64_tr_b16 v[188:189], %[va1] offset:49664\n\t"
  "ds_read_b64_tr_b16 v[190:191], %[va1] offset:51712\n\t"
  "v_mfma_f32_16x16x32_bf16 a[68:71], v[208:211], v[168:171], a[68:71]\n\t"
  "v_mfma_f32_16x16x32_bf16 a[72:75], v[212:215], v[112:115], a[72:75]\n\t"
  "ds_read_b64_tr_b16 v[192:193], %[va0] offset:53248\n\t"
  "ds_read_b64_tr_b16 v[194:195], %[va0] offset:55296\n\t"
  "v_mfma_f32_16x16x32_bf16 a[76:79], v[212:215], v[168:171], a[76:79]\n\t"
  "v_mfma_f32_16x16x32_bf16 a[80:83], v[216:219], v[112:115], a[80:83]\n\t"
  "ds_read_b64_tr_b16 v[196:197], %[va1] offset:53248\n\t"
  "ds_read_b64_tr_b16 v[198:199], %[va1] offset:55296\n\t"
  "v_mfma_f32_16x16x32_bf16 a[84:87], v[216:219], v[168:171], a[84:87]\n\t"
  "v_mfma_f32_16x16x32_bf16 a[88:91], v[220:223], v[112:115], a[88:91]\n\t"
  "ds_read_b64_tr_b16 v[200:201], %[va0] offset:53760\n\t"
  "ds_read_b64_tr_b16 v[202:203], %[va0] offset:55808\n\t"
  "v_mfma_f32_16x16x32_bf16 a[92:95], v[220:223], v[168:171], a[92:95]\n\t"
  "s_waitcnt lgkmcnt(14)\n\t"
  "v_mfma_f32_16x16x32_bf16 a[96:99], v[224:227], v[112:115], a[96:99]\n\t"
  "ds_read_b64_tr_b16 v[204:205], %[va1] offset:53760\n\t"
  "ds_read_b64_tr_b16 v[206:207], %[va1] offset:55808\n\t"
  "v_mfma_f32_16x16x32_bf16 a[100:103], v[224:227], v[168:171], a[100:103]\n\t"
  "v_mfma_f32_16x16x32_bf16 a[104:107], v[228:231], v[112:115], a[104:107]\n\t"
  "s_waitcnt vmcnt(16)\n\t"
  "s_barrier\n\t"
  "ds_read_b64_tr_b16 v[208:209], %[va0] offset:57344\n\t"
  "ds_read_b64_tr_b16 v[210:211], %[va0] offset:59392\n\t"
  "v_mfma_f32_16x16x32_bf16 a[108:111], v[228:231], v[168:171], a[108:111]\n\t"
  "s_add_u32 m0, %[m0v], 0\n\t"
  "s_nop 0\n\t"
  "buffer_load_dwordx4 %[vs], %[srdv], 0 offen offset:0 lds\n\t"
  "v_mfma_f32_16x16x32_bf16 a[112:115], v[232:235], v[112:115], a[112:115]\n\t"
  "ds_read_b64_tr_b16 v[212:213], %[va1] offset:57344\n\t"
  "ds_read_b64_tr_b16 v[214:215], %[va1] offset:59392\n\t"
  "v_mfma_f32_16x16x32_bf16 a[116:119], v[232:235], v[168:171], a[116:119]\n\t"
  "v_mfma_f32_16x16x32_bf16 a[120:123], v[236:239], v[112:115], a[120:123]\n\t"
  "ds_read_b64_tr_b16 v[216:217], %[va0] offset:57856\n\t"
  "ds_read_b64_tr_b16 v[218:219], %[va0] offset:59904\n\t"
  "v_mfma_f32_16x16x32_bf16 a[124:127], v[236:239], v[168:171], a[124:127]\n\t"
  "s_waitcnt lgkmcnt(14)\n\t"
  "v_mfma_f32_16x16x32_bf16 a[128:131], v[176:179], v[112:115], a[128:131]\n\t"
  "ds_read_b64_tr_b16 v[220:221], %[va1] offset:57856\n\t"
  "ds_read_b64_tr_b16 v[222:223], %[va1] offset:59904\n\t"
  "v_mfma_f32_16x16x32_bf16 a[132:135], v[176:179], v[168:171], a[132:135]\n\t"
  "buffer_load_dwordx4 %[vs], %[srdv], s40 offen offset:1024 lds\n\t"
  "v_mfma_f32_16x16x32_bf16 a[136:139], v[180:183], v[112:115], a[136:139]\n\t"
  "ds_read_b64_tr_b16 v[224:225], %[va0] offset:61440\n\t"
  "ds_read_b64_tr_b16 v[226:227], %[va0] offset:63488\n\t"
  "v_mfma_f32_16x16x32_bf16 a[140:143], v[180:183], v[168:171], a[140:143]\n\t"
  "v_mfma_f32_16x16x32_bf16 a[144:147], v[184:187], v[112:115], a[144:147]\n\t"
  "ds_read_b64_tr_b16 v[228:229], %[va1] offset:61440\n\t"
  "ds_read_b64_tr_b16 v[230:231], %[va1] offset:63488\n\t"
  "v_mfma_f32_16x16x32_bf16 a[148:151], v[184:187], v[168:171], a[148:151]\n\t"
  "v_mfma_f32_16x16x32_bf16 a[152:155], v[188:191], v[112:115], a[152:155]\n\t"
  "ds_read_b64_tr_b16 v[232:233], %[va0] offset:61952\n\t"
  "ds_read_b64_tr_b16 v[234:235], %[va0] offset:64000\n\t"
  "v_mfma_f32_16x16x32_bf16 a[156:159], v[188:191], v[168:171], a[156:159]\n\t"
  "buffer_load_dwordx4 %[vs], %[srdv], s41 offen offset:2048 lds\n\t"
  "s_waitcnt lgkmcnt(14)\n\t"
  "v_mfma_f32_16x16x32_bf16 a[160:163], v[192:195], v[112:115], a[160:163]\n\t"
  "ds_read_b64_tr_b16 v[236:237], %[va1] offset:61952\n\t"
  "ds_read_b64_tr_b16 v[238:239], %[va1] offset:64000\n\t"
  "v_mfma_f32_16x16x32_bf16 a[164:167], v[192:195], v[168:171], a[164:167]\n\t"
  "v_mfma_f32_16x16x32_bf16 a[168:171], v[196:199], v[112:115], a[168:171]\n\t"
  "ds_read_b64_tr_b16 v[176:177], %[vz0] offset:0\n\t"
  "ds_read_b64_tr_b16 v[178:179], %[vz0] offset:2048\n\t"
  "v_mfma_f32_16x16x32_bf16 a[172:175], v[196:199], v[168:171], a[172:175]\n\t"
  "buffer_load_dwordx4 %[vs], %[srdv], s42 offen offset:3072 lds\n\t"
  "v_add_u32 %[vs], 0x4000, %[vs]\n\t"
  "s_add_u32 m0, %[m0v], 16384\n\t"
  "v_mfma_f32_16x16x32_bf16 a[176:179], v[200:203], v[112:115], a[176:179]\n\t"
  "ds_read_b64_tr_b16 v[180:181], %[vz1] offset:0\n\t"
  "ds_read_b64_tr_b16 v[182:183], %[vz1] offset:2048\n\t"
  "v_mfma_f32_16x16x32_bf16 a[180:183], v[200:203], v[168:171], a[180:183]\n\t"
  "v_mfma_f32_16x16x32_bf16 a[184:187], v[204:207], v[112:115], a[184:187]\n\t"
  "ds_read_b64_tr_b16 v[184:185], %[vz0] offset:512\n\t"
  "ds_read_b64_tr_b16 v[186:187], %[vz0] offset:2560\n\t"
  "v_mfma_f32_16x16x32_bf16 a[188:191], v[204:207], v[168:171], a[188:191]\n\t"
  "s_waitcnt lgkmcnt(14)\n\t"
  "v_mfma_f32_16x16x32_bf16 a[0:3], v[208:211], v[116:119], a[0:3]\n\t"
  "ds_read_b64_tr_b16 v[188:189], %[vz1] offset:512\n\t"
  "ds_read_b64_tr_b16 v[190:191], %[vz1] offset:2560\n\t"
  "v_mfma_f32_16x16x32_bf16 a[4:7], v[208:211], v[172:175], a[4:7]\n\t"
  "buffer_load_dwordx4 %[vs], %[srdv], 0 offen offset:0 lds\n\t"
  "v_mfma_f32_16x16x32_bf16 a[8:11], v[212:215], v[116:119], a[8:11]\n\t"
  "ds_read_b64_tr_b16 v[192:193], %[vz0] offset:4096\n\t"
  "ds_read_b64_tr_b16 v[194:195], %[vz0] offset:6144\n\t"
  "v_mfma_f32_16x16x32_bf16 a[12:15], v[212:215], v[172:175], a[12:15]\n\t"
  "v_mfma_f32_16x16x32_bf16 a[16:19], v[216:219], v[116:119], a[16:19]\n\t"
  "ds_read_b64_tr_b16 v[196:197], %[vz1] offset:4096\n\t"
  "ds_read_b64_tr_b16 v[198:199], %[vz1] offset:6144\n\t"
  "v_mfma_f32_16x16x32_bf16 a[20:23], v[216:219], v[172:175], a[20:23]\n\t"
  "buffer_load_dwordx4 %[vs], %[srdv], s40 offen offset:1024 lds\n\t"
  "v_mfma_f32_16x16x32_bf16 a[24:27], v[220:223], v[116:119], a[24:27]\n\t"
  "ds_read_b64_tr_b16 v[200:201], %[vz0] offset:4608\n\t"
  "ds_read_b64_tr_b16 v[202:203], %[vz0] offset:6656\n\t"
  "v_mfma_f32_16x16x32_bf16 a[28:31], v[220:223], v[172:175], a[28:31]\n\t"
  "s_waitcnt lgkmcnt(14)\n\t"
  "v_mfma_f32_16x16x32_bf16 a[32:35], v[224:227], v[116:119], a[32:35]\n\t"
  "ds_read_b64_tr_b16 v[204:205], %[vz1] offset:4608\n\t"
  "ds_read_b64_tr_b16 v[206:207], %[vz1] offset:6656\n\t"
  "v_mfma_f32_16x16x32_bf16 a[36:39], v[224:227], v[172:175], a[36:39]\n\t"
  "v_mfma_f32_16x16x32_bf16 a[40:43], v[228:231], v[116:119], a[40:43]\n\t"
  "ds_read_b64_tr_b16 v[208:209], %[vz0] offset:8192\n\t"
  "ds_read_b64_tr_b16 v[210:211], %[vz0] offset:10240\n\t"
  "v_mfma_f32_16x16x32_bf16 a[44:47], v[228:231], v[172:175], a[44:47]\n\t"
  "buffer_load_dwordx4 %[vs], %[srdv], s41 offen offset:2048 lds\n\t"
  "v_mfma_f32_16x16x32_bf16 a[48:51], v[232:235], v[116:119], a[48:51]\n\t"
  "ds_read_b64_tr_b16 v[212:213], %[vz1] offset:8192\n\t"
  "ds_read_b64_tr_b16 v[214:215], %[vz1] offset:10240\n\t"
  "v_mfma_f32_16x16x32_bf16 a[52:55], v[232:235], v[172:175], a[52:55]\n\t"
  "v_mfma_f32_16x16x32_bf16 a[56:59], v[236:239], v[116:119], a[56:59]\n\t"
  "ds_read_b64_tr_b16 v[216:217], %[vz0] offset:8704\n\t"
  "ds_read_b64_tr_b16 v[218:219], %[vz0] offset:10752\n\t"
  "v_mfma_f32_16x16x32_bf16 a[60:63], v[236:239], v[172:175], a[60:63]\n\t"
  "s_waitcnt lgkmcnt(14)\n\t"
  "v_mfma_f32_16x16x32_bf16 a[64:67], v[176:179], v[116:119], a[64:67]\n\t"
  "ds_read_b64_tr_b16 v[220:221], %[vz1] offset:8704\n\t"
  "ds_read_b64_tr_b16 v[222:223], %[vz1] offset:10752\n\t"
  "v_mfma_f32_16x16x32_bf16 a[68:71], v[176:179], v[172:175], a[68:71]\n\t"
  "buffer_load_dwordx4 %[vs], %[srdv], s42 offen offset:3072 lds\n\t"
  "v_add_u32 %[vs], 0x4000, %[vs]\n\t"
  "v_mfma_f32_16x16x32_bf16 a[72:75], v[180:183], v[116:119], a[72:75]\n\t"
  "ds_read_b64_tr_b16 v[224:225], %[vz0] offset:12288\n\t"
  "ds_read_b64_tr_b16 v[226:227], %[vz0] offset:14336\n\t"
  "v_mfma_f32_16x16x32_bf16 a[76:79], v[180:183], v[172:175], a[76:79]\n\t"
  "v_mfma_f32_16x16x32_bf16 a[80:83], v[184:187], v[116:119], a[80:83]\n\t"
  "ds_read_b64_tr_b16 v[228:229], %[vz1] offset:12288\n\t"
  "ds_read_b64_tr_b16 v[230:231], %[vz1] offset:14336\n\t"
  "v_mfma_f32_16x16x32_bf16 a[84:87], v[184:187], v[172:175], a[84:87]\n\t"
  "v_mfma_f32_16x16x32_bf16 a[88:91], v[188:191], v[116:119], a[88:91]\n\t"
  "ds_read_b64_tr_b16 v[232:233], %[vz0] offset:12800\n\t"
  "ds_read_b64_tr_b16 v[234:235], %[vz0] offset:14848\n\t"
  "v_mfma_f32_16x16x32_bf16 a[92:95], v[188:191], v[172:175], a[92:95]\n\t"
  "s_waitcnt lgkmcnt(14)\n\t"
  "v_mfma_f32_16x16x32_bf16 a[96:99], v[192:195], v[116:119], a[96:99]\n\t"
  "ds_read_b64_tr_b16 v[236:237], %[vz1] offset:12800\n\t"
  "ds_read_b64_tr_b16 v[238:239], %[vz1] offset:14848\n\t"
  "v_mfma_f32_16x16x32_bf16 a[100:103], v[192:195], v[172:175], a[100:103]\n\t"
  "v_mfma_f32_16x16x32_bf16 a[104:107], v[196:199], v[116:119], a[104:107]\n\t"
  "v_mfma_f32_16x16x32_bf16 a[108:111], v[196:199], v[172:175], a[108:111]\n\t"
  "v_mfma_f32_16x16x32_bf16 a[112:115], v[200:203], v[116:119], a[112:115]\n\t"
  "v_mfma_f32_16x16x32_bf16 a[116:119], v[200:203], v[172:175], a[116:119]\n\t"
  "v_mfma_f32_16x16x32_bf16 a[120:123], v[204:207], v[116:119], a[120:123]\n\t"
  "v_mfma_f32_16x16x32_bf16 a[124:127], v[204:207], v[172:175], a[124:127]\n\t"
  "s_waitcnt lgkmcnt(8)\n\t"
  "v_mfma_f32_16x16x32_bf16 a[128:131], v[208:211], v[116:119], a[128:131]\n\t"
  "v_mfma_f32_16x16x32_bf16 a[132:135], v[208:211], v[172:175], a[132:135]\n\t"
  "v_mfma_f32_16x16x32_bf16 a[136:139], v[212:215], v[116:119], a[136:139]\n\t"
  "v_mfma_f32_16x16x32_bf16 a[140:143], v[212:215], v[172:175], a[140:143]\n\t"
  "v_mfma_f32_16x16x32_bf16 a[144:147], v[216:219], v[116:119], a[144:147]\n\t"
  "v_mfma_f32_16x16x32_bf16 a[148:151], v[216:219], v[172:175], a[148:151]\n\t"
  "v_mfma_f32_16x16x32_bf16 a[152:155], v[220:223], v[116:119], a[152:155]\n\t"
  "v_mfma_f32_16x16x32_bf16 a[156:159], v[220:223], v[172:175], a[156:159]\n\t"
  "s_waitcnt lgkmcnt(0)\n\t"
  "v_mfma_f32_16x16x32_bf16 a[160:163], v[224:227], v[116:119], a[160:163]\n\t"
  "v_mfma_f32_16x16x32_bf16 a[164:167], v[224:227], v[172:175], a[164:167]\n\t"
  "v_mfma_f32_16x16x32_bf16 a[168:171], v[228:231], v[116:119], a[168:171]\n\t"
  "v_mfma_f32_16x16x32_bf16 a[172:175], v[228:231], v[172:175], a[172:175]\n\t"
  "v_mfma_f32_16x16x32_bf16 a[176:179], v[232:235], v[116:119], a[176:179]\n\t"
  "v_mfma_f32_16x16x32_bf16 a[180:183], v[232:235], v[172:175], a[180:183]\n\t"
  "v_mfma_f32_16x16x32_bf16 a[184:187], v[236:239], v[116:119], a[184:187]\n\t"
  "v_mfma_f32_16x16x32_bf16 a[188:191], v[236:239], v[172:175], a[188:191]\n\t"
  "s_nop 7\n\t"
      : "={a[0:15]}"(acc[0]), "={a[16:31]}"(acc[1]), "={a[32:47]}"(acc[2]), "={a[48:63]}"(acc[3]), "={a[64:79]}"(acc[4]), "={a[80:95]}"(acc[5]), "={a[96:111]}"(acc[6]), "={a[112:127]}"(acc[7]), "={a[128:143]}"(acc[8]), "={a[144:159]}"(acc[9]), "={a[160:175]}"(acc[10]), "={a[176:191]}"(acc[11]), [wqs] "+v"(wqs), [wis] "+v"(wis), [ks0] "+v"(ks0), [ks1] "+v"(ks1), [vs] "+v"(vs)
      : [xs] "v"(xs), [va0] "v"(va0), [va1] "v"(va1), [vk0] "v"(vk0), [vk1] "v"(vk1), [vz0] "v"(vk0 + 131072u), [vz1] "v"(vk1 + 131072u),
        [srdq] "s"(srdq), [srdi] "s"(srdi), [srdx] "s"(srdx), [srdk] "s"(srdk), [srdv] "s"(srdv),
        [m0v] "s"(m0v), [m0k] "s"(m0k), [m0i] "s"(m0i) : "v64", "v65", "v66", "v67", "v68", "v69", "v70", "v71", "v72", "v73", "v74", "v75", "v76", "v77", "v78", "v79", "v80", "v81", "v82", "v83", "v84", "v85", "v86", "v87", "v88", "v89", "v90", "v91", "v92", "v93", "v94", "v95", "v96", "v97", "v98", "v99", "v100", "v101", "v102", "v103", "v104", "v105", "v106", "v107", "v108", "v109", "v110", "v111", "v112", "v113", "v114", "v115", "v116", "v117", "v118", "v119", "v120", "v121", "v122", "v123", "v124", "v125", "v126", "v127", "v128", "v129", "v130", "v131", "v132", "v133", "v134", "v135", "v136", "v137", "v138", "v139", "v140", "v141", "v142", "v143", "v144", "v145", "v146", "v147", "v148", "v149", "v150", "v151", "v152", "v153", "v154", "v155", "v156", "v157", "v158", "v159", "v160", "v161", "v162", "v163", "v164", "v165", "v166", "v167", "v168", "v169", "v170", "v171", "v172", "v173", "v174", "v175", "v176", "v177", "v178", "v179", "v180", "v181", "v182", "v183", "v184", "v185", "v186", "v187", "v188", "v189", "v190", "v191", "v192", "v193", "v194", "v195", "v196", "v197", "v198", "v199", "v200", "v201", "v202", "v203", "v204", "v205", "v206", "v207", "v208", "v209", "v210", "v211", "v212", "v213", "v214", "v215", "v216", "v217", "v218", "v219", "v220", "v221", "v222", "v223", "v224", "v225", "v226", "v227", "v228", "v229", "v230", "v231", "v232", "v233", "v234", "v235", "v236", "v237", "v238", "v239", "s40", "s41", "s42", "s46", "s47", "s48", "s49", "s51", "s52", "s53", "s54", "s55", "s56", "scc", "memory"); }
    else { asm volatile(
  "s_mov_b32 s46, 130048\n\t"
  "s_mov_b32 s47, 30720\n\t"
  "s_mov_b32 s48, 160768\n\t"
  "s_mov_b32 s40, 3072\n\t"
  "s_mov_b32 s41, 6144\n\t"
  "s_mov_b32 s42, 9216\n\t"
  "s_mov_b32 s49, 56320\n\t"
  "s_mov_b32 s51, 4096\n\t"
  "s_mov_b32 s52, 8192\n\t"
  "s_mov_b32 s53, 12288\n\t"
  "s_mov_b32 s54, 16384\n\t"
  "s_mov_b32 s55, 20480\n\t"
  "s_mov_b32 s56, 24576\n\t"
  "buffer_load_dwordx4 v[64:67], %[xs], %[srdx], 0 offen offset:0\n\t"
  "buffer_load_dwordx4 v[120:123], %[xs], %[srdx], s53 offen offset:2048\n\t"
  "s_add_u32 m0, %[m0k], 98304\n\t"
  "s_nop 0\n\t"
  "buffer_load_dwordx4 %[wqs], %[srdq], 0 offen offset:0 lds\n\t"
  "buffer_load_dwordx4 %[wqs], %[srdq], s46 offen offset:1024 lds\n\t"
  "buffer_load_dwordx4 %[wqs], %[srdq], s47 offen offset:2048 lds\n\t"
  "buffer_load_dwordx4 %[wqs], %[srdq], s48 offen offset:3072 lds\n\t"
  "v_add_u32 %[wqs], 0x40000, %[wqs]\n\t"
  "buffer_load_dwordx4 v[68:71], %[xs], %[srdx], 0 offen offset:1024\n\t"
  "buffer_load_dwordx4 v[124:127], %[xs], %[srdx], s53 offen offset:3072\n\t"
  "s_add_u32 m0, %[m0k], 122880\n\t"
  "s_nop 0\n\t"
  "buffer_load_dwordx4 %[wqs], %[srdq], 0 offen offset:0 lds\n\t"
  "buffer_load_dwordx4 %[wqs], %[srdq], s46 offen offset:1024 lds\n\t"
  "buffer_load_dwordx4 %[wqs], %[srdq], s47 offen offset:2048 lds\n\t"
  "buffer_load_dwordx4 %[wqs], %[srdq], s48 offen offset:3072 lds\n\t"
  "v_add_u32 %[wqs], 0x40000, %[wqs]\n\t"
  "buffer_load_dwordx4 v[72:75], %[xs], %[srdx], 0 offen offset:2048\n\t"
  "buffer_load_dwordx4 v[128:131], %[xs], %[srdx], s54 offen offset:0\n\t"
  "s_add_u32 m0, %[m0k], 0\n\t"
  "s_nop 0\n\t"
  "buffer_load_dwordx4 %[wqs], %[srdq], 0 offen offset:0 lds\n\t"
  "buffer_load_dwordx4 %[wqs], %[srdq], s46 offen offset:1024 lds\n\t"
  "buffer_load_dwordx4 %[wqs], %[srdq], s47 offen offset:2048 lds\n\t"
  "buffer_load_dwordx4 %[wqs], %[srdq], s48 offen offset:3072 lds\n\t"
  "v_add_u32 %[wqs], 0x40000, %[wqs]\n\t"
  "s_waitcnt vmcnt(12)\n\t"
  "s_barrier\n\t"
  "ds_read_b64_tr_b16 v[176:177], %[va0] offset:32768\n\t"
  "ds_read_b64_tr_b16 v[178:179], %[va0] offset:34816\n\t"
  "ds_read_b64_tr_b16 v[180:181], %[va1] offset:32768\n\t"
  "ds_read_b64_tr_b16 v[182:183], %[va1] offset:34816\n\t"
  "ds_read_b64_tr_b16 v[184:185], %[va0] offset:33280\n\t"
  "ds_read_b64_tr_b16 v[186:187], %[va0] offset:35328\n\t"
  "ds_read_b64_tr_b16 v[188:189], %[va1] offset:33280\n\t"
  "ds_read_b64_tr_b16 v[190:191], %[va1] offset:35328\n\t"
  "ds_read_b64_tr_b16 v[192:193], %[va0] offset:36864\n\t"
  "ds_read_b64_tr_b16 v[194:195], %[va0] offset:38912\n\t"
  "ds_read_b64_tr_b16 v[196:197], %[va1] offset:36864\n\t"
  "ds_read_b64_tr_b16 v[198:199], %[va1] offset:38912\n\t"
  "ds_read_b64_tr_b16 v[200:201], %[va0] offset:37376\n\t"
  "ds_read_b64_tr_b16 v[202:203], %[va0] offset:39424\n\t"
  "ds_read_b64_tr_b16 v[204:205], %[va1] offset:37376\n\t"
  "ds_read_b64_tr_b16 v[206:207], %[va1] offset:39424\n\t"
  "ds_read_b64_tr_b16 v[208:209], %[va0] offset:40960\n\t"
  "ds_read_b64_tr_b16 v[210:211], %[va0] offset:43008\n\t"
  "ds_read_b64_tr_b16 v[212:213], %[va1] offset:40960\n\t"
  "ds_read_b64_tr_b16 v[214:215], %[va1] offset:43008\n\t"
  "ds_read_b64_tr_b16 v[216:217], %[va0] offset:41472\n\t"
  "ds_read_b64_tr_b16 v[218:219], %[va0] offset:43520\n\t"
  "s_waitcnt lgkmcnt(14)\n\t"
  "v_mfma_f32_16x16x32_bf16 a[0:3], v[176:179], v[64:67], 0\n\t"
  "ds_read_b64_tr_b16 v[220:221], %[va1] offset:41472\n\t"
  "ds_read_b64_tr_b16 v[222:223], %[va1] offset:43520\n\t"
  "v_mfma_f32_16x16x32_bf16 a[4:7], v[176:179], v[120:123], 0\n\t"
  "buffer_load_dwordx4 v[76:79], %[xs], %[srdx], 0 offen offset:3072\n\t"
  "v_mfma_f32_16x16x32_bf16 a[8:11], v[180:183], v[64:67], 0\n\t"
  "ds_read_b64_tr_b16 v[224:225], %[va0] offset:45056\n\t"
  "ds_read_b64_tr_b16 v[226:227], %[va0] offset:47104\n\t"
  "v_mfma_f32_16x16x32_bf16 a[12:15], v[180:183], v[120:123], 0\n\t"
  "buffer_load_dwordx4 v[132:135], %[xs], %[srdx], s54 offen offset:1024\n\t"
  "s_add_u32 m0, %[m0k], 24576\n\t"
  "v_mfma_f32_16x16x32_bf16 a[16:19], v[184:187], v[64:67], 0\n\t"
  "ds_read_b64_tr_b16 v[228:229], %[va1] offset:45056\n\t"
  "ds_read_b64_tr_b16 v[230:231], %[va1] offset:47104\n\t"
  "v_mfma_f32_16x16x32_bf16 a[20:23], v[184:187], v[120:123], 0\n\t"
  "buffer_load_dwordx4 %[wqs], %[srdq], 0 offen offset:0 lds\n\t"
  "buffer_load_dwordx4 %[wqs], %[srdq], s46 offen offset:1024 lds\n\t"
  "v_mfma_f32_16x16x32_bf16 a[24:27], v[188:191], v[64:67], 0\n\t"
  "ds_read_b64_tr_b16 v[232:233], %[va0] offset:45568\n\t"
  "ds_read_b64_tr_b16 v[234:235], %[va0] offset:47616\n\t"
  "v_mfma_f32_16x16x32_bf16 a[28:31], v[188:191], v[120:123], 0\n\t"
  "buffer_load_dwordx4 %[wqs], %[srdq], s47 offen offset:2048 lds\n\t"
  "buffer_load_dwordx4 %[wqs], %[srdq], s48 offen offset:3072 lds\n\t"
  "v_add_u32 %[wqs], 0x40000, %[wqs]\n\t"
  "s_waitcnt lgkmcnt(14)\n\t"
  "v_mfma_f32_16x16x32_bf16 a[32:35], v[192:195], v[64:67], 0\n\t"
  "ds_read_b64_tr_b16 v[236:237], %[va1] offset:45568\n\t"
  "ds_read_b64_tr_b16 v[238:239], %[va1] offset:47616\n\t"
  "v_mfma_f32_16x16x32_bf16 a[36:39], v[192:195], v[120:123], 0\n\t"
  "v_mfma_f32_16x16x32_bf16 a[40:43], v[196:199], v[64:67], 0\n\t"
  "s_waitcnt vmcnt(12)\n\t"
  "s_barrier\n\t"
  "ds_read_b64_tr_b16 v[176:177], %[va0] offset:57344\n\t"
  "ds_read_b64_tr_b16 v[178:179], %[va0] offset:59392\n\t"
  "v_mfma_f32_16x16x32_bf16 a[44:47], v[196:199], v[120:123], 0\n\t"
  "buffer_load_dwordx4 v[80:83], %[xs], %[srdx], s51 offen offset:0\n\t"
  "v_mfma_f32_16x16x32_bf16 a[48:51], v[200:203], v[64:67], 0\n\t"
  "ds_read_b64_tr_b16 v[180:181], %[va1] offset:57344\n\t"
  "ds_read_b64_tr_b16 v[182:183], %[va1] offset:59392\n\t"
  "v_mfma_f32_16x16x32_bf16 a[52:55], v[200:203], v[120:123], 0\n\t"
  "v_mfma_f32_16x16x32_bf16 a[56:59], v[204:207], v[64:67], 0\n\t"
  "ds_read_b64_tr_b16 v[184:185], %[va0] offset:57856\n\t"
  "ds_read_b64_tr_b16 v[186:187], %[va0] offset:59904\n\t"
  "v_mfma_f32_16x16x32_bf16 a[60:63], v[204:207], v[120:123], 0\n\t"
  "s_waitcnt lgkmcnt(14)\n\t"
  "v_mfma_f32_16x16x32_bf16 a[64:67], v[208:211], v[64:67], 0\n\t"
  "ds_read_b64_tr_b16 v[188:189], %[va1] offset:57856\n\t"
  "ds_read_b64_tr_b16 v[190:191], %[va1] offset:59904\n\t"
  "v_mfma_f32_16x16x32_bf16 a[68:71], v[208:211], v[120:123], 0\n\t"
  "buffer_load_dwordx4 v[136:139], %[xs], %[srdx], s54 offen offset:2048\n\t"
  "s_add_u32 m0, %[m0k], 49152\n\t"
  "v_mfma_f32_16x16x32_bf16 a[72:75], v[212:215], v[64:67], 0\n\t"
  "ds_read_b64_tr_b16 v[192:193], %[va0] offset:61440\n\t"
  "ds_read_b64_tr_b16 v[194:195], %[va0] offset:63488\n\t"
  "v_mfma_f32_16x16x32_bf16 a[76:79], v[212:215], v[120:123], 0\n\t"
  "v_mfma_f32_16x16x32_bf16 a[80:83], v[216:219], v[64:67], 0\n\t"
  "ds_read_b64_tr_b16 v[196:197], %[va1] offset:61440\n\t"
  "ds_read_b64_tr_b16 v[198:199], %[va1] offset:63488\n\t"
  "v_mfma_f32_16x16x32_bf16 a[84:87], v[216:219], v[120:123], 0\n\t"
  "buffer_load_dwordx4 %[wqs], %[srdq], 0 offen offset:0 lds\n\t"
  "v_mfma_f32_16x16x32_bf16 a[88:91], v[220:223], v[64:67], 0\n\t"
  "ds_read_b64_tr_b16 v[200:201], %[va0] offset:61952\n\t"
  "ds_read_b64_tr_b16 v[202:203], %[va0] offset:64000\n\t"
  "v_mfma_f32_16x16x32_bf16 a[92:95], v[220:223], v[120:123], 0\n\t"
  "s_waitcnt lgkmcnt(14)\n\t"
  "v_mfma_f32_16x16x32_bf16 a[96:99], v[224:227], v[64:67], 0\n\t"
  "ds_read_b64_tr_b16 v[204:205], %[va1] offset:61952\n\t"
  "ds_read_b64_tr_b16 v[206:207], %[va1] offset:64000\n\t"
  "v_mfma_f32_16x16x32_bf16 a[100:103], v[224:227], v[120:123], 0\n\t"
  "buffer_load_dwordx4 %[wqs], %[srdq], s46 offen offset:1024 lds\n\t"
  "v_mfma_f32_16x16x32_bf16 a[104:107], v[228:231], v[64:67], 0\n\t"
  "ds_read_b64_tr_b16 v[208:209], %[vz0] offset:0\n\t"
  "ds_read_b64_tr_b16 v[210:211], %[vz0] offset:2048\n\t"
  "v_mfma_f32_16x16x32_bf16 a[108:111], v[228:231], v[120:123], 0\n\t"
  "v_mfma_f32_16x16x32_bf16 a[112:115], v[232:235], v[64:67], 0\n\t"
  "ds_read_b64_tr_b16 v[212:213], %[vz1] offset:0\n\t"
  "ds_read_b64_tr_b16 v[214:215], %[vz1] offset:2048\n\t"
  "v_mfma_f32_16x16x32_bf16 a[116:119], v[232:235], v[120:123], 0\n\t"
  "buffer_load_dwordx4 %[wqs], %[srdq], s47 offen offset:2048 lds\n\t"
  "v_mfma_f32_16x16x32_bf16 a[120:123], v[236:239], v[64:67], 0\n\t"
  "ds_read_b64_tr_b16 v[216:217], %[vz0] offset:512\n\t"
  "ds_read_b64_tr_b16 v[218:219], %[vz0] offset:2560\n\t"
  "v_mfma_f32_16x16x32_bf16 a[124:127], v[236:239], v[120:123], 0\n\t"
  "s_waitcnt lgkmcnt(14)\n\t"
  "v_mfma_f32_16x16x32_bf16 a[0:3], v[176:179], v[68:71], a[0:3]\n\t"
  "ds_read_b64_tr_b16 v[220:221], %[vz1] offset:512\n\t"
  "ds_read_b64_tr_b16 v[222:223], %[vz1] offset:2560\n\t"
  "v_mfma_f32_16x16x32_bf16 a[4:7], v[176:179], v[124:127], a[4:7]\n\t"
  "v_mfma_f32_16x16x32_bf16 a[8:11], v[180:183], v[68:71], a[8:11]\n\t"
  "ds_read_b64_tr_b16 v[224:225], %[vz0] offset:4096\n\t"
  "ds_read_b64_tr_b16 v[226:227], %[vz0] offset:6144\n\t"
  "v_mfma_f32_16x16x32_bf16 a[12:15], v[180:183], v[124:127], a[12:15]\n\t"
  "buffer_load_dwordx4 %[wqs], %[srdq], s48 offen offset:3072 lds\n\t"
  "v_add_u32 %[wqs], 0x40000, %[wqs]\n\t"
  "v_mfma_f32_16x16x32_bf16 a[16:19], v[184:187], v[68:71], a[16:19]\n\t"
  "ds_read_b64_tr_b16 v[228:229], %[vz1] offset:4096\n\t"
  "ds_read_b64_tr_b16 v[230:231], %[vz1] offset:6144\n\t"
  "v_mfma_f32_16x16x32_bf16 a[20:23], v[184:187], v[124:127], a[20:23]\n\t"
  "v_mfma_f32_16x16x32_bf16 a[24:27], v[188:191], v[68:71], a[24:27]\n\t"
  "ds_read_b64_tr_b16 v[232:233], %[vz0] offset:4608\n\t"
  "ds_read_b64_tr_b16 v[234:235], %[vz0] offset:6656\n\t"
  "v_mfma_f32_16x16x32_bf16 a[28:31], v[188:191], v[124:127], a[28:31]\n\t"
  "s_waitcnt lgkmcnt(14)\n\t"
  "v_mfma_f32_16x16x32_bf16 a[32:35], v[192:195], v[68:71], a[32:35]\n\t"
  "ds_read_b64_tr_b16 v[236:237], %[vz1] offset:4608\n\t"
  "ds_read_b64_tr_b16 v[238:239], %[vz1] offset:6656\n\t"
  "v_mfma_f32_16x16x32_bf16 a[36:39], v[192:195], v[124:127], a[36:39]\n\t"
  "v_mfma_f32_16x16x32_bf16 a[40:43], v[196:199], v[68:71], a[40:43]\n\t"
  "s_waitcnt vmcnt(12)\n\t"
  "s_barrier\n\t"
  "ds_read_b64_tr_b16 v[176:177], %[vk0] offset:0\n\t"
  "ds_read_b64_tr_b16 v[178:179], %[vk0] offset:2048\n\t"
  "v_mfma_f32_16x16x32_bf16 a[44:47], v[196:199], v[124:127], a[44:47]\n\t"
  "buffer_load_dwordx4 v[84:87], %[xs], %[srdx], s51 offen offset:1024\n\t"
  "v_mfma_f32_16x16x32_bf16 a[48:51], v[200:203], v[68:71], a[48:51]\n\t"
  "ds_read_b64_tr_b16 v[180:181], %[vk1] offset:0\n\t"
  "ds_read_b64_tr_b16 v[182:183], %[vk1] offset:2048\n\t"
  "v_mfma_f32_16x16x32_bf16 a[52:55], v[200:203], v[124:127], a[52:55]\n\t"
  "v_mfma_f32_16x16x32_bf16 a[56:59], v[204:207], v[68:71], a[56:59]\n\t"
  "ds_read_b64_tr_b16 v[184:185], %[vk0] offset:512\n\t"
  "ds_read_b64_tr_b16 v[186:187], %[vk0] offset:2560\n\t"
  "v_mfma_f32_16x16x32_bf16 a[60:63], v[204:207], v[124:127], a[60:63]\n\t"
  "s_waitcnt lgkmcnt(14)\n\t"
  "v_mfma_f32_16x16x32_bf16 a[64:67], v[208:211], v[68:71], a[64:67]\n\t"
  "ds_read_b64_tr_b16 v[188:189], %[vk1] offset:512\n\t"
  "ds_read_b64_tr_b16 v[190:191], %[vk1] offset:2560\n\t"
  "v_mfma_f32_16x16x32_bf16 a[68:71], v[208:211], v[124:127], a[68:71]\n\t"
  "buffer_load_dwordx4 v[140:143], %[xs], %[srdx], s54 offen offset:3072\n\t"
  "s_add_u32 m0, %[m0k], 73728\n\t"
  "v_mfma_f32_16x16x32_bf16 a[72:75], v[212:215], v[68:71], a[72:75]\n\t"
  "ds_read_b64_tr_b16 v[192:193], %[vk0] offset:4096\n\t"
  "ds_read_b64_tr_b16 v[194:195], %[vk0] offset:6144\n\t"
  "v_mfma_f32_16x16x32_bf16 a[76:79], v[212:215], v[124:127], a[76:79]\n\t"
  "v_mfma_f32_16x16x32_bf16 a[80:83], v[216:219], v[68:71], a[80:83]\n\t"
  "ds_read_b64_tr_b16 v[196:197], %[vk1] offset:4096\n\t"
  "ds_read_b64_tr_b16 v[198:199], %[vk1] offset:6144\n\t"
  "v_mfma_f32_16x16x32_bf16 a[84:87], v[216:219], v[124:127], a[84:87]\n\t"
  "buffer_load_dwordx4 %[wqs], %[srdq], 0 offen offset:0 lds\n\t"
  "v_mfma_f32_16x16x32_bf16 a[88:91], v[220:223], v[68:71], a[88:91]\n\t"
  "ds_read_b64_tr_b16 v[200:201], %[vk0] offset:4608\n\t"
  "ds_read_b64_tr_b16 v[202:203], %[vk0] offset:6656\n\t"
  "v_mfma_f32_16x16x32_bf16 a[92:95], v[220:223], v[124:127], a[92:95]\n\t"
  "s_waitcnt lgkmcnt(14)\n\t"
  "v_mfma_f32_16x16x32_bf16 a[96:99], v[224:227], v[68:71], a[96:99]\n\t"
  "ds_read_b64_tr_b16 v[204:205], %[vk1] offset:4608\n\t"
  "ds_read_b64_tr_b16 v[206:207], %[vk1] offset:6656\n\t"
  "v_mfma_f32_16x16x32_bf16 a[100:103], v[224:227], v[124:127], a[100:103]\n\t"
  "buffer_load_dwordx4 %[wqs], %[srdq], s46 offen offset:1024 lds\n\t"
  "v_mfma_f32_16x16x32_bf16 a[104:107], v[228:231], v[68:71], a[104:107]\n\t"
  "ds_read_b64_tr_b16 v[208:209], %[vk0] offset:8192\n\t"
  "ds_read_b64_tr_b16 v[210:211], %[vk0] offset:10240\n\t"
  "v_mfma_f32_16x16x32_bf16 a[108:111], v[228:231], v[124:127], a[108:111]\n\t"
  "v_mfma_f32_16x16x32_bf16 a[112:115], v[232:235], v[68:71], a[112:115]\n\t"
  "ds_read_b64_tr_b16 v[212:213], %[vk1] offset:8192\n\t"
  "ds_read_b64_tr_b16 v[214:215], %[vk1] offset:10240\n\t"
  "v_mfma_f32_16x16x32_bf16 a[116:119], v[232:235], v[124:127], a[116:119]\n\t"
  "buffer_load_dwordx4 %[wqs], %[srdq], s47 offen offset:2048 lds\n\t"
  "v_mfma_f32_16x16x32_bf16 a[120:123], v[236:239], v[68:71], a[120:123]\n\t"
  "ds_read_b64_tr_b16 v[216:217], %[vk0] offset:8704\n\t"
  "ds_read_b64_tr_b16 v[218:219], %[vk0] offset:10752\n\t"
  "v_mfma_f32_16x16x32_bf16 a[124:127], v[236:239], v[124:127], a[124:127]\n\t"
  "s_waitcnt lgkmcnt(14)\n\t"
  "v_mfma_f32_16x16x32_bf16 a[0:3], v[176:179], v[72:75], a[0:3]\n\t"
  "ds_read_b64_tr_b16 v[220:221], %[vk1] offset:8704\n\t"
  "ds_read_b64_tr_b16 v[222:223], %[vk1] offset:10752\n\t"
  "v_mfma_f32_16x16x32_bf16 a[4:7], v[176:179], v[128:131], a[4:7]\n\t"
  "v_mfma_f32_16x16x32_bf16 a[8:11], v[180:183], v[72:75], a[8:11]\n\t"
  "ds_read_b64_tr_b16 v[224:225], %[vk0] offset:12288\n\t"
  "ds_read_b64_tr_b16 v[226:227], %[vk0] offset:14336\n\t"
  "v_mfma_f32_16x16x32_bf16 a[12:15], v[180:183], v[128:131], a[12:15]\n\t"
  "buffer_load_dwordx4 %[wqs], %[srdq], s48 offen offset:3072 lds\n\t"
  "v_add_u32 %[wqs], 0x40000, %[wqs]\n\t"
  "v_mfma_f32_16x16x32_bf16 a[16:19], v[184:187], v[72:75], a[16:19]\n\t"
  "ds_read_b64_tr_b16 v[228:229], %[vk1] offset:12288\n\t"
  "ds_read_b64_tr_b16 v[230:231], %[vk1] offset:14336\n\t"
  "v_mfma_f32_16x16x32_bf16 a[20:23], v[184:187], v[128:131], a[20:23]\n\t"
  "v_mfma_f32_16x16x32_bf16 a[24:27], v[188:191], v[72:75], a[24:27]\n\t"
  "ds_read_b64_tr_b16 v[232:233], %[vk0] offset:12800\n\t"
  "ds_read_b64_tr_b16 v[234:235], %[vk0] offset:14848\n\t"
  "v_mfma_f32_16x16x32_bf16 a[28:31], v[188:191], v[128:131], a[28:31]\n\t"
  "s_waitcnt lgkmcnt(14)\n\t"
  "v_mfma_f32_16x16x32_bf16 a[32:35], v[192:195], v[72:75], a[32:35]\n\t"
  "ds_read_b64_tr_b16 v[236:237], %[vk1] offset:12800\n\t"
  "ds_read_b64_tr_b16 v[238:239], %[vk1] offset:14848\n\t"
  "v_mfma_f32_16x16x32_bf16 a[36:39], v[192:195], v[128:131], a[36:39]\n\t"
  "v_mfma_f32_16x16x32_bf16 a[40:43], v[196:199], v[72:75], a[40:43]\n\t"
  "s_waitcnt vmcnt(12)\n\t"
  "s_barrier\n\t"
  "ds_read_b64_tr_b16 v[176:177], %[vk0] offset:24576\n\t"
  "ds_read_b64_tr_b16 v[178:179], %[vk0] offset:26624\n\t"
  "v_mfma_f32_16x16x32_bf16 a[44:47], v[196:199], v[128:131], a[44:47]\n\t"
  "buffer_load_dwordx4 v[88:91], %[xs], %[srdx], s51 offen offset:2048\n\t"
  "v_mfma_f32_16x16x32_bf16 a[48:51], v[200:203], v[72:75], a[48:51]\n\t"
  "ds_read_b64_tr_b16 v[180:181], %[vk1] offset:24576\n\t"
  "ds_read_b64_tr_b16 v[182:183], %[vk1] offset:26624\n\t"
  "v_mfma_f32_16x16x32_bf16 a[52:55], v[200:203], v[128:131], a[52:55]\n\t"
  "v_mfma_f32_16x16x32_bf16 a[56:59], v[204:207], v[72:75], a[56:59]\n\t"
  "ds_read_b64_tr_b16 v[184:185], %[vk0] offset:25088\n\t"
  "ds_read_b64_tr_b16 v[186:187], %[vk0] offset:27136\n\t"
  "v_mfma_f32_16x16x32_bf16 a[60:63], v[204:207], v[128:131], a[60:63]\n\t"
  "s_waitcnt lgkmcnt(14)\n\t"
  "v_mfma_f32_16x16x32_bf16 a[64:67], v[208:211], v[72:75], a[64:67]\n\t"
  "ds_read_b64_tr_b16 v[188:189], %[vk1] offset:25088\n\t"
  "ds_read_b64_tr_b16 v[190:191], %[vk1] offset:27136\n\t"
  "v_mfma_f32_16x16x32_bf16 a[68:71], v[208:211], v[128:131], a[68:71]\n\t"
  "buffer_load_dwordx4 v[144:147], %[xs], %[srdx], s55 offen offset:0\n\t"
  "s_add_u32 m0, %[m0k], 98304\n\t"
  "v_mfma_f32_16x16x32_bf16 a[72:75], v[212:215], v[72:75], a[72:75]\n\t"
  "ds_read_b64_tr_b16 v[192:193], %[vk0] offset:28672\n\t"
  "ds_read_b64_tr_b16 v[194:195], %[vk0] offset:30720\n\t"
  "v_mfma_f32_16x16x32_bf16 a[76:79], v[212:215], v[128:131], a[76:79]\n\t"
  "v_mfma_f32_16x16x32_bf16 a[80:83], v[216:219], v[72:75], a[80:83]\n\t"
  "ds_read_b64_tr_b16 v[196:197], %[vk1] offset:28672\n\t"
  "ds_read_b64_tr_b16 v[198:199], %[vk1] offset:30720\n\t"
  "v_mfma_f32_16x16x32_bf16 a[84:87], v[216:219], v[128:131], a[84:87]\n\t"
  "buffer_load_dwordx4 %[wqs], %[srdq], 0 offen offset:0 lds\n\t"
  "v_mfma_f32_16x16x32_bf16 a[88:91], v[220:223], v[72:75], a[88:91]\n\t"
  "ds_read_b64_tr_b16 v[200:201], %[vk0] offset:29184\n\t"
  "ds_read_b64_tr_b16 v[202:203], %[vk0] offset:31232\n\t"
  "v_mfma_f32_16x16x32_bf16 a[92:95], v[220:223], v[128:131], a[92:95]\n\t"
  "s_waitcnt lgkmcnt(14)\n\t"
  "v_mfma_f32_16x16x32_bf16 a[96:99], v[224:227], v[72:75], a[96:99]\n\t"
  "ds_read_b64_tr_b16 v[204:205], %[vk1] offset:29184\n\t"
  "ds_read_b64_tr_b16 v[206:207], %[vk1] offset:31232\n\t"
  "v_mfma_f32_16x16x32_bf16 a[100:103], v[224:227], v[128:131], a[100:103]\n\t"
  "buffer_load_dwordx4 %[wqs], %[srdq], s46 offen offset:1024 lds\n\t"
  "v_mfma_f32_16x16x32_bf16 a[104:107], v[228:231], v[72:75], a[104:107]\n\t"
  "ds_read_b64_tr_b16 v[208:209], %[vk0] offset:32768\n\t"
  "ds_read_b64_tr_b16 v[210:211], %[vk0] offset:34816\n\t"
  "v_mfma_f32_16x16x32_bf16 a[108:111], v[228:231], v[128:131], a[108:111]\n\t"
  "v_mfma_f32_16x16x32_bf16 a[112:115], v[232:235], v[72:75], a[112:115]\n\t"
  "ds_read_b64_tr_b16 v[212:213], %[vk1] offset:32768\n\t"
  "ds_read_b64_tr_b16 v[214:215], %[vk1] offset:34816\n\t"
  "v_mfma_f32_16x16x32_bf16 a[116:119], v[232:235], v[128:131], a[116:119]\n\t"
  "buffer_load_dwordx4 %[wqs], %[srdq], s47 offen offset:2048 lds\n\t"
  "v_mfma_f32_16x16x32_bf16 a[120:123], v[236:239], v[72:75], a[120:123]\n\t"
  "ds_read_b64_tr_b16 v[216:217], %[vk0] offset:33280\n\t"
  "ds_read_b64_tr_b16 v[218:219], %[vk0] offset:35328\n\t"
  "v_mfma_f32_16x16x32_bf16 a[124:127], v[236:239], v[128:131], a[124:127]\n\t"
  "s_waitcnt lgkmcnt(14)\n\t"
  "v_mfma_f32_16x16x32_bf16 a[0:3], v[176:179], v[76:79], a[0:3]\n\t"
  "ds_read_b64_tr_b16 v[220:221], %[vk1] offset:33280\n\t"
  "ds_read_b64_tr_b16 v[222:223], %[vk1] offset:35328\n\t"
  "v_mfma_f32_16x16x32_bf16 a[4:7], v[176:179], v[132:135], a[4:7]\n\t"
  "v_mfma_f32_16x16x32_bf16 a[8:11], v[180:183], v[76:79], a[8:11]\n\t"
  "ds_read_b64_tr_b16 v[224:225], %[vk0] offset:36864\n\t"
  "ds_read_b64_tr_b16 v[226:227], %[vk0] offset:38912\n\t"
  "v_mfma_f32_16x16x32_bf16 a[12:15], v[180:183], v[132:135], a[12:15]\n\t"
  "buffer_load_dwordx4 %[wqs], %[srdq], s48 offen offset:3072 lds\n\t"
  "v_add_u32 %[wqs], 0x40000, %[wqs]\n\t"
  "v_mfma_f32_16x16x32_bf16 a[16:19], v[184:187], v[76:79], a[16:19]\n\t"
  "ds_read_b64_tr_b16 v[228:229], %[vk1] offset:36864\n\t"
  "ds_read_b64_tr_b16 v[230:231], %[vk1] offset:38912\n\t"
  "v_mfma_f32_16x16x32_bf16 a[20:23], v[184:187], v[132:135], a[20:23]\n\t"
  "v_mfma_f32_16x16x32_bf16 a[24:27], v[188:191], v[76:79], a[24:27]\n\t"
  "ds_read_b64_tr_b16 v[232:233], %[vk0] offset:37376\n\t"
  "ds_read_b64_tr_b16 v[234:235], %[vk0] offset:39424\n\t"
  "v_mfma_f32_16x16x32_bf16 a[28:31], v[188:191], v[132:135], a[28:31]\n\t"
  "s_waitcnt lgkmcnt(14)\n\t"
  "v_mfma_f32_16x16x32_bf16 a[32:35], v[192:195], v[76:79], a[32:35]\n\t"
  "ds_read_b64_tr_b16 v[236:237], %[vk1] offset:37376\n\t"
  "ds_read_b64_tr_b16 v[238:239], %[vk1] offset:39424\n\t"
  "v_mfma_f32_16x16x32_bf16 a[36:39], v[192:195], v[132:135], a[36:39]\n\t"
  "v_mfma_f32_16x16x32_bf16 a[40:43], v[196:199], v[76:79], a[40:43]\n\t"
  "s_waitcnt vmcnt(12)\n\t"
  "s_barrier\n\t"
  "ds_read_b64_tr_b16 v[176:177], %[vk0] offset:49152\n\t"
  "ds_read_b64_tr_b16 v[178:179], %[vk0] offset:51200\n\t"
  "v_mfma_f32_16x16x32_bf16 a[44:47], v[196:199], v[132:135], a[44:47]\n\t"
  "buffer_load_dwordx4 v[92:95], %[xs], %[srdx], s51 offen offset:3072\n\t"
  "v_mfma_f32_16x16x32_bf16 a[48:51], v[200:203], v[76:79], a[48:51]\n\t"
  "ds_read_b64_tr_b16 v[180:181], %[vk1] offset:49152\n\t"
  "ds_read_b64_tr_b16 v[182:183], %[vk1] offset:51200\n\t"
  "v_mfma_f32_16x16x32_bf16 a[52:55], v[200:203], v[132:135], a[52:55]\n\t"
  "v_mfma_f32_16x16x32_bf16 a[56:59], v[204:207], v[76:79], a[56:59]\n\t"
  "ds_read_b64_tr_b16 v[184:185], %[vk0] offset:49664\n\t"
  "ds_read_b64_tr_b16 v[186:187], %[vk0] offset:51712\n\t"
  "v_mfma_f32_16x16x32_bf16 a[60:63], v[204:207], v[132:135], a[60:63]\n\t"
  "s_waitcnt lgkmcnt(14)\n\t"
  "v_mfma_f32_16x16x32_bf16 a[64:67], v[208:211], v[76:79], a[64:67]\n\t"
  "ds_read_b64_tr_b16 v[188:189], %[vk1] offset:49664\n\t"
  "ds_read_b64_tr_b16 v[190:191], %[vk1] offset:51712\n\t"
  "v_mfma_f32_16x16x32_bf16 a[68:71], v[208:211], v[132:135], a[68:71]\n\t"
  "buffer_load_dwordx4 v[148:151], %[xs], %[srdx], s55 offen offset:1024\n\t"
  "s_add_u32 m0, %[m0k], 122880\n\t"
  "v_mfma_f32_16x16x32_bf16 a[72:75], v[212:215], v[76:79], a[72:75]\n\t"
  "ds_read_b64_tr_b16 v[192:193], %[vk0] offset:53248\n\t"
  "ds_read_b64_tr_b16 v[194:195], %[vk0] offset:55296\n\t"
  "v_mfma_f32_16x16x32_bf16 a[76:79], v[212:215], v[132:135], a[76:79]\n\t"
  "v_mfma_f32_16x16x32_bf16 a[80:83], v[216:219], v[76:79], a[80:83]\n\t"
  "ds_read_b64_tr_b16 v[196:197], %[vk1] offset:53248\n\t"
  "ds_read_b64_tr_b16 v[198:199], %[vk1] offset:55296\n\t"
  "v_mfma_f32_16x16x32_bf16 a[84:87], v[216:219], v[132:135], a[84:87]\n\t"
  "buffer_load_dwordx4 %[wqs], %[srdq], 0 offen offset:0 lds\n\t"
  "v_mfma_f32_16x16x32_bf16 a[88:91], v[220:223], v[76:79], a[88:91]\n\t"
  "ds_read_b64_tr_b16 v[200:201], %[vk0] offset:53760\n\t"
  "ds_read_b64_tr_b16 v[202:203], %[vk0] offset:55808\n\t"
  "v_mfma_f32_16x16x32_bf16 a[92:95], v[220:223], v[132:135], a[92:95]\n\t"
  "s_waitcnt lgkmcnt(14)\n\t"
  "v_mfma_f32_16x16x32_bf16 a[96:99], v[224:227], v[76:79], a[96:99]\n\t"
  "ds_read_b64_tr_b16 v[204:205], %[vk1] offset:53760\n\t"
  "ds_read_b64_tr_b16 v[206:207], %[vk1] offset:55808\n\t"
  "v_mfma_f32_16x16x32_bf16 a[100:103], v[224:227], v[132:135], a[100:103]\n\t"
  "buffer_load_dwordx4 %[wqs], %[srdq], s46 offen offset:1024 lds\n\t"
  "v_mfma_f32_16x16x32_bf16 a[104:107], v[228:231], v[76:79], a[104:107]\n\t"
  "ds_read_b64_tr_b16 v[208:209], %[vk0] offset:57344\n\t"
  "ds_read_b64_tr_b16 v[210:211], %[vk0] offset:59392\n\t"
  "v_mfma_f32_16x16x32_bf16 a[108:111], v[228:231], v[132:135], a[108:111]\n\t"
  "v_mfma_f32_16x16x32_bf16 a[112:115], v[232:235], v[76:79], a[112:115]\n\t"
  "ds_read_b64_tr_b16 v[212:213], %[vk1] offset:57344\n\t"
  "ds_read_b64_tr_b16 v[214:215], %[vk1] offset:59392\n\t"
  "v_mfma_f32_16x16x32_bf16 a[116:119], v[232:235], v[132:135], a[116:119]\n\t"
  "buffer_load_dwordx4 %[wqs], %[srdq], s47 offen offset:2048 lds\n\t"
  "v_mfma_f32_16x16x32_bf16 a[120:123], v[236:239], v[76:79], a[120:123]\n\t"
  "ds_read_b64_tr_b16 v[216:217], %[vk0] offset:57856\n\t"
  "ds_read_b64_tr_b16 v[218:219], %[vk0] offset:59904\n\t"
  "v_mfma_f32_16x16x32_bf16 a[124:127], v[236:239], v[132:135], a[124:127]\n\t"
  "s_waitcnt lgkmcnt(14)\n\t"
  "v_mfma_f32_16x16x32_bf16 a[0:3], v[176:179], v[80:83], a[0:3]\n\t"
  "ds_read_b64_tr_b16 v[220:221], %[vk1] offset:57856\n\t"
  "ds_read_b64_tr_b16 v[222:223], %[vk1] offset:59904\n\t"
  "v_mfma_f32_16x16x32_bf16 a[4:7], v[176:179], v[136:139], a[4:7]\n\t"
  "v_mfma_f32_16x16x32_bf16 a[8:11], v[180:183], v[80:83], a[8:11]\n\t"
  "ds_read_b64_tr_b16 v[224:225], %[vk0] offset:61440\n\t"
  "ds_read_b64_tr_b16 v[226:227], %[vk0] offset:63488\n\t"
  "v_mfma_f32_16x16x32_bf16 a[12:15], v[180:183], v[136:139], a[12:15]\n\t"
  "buffer_load_dwordx4 %[wqs], %[srdq], s48 offen offset:3072 lds\n\t"
  "v_add_u32 %[wqs], 0x40000, %[wqs]\n\t"
  "v_mfma_f32_16x16x32_bf16 a[16:19], v[184:187], v[80:83], a[16:19]\n\t"
  "ds_read_b64_tr_b16 v[228:229], %[vk1] offset:61440\n\t"
  "ds_read_b64_tr_b16 v[230:231], %[vk1] offset:63488\n\t"
  "v_mfma_f32_16x16x32_bf16 a[20:23], v[184:187], v[136:139], a[20:23]\n\t"
  "v_mfma_f32_16x16x32_bf16 a[24:27], v[188:191], v[80:83], a[24:27]\n\t"
  "ds_read_b64_tr_b16 v[232:233], %[vk0] offset:61952\n\t"
  "ds_read_b64_tr_b16 v[234:235], %[vk0] offset:64000\n\t"
  "v_mfma_f32_16x16x32_bf16 a[28:31], v[188:191], v[136:139], a[28:31]\n\t"
  "s_waitcnt lgkmcnt(14)\n\t"
  "v_mfma_f32_16x16x32_bf16 a[32:35], v[192:195], v[80:83], a[32:35]\n\t"
  "ds_read_b64_tr_b16 v[236:237], %[vk1] offset:61952\n\t"
  "ds_read_b64_tr_b16 v[238:239], %[vk1] offset:64000\n\t"
  "v_mfma_f32_16x16x32_bf16 a[36:39], v[192:195], v[136:139], a[36:39]\n\t"
  "v_mfma_f32_16x16x32_bf16 a[40:43], v[196:199], v[80:83], a[40:43]\n\t"
  "s_waitcnt vmcnt(12)\n\t"
  "s_barrier\n\t"
  "ds_read_b64_tr_b16 v[176:177], %[va0] offset:8192\n\t"
  "ds_read_b64_tr_b16 v[178:179], %[va0] offset:10240\n\t"
  "v_mfma_f32_16x16x32_bf16 a[44:47], v[196:199], v[136:139], a[44:47]\n\t"
  "buffer_load_dwordx4 v[96:99], %[xs], %[srdx], s52 offen offset:0\n\t"
  "v_mfma_f32_16x16x32_bf16 a[48:51], v[200:203], v[80:83], a[48:51]\n\t"
  "ds_read_b64_tr_b16 v[180:181], %[va1] offset:8192\n\t"
  "ds_read_b64_tr_b16 v[182:183], %[va1] offset:10240\n\t"
  "v_mfma_f32_16x16x32_bf16 a[52:55], v[200:203], v[136:139], a[52:55]\n\t"
  "v_mfma_f32_16x16x32_bf16 a[56:59], v[204:207], v[80:83], a[56:59]\n\t"
  "ds_read_b64_tr_b16 v[184:185], %[va0] offset:8704\n\t"
  "ds_read_b64_tr_b16 v[186:187], %[va0] offset:10752\n\t"
  "v_mfma_f32_16x16x32_bf16 a[60:63], v[204:207], v[136:139], a[60:63]\n\t"
  "s_waitcnt lgkmcnt(14)\n\t"
  "v_mfma_f32_16x16x32_bf16 a[64:67], v[208:211], v[80:83], a[64:67]\n\t"
  "ds_read_b64_tr_b16 v[188:189], %[va1] offset:8704\n\t"
  "ds_read_b64_tr_b16 v[190:191], %[va1] offset:10752\n\t"
  "v_mfma_f32_16x16x32_bf16 a[68:71], v[208:211], v[136:139], a[68:71]\n\t"
  "buffer_load_dwordx4 v[152:155], %[xs], %[srdx], s55 offen offset:2048\n\t"
  "s_add_u32 m0, %[m0k], 0\n\t"
  "v_mfma_f32_16x16x32_bf16 a[72:75], v[212:215], v[80:83], a[72:75]\n\t"
  "ds_read_b64_tr_b16 v[192:193], %[va0] offset:12288\n\t"
  "ds_read_b64_tr_b16 v[194:195], %[va0] offset:14336\n\t"
  "v_mfma_f32_16x16x32_bf16 a[76:79], v[212:215], v[136:139], a[76:79]\n\t"
  "v_mfma_f32_16x16x32_bf16 a[80:83], v[216:219], v[80:83], a[80:83]\n\t"
  "ds_read_b64_tr_b16 v[196:197], %[va1] offset:12288\n\t"
  "ds_read_b64_tr_b16 v[198:199], %[va1] offset:14336\n\t"
  "v_mfma_f32_16x16x32_bf16 a[84:87], v[216:219], v[136:139], a[84:87]\n\t"
  "buffer_load_dwordx4 %[wqs], %[srdq], 0 offen offset:0 lds\n\t"
  "v_mfma_f32_16x16x32_bf16 a[88:91], v[220:223], v[80:83], a[88:91]\n\t"
  "ds_read_b64_tr_b16 v[200:201], %[va0] offset:12800\n\t"
  "ds_read_b64_tr_b16 v[202:203], %[va0] offset:14848\n\t"
  "v_mfma_f32_16x16x32_bf16 a[92:95], v[220:223], v[136:139], a[92:95]\n\t"
  "s_waitcnt lgkmcnt(14)\n\t"
  "v_mfma_f32_16x16x32_bf16 a[96:99], v[224:227], v[80:83], a[96:99]\n\t"
  "ds_read_b64_tr_b16 v[204:205], %[va1] offset:12800\n\t"
  "ds_read_b64_tr_b16 v[206:207], %[va1] offset:14848\n\t"
  "v_mfma_f32_16x16x32_bf16 a[100:103], v[224:227], v[136:139], a[100:103]\n\t"
  "buffer_load_dwordx4 %[wqs], %[srdq], s46 offen offset:1024 lds\n\t"
  "v_mfma_f32_16x16x32_bf16 a[104:107], v[228:231], v[80:83], a[104:107]\n\t"
  "ds_read_b64_tr_b16 v[208:209], %[va0] offset:16384\n\t"
  "ds_read_b64_tr_b16 v[210:211], %[va0] offset:18432\n\t"
  "v_mfma_f32_16x16x32_bf16 a[108:111], v[228:231], v[136:139], a[108:111]\n\t"
  "v_mfma_f32_16x16x32_bf16 a[112:115], v[232:235], v[80:83], a[112:115]\n\t"
  "ds_read_b64_tr_b16 v[212:213], %[va1] offset:16384\n\t"
  "ds_read_b64_tr_b16 v[214:215], %[va1] offset:18432\n\t"
  "v_mfma_f32_16x16x32_bf16 a[116:119], v[232:235], v[136:139], a[116:119]\n\t"
  "buffer_load_dwordx4 %[wqs], %[srdq], s47 offen offset:2048 lds\n\t"
  "v_mfma_f32_16x16x32_bf16 a[120:123], v[236:239], v[80:83], a[120:123]\n\t"
  "ds_read_b64_tr_b16 v[216:217], %[va0] offset:16896\n\t"
  "ds_read_b64_tr_b16 v[218:219], %[va0] offset:18944\n\t"
  "v_mfma_f32_16x16x32_bf16 a[124:127], v[236:239], v[136:139], a[124:127]\n\t"
  "s_waitcnt lgkmcnt(14)\n\t"
  "v_mfma_f32_16x16x32_bf16 a[0:3], v[176:179], v[84:87], a[0:3]\n\t"
  "ds_read_b64_tr_b16 v[220:221], %[va1] offset:16896\n\t"
  "ds_read_b64_tr_b16 v[222:223], %[va1] offset:18944\n\t"
  "v_mfma_f32_16x16x32_bf16 a[4:7], v[176:179], v[140:143], a[4:7]\n\t"
  "v_mfma_f32_16x16x32_bf16 a[8:11], v[180:183], v[84:87], a[8:11]\n\t"
  "ds_read_b64_tr_b16 v[224:225], %[va0] offset:20480\n\t"
  "ds_read_b64_tr_b16 v[226:227], %[va0] offset:22528\n\t"
  "v_mfma_f32_16x16x32_bf16 a[12:15], v[180:183], v[140:143], a[12:15]\n\t"
  "buffer_load_dwordx4 %[wqs], %[srdq], s48 offen offset:3072 lds\n\t"
  "v_add_u32 %[wqs], 0x40000, %[wqs]\n\t"
  "v_mfma_f32_16x16x32_bf16 a[16:19], v[184:187], v[84:87], a[16:19]\n\t"
  "ds_read_b64_tr_b16 v[228:229], %[va1] offset:20480\n\t"
  "ds_read_b64_tr_b16 v[230:231], %[va1] offset:22528\n\t"
  "v_mfma_f32_16x16x32_bf16 a[20:23], v[184:187], v[140:143], a[20:23]\n\t"
  "v_mfma_f32_16x16x32_bf16 a[24:27], v[188:191], v[84:87], a[24:27]\n\t"
  "ds_read_b64_tr_b16 v[232:233], %[va0] offset:20992\n\t"
  "ds_read_b64_tr_b16 v[234:235], %[va0] offset:23040\n\t"
  "v_mfma_f32_16x16x32_bf16 a[28:31], v[188:191], v[140:143], a[28:31]\n\t"
  "s_waitcnt lgkmcnt(14)\n\t"
  "v_mfma_f32_16x16x32_bf16 a[32:35], v[192:195], v[84:87], a[32:35]\n\t"
  "ds_read_b64_tr_b16 v[236:237], %[va1] offset:20992\n\t"
  "ds_read_b64_tr_b16 v[238:239], %[va1] offset:23040\n\t"
  "v_mfma_f32_16x16x32_bf16 a[36:39], v[192:195], v[140:143], a[36:39]\n\t"
  "v_mfma_f32_16x16x32_bf16 a[40:43], v[196:199], v[84:87], a[40:43]\n\t"
  "s_waitcnt vmcnt(12)\n\t"
  "s_barrier\n\t"
  "ds_read_b64_tr_b16 v[176:177], %[va0] offset:32768\n\t"
  "ds_read_b64_tr_b16 v[178:179], %[va0] offset:34816\n\t"
  "v_mfma_f32_16x16x32_bf16 a[44:47], v[196:199], v[140:143], a[44:47]\n\t"
  "buffer_load_dwordx4 v[100:103], %[xs], %[srdx], s52 offen offset:1024\n\t"
  "v_mfma_f32_16x16x32_bf16 a[48:51], v[200:203], v[84:87], a[48:51]\n\t"
  "ds_read_b64_tr_b16 v[180:181], %[va1] offset:32768\n\t"
  "ds_read_b64_tr_b16 v[182:183], %[va1] offset:34816\n\t"
  "v_mfma_f32_16x16x32_bf16 a[52:55], v[200:203], v[140:143], a[52:55]\n\t"
  "v_mfma_f32_16x16x32_bf16 a[56:59], v[204:207], v[84:87], a[56:59]\n\t"
  "ds_read_b64_tr_b16 v[184:185], %[va0] offset:33280\n\t"
  "ds_read_b64_tr_b16 v[186:187], %[va0] offset:35328\n\t"
  "v_mfma_f32_16x16x32_bf16 a[60:63], v[204:207], v[140:143], a[60:63]\n\t"
  "s_waitcnt lgkmcnt(14)\n\t"
  "v_mfma_f32_16x16x32_bf16 a[64:67], v[208:211], v[84:87], a[64:67]\n\t"
  "ds_read_b64_tr_b16 v[188:189], %[va1] offset:33280\n\t"
  "ds_read_b64_tr_b16 v[190:191], %[va1] offset:35328\n\t"
  "v_mfma_f32_16x16x32_bf16 a[68:71], v[208:211], v[140:143], a[68:71]\n\t"
  "buffer_load_dwordx4 v[156:159], %[xs], %[srdx], s55 offen offset:3072\n\t"
  "s_add_u32 m0, %[m0k], 24576\n\t"
  "v_mfma_f32_16x16x32_bf16 a[72:75], v[212:215], v[84:87], a[72:75]\n\t"
  "ds_read_b64_tr_b16 v[192:193], %[va0] offset:36864\n\t"
  "ds_read_b64_tr_b16 v[194:195], %[va0] offset:38912\n\t"
  "v_mfma_f32_16x16x32_bf16 a[76:79], v[212:215], v[140:143], a[76:79]\n\t"
  "v_mfma_f32_16x16x32_bf16 a[80:83], v[216:219], v[84:87], a[80:83]\n\t"
  "ds_read_b64_tr_b16 v[196:197], %[va1] offset:36864\n\t"
  "ds_read_b64_tr_b16 v[198:199], %[va1] offset:38912\n\t"
  "v_mfma_f32_16x16x32_bf16 a[84:87], v[216:219], v[140:143], a[84:87]\n\t"
  "buffer_load_dwordx4 %[wqs], %[srdq], 0 offen offset:0 lds\n\t"
  "v_mfma_f32_16x16x32_bf16 a[88:91], v[220:223], v[84:87], a[88:91]\n\t"
  "ds_read_b64_tr_b16 v[200:201], %[va0] offset:37376\n\t"
  "ds_read_b64_tr_b16 v[202:203], %[va0] offset:39424\n\t"
  "v_mfma_f32_16x16x32_bf16 a[92:95], v[220:223], v[140:143], a[92:95]\n\t"
  "s_waitcnt lgkmcnt(14)\n\t"
  "v_mfma_f32_16x16x32_bf16 a[96:99], v[224:227], v[84:87], a[96:99]\n\t"
  "ds_read_b64_tr_b16 v[204:205], %[va1] offset:37376\n\t"
  "ds_read_b64_tr_b16 v[206:207], %[va1] offset:39424\n\t"
  "v_mfma_f32_16x16x32_bf16 a[100:103], v[224:227], v[140:143], a[100:103]\n\t"
  "buffer_load_dwordx4 %[wqs], %[srdq], s46 offen offset:1024 lds\n\t"
  "v_mfma_f32_16x16x32_bf16 a[104:107], v[228:231], v[84:87], a[104:107]\n\t"
  "ds_read_b64_tr_b16 v[208:209], %[va0] offset:40960\n\t"
  "ds_read_b64_tr_b16 v[210:211], %[va0] offset:43008\n\t"
  "v_mfma_f32_16x16x32_bf16 a[108:111], v[228:231], v[140:143], a[108:111]\n\t"
  "v_mfma_f32_16x16x32_bf16 a[112:115], v[232:235], v[84:87], a[112:115]\n\t"
  "ds_read_b64_tr_b16 v[212:213], %[va1] offset:40960\n\t"
  "ds_read_b64_tr_b16 v[214:215], %[va1] offset:43008\n\t"
  "v_mfma_f32_16x16x32_bf16 a[116:119], v[232:235], v[140:143], a[116:119]\n\t"
  "buffer_load_dwordx4 %[wqs], %[srdq], s47 offen offset:2048 lds\n\t"
  "v_mfma_f32_16x16x32_bf16 a[120:123], v[236:239], v[84:87], a[120:123]\n\t"
  "ds_read_b64_tr_b16 v[216:217], %[va0] offset:41472\n\t"
  "ds_read_b64_tr_b16 v[218:219], %[va0] offset:43520\n\t"
  "v_mfma_f32_16x16x32_bf16 a[124:127], v[236:239], v[140:143], a[124:127]\n\t"
  "s_waitcnt lgkmcnt(14)\n\t"
  "v_mfma_f32_16x16x32_bf16 a[0:3], v[176:179], v[88:91], a[0:3]\n\t"
  "ds_read_b64_tr_b16 v[220:221], %[va1] offset:41472\n\t"
  "ds_read_b64_tr_b16 v[222:223], %[va1] offset:43520\n\t"
  "v_mfma_f32_16x16x32_bf16 a[4:7], v[176:179], v[144:147], a[4:7]\n\t"
  "v_mfma_f32_16x16x32_bf16 a[8:11], v[180:183], v[88:91], a[8:11]\n\t"
  "ds_read_b64_tr_b16 v[224:225], %[va0] offset:45056\n\t"
  "ds_read_b64_tr_b16 v[226:227], %[va0] offset:47104\n\t"
  "v_mfma_f32_16x16x32_bf16 a[12:15], v[180:183], v[144:147], a[12:15]\n\t"
  "buffer_load_dwordx4 %[wqs], %[srdq], s48 offen offset:3072 lds\n\t"
  "v_add_u32 %[wqs], 0x40000, %[wqs]\n\t"
  "v_mfma_f32_16x16x32_bf16 a[16:19], v[184:187], v[88:91], a[16:19]\n\t"
  "ds_read_b64_tr_b16 v[228:229], %[va1] offset:45056\n\t"
  "ds_read_b64_tr_b16 v[230:231], %[va1] offset:47104\n\t"
  "v_mfma_f32_16x16x32_bf16 a[20:23], v[184:187], v[144:147], a[20:23]\n\t"
  "v_mfma_f32_16x16x32_bf16 a[24:27], v[188:191], v[88:91], a[24:27]\n\t"
  "ds_read_b64_tr_b16 v[232:233], %[va0] offset:45568\n\t"
  "ds_read_b64_tr_b16 v[234:235], %[va0] offset:47616\n\t"
  "v_mfma_f32_16x16x32_bf16 a[28:31], v[188:191], v[144:147], a[28:31]\n\t"
  "s_waitcnt lgkmcnt(14)\n\t"
  "v_mfma_f32_16x16x32_bf16 a[32:35], v[192:195], v[88:91], a[32:35]\n\t"
  "ds_read_b64_tr_b16 v[236:237], %[va1] offset:45568\n\t"
  "ds_read_b64_tr_b16 v[238:239], %[va1] offset:47616\n\t"
  "v_mfma_f32_16x16x32_bf16 a[36:39], v[192:195], v[144:147], a[36:39]\n\t"
  "v_mfma_f32_16x16x32_bf16 a[40:43], v[196:199], v[88:91], a[40:43]\n\t"
  "s_waitcnt vmcnt(12)\n\t"
  "s_barrier\n\t"
  "ds_read_b64_tr_b16 v[176:177], %[va0] offset:57344\n\t"
  "ds_read_b64_tr_b16 v[178:179], %[va0] offset:59392\n\t"
  "v_mfma_f32_16x16x32_bf16 a[44:47], v[196:199], v[144:147], a[44:47]\n\t"
  "buffer_load_dwordx4 v[104:107], %[xs], %[srdx], s52 offen offset:2048\n\t"
  "v_mfma_f32_16x16x32_bf16 a[48:51], v[200:203], v[88:91], a[48:51]\n\t"
  "ds_read_b64_tr_b16 v[180:181], %[va1] offset:57344\n\t"
  "ds_read_b64_tr_b16 v[182:183], %[va1] offset:59392\n\t"
  "v_mfma_f32_16x16x32_bf16 a[52:55], v[200:203], v[144:147], a[52:55]\n\t"
  "v_mfma_f32_16x16x32_bf16 a[56:59], v[204:207], v[88:91], a[56:59]\n\t"
  "ds_read_b64_tr_b16 v[184:185], %[va0] offset:57856\n\t"
  "ds_read_b64_tr_b16 v[186:187], %[va0] offset:59904\n\t"
  "v_mfma_f32_16x16x32_bf16 a[60:63], v[204:207], v[144:147], a[60:63]\n\t"
  "s_waitcnt lgkmcnt(14)\n\t"
  "v_mfma_f32_16x16x32_bf16 a[64:67], v[208:211], v[88:91], a[64:67]\n\t"
  "ds_read_b64_tr_b16 v[188:189], %[va1] offset:57856\n\t"
  "ds_read_b64_tr_b16 v[190:191], %[va1] offset:59904\n\t"
  "v_mfma_f32_16x16x32_bf16 a[68:71], v[208:211], v[144:147], a[68:71]\n\t"
  "buffer_load_dwordx4 v[160:163], %[xs], %[srdx], s56 offen offset:0\n\t"
  "s_add_u32 m0, %[m0k], 49152\n\t"
  "v_mfma_f32_16x16x32_bf16 a[72:75], v[212:215], v[88:91], a[72:75]\n\t"
  "ds_read_b64_tr_b16 v[192:193], %[va0] offset:61440\n\t"
  "ds_read_b64_tr_b16 v[194:195], %[va0] offset:63488\n\t"
  "v_mfma_f32_16x16x32_bf16 a[76:79], v[212:215], v[144:147], a[76:79]\n\t"
  "v_mfma_f32_16x16x32_bf16 a[80:83], v[216:219], v[88:91], a[80:83]\n\t"
  "ds_read_b64_tr_b16 v[196:197], %[va1] offset:61440\n\t"
  "ds_read_b64_tr_b16 v[198:199], %[va1] offset:63488\n\t"
  "v_mfma_f32_16x16x32_bf16 a[84:87], v[216:219], v[144:147], a[84:87]\n\t"
  "buffer_load_dwordx4 %[wqs], %[srdq], 0 offen offset:0 lds\n\t"
  "v_mfma_f32_16x16x32_bf16 a[88:91], v[220:223], v[88:91], a[88:91]\n\t"
  "ds_read_b64_tr_b16 v[200:201], %[va0] offset:61952\n\t"
  "ds_read_b64_tr_b16 v[202:203], %[va0] offset:64000\n\t"
  "v_mfma_f32_16x16x32_bf16 a[92:95], v[220:223], v[144:147], a[92:95]\n\t"
  "s_waitcnt lgkmcnt(14)\n\t"
  "v_mfma_f32_16x16x32_bf16 a[96:99], v[224:227], v[88:91], a[96:99]\n\t"
  "ds_read_b64_tr_b16 v[204:205], %[va1] offset:61952\n\t"
  "ds_read_b64_tr_b16 v[206:207], %[va1] offset:64000\n\t"
  "v_mfma_f32_16x16x32_bf16 a[100:103], v[224:227], v[144:147], a[100:103]\n\t"
  "buffer_load_dwordx4 %[wqs], %[srdq], s46 offen offset:1024 lds\n\t"
  "v_mfma_f32_16x16x32_bf16 a[104:107], v[228:231], v[88:91], a[104:107]\n\t"
  "ds_read_b64_tr_b16 v[208:209], %[vz0] offset:0\n\t"
  "ds_read_b64_tr_b16 v[210:211], %[vz0] offset:2048\n\t"
  "v_mfma_f32_16x16x32_bf16 a[108:111], v[228:231], v[144:147], a[108:111]\n\t"
  "v_mfma_f32_16x16x32_bf16 a[112:115], v[232:235], v[88:91], a[112:115]\n\t"
  "ds_read_b64_tr_b16 v[212:213], %[vz1] offset:0\n\t"
  "ds_read_b64_tr_b16 v[214:215], %[vz1] offset:2048\n\t"
  "v_mfma_f32_16x16x32_bf16 a[116:119], v[232:235], v[144:147], a[116:119]\n\t"
  "buffer_load_dwordx4 %[wqs], %[srdq], s47 offen offset:2048 lds\n\t"
  "v_mfma_f32_16x16x32_bf16 a[120:123], v[236:239], v[88:91], a[120:123]\n\t"
  "ds_read_b64_tr_b16 v[216:217], %[vz0] offset:512\n\t"
  "ds_read_b64_tr_b16 v[218:219], %[vz0] offset:2560\n\t"
  "v_mfma_f32_16x16x32_bf16 a[124:127], v[236:239], v[144:147], a[124:127]\n\t"
  "s_waitcnt lgkmcnt(14)\n\t"
  "v_mfma_f32_16x16x32_bf16 a[0:3], v[176:179], v[92:95], a[0:3]\n\t"
  "ds_read_b64_tr_b16 v[220:221], %[vz1] offset:512\n\t"
  "ds_read_b64_tr_b16 v[222:223], %[vz1] offset:2560\n\t"
  "v_mfma_f32_16x16x32_bf16 a[4:7], v[176:179], v[148:151], a[4:7]\n\t"
  "v_mfma_f32_16x16x32_bf16 a[8:11], v[180:183], v[92:95], a[8:11]\n\t"
  "ds_read_b64_tr_b16 v[224:225], %[vz0] offset:4096\n\t"
  "ds_read_b64_tr_b16 v[226:227], %[vz0] offset:6144\n\t"
  "v_mfma_f32_16x16x32_bf16 a[12:15], v[180:183], v[148:151], a[12:15]\n\t"
  "buffer_load_dwordx4 %[wqs], %[srdq], s48 offen offset:3072 lds\n\t"
  "v_add_u32 %[wqs], 0x40000, %[wqs]\n\t"
  "v_mfma_f32_16x16x32_bf16 a[16:19], v[184:187], v[92:95], a[16:19]\n\t"
  "ds_read_b64_tr_b16 v[228:229], %[vz1] offset:4096\n\t"
  "ds_read_b64_tr_b16 v[230:231], %[vz1] offset:6144\n\t"
  "v_mfma_f32_16x16x32_bf16 a[20:23], v[184:187], v[148:151], a[20:23]\n\t"
  "v_mfma_f32_16x16x32_bf16 a[24:27], v[188:191], v[92:95], a[24:27]\n\t"
  "ds_read_b64_tr_b16 v[232:233], %[vz0] offset:4608\n\t"
  "ds_read_b64_tr_b16 v[234:235], %[vz0] offset:6656\n\t"
  "v_mfma_f32_16x16x32_bf16 a[28:31], v[188:191], v[148:151], a[28:31]\n\t"
  "s_waitcnt lgkmcnt(14)\n\t"
  "v_mfma_f32_16x16x32_bf16 a[32:35], v[192:195], v[92:95], a[32:35]\n\t"
  "ds_read_b64_tr_b16 v[236:237], %[vz1] offset:4608\n\t"
  "ds_read_b64_tr_b16 v[238:239], %[vz1] offset:6656\n\t"
  "v_mfma_f32_16x16x32_bf16 a[36:39], v[192:195], v[148:151], a[36:39]\n\t"
  "v_mfma_f32_16x16x32_bf16 a[40:43], v[196:199], v[92:95], a[40:43]\n\t"
  "s_waitcnt vmcnt(12)\n\t"
  "s_barrier\n\t"
  "ds_read_b64_tr_b16 v[176:177], %[vk0] offset:0\n\t"
  "ds_read_b64_tr_b16 v[178:179], %[vk0] offset:2048\n\t"
  "v_mfma_f32_16x16x32_bf16 a[44:47], v[196:199], v[148:151], a[44:47]\n\t"
  "buffer_load_dwordx4 v[108:111], %[xs], %[srdx], s52 offen offset:3072\n\t"
  "v_mfma_f32_16x16x32_bf16 a[48:51], v[200:203], v[92:95], a[48:51]\n\t"
  "ds_read_b64_tr_b16 v[180:181], %[vk1] offset:0\n\t"
  "ds_read_b64_tr_b16 v[182:183], %[vk1] offset:2048\n\t"
  "v_mfma_f32_16x16x32_bf16 a[52:55], v[200:203], v[148:151], a[52:55]\n\t"
  "v_mfma_f32_16x16x32_bf16 a[56:59], v[204:207], v[92:95], a[56:59]\n\t"
  "ds_read_b64_tr_b16 v[184:185], %[vk0] offset:512\n\t"
  "ds_read_b64_tr_b16 v[186:187], %[vk0] offset:2560\n\t"
  "v_mfma_f32_16x16x32_bf16 a[60:63], v[204:207], v[148:151], a[60:63]\n\t"
  "s_waitcnt lgkmcnt(14)\n\t"
  "v_mfma_f32_16x16x32_bf16 a[64:67], v[208:211], v[92:95], a[64:67]\n\t"
  "ds_read_b64_tr_b16 v[188:189], %[vk1] offset:512\n\t"
  "ds_read_b64_tr_b16 v[190:191], %[vk1] offset:2560\n\t"
  "v_mfma_f32_16x16x32_bf16 a[68:71], v[208:211], v[148:151], a[68:71]\n\t"
  "buffer_load_dwordx4 v[164:167], %[xs], %[srdx], s56 offen offset:1024\n\t"
  "s_add_u32 m0, %[m0k], 73728\n\t"
  "v_mfma_f32_16x16x32_bf16 a[72:75], v[212:215], v[92:95], a[72:75]\n\t"
  "ds_read_b64_tr_b16 v[192:193], %[vk0] offset:4096\n\t"
  "ds_read_b64_tr_b16 v[194:195], %[vk0] offset:6144\n\t"
  "v_mfma_f32_16x16x32_bf16 a[76:79], v[212:215], v[148:151], a[76:79]\n\t"
  "v_mfma_f32_16x16x32_bf16 a[80:83], v[216:219], v[92:95], a[80:83]\n\t"
  "ds_read_b64_tr_b16 v[196:197], %[vk1] offset:4096\n\t"
  "ds_read_b64_tr_b16 v[198:199], %[vk1] offset:6144\n\t"
  "v_mfma_f32_16x16x32_bf16 a[84:87], v[216:219], v[148:151], a[84:87]\n\t"
  "buffer_load_dwordx4 %[wqs], %[srdq], 0 offen offset:0 lds\n\t"
  "v_mfma_f32_16x16x32_bf16 a[88:91], v[220:223], v[92:95], a[88:91]\n\t"
  "ds_read_b64_tr_b16 v[200:201], %[vk0] offset:4608\n\t"
  "ds_read_b64_tr_b16 v[202:203], %[vk0] offset:6656\n\t"
  "v_mfma_f32_16x16x32_bf16 a[92:95], v[220:223], v[148:151], a[92:95]\n\t"
  "s_waitcnt lgkmcnt(14)\n\t"
  "v_mfma_f32_16x16x32_bf16 a[96:99], v[224:227], v[92:95], a[96:99]\n\t"
  "ds_read_b64_tr_b16 v[204:205], %[vk1] offset:4608\n\t"
  "ds_read_b64_tr_b16 v[206:207], %[vk1] offset:6656\n\t"
  "v_mfma_f32_16x16x32_bf16 a[100:103], v[224:227], v[148:151], a[100:103]\n\t"
  "buffer_load_dwordx4 %[wqs], %[srdq], s46 offen offset:1024 lds\n\t"
  "v_mfma_f32_16x16x32_bf16 a[104:107], v[228:231], v[92:95], a[104:107]\n\t"
  "ds_read_b64_tr_b16 v[208:209], %[vk0] offset:8192\n\t"
  "ds_read_b64_tr_b16 v[210:211], %[vk0] offset:10240\n\t"
  "v_mfma_f32_16x16x32_bf16 a[108:111], v[228:231], v[148:151], a[108:111]\n\t"
  "v_mfma_f32_16x16x32_bf16 a[112:115], v[232:235], v[92:95], a[112:115]\n\t"
  "ds_read_b64_tr_b16 v[212:213], %[vk1] offset:8192\n\t"
  "ds_read_b64_tr_b16 v[214:215], %[vk1] offset:10240\n\t"
  "v_mfma_f32_16x16x32_bf16 a[116:119], v[232:235], v[148:151], a[116:119]\n\t"
  "buffer_load_dwordx4 %[wqs], %[srdq], s47 offen offset:2048 lds\n\t"
  "v_mfma_f32_16x16x32_bf16 a[120:123], v[236:239], v[92:95], a[120:123]\n\t"
  "ds_read_b64_tr_b16 v[216:217], %[vk0] offset:8704\n\t"
  "ds_read_b64_tr_b16 v[218:219], %[vk0] offset:10752\n\t"
  "v_mfma_f32_16x16x32_bf16 a[124:127], v[236:239], v[148:151], a[124:127]\n\t"
  "s_waitcnt lgkmcnt(14)\n\t"
  "v_mfma_f32_16x16x32_bf16 a[0:3], v[176:179], v[96:99], a[0:3]\n\t"
  "ds_read_b64_tr_b16 v[220:221], %[vk1] offset:8704\n\t"
  "ds_read_b64_tr_b16 v[222:223], %[vk1] offset:10752\n\t"
  "v_mfma_f32_16x16x32_bf16 a[4:7], v[176:179], v[152:155], a[4:7]\n\t"
  "v_mfma_f32_16x16x32_bf16 a[8:11], v[180:183], v[96:99], a[8:11]\n\t"
  "ds_read_b64_tr_b16 v[224:225], %[vk0] offset:12288\n\t"
  "ds_read_b64_tr_b16 v[226:227], %[vk0] offset:14336\n\t"
  "v_mfma_f32_16x16x32_bf16 a[12:15], v[180:183], v[152:155], a[12:15]\n\t"
  "buffer_load_dwordx4 %[wqs], %[srdq], s48 offen offset:3072 lds\n\t"
  "v_add_u32 %[wqs], 0x40000, %[wqs]\n\t"
  "v_mfma_f32_16x16x32_bf16 a[16:19], v[184:187], v[96:99], a[16:19]\n\t"
  "ds_read_b64_tr_b16 v[228:229], %[vk1] offset:12288\n\t"
  "ds_read_b64_tr_b16 v[230:231], %[vk1] offset:14336\n\t"
  "v_mfma_f32_16x16x32_bf16 a[20:23], v[184:187], v[152:155], a[20:23]\n\t"
  "v_mfma_f32_16x16x32_bf16 a[24:27], v[188:191], v[96:99], a[24:27]\n\t"
  "ds_read_b64_tr_b16 v[232:233], %[vk0] offset:12800\n\t"
  "ds_read_b64_tr_b16 v[234:235], %[vk0] offset:14848\n\t"
  "v_mfma_f32_16x16x32_bf16 a[28:31], v[188:191], v[152:155], a[28:31]\n\t"
  "s_waitcnt lgkmcnt(14)\n\t"
  "v_mfma_f32_16x16x32_bf16 a[32:35], v[192:195], v[96:99], a[32:35]\n\t"
  "ds_read_b64_tr_b16 v[236:237], %[vk1] offset:12800\n\t"
  "ds_read_b64_tr_b16 v[238:239], %[vk1] offset:14848\n\t"
  "v_mfma_f32_16x16x32_bf16 a[36:39], v[192:195], v[152:155], a[36:39]\n\t"
  "v_mfma_f32_16x16x32_bf16 a[40:43], v[196:199], v[96:99], a[40:43]\n\t"
  "s_waitcnt vmcnt(12)\n\t"
  "s_barrier\n\t"
  "ds_read_b64_tr_b16 v[176:177], %[vk0] offset:24576\n\t"
  "ds_read_b64_tr_b16 v[178:179], %[vk0] offset:26624\n\t"
  "v_mfma_f32_16x16x32_bf16 a[44:47], v[196:199], v[152:155], a[44:47]\n\t"
  "buffer_load_dwordx4 v[112:115], %[xs], %[srdx], s53 offen offset:0\n\t"
  "v_mfma_f32_16x16x32_bf16 a[48:51], v[200:203], v[96:99], a[48:51]\n\t"
  "ds_read_b64_tr_b16 v[180:181], %[vk1] offset:24576\n\t"
  "ds_read_b64_tr_b16 v[182:183], %[vk1] offset:26624\n\t"
  "v_mfma_f32_16x16x32_bf16 a[52:55], v[200:203], v[152:155], a[52:55]\n\t"
  "v_mfma_f32_16x16x32_bf16 a[56:59], v[204:207], v[96:99], a[56:59]\n\t"
  "ds_read_b64_tr_b16 v[184:185], %[vk0] offset:25088\n\t"
  "ds_read_b64_tr_b16 v[186:187], %[vk0] offset:27136\n\t"
  "v_mfma_f32_16x16x32_bf16 a[60:63], v[204:207], v[152:155], a[60:63]\n\t"
  "s_waitcnt lgkmcnt(14)\n\t"
  "v_mfma_f32_16x16x32_bf16 a[64:67], v[208:211], v[96:99], a[64:67]\n\t"
  "ds_read_b64_tr_b16 v[188:189], %[vk1] offset:25088\n\t"
  "ds_read_b64_tr_b16 v[190:191], %[vk1] offset:27136\n\t"
  "v_mfma_f32_16x16x32_bf16 a[68:71], v[208:211], v[152:155], a[68:71]\n\t"
  "buffer_load_dwordx4 v[168:171], %[xs], %[srdx], s56 offen offset:2048\n\t"
  "s_add_u32 m0, %[m0k], 98304\n\t"
  "v_mfma_f32_16x16x32_bf16 a[72:75], v[212:215], v[96:99], a[72:75]\n\t"
  "ds_read_b64_tr_b16 v[192:193], %[vk0] offset:28672\n\t"
  "ds_read_b64_tr_b16 v[194:195], %[vk0] offset:30720\n\t"
  "v_mfma_f32_16x16x32_bf16 a[76:79], v[212:215], v[152:155], a[76:79]\n\t"
  "v_mfma_f32_16x16x32_bf16 a[80:83], v[216:219], v[96:99], a[80:83]\n\t"
  "ds_read_b64_tr_b16 v[196:197], %[vk1] offset:28672\n\t"
  "ds_read_b64_tr_b16 v[198:199], %[vk1] offset:30720\n\t"
  "v_mfma_f32_16x16x32_bf16 a[84:87], v[216:219], v[152:155], a[84:87]\n\t"
  "buffer_load_dwordx4 %[wqs], %[srdq], 0 offen offset:0 lds\n\t"
  "v_mfma_f32_16x16x32_bf16 a[88:91], v[220:223], v[96:99], a[88:91]\n\t"
  "ds_read_b64_tr_b16 v[200:201], %[vk0] offset:29184\n\t"
  "ds_read_b64_tr_b16 v[202:203], %[vk0] offset:31232\n\t"
  "v_mfma_f32_16x16x32_bf16 a[92:95], v[220:223], v[152:155], a[92:95]\n\t"
  "s_waitcnt lgkmcnt(14)\n\t"
  "v_mfma_f32_16x16x32_bf16 a[96:99], v[224:227], v[96:99], a[96:99]\n\t"
  "ds_read_b64_tr_b16 v[204:205], %[vk1] offset:29184\n\t"
  "ds_read_b64_tr_b16 v[206:207], %[vk1] offset:31232\n\t"
  "v_mfma_f32_16x16x32_bf16 a[100:103], v[224:227], v[152:155], a[100:103]\n\t"
  "buffer_load_dwordx4 %[wqs], %[srdq], s46 offen offset:1024 lds\n\t"
  "v_mfma_f32_16x16x32_bf16 a[104:107], v[228:231], v[96:99], a[104:107]\n\t"
  "ds_read_b64_tr_b16 v[208:209], %[vk0] offset:32768\n\t"
  "ds_read_b64_tr_b16 v[210:211], %[vk0] offset:34816\n\t"
  "v_mfma_f32_16x16x32_bf16 a[108:111], v[228:231], v[152:155], a[108:111]\n\t"
  "v_mfma_f32_16x16x32_bf16 a[112:115], v[232:235], v[96:99], a[112:115]\n\t"
  "ds_read_b64_tr_b16 v[212:213], %[vk1] offset:32768\n\t"
  "ds_read_b64_tr_b16 v[214:215], %[vk1] offset:34816\n\t"
  "v_mfma_f32_16x16x32_bf16 a[116:119], v[232:235], v[152:155], a[116:119]\n\t"
  "buffer_load_dwordx4 %[wqs], %[srdq], s47 offen offset:2048 lds\n\t"
  "v_mfma_f32_16x16x32_bf16 a[120:123], v[236:239], v[96:99], a[120:123]\n\t"
  "ds_read_b64_tr_b16 v[216:217], %[vk0] offset:33280\n\t"
  "ds_read_b64_tr_b16 v[218:219], %[vk0] offset:35328\n\t"
  "v_mfma_f32_16x16x32_bf16 a[124:127], v[236:239], v[152:155], a[124:127]\n\t"
  "s_waitcnt lgkmcnt(14)\n\t"
  "v_mfma_f32_16x16x32_bf16 a[0:3], v[176:179], v[100:103], a[0:3]\n\t"
  "ds_read_b64_tr_b16 v[220:221], %[vk1] offset:33280\n\t"
  "ds_read_b64_tr_b16 v[222:223], %[vk1] offset:35328\n\t"
  "v_mfma_f32_16x16x32_bf16 a[4:7], v[176:179], v[156:159], a[4:7]\n\t"
  "v_mfma_f32_16x16x32_bf16 a[8:11], v[180:183], v[100:103], a[8:11]\n\t"
  "ds_read_b64_tr_b16 v[224:225], %[vk0] offset:36864\n\t"
  "ds_read_b64_tr_b16 v[226:227], %[vk0] offset:38912\n\t"
  "v_mfma_f32_16x16x32_bf16 a[12:15], v[180:183], v[156:159], a[12:15]\n\t"
  "buffer_load_dwordx4 %[wqs], %[srdq], s48 offen offset:3072 lds\n\t"
  "v_add_u32 %[wqs], 0x40000, %[wqs]\n\t"
  "v_mfma_f32_16x16x32_bf16 a[16:19], v[184:187], v[100:103], a[16:19]\n\t"
  "ds_read_b64_tr_b16 v[228:229], %[vk1] offset:36864\n\t"
  "ds_read_b64_tr_b16 v[230:231], %[vk1] offset:38912\n\t"
  "v_mfma_f32_16x16x32_bf16 a[20:23], v[184:187], v[156:159], a[20:23]\n\t"
  "v_mfma_f32_16x16x32_bf16 a[24:27], v[188:191], v[100:103], a[24:27]\n\t"
  "ds_read_b64_tr_b16 v[232:233], %[vk0] offset:37376\n\t"
  "ds_read_b64_tr_b16 v[234:235], %[vk0] offset:39424\n\t"
  "v_mfma_f32_16x16x32_bf16 a[28:31], v[188:191], v[156:159], a[28:31]\n\t"
  "s_waitcnt lgkmcnt(14)\n\t"
  "v_mfma_f32_16x16x32_bf16 a[32:35], v[192:195], v[100:103], a[32:35]\n\t"
  "ds_read_b64_tr_b16 v[236:237], %[vk1] offset:37376\n\t"
  "ds_read_b64_tr_b16 v[238:239], %[vk1] offset:39424\n\t"
  "v_mfma_f32_16x16x32_bf16 a[36:39], v[192:195], v[156:159], a[36:39]\n\t"
  "v_mfma_f32_16x16x32_bf16 a[40:43], v[196:199], v[100:103], a[40:43]\n\t"
  "s_waitcnt vmcnt(12)\n\t"
  "s_barrier\n\t"
  "ds_read_b64_tr_b16 v[176:177], %[vk0] offset:49152\n\t"
  "ds_read_b64_tr_b16 v[178:179], %[vk0] offset:51200\n\t"
  "v_mfma_f32_16x16x32_bf16 a[44:47], v[196:199], v[156:159], a[44:47]\n\t"
  "buffer_load_dwordx4 v[116:119], %[xs], %[srdx], s53 offen offset:1024\n\t"
  "v_mfma_f32_16x16x32_bf16 a[48:51], v[200:203], v[100:103], a[48:51]\n\t"
  "ds_read_b64_tr_b16 v[180:181], %[vk1] offset:49152\n\t"
  "ds_read_b64_tr_b16 v[182:183], %[vk1] offset:51200\n\t"
  "v_mfma_f32_16x16x32_bf16 a[52:55], v[200:203], v[156:159], a[52:55]\n\t"
  "v_mfma_f32_16x16x32_bf16 a[56:59], v[204:207], v[100:103], a[56:59]\n\t"
  "ds_read_b64_tr_b16 v[184:185], %[vk0] offset:49664\n\t"
  "ds_read_b64_tr_b16 v[186:187], %[vk0] offset:51712\n\t"
  "v_mfma_f32_16x16x32_bf16 a[60:63], v[204:207], v[156:159], a[60:63]\n\t"
  "buffer_load_dwordx4 v[172:175], %[xs], %[srdx], s56 offen offset:3072\n\t"
  "s_add_u32 m0, %[m0k], 122880\n\t"
  "s_waitcnt lgkmcnt(14)\n\t"
  "v_mfma_f32_16x16x32_bf16 a[64:67], v[208:211], v[100:103], a[64:67]\n\t"
  "ds_read_b64_tr_b16 v[188:189], %[vk1] offset:49664\n\t"
  "ds_read_b64_tr_b16 v[190:191], %[vk1] offset:51712\n\t"
  "v_mfma_f32_16x16x32_bf16 a[68:71], v[208:211], v[156:159], a[68:71]\n\t"
  "buffer_load_dwordx4 %[wqs], %[srdq], 0 offen offset:0 lds\n\t"
  "v_mfma_f32_16x16x32_bf16 a[72:75], v[212:215], v[100:103], a[72:75]\n\t"
  "ds_read_b64_tr_b16 v[192:193], %[vk0] offset:53248\n\t"
  "ds_read_b64_tr_b16 v[194:195], %[vk0] offset:55296\n\t"
  "v_mfma_f32_16x16x32_bf16 a[76:79], v[212:215], v[156:159], a[76:79]\n\t"
  "v_mfma_f32_16x16x32_bf16 a[80:83], v[216:219], v[100:103], a[80:83]\n\t"
  "ds_read_b64_tr_b16 v[196:197], %[vk1] offset:53248\n\t"
  "ds_read_b64_tr_b16 v[198:199], %[vk1] offset:55296\n\t"
  "v_mfma_f32_16x16x32_bf16 a[84:87], v[216:219], v[156:159], a[84:87]\n\t"
  "buffer_load_dwordx4 %[wqs], %[srdq], s46 offen offset:1024 lds\n\t"
  "v_mfma_f32_16x16x32_bf16 a[88:91], v[220:223], v[100:103], a[88:91]\n\t"
  "ds_read_b64_tr_b16 v[200:201], %[vk0] offset:53760\n\t"
  "ds_read_b64_tr_b16 v[202:203], %[vk0] offset:55808\n\t"
  "v_mfma_f32_16x16x32_bf16 a[92:95], v[220:223], v[156:159], a[92:95]\n\t"
  "buffer_load_dwordx4 %[wqs], %[srdq], s47 offen offset:2048 lds\n\t"
  "s_waitcnt lgkmcnt(14)\n\t"
  "v_mfma_f32_16x16x32_bf16 a[96:99], v[224:227], v[100:103], a[96:99]\n\t"
  "ds_read_b64_tr_b16 v[204:205], %[vk1] offset:53760\n\t"
  "ds_read_b64_tr_b16 v[206:207], %[vk1] offset:55808\n\t"
  "v_mfma_f32_16x16x32_bf16 a[100:103], v[224:227], v[156:159], a[100:103]\n\t"
  "buffer_load_dwordx4 %[wqs], %[srdq], s48 offen offset:3072 lds\n\t"
  "v_add_u32 %[wqs], 0x40000, %[wqs]\n\t"
  "s_add_u32 m0, %[m0k], 0\n\t"
  "v_mfma_f32_16x16x32_bf16 a[104:107], v[228:231], v[100:103], a[104:107]\n\t"
  "ds_read_b64_tr_b16 v[208:209], %[vk0] offset:57344\n\t"
  "ds_read_b64_tr_b16 v[210:211], %[vk0] offset:59392\n\t"
  "v_mfma_f32_16x16x32_bf16 a[108:111], v[228:231], v[156:159], a[108:111]\n\t"
  "v_mfma_f32_16x16x32_bf16 a[112:115], v[232:235], v[100:103], a[112:115]\n\t"
  "ds_read_b64_tr_b16 v[212:213], %[vk1] offset:57344\n\t"
  "ds_read_b64_tr_b16 v[214:215], %[vk1] offset:59392\n\t"
  "v_mfma_f32_16x16x32_bf16 a[116:119], v[232:235], v[156:159], a[116:119]\n\t"
  "buffer_load_dwordx4 %[ks0], %[srdk], 0 offen offset:0 lds\n\t"
  "v_mfma_f32_16x16x32_bf16 a[120:123], v[236:239], v[100:103], a[120:123]\n\t"
  "ds_read_b64_tr_b16 v[216:217], %[vk0] offset:57856\n\t"
  "ds_read_b64_tr_b16 v[218:219], %[vk0] offset:59904\n\t"
  "v_mfma_f32_16x16x32_bf16 a[124:127], v[236:239], v[156:159], a[124:127]\n\t"
  "buffer_load_dwordx4 %[ks1], %[srdk], s40 offen offset:1024 lds\n\t"
  "s_waitcnt lgkmcnt(14)\n\t"
  "v_mfma_f32_16x16x32_bf16 a[0:3], v[176:179], v[104:107], a[0:3]\n\t"
  "ds_read_b64_tr_b16 v[220:221], %[vk1] offset:57856\n\t"
  "ds_read_b64_tr_b16 v[222:223], %[vk1] offset:59904\n\t"
  "v_mfma_f32_16x16x32_bf16 a[4:7], v[176:179], v[160:163], a[4:7]\n\t"
  "buffer_load_dwordx4 %[ks0], %[srdk], s41 offen offset:2048 lds\n\t"
  "v_mfma_f32_16x16x32_bf16 a[8:11], v[180:183], v[104:107], a[8:11]\n\t"
  "ds_read_b64_tr_b16 v[224:225], %[vk0] offset:61440\n\t"
  "ds_read_b64_tr_b16 v[226:227], %[vk0] offset:63488\n\t"
  "v_mfma_f32_16x16x32_bf16 a[12:15], v[180:183], v[160:163], a[12:15]\n\t"
  "v_mfma_f32_16x16x32_bf16 a[16:19], v[184:187], v[104:107], a[16:19]\n\t"
  "ds_read_b64_tr_b16 v[228:229], %[vk1] offset:61440\n\t"
  "ds_read_b64_tr_b16 v[230:231], %[vk1] offset:63488\n\t"
  "v_mfma_f32_16x16x32_bf16 a[20:23], v[184:187], v[160:163], a[20:23]\n\t"
  "buffer_load_dwordx4 %[ks1], %[srdk], s42 offen offset:3072 lds\n\t"
  "v_add_u32 %[ks0], 0x4000, %[ks0]\n\t"
  "v_add_u32 %[ks1], 0x4000, %[ks1]\n\t"
  "v_mfma_f32_16x16x32_bf16 a[24:27], v[188:191], v[104:107], a[24:27]\n\t"
  "ds_read_b64_tr_b16 v[232:233], %[vk0] offset:61952\n\t"
  "ds_read_b64_tr_b16 v[234:235], %[vk0] offset:64000\n\t"
  "v_mfma_f32_16x16x32_bf16 a[28:31], v[188:191], v[160:163], a[28:31]\n\t"
  "s_waitcnt lgkmcnt(14)\n\t"
  "v_mfma_f32_16x16x32_bf16 a[32:35], v[192:195], v[104:107], a[32:35]\n\t"
  "ds_read_b64_tr_b16 v[236:237], %[vk1] offset:61952\n\t"
  "ds_read_b64_tr_b16 v[238:239], %[vk1] offset:64000\n\t"
  "v_mfma_f32_16x16x32_bf16 a[36:39], v[192:195], v[160:163], a[36:39]\n\t"
  "v_mfma_f32_16x16x32_bf16 a[40:43], v[196:199], v[104:107], a[40:43]\n\t"
  "s_waitcnt vmcnt(16)\n\t"
  "s_barrier\n\t"
  "ds_read_b64_tr_b16 v[176:177], %[va0] offset:8192\n\t"
  "ds_read_b64_tr_b16 v[178:179], %[va0] offset:10240\n\t"
  "v_mfma_f32_16x16x32_bf16 a[44:47], v[196:199], v[160:163], a[44:47]\n\t"
  "s_add_u32 m0, %[m0k], 16384\n\t"
  "s_nop 0\n\t"
  "buffer_load_dwordx4 %[ks0], %[srdk], 0 offen offset:0 lds\n\t"
  "v_mfma_f32_16x16x32_bf16 a[48:51], v[200:203], v[104:107], a[48:51]\n\t"
  "ds_read_b64_tr_b16 v[180:181], %[va1] offset:8192\n\t"
  "ds_read_b64_tr_b16 v[182:183], %[va1] offset:10240\n\t"
  "v_mfma_f32_16x16x32_bf16 a[52:55], v[200:203], v[160:163], a[52:55]\n\t"
  "v_mfma_f32_16x16x32_bf16 a[56:59], v[204:207], v[104:107], a[56:59]\n\t"
  "ds_read_b64_tr_b16 v[184:185], %[va0] offset:8704\n\t"
  "ds_read_b64_tr_b16 v[186:187], %[va0] offset:10752\n\t"
  "v_mfma_f32_16x16x32_bf16 a[60:63], v[204:207], v[160:163], a[60:63]\n\t"
  "buffer_load_dwordx4 %[ks1], %[srdk], s40 offen offset:1024 lds\n\t"
  "s_waitcnt lgkmcnt(14)\n\t"
  "v_mfma_f32_16x16x32_bf16 a[64:67], v[208:211], v[104:107], a[64:67]\n\t"
  "ds_read_b64_tr_b16 v[188:189], %[va1] offset:8704\n\t"
  "ds_read_b64_tr_b16 v[190:191], %[va1] offset:10752\n\t"
  "v_mfma_f32_16x16x32_bf16 a[68:71], v[208:211], v[160:163], a[68:71]\n\t"
  "v_mfma_f32_16x16x32_bf16 a[72:75], v[212:215], v[104:107], a[72:75]\n\t"
  "ds_read_b64_tr_b16 v[192:193], %[va0] offset:12288\n\t"
  "ds_read_b64_tr_b16 v[194:195], %[va0] offset:14336\n\t"
  "v_mfma_f32_16x16x32_bf16 a[76:79], v[212:215], v[160:163], a[76:79]\n\t"
  "buffer_load_dwordx4 %[ks0], %[srdk], s41 offen offset:2048 lds\n\t"
  "v_mfma_f32_16x16x32_bf16 a[80:83], v[216:219], v[104:107], a[80:83]\n\t"
  "ds_read_b64_tr_b16 v[196:197], %[va1] offset:12288\n\t"
  "ds_read_b64_tr_b16 v[198:199], %[va1] offset:14336\n\t"
  "v_mfma_f32_16x16x32_bf16 a[84:87], v[216:219], v[160:163], a[84:87]\n\t"
  "v_mfma_f32_16x16x32_bf16 a[88:91], v[220:223], v[104:107], a[88:91]\n\t"
  "ds_read_b64_tr_b16 v[200:201], %[va0] offset:12800\n\t"
  "ds_read_b64_tr_b16 v[202:203], %[va0] offset:14848\n\t"
  "v_mfma_f32_16x16x32_bf16 a[92:95], v[220:223], v[160:163], a[92:95]\n\t"
  "buffer_load_dwordx4 %[ks1], %[srdk], s42 offen offset:3072 lds\n\t"
  "v_add_u32 %[ks0], 0x4000, %[ks0]\n\t"
  "v_add_u32 %[ks1], 0x4000, %[ks1]\n\t"
  "s_add_u32 m0, %[m0k], 32768\n\t"
  "s_waitcnt lgkmcnt(14)\n\t"
  "v_mfma_f32_16x16x32_bf16 a[96:99], v[224:227], v[104:107], a[96:99]\n\t"
  "ds_read_b64_tr_b16 v[204:205], %[va1] offset:12800\n\t"
  "ds_read_b64_tr_b16 v[206:207], %[va1] offset:14848\n\t"
  "v_mfma_f32_16x16x32_bf16 a[100:103], v[224:227], v[160:163], a[100:103]\n\t"
  "buffer_load_dwordx4 %[ks0], %[srdk], 0 offen offset:0 lds\n\t"
  "v_mfma_f32_16x16x32_bf16 a[104:107], v[228:231], v[104:107], a[104:107]\n\t"
  "ds_read_b64_tr_b16 v[208:209], %[va0] offset:16384\n\t"
  "ds_read_b64_tr_b16 v[210:211], %[va0] offset:18432\n\t"
  "v_mfma_f32_16x16x32_bf16 a[108:111], v[228:231], v[160:163], a[108:111]\n\t"
  "v_mfma_f32_16x16x32_bf16 a[112:115], v[232:235], v[104:107], a[112:115]\n\t"
  "ds_read_b64_tr_b16 v[212:213], %[va1] offset:16384\n\t"
  "ds_read_b64_tr_b16 v[214:215], %[va1] offset:18432\n\t"
  "v_mfma_f32_16x16x32_bf16 a[116:119], v[232:235], v[160:163], a[116:119]\n\t"
  "buffer_load_dwordx4 %[ks1], %[srdk], s40 offen offset:1024 lds\n\t"
  "v_mfma_f32_16x16x32_bf16 a[120:123], v[236:239], v[104:107], a[120:123]\n\t"
  "ds_read_b64_tr_b16 v[216:217], %[va0] offset:16896\n\t"
  "ds_read_b64_tr_b16 v[218:219], %[va0] offset:18944\n\t"
  "v_mfma_f32_16x16x32_bf16 a[124:127], v[236:239], v[160:163], a[124:127]\n\t"
  "s_waitcnt lgkmcnt(14)\n\t"
  "v_mfma_f32_16x16x32_bf16 a[0:3], v[176:179], v[108:111], a[0:3]\n\t"
  "ds_read_b64_tr_b16 v[220:221], %[va1] offset:16896\n\t"
  "ds_read_b64_tr_b16 v[222:223], %[va1] offset:18944\n\t"
  "v_mfma_f32_16x16x32_bf16 a[4:7], v[176:179], v[164:167], a[4:7]\n\t"
  "buffer_load_dwordx4 %[ks0], %[srdk], s41 offen offset:2048 lds\n\t"
  "v_mfma_f32_16x16x32_bf16 a[8:11], v[180:183], v[108:111], a[8:11]\n\t"
  "ds_read_b64_tr_b16 v[224:225], %[va0] offset:20480\n\t"
  "ds_read_b64_tr_b16 v[226:227], %[va0] offset:22528\n\t"
  "v_mfma_f32_16x16x32_bf16 a[12:15], v[180:183], v[164:167], a[12:15]\n\t"
  "buffer_load_dwordx4 %[ks1], %[srdk], s42 offen offset:3072 lds\n\t"
  "v_add_u32 %[ks0], 0x4000, %[ks0]\n\t"
  "v_add_u32 %[ks1], 0x4000, %[ks1]\n\t"
  "v_mfma_f32_16x16x32_bf16 a[16:19], v[184:187], v[108:111], a[16:19]\n\t"
  "ds_read_b64_tr_b16 v[228:229], %[va1] offset:20480\n\t"
  "ds_read_b64_tr_b16 v[230:231], %[va1] offset:22528\n\t"
  "v_mfma_f32_16x16x32_bf16 a[20:23], v[184:187], v[164:167], a[20:23]\n\t"
  "v_mfma_f32_16x16x32_bf16 a[24:27], v[188:191], v[108:111], a[24:27]\n\t"
  "ds_read_b64_tr_b16 v[232:233], %[va0] offset:20992\n\t"
  "ds_read_b64_tr_b16 v[234:235], %[va0] offset:23040\n\t"
  "v_mfma_f32_16x16x32_bf16 a[28:31], v[188:191], v[164:167], a[28:31]\n\t"
  "s_waitcnt lgkmcnt(14)\n\t"
  "v_mfma_f32_16x16x32_bf16 a[32:35], v[192:195], v[108:111], a[32:35]\n\t"
  "ds_read_b64_tr_b16 v[236:237], %[va1] offset:20992\n\t"
  "ds_read_b64_tr_b16 v[238:239], %[va1] offset:23040\n\t"
  "v_mfma_f32_16x16x32_bf16 a[36:39], v[192:195], v[164:167], a[36:39]\n\t"
  "v_mfma_f32_16x16x32_bf16 a[40:43], v[196:199], v[108:111], a[40:43]\n\t"
  "s_waitcnt vmcnt(18)\n\t"
  "s_barrier\n\t"
  "ds_read_b64_tr_b16 v[176:177], %[va0] offset:32768\n\t"
  "ds_read_b64_tr_b16 v[178:179], %[va0] offset:34816\n\t"
  "v_mfma_f32_16x16x32_bf16 a[44:47], v[196:199], v[164:167], a[44:47]\n\t"
  "s_add_u32 m0, %[m0k], 49152\n\t"
  "s_nop 0\n\t"
  "buffer_load_dwordx4 %[ks0], %[srdk], 0 offen offset:0 lds\n\t"
  "v_mfma_f32_16x16x32_bf16 a[48:51], v[200:203], v[108:111], a[48:51]\n\t"
  "ds_read_b64_tr_b16 v[180:181], %[va1] offset:32768\n\t"
  "ds_read_b64_tr_b16 v[182:183], %[va1] offset:34816\n\t"
  "v_mfma_f32_16x16x32_bf16 a[52:55], v[200:203], v[164:167], a[52:55]\n\t"
  "v_mfma_f32_16x16x32_bf16 a[56:59], v[204:207], v[108:111], a[56:59]\n\t"
  "ds_read_b64_tr_b16 v[184:185], %[va0] offset:33280\n\t"
  "ds_read_b64_tr_b16 v[186:187], %[va0] offset:35328\n\t"
  "v_mfma_f32_16x16x32_bf16 a[60:63], v[204:207], v[164:167], a[60:63]\n\t"
  "s_waitcnt lgkmcnt(14)\n\t"
  "v_mfma_f32_16x16x32_bf16 a[64:67], v[208:211], v[108:111], a[64:67]\n\t"
  "ds_read_b64_tr_b16 v[188:189], %[va1] offset:33280\n\t"
  "ds_read_b64_tr_b16 v[190:191], %[va1] offset:35328\n\t"
  "v_mfma_f32_16x16x32_bf16 a[68:71], v[208:211], v[164:167], a[68:71]\n\t"
  "v_mfma_f32_16x16x32_bf16 a[72:75], v[212:215], v[108:111], a[72:75]\n\t"
  "ds_read_b64_tr_b16 v[192:193], %[va0] offset:36864\n\t"
  "ds_read_b64_tr_b16 v[194:195], %[va0] offset:38912\n\t"
  "v_mfma_f32_16x16x32_bf16 a[76:79], v[212:215], v[164:167], a[76:79]\n\t"
  "buffer_load_dwordx4 %[ks1], %[srdk], s40 offen offset:1024 lds\n\t"
  "v_mfma_f32_16x16x32_bf16 a[80:83], v[216:219], v[108:111], a[80:83]\n\t"
  "ds_read_b64_tr_b16 v[196:197], %[va1] offset:36864\n\t"
  "ds_read_b64_tr_b16 v[198:199], %[va1] offset:38912\n\t"
  "v_mfma_f32_16x16x32_bf16 a[84:87], v[216:219], v[164:167], a[84:87]\n\t"
  "v_mfma_f32_16x16x32_bf16 a[88:91], v[220:223], v[108:111], a[88:91]\n\t"
  "ds_read_b64_tr_b16 v[200:201], %[va0] offset:37376\n\t"
  "ds_read_b64_tr_b16 v[202:203], %[va0] offset:39424\n\t"
  "v_mfma_f32_16x16x32_bf16 a[92:95], v[220:223], v[164:167], a[92:95]\n\t"
  "s_waitcnt lgkmcnt(14)\n\t"
  "v_mfma_f32_16x16x32_bf16 a[96:99], v[224:227], v[108:111], a[96:99]\n\t"
  "ds_read_b64_tr_b16 v[204:205], %[va1] offset:37376\n\t"
  "ds_read_b64_tr_b16 v[206:207], %[va1] offset:39424\n\t"
  "v_mfma_f32_16x16x32_bf16 a[100:103], v[224:227], v[164:167], a[100:103]\n\t"
  "buffer_load_dwordx4 %[ks0], %[srdk], s41 offen offset:2048 lds\n\t"
  "v_mfma_f32_16x16x32_bf16 a[104:107], v[228:231], v[108:111], a[104:107]\n\t"
  "ds_read_b64_tr_b16 v[208:209], %[va0] offset:40960\n\t"
  "ds_read_b64_tr_b16 v[210:211], %[va0] offset:43008\n\t"
  "v_mfma_f32_16x16x32_bf16 a[108:111], v[228:231], v[164:167], a[108:111]\n\t"
  "v_mfma_f32_16x16x32_bf16 a[112:115], v[232:235], v[108:111], a[112:115]\n\t"
  "ds_read_b64_tr_b16 v[212:213], %[va1] offset:40960\n\t"
  "ds_read_b64_tr_b16 v[214:215], %[va1] offset:43008\n\t"
  "v_mfma_f32_16x16x32_bf16 a[116:119], v[232:235], v[164:167], a[116:119]\n\t"
  "v_mfma_f32_16x16x32_bf16 a[120:123], v[236:239], v[108:111], a[120:123]\n\t"
  "ds_read_b64_tr_b16 v[216:217], %[va0] offset:41472\n\t"
  "ds_read_b64_tr_b16 v[218:219], %[va0] offset:43520\n\t"
  "v_mfma_f32_16x16x32_bf16 a[124:127], v[236:239], v[164:167], a[124:127]\n\t"
  "s_waitcnt lgkmcnt(14)\n\t"
  "v_mfma_f32_16x16x32_bf16 a[0:3], v[176:179], v[112:115], a[0:3]\n\t"
  "ds_read_b64_tr_b16 v[220:221], %[va1] offset:41472\n\t"
  "ds_read_b64_tr_b16 v[222:223], %[va1] offset:43520\n\t"
  "v_mfma_f32_16x16x32_bf16 a[4:7], v[176:179], v[168:171], a[4:7]\n\t"
  "buffer_load_dwordx4 %[ks1], %[srdk], s42 offen offset:3072 lds\n\t"
  "v_add_u32 %[ks0], 0x4000, %[ks0]\n\t"
  "v_add_u32 %[ks1], 0x4000, %[ks1]\n\t"
  "v_mfma_f32_16x16x32_bf16 a[8:11], v[180:183], v[112:115], a[8:11]\n\t"
  "ds_read_b64_tr_b16 v[224:225], %[va0] offset:45056\n\t"
  "ds_read_b64_tr_b16 v[226:227], %[va0] offset:47104\n\t"
  "v_mfma_f32_16x16x32_bf16 a[12:15], v[180:183], v[168:171], a[12:15]\n\t"
  "v_mfma_f32_16x16x32_bf16 a[16:19], v[184:187], v[112:115], a[16:19]\n\t"
  "ds_read_b64_tr_b16 v[228:229], %[va1] offset:45056\n\t"
  "ds_read_b64_tr_b16 v[230:231], %[va1] offset:47104\n\t"
  "v_mfma_f32_16x16x32_bf16 a[20:23], v[184:187], v[168:171], a[20:23]\n\t"
  "v_mfma_f32_16x16x32_bf16 a[24:27], v[188:191], v[112:115], a[24:27]\n\t"
  "ds_read_b64_tr_b16 v[232:233], %[va0] offset:45568\n\t"
  "ds_read_b64_tr_b16 v[234:235], %[va0] offset:47616\n\t"
  "v_mfma_f32_16x16x32_bf16 a[28:31], v[188:191], v[168:171], a[28:31]\n\t"
  "s_waitcnt lgkmcnt(14)\n\t"
  "v_mfma_f32_16x16x32_bf16 a[32:35], v[192:195], v[112:115], a[32:35]\n\t"
  "ds_read_b64_tr_b16 v[236:237], %[va1] offset:45568\n\t"
  "ds_read_b64_tr_b16 v[238:239], %[va1] offset:47616\n\t"
  "v_mfma_f32_16x16x32_bf16 a[36:39], v[192:195], v[168:171], a[36:39]\n\t"
  "v_mfma_f32_16x16x32_bf16 a[40:43], v[196:199], v[112:115], a[40:43]\n\t"
  "s_waitcnt vmcnt(16)\n\t"
  "s_barrier\n\t"
  "ds_read_b64_tr_b16 v[176:177], %[va0] offset:57344\n\t"
  "ds_read_b64_tr_b16 v[178:179], %[va0] offset:59392\n\t"
  "v_mfma_f32_16x16x32_bf16 a[44:47], v[196:199], v[168:171], a[44:47]\n\t"
  "s_add_u32 m0, %[m0v], 0\n\t"
  "s_nop 0\n\t"
  "buffer_load_dwordx4 %[vs], %[srdv], 0 offen offset:0 lds\n\t"
  "v_mfma_f32_16x16x32_bf16 a[48:51], v[200:203], v[112:115], a[48:51]\n\t"
  "ds_read_b64_tr_b16 v[180:181], %[va1] offset:57344\n\t"
  "ds_read_b64_tr_b16 v[182:183], %[va1] offset:59392\n\t"
  "v_mfma_f32_16x16x32_bf16 a[52:55], v[200:203], v[168:171], a[52:55]\n\t"
  "v_mfma_f32_16x16x32_bf16 a[56:59], v[204:207], v[112:115], a[56:59]\n\t"
  "ds_read_b64_tr_b16 v[184:185], %[va0] offset:57856\n\t"
  "ds_read_b64_tr_b16 v[186:187], %[va0] offset:59904\n\t"
  "v_mfma_f32_16x16x32_bf16 a[60:63], v[204:207], v[168:171], a[60:63]\n\t"
  "buffer_load_dwordx4 %[vs], %[srdv], s40 offen offset:1024 lds\n\t"
  "s_waitcnt lgkmcnt(14)\n\t"
  "v_mfma_f32_16x16x32_bf16 a[64:67], v[208:211], v[112:115], a[64:67]\n\t"
  "ds_read_b64_tr_b16 v[188:189], %[va1] offset:57856\n\t"
  "ds_read_b64_tr_b16 v[190:191], %[va1] offset:59904\n\t"
  "v_mfma_f32_16x16x32_bf16 a[68:71], v[208:211], v[168:171], a[68:71]\n\t"
  "v_mfma_f32_16x16x32_bf16 a[72:75], v[212:215], v[112:115], a[72:75]\n\t"
  "ds_read_b64_tr_b16 v[192:193], %[va0] offset:61440\n\t"
  "ds_read_b64_tr_b16 v[194:195], %[va0] offset:63488\n\t"
  "v_mfma_f32_16x16x32_bf16 a[76:79], v[212:215], v[168:171], a[76:79]\n\t"
  "buffer_load_dwordx4 %[vs], %[srdv], s41 offen offset:2048 lds\n\t"
  "v_mfma_f32_16x16x32_bf16 a[80:83], v[216:219], v[112:115], a[80:83]\n\t"
  "ds_read_b64_tr_b16 v[196:197], %[va1] offset:61440\n\t"
  "ds_read_b64_tr_b16 v[198:199], %[va1] offset:63488\n\t"
  "v_mfma_f32_16x16x32_bf16 a[84:87], v[216:219], v[168:171], a[84:87]\n\t"
  "v_mfma_f32_16x16x32_bf16 a[88:91], v[220:223], v[112:115], a[88:91]\n\t"
  "ds_read_b64_tr_b16 v[200:201], %[va0] offset:61952\n\t"
  "ds_read_b64_tr_b16 v[202:203], %[va0] offset:64000\n\t"
  "v_mfma_f32_16x16x32_bf16 a[92:95], v[220:223], v[168:171], a[92:95]\n\t"
  "buffer_load_dwordx4 %[vs], %[srdv], s42 offen offset:3072 lds\n\t"
  "v_add_u32 %[vs], 0x4000, %[vs]\n\t"
  "s_add_u32 m0, %[m0v], 16384\n\t"
  "s_waitcnt lgkmcnt(14)\n\t"
  "v_mfma_f32_16x16x32_bf16 a[96:99], v[224:227], v[112:115], a[96:99]\n\t"
  "ds_read_b64_tr_b16 v[204:205], %[va1] offset:61952\n\t"
  "ds_read_b64_tr_b16 v[206:207], %[va1] offset:64000\n\t"
  "v_mfma_f32_16x16x32_bf16 a[100:103], v[224:227], v[168:171], a[100:103]\n\t"
  "buffer_load_dwordx4 %[vs], %[srdv], 0 offen offset:0 lds\n\t"
  "v_mfma_f32_16x16x32_bf16 a[104:107], v[228:231], v[112:115], a[104:107]\n\t"
  "ds_read_b64_tr_b16 v[208:209], %[vz0] offset:0\n\t"
  "ds_read_b64_tr_b16 v[210:211], %[vz0] offset:2048\n\t"
  "v_mfma_f32_16x16x32_bf16 a[108:111], v[228:231], v[168:171], a[108:111]\n\t"
  "v_mfma_f32_16x16x32_bf16 a[112:115], v[232:235], v[112:115], a[112:115]\n\t"
  "ds_read_b64_tr_b16 v[212:213], %[vz1] offset:0\n\t"
  "ds_read_b64_tr_b16 v[214:215], %[vz1] offset:2048\n\t"
  "v_mfma_f32_16x16x32_bf16 a[116:119], v[232:235], v[168:171], a[116:119]\n\t"
  "buffer_load_dwordx4 %[vs], %[srdv], s40 offen offset:1024 lds\n\t"
  "v_mfma_f32_16x16x32_bf16 a[120:123], v[236:239], v[112:115], a[120:123]\n\t"
  "ds_read_b64_tr_b16 v[216:217], %[vz0] offset:512\n\t"
  "ds_read_b64_tr_b16 v[218:219], %[vz0] offset:2560\n\t"
  "v_mfma_f32_16x16x32_bf16 a[124:127], v[236:239], v[168:171], a[124:127]\n\t"
  "s_waitcnt lgkmcnt(14)\n\t"
  "v_mfma_f32_16x16x32_bf16 a[0:3], v[176:179], v[116:119], a[0:3]\n\t"
  "ds_read_b64_tr_b16 v[220:221], %[vz1] offset:512\n\t"
  "ds_read_b64_tr_b16 v[222:223], %[vz1] offset:2560\n\t"
  "v_mfma_f32_16x16x32_bf16 a[4:7], v[176:179], v[172:175], a[4:7]\n\t"
  "buffer_load_dwordx4 %[vs], %[srdv], s41 offen offset:2048 lds\n\t"
  "v_mfma_f32_16x16x32_bf16 a[8:11], v[180:183], v[116:119], a[8:11]\n\t"
  "ds_read_b64_tr_b16 v[224:225], %[vz0] offset:4096\n\t"
  "ds_read_b64_tr_b16 v[226:227], %[vz0] offset:6144\n\t"
  "v_mfma_f32_16x16x32_bf16 a[12:15], v[180:183], v[172:175], a[12:15]\n\t"
  "buffer_load_dwordx4 %[vs], %[srdv], s42 offen offset:3072 lds\n\t"
  "v_add_u32 %[vs], 0x4000, %[vs]\n\t"
  "v_mfma_f32_16x16x32_bf16 a[16:19], v[184:187], v[116:119], a[16:19]\n\t"
  "ds_read_b64_tr_b16 v[228:229], %[vz1] offset:4096\n\t"
  "ds_read_b64_tr_b16 v[230:231], %[vz1] offset:6144\n\t"
  "v_mfma_f32_16x16x32_bf16 a[20:23], v[184:187], v[172:175], a[20:23]\n\t"
  "v_mfma_f32_16x16x32_bf16 a[24:27], v[188:191], v[116:119], a[24:27]\n\t"
  "ds_read_b64_tr_b16 v[232:233], %[vz0] offset:4608\n\t"
  "ds_read_b64_tr_b16 v[234:235], %[vz0] offset:6656\n\t"
  "v_mfma_f32_16x16x32_bf16 a[28:31], v[188:191], v[172:175], a[28:31]\n\t"
  "s_waitcnt lgkmcnt(14)\n\t"
  "v_mfma_f32_16x16x32_bf16 a[32:35], v[192:195], v[116:119], a[32:35]\n\t"
  "ds_read_b64_tr_b16 v[236:237], %[vz1] offset:4608\n\t"
  "ds_read_b64_tr_b16 v[238:239], %[vz1] offset:6656\n\t"
  "v_mfma_f32_16x16x32_bf16 a[36:39], v[192:195], v[172:175], a[36:39]\n\t"
  "v_mfma_f32_16x16x32_bf16 a[40:43], v[196:199], v[116:119], a[40:43]\n\t"
  "v_mfma_f32_16x16x32_bf16 a[44:47], v[196:199], v[172:175], a[44:47]\n\t"
  "v_mfma_f32_16x16x32_bf16 a[48:51], v[200:203], v[116:119], a[48:51]\n\t"
  "v_mfma_f32_16x16x32_bf16 a[52:55], v[200:203], v[172:175], a[52:55]\n\t"
  "v_mfma_f32_16x16x32_bf16 a[56:59], v[204:207], v[116:119], a[56:59]\n\t"
  "v_mfma_f32_16x16x32_bf16 a[60:63], v[204:207], v[172:175], a[60:63]\n\t"
  "s_waitcnt lgkmcnt(8)\n\t"
  "v_mfma_f32_16x16x32_bf16 a[64:67], v[208:211], v[116:119], a[64:67]\n\t"
  "v_mfma_f32_16x16x32_bf16 a[68:71], v[208:211], v[172:175], a[68:71]\n\t"
  "v_mfma_f32_16x16x32_bf16 a[72:75], v[212:215], v[116:119], a[72:75]\n\t"
  "v_mfma_f32_16x16x32_bf16 a[76:79], v[212:215], v[172:175], a[76:79]\n\t"
  "v_mfma_f32_16x16x32_bf16 a[80:83], v[216:219], v[116:119], a[80:83]\n\t"
  "v_mfma_f32_16x16x32_bf16 a[84:87], v[216:219], v[172:175], a[84:87]\n\t"
  "v_mfma_f32_16x16x32_bf16 a[88:91], v[220:223], v[116:119], a[88:91]\n\t"
  "v_mfma_f32_16x16x32_bf16 a[92:95], v[220:223], v[172:175], a[92:95]\n\t"
  "s_waitcnt lgkmcnt(0)\n\t"
  "v_mfma_f32_16x16x32_bf16 a[96:99], v[224:227], v[116:119], a[96:99]\n\t"
  "v_mfma_f32_16x16x32_bf16 a[100:103], v[224:227], v[172:175], a[100:103]\n\t"
  "v_mfma_f32_16x16x32_bf16 a[104:107], v[228:231], v[116:119], a[104:107]\n\t"
  "v_mfma_f32_16x16x32_bf16 a[108:111], v[228:231], v[172:175], a[108:111]\n\t"
  "v_mfma_f32_16x16x32_bf16 a[112:115], v[232:235], v[116:119], a[112:115]\n\t"
  "v_mfma_f32_16x16x32_bf16 a[116:119], v[232:235], v[172:175], a[116:119]\n\t"
  "v_mfma_f32_16x16x32_bf16 a[120:123], v[236:239], v[116:119], a[120:123]\n\t"
  "v_mfma_f32_16x16x32_bf16 a[124:127], v[236:239], v[172:175], a[124:127]\n\t"
  "s_nop 7\n\t"
      : "={a[0:15]}"(acc[0]), "={a[16:31]}"(acc[1]), "={a[32:47]}"(acc[2]), "={a[48:63]}"(acc[3]), "={a[64:79]}"(acc[4]), "={a[80:95]}"(acc[5]), "={a[96:111]}"(acc[6]), "={a[112:127]}"(acc[7]), [wqs] "+v"(wqs), [wis] "+v"(wis), [ks0] "+v"(ks0), [ks1] "+v"(ks1), [vs] "+v"(vs)
      : [xs] "v"(xs), [va0] "v"(va0), [va1] "v"(va1), [vk0] "v"(vk0), [vk1] "v"(vk1), [vz0] "v"(vk0 + 131072u), [vz1] "v"(vk1 + 131072u),
        [srdq] "s"(srdq), [srdi] "s"(srdi), [srdx] "s"(srdx), [srdk] "s"(srdk), [srdv] "s"(srdv),
        [m0v] "s"(m0v), [m0k] "s"(m0k), [m0i] "s"(m0i) : "v64", "v65", "v66", "v67", "v68", "v69", "v70", "v71", "v72", "v73", "v74", "v75", "v76", "v77", "v78", "v79", "v80", "v81", "v82", "v83", "v84", "v85", "v86", "v87", "v88", "v89", "v90", "v91", "v92", "v93", "v94", "v95", "v96", "v97", "v98", "v99", "v100", "v101", "v102", "v103", "v104", "v105", "v106", "v107", "v108", "v109", "v110", "v111", "v112", "v113", "v114", "v115", "v116", "v117", "v118", "v119", "v120", "v121", "v122", "v123", "v124", "v125", "v126", "v127", "v128", "v129", "v130", "v131", "v132", "v133", "v134", "v135", "v136", "v137", "v138", "v139", "v140", "v141", "v142", "v143", "v144", "v145", "v146", "v147", "v148", "v149", "v150", "v151", "v152", "v153", "v154", "v155", "v156", "v157", "v158", "v159", "v160", "v161", "v162", "v163", "v164", "v165", "v166", "v167", "v168", "v169", "v170", "v171", "v172", "v173", "v174", "v175", "v176", "v177", "v178", "v179", "v180", "v181", "v182", "v183", "v184", "v185", "v186", "v187", "v188", "v189", "v190", "v191", "v192", "v193", "v194", "v195", "v196", "v197", "v198", "v199", "v200", "v201", "v202", "v203", "v204", "v205", "v206", "v207", "v208", "v209", "v210", "v211", "v212", "v213", "v214", "v215", "v216", "v217", "v218", "v219", "v220", "v221", "v222", "v223", "v224", "v225", "v226", "v227", "v228", "v229", "v230", "v231", "v232", "v233", "v234", "v235", "v236", "v237", "v238", "v239", "s40", "s41", "s42", "s46", "s47", "s48", "s49", "s51", "s52", "s53", "s54", "s55", "s56", "scc", "memory"); }
    TS(1, 2);
    const float qs = 0.09016844005556021f;
    #pragma unroll
    for (int qb = 0; qb < 2; ++qb)
      #pragma unroll
      for (int s = 0; s < 8; ++s) {
        const int ra = ((2 * s) * 2 + qb) * 4, rb = ((2 * s + 1) * 2 + qb) * 4;
        const u32x4 w = regroup(cvtpk(acc[ra >> 4][ra & 15] * qs, acc[ra >> 4][(ra & 15) + 1] * qs), cvtpk(acc[ra >> 4][(ra & 15) + 2] * qs, acc[ra >> 4][(ra & 15) + 3] * qs),
                                cvtpk(acc[rb >> 4][rb & 15] * qs, acc[rb >> 4][(rb & 15) + 1] * qs), cvtpk(acc[rb >> 4][(rb & 15) + 2] * qs, acc[rb >> 4][(rb & 15) + 3] * qs));
        const int f = qb * 8 + s;
        #pragma unroll
        for (int e = 0; e < 4; ++e) q[f >> 2][(f & 3) * 4 + e] = w[e];
      }
    asm volatile("s_waitcnt vmcnt(0)" ::: "memory");
    if (has_s1) {
      #pragma unroll
      for (int qb = 0; qb < 2; ++qb) {
        #pragma unroll
        for (int nt = 0; nt < 8; ++nt) {
          const int r = ((16 + nt) * 2 + qb) * 4;
          zp[qb][2 * nt] = cvtpk(gelu_t(acc[r >> 4][r & 15]), gelu_t(acc[r >> 4][(r & 15) + 1]));
          zp[qb][2 * nt + 1] = cvtpk(gelu_t(acc[r >> 4][(r & 15) + 2]), gelu_t(acc[r >> 4][(r & 15) + 3]));
        }
        const unsigned t16 = (row0 >> 4) + qb;
        if (is2) {
          float s = 0.f;
          #pragma unroll
          for (int i = 0; i < 16; ++i) s = dot2(zp[qb][i], 0x3f803f80u, s);
          s = gsum(s);
          const float mp = s * (1.0f / 128.0f);
          float v = 0.f;
          #pragma unroll
          for (int i = 0; i < 16; ++i) { const float a = bflo(zp[qb][i]) - mp, bb = bfhi(zp[qb][i]) - mp; v = fmaf(a, a, v); v = fmaf(bb, bb, v); }
          v = gsum(v);
          if (g == 0) { u32x4 st = {__float_as_uint(s), 1u, __float_as_uint(v), 1u}; __builtin_amdgcn_raw_buffer_store_b128(st, srdws, (unsigned)WS_ST + ((t16 * 16u + c) * 8u + fb1) * 16u, 0, 16); }
        } else {
          #pragma unroll
          for (int p = 0; p < 4; ++p) {
            u32x4 w = {zp[qb][4 * p], zp[qb][4 * p + 1], zp[qb][4 * p + 2], zp[qb][4 * p + 3]};
            __builtin_amdgcn_raw_buffer_store_b128(w, srdws, (unsigned)WS_Z1 + ((t16 * 28u + fb1 * 4u + p) * 64u + lane) * 16u, 0, 16);
          }
        }
      }
    }
  }
  TS(1, 3);
  f32x16 o[8]; float l0, l1;
  float mi0 = -INFINITY, mi1 = -INFINITY;
  {
    const unsigned fla = ldsb + 65536u + wid * 4u, flb = ldsb + 65536u;
    const unsigned kmx = hb + c * 512u + g * 16u;
    typedef unsigned u32x8 __attribute__((ext_vector_type(8)));
    u32x8 hp;
    #pragma unroll
    for (int qb = 0; qb < 2; ++qb) {
      const unsigned tok = row0 + 16u * qb + c;
      const unsigned ks = tok >> 5, gg = (tok >> 3) & 3u, j = tok & 7u;
      const unsigned rho = 16u * (j >> 2) + 4u * gg + (j & 3u), kg = rho >> 3, rr = rho & 7u;
      hp[qb] = (unsigned)WS_ST + (row0 + c) * 128u + g * 16u;
      hp[2 + qb] = (unsigned)WS_Z2N + (fb1 * 64u + ks) * 8192u + kg * 1024u + rr * 64u + ((g * 16u) ^ ((rr >> 2) << 5));
    }
    const unsigned wl = idx * 256u + tid;
    hp[4] = xcd * 1048576u + wl * 128u;
    hp[5] = xcd * 458752u + (wl < 3584u ? wl : 3583u) * 128u;
    hp[6] = 0u;
    hp[7] = (wl < 6272u ? wl : 6271u) * 128u;
    u32x2 hq = {g < 3u ? 0x3f800000u : 0u, (unsigned)WS_ST + (row0 + c) * 128u + (g < 3u ? g + 4u : 6u) * 16u};
    const unsigned hs1u = 1u - ((head + 2u) >> 4), is2u = hs1u & ((head + 9u) >> 4), cntb = (qblk & 7u) * 128u, wit = 1u + (idx & 3u);
    asm volatile(
  "s_mov_b32 s47, 0\n\t"
  ".Lstart_%=:\n\t"
  "s_mov_b32 s50, 0\n\t"
  "s_mov_b32 s40, 3072\n\t"
  "s_mov_b32 s41, 6144\n\t"
  "s_mov_b32 s42, 9216\n\t"
  "s_barrier\n\t"
  "s_add_u32 m0, %[m0v], 32768\n\t"
  "s_nop 0\n\t"
  "buffer_load_dwordx4 %[vs], %[srdv], 0 offen offset:0 lds\n\t"
  "buffer_load_dwordx4 %[vs], %[srdv], s40 offen offset:1024 lds\n\t"
  "buffer_load_dwordx4 %[vs], %[srdv], s41 offen offset:2048 lds\n\t"
  "buffer_load_dwordx4 %[vs], %[srdv], s42 offen offset:3072 lds\n\t"
  "v_add_u32 %[vs], 0x4000, %[vs]\n\t"
  "s_mov_b32 s44, 0x4f800000\n\t"
  "v_mov_b32 v112, 0x3f803f80\n\t"
  "v_mov_b32 v113, 0x3f803f80\n\t"
  "v_mov_b32 v114, 0x3f803f80\n\t"
  "v_mov_b32 v115, 0x3f803f80\n\t"
  "ds_read_b128 a[192:195], %[ka0] offset:0\n\t"
  "ds_read_b128 a[196:199], %[ka1] offset:0\n\t"
  "ds_read_b128 a[200:203], %[ka0] offset:4096\n\t"
  "ds_read_b128 a[204:207], %[ka1] offset:4096\n\t"
  "ds_read_b128 a[208:211], %[ka0] offset:8192\n\t"
  "ds_read_b128 a[212:215], %[ka1] offset:8192\n\t"
  "ds_read_b128 a[216:219], %[ka0] offset:12288\n\t"
  "ds_read_b128 a[220:223], %[ka1] offset:12288\n\t"
  "ds_read_b128 a[224:227], %[ka0] offset:2048\n\t"
  "ds_read_b128 a[228:231], %[ka1] offset:2048\n\t"
  "ds_read_b128 a[232:235], %[ka0] offset:6144\n\t"
  "ds_read_b128 a[236:239], %[ka1] offset:6144\n\t"
  "ds_read_b128 a[240:243], %[ka0] offset:10240\n\t"
  "ds_read_b128 a[244:247], %[ka1] offset:10240\n\t"
  "ds_read_b128 a[248:251], %[ka0] offset:14336\n\t"
  "ds_read_b128 a[252:255], %[ka1] offset:14336\n\t"
  "s_waitcnt lgkmcnt(0)\n\t"
  "v_mfma_f32_16x16x32_bf16 v[64:67], a[192:195], a[128:131], 0\n\t"
  "v_mfma_f32_16x16x32_bf16 v[68:71], a[192:195], a[160:163], 0\n\t"
  "v_mfma_f32_16x16x32_bf16 v[64:67], a[196:199], a[132:135], v[64:67]\n\t"
  "v_mfma_f32_16x16x32_bf16 v[68:71], a[196:199], a[164:167], v[68:71]\n\t"
  "v_mfma_f32_16x16x32_bf16 v[64:67], a[200:203], a[136:139], v[64:67]\n\t"
  "v_mfma_f32_16x16x32_bf16 v[68:71], a[200:203], a[168:171], v[68:71]\n\t"
  "v_mfma_f32_16x16x32_bf16 v[64:67], a[204:207], a[140:143], v[64:67]\n\t"
  "v_mfma_f32_16x16x32_bf16 v[68:71], a[204:207], a[172:175], v[68:71]\n\t"
  "v_mfma_f32_16x16x32_bf16 v[64:67], a[208:211], a[144:147], v[64:67]\n\t"
  "v_mfma_f32_16x16x32_bf16 v[68:71], a[208:211], a[176:179], v[68:71]\n\t"
  "v_mfma_f32_16x16x32_bf16 v[64:67], a[212:215], a[148:151], v[64:67]\n\t"
  "v_mfma_f32_16x16x32_bf16 v[68:71], a[212:215], a[180:183], v[68:71]\n\t"
  "v_mfma_f32_16x16x32_bf16 v[64:67], a[216:219], a[152:155], v[64:67]\n\t"
  "v_mfma_f32_16x16x32_bf16 v[68:71], a[216:219], a[184:187], v[68:71]\n\t"
  "v_mfma_f32_16x16x32_bf16 v[64:67], a[220:223], a[156:159], v[64:67]\n\t"
  "v_mfma_f32_16x16x32_bf16 v[68:71], a[220:223], a[188:191], v[68:71]\n\t"
  "v_mfma_f32_16x16x32_bf16 v[72:75], a[224:227], a[128:131], 0\n\t"
  "v_mfma_f32_16x16x32_bf16 v[76:79], a[224:227], a[160:163], 0\n\t"
  "v_mfma_f32_16x16x32_bf16 v[72:75], a[228:231], a[132:135], v[72:75]\n\t"
  "v_mfma_f32_16x16x32_bf16 v[76:79], a[228:231], a[164:167], v[76:79]\n\t"
  "v_mfma_f32_16x16x32_bf16 v[72:75], a[232:235], a[136:139], v[72:75]\n\t"
  "v_mfma_f32_16x16x32_bf16 v[76:79], a[232:235], a[168:171], v[76:79]\n\t"
  "v_mfma_f32_16x16x32_bf16 v[72:75], a[236:239], a[140:143], v[72:75]\n\t"
  "v_mfma_f32_16x16x32_bf16 v[76:79], a[236:239], a[172:175], v[76:79]\n\t"
  "v_mfma_f32_16x16x32_bf16 v[72:75], a[240:243], a[144:147], v[72:75]\n\t"
  "v_mfma_f32_16x16x32_bf16 v[76:79], a[240:243], a[176:179], v[76:79]\n\t"
  "v_mfma_f32_16x16x32_bf16 v[72:75], a[244:247], a[148:151], v[72:75]\n\t"
  "v_mfma_f32_16x16x32_bf16 v[76:79], a[244:247], a[180:183], v[76:79]\n\t"
  "v_mfma_f32_16x16x32_bf16 v[72:75], a[248:251], a[152:155], v[72:75]\n\t"
  "v_mfma_f32_16x16x32_bf16 v[76:79], a[248:251], a[184:187], v[76:79]\n\t"
  "v_mfma_f32_16x16x32_bf16 v[72:75], a[252:255], a[156:159], v[72:75]\n\t"
  "v_mfma_f32_16x16x32_bf16 v[76:79], a[252:255], a[188:191], v[76:79]\n\t"
  "s_nop 15\n\t"
  "v_max3_f32 v200, v64, v65, v66\n\t"
  "v_max3_f32 v200, v200, v67, v72\n\t"
  "v_max3_f32 v200, v200, v73, v74\n\t"
  "v_max3_f32 v200, v200, v75, %[mi0]\n\t"
  "v_mov_b32 v201, v200\n\t"
  "s_nop 1\n\t"
  "v_permlane32_swap_b32 v200, v201\n\t"
  "s_nop 1\n\t"
  "v_max_f32 v200, v200, v201\n\t"
  "v_mov_b32 v201, v200\n\t"
  "s_nop 1\n\t"
  "v_permlane16_swap_b32 v200, v201\n\t"
  "s_nop 1\n\t"
  "v_max_f32 v200, v200, v201\n\t"
  "v_sub_f32 v96, 0, v200\n\t"
  "v_sub_f32 v64, v64, v200\n\t"
  "v_sub_f32 v72, v72, v200\n\t"
  "v_sub_f32 v97, 0, v200\n\t"
  "v_sub_f32 v65, v65, v200\n\t"
  "v_sub_f32 v73, v73, v200\n\t"
  "v_sub_f32 v98, 0, v200\n\t"
  "v_sub_f32 v66, v66, v200\n\t"
  "v_sub_f32 v74, v74, v200\n\t"
  "v_sub_f32 v99, 0, v200\n\t"
  "v_sub_f32 v67, v67, v200\n\t"
  "v_sub_f32 v75, v75, v200\n\t"
  "v_max3_f32 v202, v68, v69, v70\n\t"
  "v_max3_f32 v202, v202, v71, v76\n\t"
  "v_max3_f32 v202, v202, v77, v78\n\t"
  "v_max3_f32 v202, v202, v79, %[mi1]\n\t"
  "v_mov_b32 v203, v202\n\t"
  "s_nop 1\n\t"
  "v_permlane32_swap_b32 v202, v203\n\t"
  "s_nop 1\n\t"
  "v_max_f32 v202, v202, v203\n\t"
  "v_mov_b32 v203, v202\n\t"
  "s_nop 1\n\t"
  "v_permlane16_swap_b32 v202, v203\n\t"
  "s_nop 1\n\t"
  "v_max_f32 v202, v202, v203\n\t"
  "v_sub_f32 v100, 0, v202\n\t"
  "v_sub_f32 v68, v68, v202\n\t"
  "v_sub_f32 v76, v76, v202\n\t"
  "v_sub_f32 v101, 0, v202\n\t"
  "v_sub_f32 v69, v69, v202\n\t"
  "v_sub_f32 v77, v77, v202\n\t"
  "v_sub_f32 v102, 0, v202\n\t"
  "v_sub_f32 v70, v70, v202\n\t"
  "v_sub_f32 v78, v78, v202\n\t"
  "v_sub_f32 v103, 0, v202\n\t"
  "v_sub_f32 v71, v71, v202\n\t"
  "v_sub_f32 v79, v79, v202\n\t"
  "s_waitcnt lgkmcnt(0)\n\t"
  "s_barrier\n\t"
  "ds_read_b128 a[192:195], %[ka0] offset:16384\n\t"
  "s_add_u32 m0, %[m0k], 0\n\t"
  "ds_read_b128 a[196:199], %[ka1] offset:16384\n\t"
  "ds_read_b128 a[200:203], %[ka0] offset:20480\n\t"
  "buffer_load_dwordx4 %[ks0], %[srdk], 0 offen offset:0 lds\n\t"
  "ds_read_b128 a[204:207], %[ka1] offset:20480\n\t"
  "ds_read_b128 a[208:211], %[ka0] offset:24576\n\t"
  "v_exp_f32 v64, v64\n\t"
  "ds_read_b128 a[212:215], %[ka1] offset:24576\n\t"
  "ds_read_b128 a[216:219], %[ka0] offset:28672\n\t"
  "v_exp_f32 v65, v65\n\t"
  "ds_read_b128 a[220:223], %[ka1] offset:28672\n\t"
  "ds_read_b128 a[224:227], %[ka0] offset:18432\n\t"
  "buffer_load_dwordx4 %[ks1], %[srdk], s40 offen offset:1024 lds\n\t"
  "v_exp_f32 v66, v66\n\t"
  "ds_read_b128 a[228:231], %[ka1] offset:18432\n\t"
  "ds_read_b128 a[232:235], %[ka0] offset:22528\n\t"
  "v_cvt_pk_bf16_f32 v116, v64, v65\n\t"
  "ds_read_b128 a[236:239], %[ka1] offset:22528\n\t"
  "ds_read_b128 a[240:243], %[ka0] offset:26624\n\t"
  "v_exp_f32 v67, v67\n\t"
  "ds_read_b128 a[244:247], %[ka1] offset:26624\n\t"
  "ds_read_b128 a[248:251], %[ka0] offset:30720\n\t"
  "v_exp_f32 v68, v68\n\t"
  "buffer_load_dwordx4 %[ks0], %[srdk], s41 offen offset:2048 lds\n\t"
  "ds_read_b128 a[252:255], %[ka1] offset:30720\n\t"
  "ds_read_b64_tr_b16 v[136:137], %[va0] offset:0\n\t"
  "v_cvt_pk_bf16_f32 v117, v66, v67\n\t"
  "ds_read_b64_tr_b16 v[138:139], %[va0] offset:2048\n\t"
  "v_exp_f32 v69, v69\n\t"
  "ds_read_b64_tr_b16 v[140:141], %[va1] offset:0\n\t"
  "ds_read_b64_tr_b16 v[142:143], %[va1] offset:2048\n\t"
  "ds_read_b64_tr_b16 v[144:145], %[va0] offset:512\n\t"
  "v_exp_f32 v70, v70\n\t"
  "ds_read_b64_tr_b16 v[146:147], %[va0] offset:2560\n\t"
  "buffer_load_dwordx4 %[ks1], %[srdk], s42 offen offset:3072 lds\n\t"
  "v_add_u32 %[ks0], 0x4000, %[ks0]\n\t"
  "ds_read_b64_tr_b16 v[148:149], %[va1] offset:512\n\t"
  "v_cvt_pk_bf16_f32 v120, v68, v69\n\t"
  "v_add_u32 %[ks1], 0x4000, %[ks1]\n\t"
  "ds_read_b64_tr_b16 v[150:151], %[va1] offset:2560\n\t"
  "v_exp_f32 v71, v71\n\t"
  "ds_read_b64_tr_b16 v[152:153], %[va0] offset:4096\n\t"
  "ds_read_b64_tr_b16 v[154:155], %[va0] offset:6144\n\t"
  "v_exp_f32 v72, v72\n\t"
  "ds_read_b64_tr_b16 v[156:157], %[va1] offset:4096\n\t"
  "s_add_u32 m0, %[m0v], 49152\n\t"
  "ds_read_b64_tr_b16 v[158:159], %[va1] offset:6144\n\t"
  "s_waitcnt lgkmcnt(5)\n\t"
  "v_mfma_f32_16x16x32_bf16 v[80:83], a[192:195], a[128:131], v[96:99]\n\t"
  "buffer_load_dwordx4 %[vs], %[srdv], 0 offen offset:0 lds\n\t"
  "ds_read_b64_tr_b16 v[160:161], %[va0] offset:4608\n\t"
  "v_mfma_f32_16x16x32_bf16 v[84:87], a[192:195], a[160:163], v[100:103]\n\t"
  "v_cvt_pk_bf16_f32 v121, v70, v71\n\t"
  "ds_read_b64_tr_b16 v[162:163], %[va0] offset:6656\n\t"
  "v_mfma_f32_16x16x32_bf16 v[80:83], a[196:199], a[132:135], v[80:83]\n\t"
  "v_exp_f32 v73, v73\n\t"
  "v_mfma_f32_16x16x32_bf16 v[84:87], a[196:199], a[164:167], v[84:87]\n\t"
  "ds_read_b64_tr_b16 v[164:165], %[va1] offset:4608\n\t"
  "ds_read_b64_tr_b16 v[166:167], %[va1] offset:6656\n\t"
  "v_mfma_f32_16x16x32_bf16 v[80:83], a[200:203], a[136:139], v[80:83]\n\t"
  "v_exp_f32 v74, v74\n\t"
  "v_mfma_f32_16x16x32_bf16 v[84:87], a[200:203], a[168:171], v[84:87]\n\t"
  "ds_read_b64_tr_b16 v[168:169], %[va0] offset:8192\n\t"
  "v_mfma_f32_16x16x32_bf16 v[80:83], a[204:207], a[140:143], v[80:83]\n\t"
  "ds_read_b64_tr_b16 v[170:171], %[va0] offset:10240\n\t"
  "v_mfma_f32_16x16x32_bf16 v[84:87], a[204:207], a[172:175], v[84:87]\n\t"
  "v_cvt_pk_bf16_f32 v118, v72, v73\n\t"
  "ds_read_b64_tr_b16 v[172:173], %[va1] offset:8192\n\t"
  "v_mfma_f32_16x16x32_bf16 v[80:83], a[208:211], a[144:147], v[80:83]\n\t"
  "buffer_load_dwordx4 %[vs], %[srdv], s40 offen offset:1024 lds\n\t"
  "ds_read_b64_tr_b16 v[174:175], %[va1] offset:10240\n\t"
  "v_mfma_f32_16x16x32_bf16 v[84:87], a[208:211], a[176:179], v[84:87]\n\t"
  "v_exp_f32 v75, v75\n\t"
  "v_mfma_f32_16x16x32_bf16 v[80:83], a[212:215], a[148:151], v[80:83]\n\t"
  "ds_read_b64_tr_b16 v[176:177], %[va0] offset:8704\n\t"
  "v_mfma_f32_16x16x32_bf16 v[84:87], a[212:215], a[180:183], v[84:87]\n\t"
  "ds_read_b64_tr_b16 v[178:179], %[va0] offset:10752\n\t"
  "v_mfma_f32_16x16x32_bf16 v[80:83], a[216:219], a[152:155], v[80:83]\n\t"
  "v_exp_f32 v76, v76\n\t"
  "v_mfma_f32_16x16x32_bf16 v[84:87], a[216:219], a[184:187], v[84:87]\n\t"
  "ds_read_b64_tr_b16 v[180:181], %[va1] offset:8704\n\t"
  "ds_read_b64_tr_b16 v[182:183], %[va1] offset:10752\n\t"
  "v_mfma_f32_16x16x32_bf16 v[80:83], a[220:223], a[156:159], v[80:83]\n\t"
  "v_cvt_pk_bf16_f32 v119, v74, v75\n\t"
  "v_mfma_f32_16x16x32_bf16 v[84:87], a[220:223], a[188:191], v[84:87]\n\t"
  "ds_read_b64_tr_b16 v[184:185], %[va0] offset:12288\n\t"
  "v_mfma_f32_16x16x32_bf16 v[88:91], a[224:227], a[128:131], v[96:99]\n\t"
  "buffer_load_dwordx4 %[vs], %[srdv], s41 offen offset:2048 lds\n\t"
  "ds_read_b64_tr_b16 v[186:187], %[va0] offset:14336\n\t"
  "v_mfma_f32_16x16x32_bf16 v[92:95], a[224:227], a[160:163], v[100:103]\n\t"
  "v_exp_f32 v77, v77\n\t"
  "v_mfma_f32_16x16x32_bf16 v[88:91], a[228:231], a[132:135], v[88:91]\n\t"
  "ds_read_b64_tr_b16 v[188:189], %[va1] offset:12288\n\t"
  "ds_read_b64_tr_b16 v[190:191], %[va1] offset:14336\n\t"
  "v_mfma_f32_16x16x32_bf16 v[92:95], a[228:231], a[164:167], v[92:95]\n\t"
  "v_exp_f32 v78, v78\n\t"
  "v_mfma_f32_16x16x32_bf16 v[88:91], a[232:235], a[136:139], v[88:91]\n\t"
  "ds_read_b64_tr_b16 v[192:193], %[va0] offset:12800\n\t"
  "v_mfma_f32_16x16x32_bf16 v[92:95], a[232:235], a[168:171], v[92:95]\n\t"
  "ds_read_b64_tr_b16 v[194:195], %[va0] offset:14848\n\t"
  "v_cvt_pk_bf16_f32 v122, v76, v77\n\t"
  "v_mfma_f32_16x16x32_bf16 v[88:91], a[236:239], a[140:143], v[88:91]\n\t"
  "ds_read_b64_tr_b16 v[196:197], %[va1] offset:12800\n\t"
  "v_mfma_f32_16x16x32_bf16 v[92:95], a[236:239], a[172:175], v[92:95]\n\t"
  "v_exp_f32 v79, v79\n\t"
  "v_mfma_f32_16x16x32_bf16 v[88:91], a[240:243], a[144:147], v[88:91]\n\t"
  "buffer_load_dwordx4 %[vs], %[srdv], s42 offen offset:3072 lds\n\t"
  "ds_read_b64_tr_b16 v[198:199], %[va1] offset:14848\n\t"
  "v_mfma_f32_16x16x32_bf16 v[92:95], a[240:243], a[176:179], v[92:95]\n\t"
  "v_add_u32 %[vs], 0x4000, %[vs]\n\t"
  "v_mfma_f32_16x16x32_bf16 v[88:91], a[244:247], a[148:151], v[88:91]\n\t"
  "v_cvt_pk_bf16_f32 v123, v78, v79\n\t"
  "v_mfma_f32_16x16x32_bf16 v[92:95], a[244:247], a[180:183], v[92:95]\n\t"
  "v_mfma_f32_16x16x32_bf16 v[88:91], a[248:251], a[152:155], v[88:91]\n\t"
  "v_mfma_f32_16x16x32_bf16 v[92:95], a[248:251], a[184:187], v[92:95]\n\t"
  "v_mfma_f32_16x16x32_bf16 v[88:91], a[252:255], a[156:159], v[88:91]\n\t"
  "v_mfma_f32_16x16x32_bf16 v[92:95], a[252:255], a[188:191], v[92:95]\n\t"
  "s_waitcnt vmcnt(16) lgkmcnt(0)\n\t"
  "s_barrier\n\t"
  "v_mfma_f32_16x16x32_bf16 a[0:3], v[136:139], v[116:119], 0\n\t"
  "ds_read_b128 a[192:195], %[ka0] offset:32768\n\t"
  "s_add_u32 m0, %[m0k], 16384\n\t"
  "v_mfma_f32_16x16x32_bf16 a[4:7], v[136:139], v[120:123], 0\n\t"
  "ds_read_b128 a[196:199], %[ka1] offset:32768\n\t"
  "ds_read_b128 a[200:203], %[ka0] offset:36864\n\t"
  "v_mfma_f32_16x16x32_bf16 a[8:11], v[140:143], v[116:119], 0\n\t"
  "buffer_load_dwordx4 %[ks0], %[srdk], 0 offen offset:0 lds\n\t"
  "v_mfma_f32_16x16x32_bf16 a[12:15], v[140:143], v[120:123], 0\n\t"
  "ds_read_b128 a[204:207], %[ka1] offset:36864\n\t"
  "v_mfma_f32_16x16x32_bf16 a[16:19], v[144:147], v[116:119], 0\n\t"
  "ds_read_b128 a[208:211], %[ka0] offset:40960\n\t"
  "v_mfma_f32_16x16x32_bf16 a[20:23], v[144:147], v[120:123], 0\n\t"
  "v_exp_f32 v80, v80\n\t"
  "v_mfma_f32_16x16x32_bf16 a[24:27], v[148:151], v[116:119], 0\n\t"
  "ds_read_b128 a[212:215], %[ka1] offset:40960\n\t"
  "ds_read_b128 a[216:219], %[ka0] offset:45056\n\t"
  "v_mfma_f32_16x16x32_bf16 a[28:31], v[148:151], v[120:123], 0\n\t"
  "v_exp_f32 v81, v81\n\t"
  "v_mfma_f32_16x16x32_bf16 a[32:35], v[152:155], v[116:119], 0\n\t"
  "ds_read_b128 a[220:223], %[ka1] offset:45056\n\t"
  "v_mfma_f32_16x16x32_bf16 a[36:39], v[152:155], v[120:123], 0\n\t"
  "ds_read_b128 a[224:227], %[ka0] offset:34816\n\t"
  "v_mfma_f32_16x16x32_bf16 a[40:43], v[156:159], v[116:119], 0\n\t"
  "buffer_load_dwordx4 %[ks1], %[srdk], s40 offen offset:1024 lds\n\t"
  "ds_read_b128 a[228:231], %[ka1] offset:34816\n\t"
  "v_mfma_f32_16x16x32_bf16 a[44:47], v[156:159], v[120:123], 0\n\t"
  "v_exp_f32 v82, v82\n\t"
  "v_mfma_f32_16x16x32_bf16 a[48:51], v[160:163], v[116:119], 0\n\t"
  "ds_read_b128 a[232:235], %[ka0] offset:38912\n\t"
  "v_mfma_f32_16x16x32_bf16 a[52:55], v[160:163], v[120:123], 0\n\t"
  "v_cvt_pk_bf16_f32 v124, v80, v81\n\t"
  "ds_read_b128 a[236:239], %[ka1] offset:38912\n\t"
  "v_mfma_f32_16x16x32_bf16 a[56:59], v[164:167], v[116:119], 0\n\t"
  "ds_read_b128 a[240:243], %[ka0] offset:43008\n\t"
  "v_mfma_f32_16x16x32_bf16 a[60:63], v[164:167], v[120:123], 0\n\t"
  "v_exp_f32 v83, v83\n\t"
  "v_mfma_f32_16x16x32_bf16 v[104:107], v[112:115], v[116:119], 0\n\t"
  "ds_read_b128 a[244:247], %[ka1] offset:43008\n\t"
  "ds_read_b128 a[248:251], %[ka0] offset:47104\n\t"
  "v_mfma_f32_16x16x32_bf16 v[108:111], v[112:115], v[120:123], 0\n\t"
  "v_exp_f32 v84, v84\n\t"
  "v_mfma_f32_16x16x32_bf16 a[64:67], v[168:171], v[116:119], 0\n\t"
  "buffer_load_dwordx4 %[ks0], %[srdk], s41 offen offset:2048 lds\n\t"
  "ds_read_b128 a[252:255], %[ka1] offset:47104\n\t"
  "v_mfma_f32_16x16x32_bf16 a[68:71], v[168:171], v[120:123], 0\n\t"
  "ds_read_b64_tr_b16 v[136:137], %[va0] offset:16384\n\t"
  "v_mfma_f32_16x16x32_bf16 a[72:75], v[172:175], v[116:119], 0\n\t"
  "v_cvt_pk_bf16_f32 v125, v82, v83\n\t"
  "ds_read_b64_tr_b16 v[138:139], %[va0] offset:18432\n\t"
  "v_mfma_f32_16x16x32_bf16 a[76:79], v[172:175], v[120:123], 0\n\t"
  "v_exp_f32 v85, v85\n\t"
  "v_mfma_f32_16x16x32_bf16 a[80:83], v[176:179], v[116:119], 0\n\t"
  "ds_read_b64_tr_b16 v[140:141], %[va1] offset:16384\n\t"
  "v_mfma_f32_16x16x32_bf16 a[84:87], v[176:179], v[120:123], 0\n\t"
  "ds_read_b64_tr_b16 v[142:143], %[va1] offset:18432\n\t"
  "v_mfma_f32_16x16x32_bf16 a[88:91], v[180:183], v[116:119], 0\n\t"
  "ds_read_b64_tr_b16 v[144:145], %[va0] offset:16896\n\t"
  "v_mfma_f32_16x16x32_bf16 a[92:95], v[180:183], v[120:123], 0\n\t"
  "v_exp_f32 v86, v86\n\t"
  "v_mfma_f32_16x16x32_bf16 a[96:99], v[184:187], v[116:119], 0\n\t"
  "buffer_load_dwordx4 %[ks1], %[srdk], s42 offen offset:3072 lds\n\t"
  "ds_read_b64_tr_b16 v[146:147], %[va0] offset:18944\n\t"
  "v_mfma_f32_16x16x32_bf16 a[100:103], v[184:187], v[120:123], 0\n\t"
  "v_add_u32 %[ks0], 0x4000, %[ks0]\n\t"
  "ds_read_b64_tr_b16 v[148:149], %[va1] offset:16896\n\t"
  "v_mfma_f32_16x16x32_bf16 a[104:107], v[188:191], v[116:119], 0\n\t"
  "v_cvt_pk_bf16_f32 v128, v84, v85\n\t"
  "v_add_u32 %[ks1], 0x4000, %[ks1]\n\t"
  "v_mfma_f32_16x16x32_bf16 a[108:111], v[188:191], v[120:123], 0\n\t"
  "ds_read_b64_tr_b16 v[150:151], %[va1] offset:18944\n\t"
  "ds_read_b64_tr_b16 v[152:153], %[va0] offset:20480\n\t"
  "v_mfma_f32_16x16x32_bf16 a[112:115], v[192:195], v[116:119], 0\n\t"
  "v_exp_f32 v87, v87\n\t"
  "v_mfma_f32_16x16x32_bf16 a[116:119], v[192:195], v[120:123], 0\n\t"
  "ds_read_b64_tr_b16 v[154:155], %[va0] offset:22528\n\t"
  "ds_read_b64_tr_b16 v[156:157], %[va1] offset:20480\n\t"
  "v_mfma_f32_16x16x32_bf16 a[120:123], v[196:199], v[116:119], 0\n\t"
  "v_exp_f32 v88, v88\n\t"
  "v_mfma_f32_16x16x32_bf16 a[124:127], v[196:199], v[120:123], 0\n\t"
  "s_add_u32 m0, %[m0v], 0\n\t"
  "ds_read_b64_tr_b16 v[158:159], %[va1] offset:22528\n\t"
  "s_waitcnt lgkmcnt(6)\n\t"
  "v_mfma_f32_16x16x32_bf16 v[64:67], a[192:195], a[128:131], v[96:99]\n\t"
  "buffer_load_dwordx4 %[vs], %[srdv], 0 offen offset:0 lds\n\t"
  "ds_read_b64_tr_b16 v[160:161], %[va0] offset:20992\n\t"
  "v_mfma_f32_16x16x32_bf16 v[68:71], a[192:195], a[160:163], v[100:103]\n\t"
  "v_cvt_pk_bf16_f32 v129, v86, v87\n\t"
  "ds_read_b64_tr_b16 v[162:163], %[va0] offset:23040\n\t"
  "v_mfma_f32_16x16x32_bf16 v[64:67], a[196:199], a[132:135], v[64:67]\n\t"
  "v_exp_f32 v89, v89\n\t"
  "v_mfma_f32_16x16x32_bf16 v[68:71], a[196:199], a[164:167], v[68:71]\n\t"
  "ds_read_b64_tr_b16 v[164:165], %[va1] offset:20992\n\t"
  "ds_read_b64_tr_b16 v[166:167], %[va1] offset:23040\n\t"
  "v_mfma_f32_16x16x32_bf16 v[64:67], a[200:203], a[136:139], v[64:67]\n\t"
  "v_exp_f32 v90, v90\n\t"
  "v_mfma_f32_16x16x32_bf16 v[68:71], a[200:203], a[168:171], v[68:71]\n\t"
  "ds_read_b64_tr_b16 v[168:169], %[va0] offset:24576\n\t"
  "v_mfma_f32_16x16x32_bf16 v[64:67], a[204:207], a[140:143], v[64:67]\n\t"
  "ds_read_b64_tr_b16 v[170:171], %[va0] offset:26624\n\t"
  "v_mfma_f32_16x16x32_bf16 v[68:71], a[204:207], a[172:175], v[68:71]\n\t"
  "v_cvt_pk_bf16_f32 v126, v88, v89\n\t"
  "ds_read_b64_tr_b16 v[172:173], %[va1] offset:24576\n\t"
  "v_mfma_f32_16x16x32_bf16 v[64:67], a[208:211], a[144:147], v[64:67]\n\t"
  "buffer_load_dwordx4 %[vs], %[srdv], s40 offen offset:1024 lds\n\t"
  "ds_read_b64_tr_b16 v[174:175], %[va1] offset:26624\n\t"
  "v_mfma_f32_16x16x32_bf16 v[68:71], a[208:211], a[176:179], v[68:71]\n\t"
  "v_exp_f32 v91, v91\n\t"
  "v_mfma_f32_16x16x32_bf16 v[64:67], a[212:215], a[148:151], v[64:67]\n\t"
  "ds_read_b64_tr_b16 v[176:177], %[va0] offset:25088\n\t"
  "v_mfma_f32_16x16x32_bf16 v[68:71], a[212:215], a[180:183], v[68:71]\n\t"
  "ds_read_b64_tr_b16 v[178:179], %[va0] offset:27136\n\t"
  "v_mfma_f32_16x16x32_bf16 v[64:67], a[216:219], a[152:155], v[64:67]\n\t"
  "v_exp_f32 v92, v92\n\t"
  "v_mfma_f32_16x16x32_bf16 v[68:71], a[216:219], a[184:187], v[68:71]\n\t"
  "ds_read_b64_tr_b16 v[180:181], %[va1] offset:25088\n\t"
  "ds_read_b64_tr_b16 v[182:183], %[va1] offset:27136\n\t"
  "v_mfma_f32_16x16x32_bf16 v[64:67], a[220:223], a[156:159], v[64:67]\n\t"
  "v_cvt_pk_bf16_f32 v127, v90, v91\n\t"
  "v_mfma_f32_16x16x32_bf16 v[68:71], a[220:223], a[188:191], v[68:71]\n\t"
  "ds_read_b64_tr_b16 v[184:185], %[va0] offset:28672\n\t"
  "v_mfma_f32_16x16x32_bf16 v[72:75], a[224:227], a[128:131], v[96:99]\n\t"
  "buffer_load_dwordx4 %[vs], %[srdv], s41 offen offset:2048 lds\n\t"
  "ds_read_b64_tr_b16 v[186:187], %[va0] offset:30720\n\t"
  "v_mfma_f32_16x16x32_bf16 v[76:79], a[224:227], a[160:163], v[100:103]\n\t"
  "v_exp_f32 v93, v93\n\t"
  "v_mfma_f32_16x16x32_bf16 v[72:75], a[228:231], a[132:135], v[72:75]\n\t"
  "ds_read_b64_tr_b16 v[188:189], %[va1] offset:28672\n\t"
  "ds_read_b64_tr_b16 v[190:191], %[va1] offset:30720\n\t"
  "v_mfma_f32_16x16x32_bf16 v[76:79], a[228:231], a[164:167], v[76:79]\n\t"
  "v_exp_f32 v94, v94\n\t"
  "v_mfma_f32_16x16x32_bf16 v[72:75], a[232:235], a[136:139], v[72:75]\n\t"
  "ds_read_b64_tr_b16 v[192:193], %[va0] offset:29184\n\t"
  "v_mfma_f32_16x16x32_bf16 v[76:79], a[232:235], a[168:171], v[76:79]\n\t"
  "ds_read_b64_tr_b16 v[194:195], %[va0] offset:31232\n\t"
  "v_cvt_pk_bf16_f32 v130, v92, v93\n\t"
  "v_mfma_f32_16x16x32_bf16 v[72:75], a[236:239], a[140:143], v[72:75]\n\t"
  "ds_read_b64_tr_b16 v[196:197], %[va1] offset:29184\n\t"
  "v_mfma_f32_16x16x32_bf16 v[76:79], a[236:239], a[172:175], v[76:79]\n\t"
  "v_exp_f32 v95, v95\n\t"
  "v_mfma_f32_16x16x32_bf16 v[72:75], a[240:243], a[144:147], v[72:75]\n\t"
  "buffer_load_dwordx4 %[vs], %[srdv], s42 offen offset:3072 lds\n\t"
  "ds_read_b64_tr_b16 v[198:199], %[va1] offset:31232\n\t"
  "v_mfma_f32_16x16x32_bf16 v[76:79], a[240:243], a[176:179], v[76:79]\n\t"
  "v_add_u32 %[vs], 0x4000, %[vs]\n\t"
  "v_mfma_f32_16x16x32_bf16 v[72:75], a[244:247], a[148:151], v[72:75]\n\t"
  "v_cvt_pk_bf16_f32 v131, v94, v95\n\t"
  "v_mfma_f32_16x16x32_bf16 v[76:79], a[244:247], a[180:183], v[76:79]\n\t"
  "v_mfma_f32_16x16x32_bf16 v[72:75], a[248:251], a[152:155], v[72:75]\n\t"
  "v_mfma_f32_16x16x32_bf16 v[76:79], a[248:251], a[184:187], v[76:79]\n\t"
  "v_mfma_f32_16x16x32_bf16 v[72:75], a[252:255], a[156:159], v[72:75]\n\t"
  "v_mfma_f32_16x16x32_bf16 v[76:79], a[252:255], a[188:191], v[76:79]\n\t"
  "s_waitcnt vmcnt(16) lgkmcnt(0)\n\t"
  "s_barrier\n\t"
  "s_cmp_eq_u32 %[is2], 0\n\t"
  "s_cbranch_scc1 .Lnl_%=\n\t"
  "buffer_load_dwordx4 v[32:35], v248, %[srdws], 0 offen offset:0 sc1\n\t"
  "buffer_load_dwordx4 v[36:39], v49, %[srdws], 0 offen offset:0 sc1\n\t"
  "buffer_load_dwordx4 v[40:43], v248, %[srdws], 0 offen offset:2048 sc1\n\t"
  "buffer_load_dwordx4 v[44:47], v49, %[srdws], 0 offen offset:2048 sc1\n\t"
  ".Lnl_%=:\n\t"
  "v_mfma_f32_16x16x32_bf16 a[0:3], v[136:139], v[124:127], a[0:3]\n\t"
  "ds_read_b128 a[192:195], %[ka0] offset:49152\n\t"
  "s_add_u32 m0, %[m0k], 32768\n\t"
  "v_mfma_f32_16x16x32_bf16 a[4:7], v[136:139], v[128:131], a[4:7]\n\t"
  "ds_read_b128 a[196:199], %[ka1] offset:49152\n\t"
  "ds_read_b128 a[200:203], %[ka0] offset:53248\n\t"
  "v_mfma_f32_16x16x32_bf16 a[8:11], v[140:143], v[124:127], a[8:11]\n\t"
  "buffer_load_dwordx4 %[ks0], %[srdk], 0 offen offset:0 lds\n\t"
  "v_mfma_f32_16x16x32_bf16 a[12:15], v[140:143], v[128:131], a[12:15]\n\t"
  "ds_read_b128 a[204:207], %[ka1] offset:53248\n\t"
  "v_mfma_f32_16x16x32_bf16 a[16:19], v[144:147], v[124:127], a[16:19]\n\t"
  "ds_read_b128 a[208:211], %[ka0] offset:57344\n\t"
  "v_mfma_f32_16x16x32_bf16 a[20:23], v[144:147], v[128:131], a[20:23]\n\t"
  "v_exp_f32 v64, v64\n\t"
  "v_mfma_f32_16x16x32_bf16 a[24:27], v[148:151], v[124:127], a[24:27]\n\t"
  "ds_read_b128 a[212:215], %[ka1] offset:57344\n\t"
  "ds_read_b128 a[216:219], %[ka0] offset:61440\n\t"
  "v_mfma_f32_16x16x32_bf16 a[28:31], v[148:151], v[128:131], a[28:31]\n\t"
  "v_exp_f32 v65, v65\n\t"
  "v_mfma_f32_16x16x32_bf16 a[32:35], v[152:155], v[124:127], a[32:35]\n\t"
  "ds_read_b128 a[220:223], %[ka1] offset:61440\n\t"
  "v_mfma_f32_16x16x32_bf16 a[36:39], v[152:155], v[128:131], a[36:39]\n\t"
  "ds_read_b128 a[224:227], %[ka0] offset:51200\n\t"
  "v_mfma_f32_16x16x32_bf16 a[40:43], v[156:159], v[124:127], a[40:43]\n\t"
  "buffer_load_dwordx4 %[ks1], %[srdk], s40 offen offset:1024 lds\n\t"
  "ds_read_b128 a[228:231], %[ka1] offset:51200\n\t"
  "v_mfma_f32_16x16x32_bf16 a[44:47], v[156:159], v[128:131], a[44:47]\n\t"
  "v_exp_f32 v66, v66\n\t"
  "v_mfma_f32_16x16x32_bf16 a[48:51], v[160:163], v[124:127], a[48:51]\n\t"
  "ds_read_b128 a[232:235], %[ka0] offset:55296\n\t"
  "v_mfma_f32_16x16x32_bf16 a[52:55], v[160:163], v[128:131], a[52:55]\n\t"
  "v_cvt_pk_bf16_f32 v116, v64, v65\n\t"
  "ds_read_b128 a[236:239], %[ka1] offset:55296\n\t"
  "v_mfma_f32_16x16x32_bf16 a[56:59], v[164:167], v[124:127], a[56:59]\n\t"
  "ds_read_b128 a[240:243], %[ka0] offset:59392\n\t"
  "v_mfma_f32_16x16x32_bf16 a[60:63], v[164:167], v[128:131], a[60:63]\n\t"
  "v_exp_f32 v67, v67\n\t"
  "v_mfma_f32_16x16x32_bf16 v[104:107], v[112:115], v[124:127], v[104:107]\n\t"
  "ds_read_b128 a[244:247], %[ka1] offset:59392\n\t"
  "ds_read_b128 a[248:251], %[ka0] offset:63488\n\t"
  "v_mfma_f32_16x16x32_bf16 v[108:111], v[112:115], v[128:131], v[108:111]\n\t"
  "v_exp_f32 v68, v68\n\t"
  "v_mfma_f32_16x16x32_bf16 a[64:67], v[168:171], v[124:127], a[64:67]\n\t"
  "buffer_load_dwordx4 %[ks0], %[srdk], s41 offen offset:2048 lds\n\t"
  "ds_read_b128 a[252:255], %[ka1] offset:63488\n\t"
  "v_mfma_f32_16x16x32_bf16 a[68:71], v[168:171], v[128:131], a[68:71]\n\t"
  "ds_read_b64_tr_b16 v[136:137], %[va0] offset:32768\n\t"
  "v_mfma_f32_16x16x32_bf16 a[72:75], v[172:175], v[124:127], a[72:75]\n\t"
  "v_cvt_pk_bf16_f32 v117, v66, v67\n\t"
  "ds_read_b64_tr_b16 v[138:139], %[va0] offset:34816\n\t"
  "v_mfma_f32_16x16x32_bf16 a[76:79], v[172:175], v[128:131], a[76:79]\n\t"
  "v_exp_f32 v69, v69\n\t"
  "v_mfma_f32_16x16x32_bf16 a[80:83], v[176:179], v[124:127], a[80:83]\n\t"
  "ds_read_b64_tr_b16 v[140:141], %[va1] offset:32768\n\t"
  "v_mfma_f32_16x16x32_bf16 a[84:87], v[176:179], v[128:131], a[84:87]\n\t"
  "ds_read_b64_tr_b16 v[142:143], %[va1] offset:34816\n\t"
  "v_mfma_f32_16x16x32_bf16 a[88:91], v[180:183], v[124:127], a[88:91]\n\t"
  "ds_read_b64_tr_b16 v[144:145], %[va0] offset:33280\n\t"
  "v_mfma_f32_16x16x32_bf16 a[92:95], v[180:183], v[128:131], a[92:95]\n\t"
  "v_exp_f32 v70, v70\n\t"
  "v_mfma_f32_16x16x32_bf16 a[96:99], v[184:187], v[124:127], a[96:99]\n\t"
  "buffer_load_dwordx4 %[ks1], %[srdk], s42 offen offset:3072 lds\n\t"
  "ds_read_b64_tr_b16 v[146:147], %[va0] offset:35328\n\t"
  "v_mfma_f32_16x16x32_bf16 a[100:103], v[184:187], v[128:131], a[100:103]\n\t"
  "v_add_u32 %[ks0], 0x4000, %[ks0]\n\t"
  "ds_read_b64_tr_b16 v[148:149], %[va1] offset:33280\n\t"
  "v_mfma_f32_16x16x32_bf16 a[104:107], v[188:191], v[124:127], a[104:107]\n\t"
  "v_cvt_pk_bf16_f32 v120, v68, v69\n\t"
  "v_add_u32 %[ks1], 0x4000, %[ks1]\n\t"
  "v_mfma_f32_16x16x32_bf16 a[108:111], v[188:191], v[128:131], a[108:111]\n\t"
  "ds_read_b64_tr_b16 v[150:151], %[va1] offset:35328\n\t"
  "ds_read_b64_tr_b16 v[152:153], %[va0] offset:36864\n\t"
  "v_mfma_f32_16x16x32_bf16 a[112:115], v[192:195], v[124:127], a[112:115]\n\t"
  "v_exp_f32 v71, v71\n\t"
  "v_mfma_f32_16x16x32_bf16 a[116:119], v[192:195], v[128:131], a[116:119]\n\t"
  "ds_read_b64_tr_b16 v[154:155], %[va0] offset:38912\n\t"
  "ds_read_b64_tr_b16 v[156:157], %[va1] offset:36864\n\t"
  "v_mfma_f32_16x16x32_bf16 a[120:123], v[196:199], v[124:127], a[120:123]\n\t"
  "v_exp_f32 v72, v72\n\t"
  "v_mfma_f32_16x16x32_bf16 a[124:127], v[196:199], v[128:131], a[124:127]\n\t"
  "s_add_u32 m0, %[m0v], 16384\n\t"
  "ds_read_b64_tr_b16 v[158:159], %[va1] offset:38912\n\t"
  "s_waitcnt lgkmcnt(6)\n\t"
  "v_mfma_f32_16x16x32_bf16 v[80:83], a[192:195], a[128:131], v[96:99]\n\t"
  "buffer_load_dwordx4 %[vs], %[srdv], 0 offen offset:0 lds\n\t"
  "ds_read_b64_tr_b16 v[160:161], %[va0] offset:37376\n\t"
  "v_mfma_f32_16x16x32_bf16 v[84:87], a[192:195], a[160:163], v[100:103]\n\t"
  "v_cvt_pk_bf16_f32 v121, v70, v71\n\t"
  "ds_read_b64_tr_b16 v[162:163], %[va0] offset:39424\n\t"
  "v_mfma_f32_16x16x32_bf16 v[80:83], a[196:199], a[132:135], v[80:83]\n\t"
  "v_exp_f32 v73, v73\n\t"
  "v_mfma_f32_16x16x32_bf16 v[84:87], a[196:199], a[164:167], v[84:87]\n\t"
  "ds_read_b64_tr_b16 v[164:165], %[va1] offset:37376\n\t"
  "ds_read_b64_tr_b16 v[166:167], %[va1] offset:39424\n\t"
  "v_mfma_f32_16x16x32_bf16 v[80:83], a[200:203], a[136:139], v[80:83]\n\t"
  "v_exp_f32 v74, v74\n\t"
  "v_mfma_f32_16x16x32_bf16 v[84:87], a[200:203], a[168:171], v[84:87]\n\t"
  "ds_read_b64_tr_b16 v[168:169], %[va0] offset:40960\n\t"
  "v_mfma_f32_16x16x32_bf16 v[80:83], a[204:207], a[140:143], v[80:83]\n\t"
  "ds_read_b64_tr_b16 v[170:171], %[va0] offset:43008\n\t"
  "v_mfma_f32_16x16x32_bf16 v[84:87], a[204:207], a[172:175], v[84:87]\n\t"
  "v_cvt_pk_bf16_f32 v118, v72, v73\n\t"
  "ds_read_b64_tr_b16 v[172:173], %[va1] offset:40960\n\t"
  "v_mfma_f32_16x16x32_bf16 v[80:83], a[208:211], a[144:147], v[80:83]\n\t"
  "buffer_load_dwordx4 %[vs], %[srdv], s40 offen offset:1024 lds\n\t"
  "ds_read_b64_tr_b16 v[174:175], %[va1] offset:43008\n\t"
  "v_mfma_f32_16x16x32_bf16 v[84:87], a[208:211], a[176:179], v[84:87]\n\t"
  "v_exp_f32 v75, v75\n\t"
  "v_mfma_f32_16x16x32_bf16 v[80:83], a[212:215], a[148:151], v[80:83]\n\t"
  "ds_read_b64_tr_b16 v[176:177], %[va0] offset:41472\n\t"
  "v_mfma_f32_16x16x32_bf16 v[84:87], a[212:215], a[180:183], v[84:87]\n\t"
  "ds_read_b64_tr_b16 v[178:179], %[va0] offset:43520\n\t"
  "v_mfma_f32_16x16x32_bf16 v[80:83], a[216:219], a[152:155], v[80:83]\n\t"
  "v_exp_f32 v76, v76\n\t"
  "v_mfma_f32_16x16x32_bf16 v[84:87], a[216:219], a[184:187], v[84:87]\n\t"
  "ds_read_b64_tr_b16 v[180:181], %[va1] offset:41472\n\t"
  "ds_read_b64_tr_b16 v[182:183], %[va1] offset:43520\n\t"
  "v_mfma_f32_16x16x32_bf16 v[80:83], a[220:223], a[156:159], v[80:83]\n\t"
  "v_cvt_pk_bf16_f32 v119, v74, v75\n\t"
  "v_mfma_f32_16x16x32_bf16 v[84:87], a[220:223], a[188:191], v[84:87]\n\t"
  "ds_read_b64_tr_b16 v[184:185], %[va0] offset:45056\n\t"
  "v_mfma_f32_16x16x32_bf16 v[88:91], a[224:227], a[128:131], v[96:99]\n\t"
  "buffer_load_dwordx4 %[vs], %[srdv], s41 offen offset:2048 lds\n\t"
  "ds_read_b64_tr_b16 v[186:187], %[va0] offset:47104\n\t"
  "v_mfma_f32_16x16x32_bf16 v[92:95], a[224:227], a[160:163], v[100:103]\n\t"
  "v_exp_f32 v77, v77\n\t"
  "v_mfma_f32_16x16x32_bf16 v[88:91], a[228:231], a[132:135], v[88:91]\n\t"
  "ds_read_b64_tr_b16 v[188:189], %[va1] offset:45056\n\t"
  "ds_read_b64_tr_b16 v[190:191], %[va1] offset:47104\n\t"
  "v_mfma_f32_16x16x32_bf16 v[92:95], a[228:231], a[164:167], v[92:95]\n\t"
  "v_exp_f32 v78, v78\n\t"
  "v_mfma_f32_16x16x32_bf16 v[88:91], a[232:235], a[136:139], v[88:91]\n\t"
  "ds_read_b64_tr_b16 v[192:193], %[va0] offset:45568\n\t"
  "v_mfma_f32_16x16x32_bf16 v[92:95], a[232:235], a[168:171], v[92:95]\n\t"
  "ds_read_b64_tr_b16 v[194:195], %[va0] offset:47616\n\t"
  "v_cvt_pk_bf16_f32 v122, v76, v77\n\t"
  "v_mfma_f32_16x16x32_bf16 v[88:91], a[236:239], a[140:143], v[88:91]\n\t"
  "ds_read_b64_tr_b16 v[196:197], %[va1] offset:45568\n\t"
  "v_mfma_f32_16x16x32_bf16 v[92:95], a[236:239], a[172:175], v[92:95]\n\t"
  "v_exp_f32 v79, v79\n\t"
  "v_mfma_f32_16x16x32_bf16 v[88:91], a[240:243], a[144:147], v[88:91]\n\t"
  "buffer_load_dwordx4 %[vs], %[srdv], s42 offen offset:3072 lds\n\t"
  "ds_read_b64_tr_b16 v[198:199], %[va1] offset:47616\n\t"
  "v_mfma_f32_16x16x32_bf16 v[92:95], a[240:243], a[176:179], v[92:95]\n\t"
  "v_add_u32 %[vs], 0x4000, %[vs]\n\t"
  "v_mfma_f32_16x16x32_bf16 v[88:91], a[244:247], a[148:151], v[88:91]\n\t"
  "v_cvt_pk_bf16_f32 v123, v78, v79\n\t"
  "v_mfma_f32_16x16x32_bf16 v[92:95], a[244:247], a[180:183], v[92:95]\n\t"
  "v_mfma_f32_16x16x32_bf16 v[88:91], a[248:251], a[152:155], v[88:91]\n\t"
  "v_mfma_f32_16x16x32_bf16 v[92:95], a[248:251], a[184:187], v[92:95]\n\t"
  "v_mfma_f32_16x16x32_bf16 v[88:91], a[252:255], a[156:159], v[88:91]\n\t"
  "v_mfma_f32_16x16x32_bf16 v[92:95], a[252:255], a[188:191], v[92:95]\n\t"
  "s_waitcnt vmcnt(16) lgkmcnt(0)\n\t"
  "s_barrier\n\t"
  "v_mfma_f32_16x16x32_bf16 a[0:3], v[136:139], v[116:119], a[0:3]\n\t"
  "ds_read_b128 a[192:195], %[ka0] offset:0\n\t"
  "s_add_u32 m0, %[m0k], 49152\n\t"
  "v_mfma_f32_16x16x32_bf16 a[4:7], v[136:139], v[120:123], a[4:7]\n\t"
  "ds_read_b128 a[196:199], %[ka1] offset:0\n\t"
  "ds_read_b128 a[200:203], %[ka0] offset:4096\n\t"
  "v_mfma_f32_16x16x32_bf16 a[8:11], v[140:143], v[116:119], a[8:11]\n\t"
  "buffer_load_dwordx4 %[ks0], %[srdk], 0 offen offset:0 lds\n\t"
  "v_mfma_f32_16x16x32_bf16 a[12:15], v[140:143], v[120:123], a[12:15]\n\t"
  "ds_read_b128 a[204:207], %[ka1] offset:4096\n\t"
  "v_mfma_f32_16x16x32_bf16 a[16:19], v[144:147], v[116:119], a[16:19]\n\t"
  "ds_read_b128 a[208:211], %[ka0] offset:8192\n\t"
  "v_mfma_f32_16x16x32_bf16 a[20:23], v[144:147], v[120:123], a[20:23]\n\t"
  "v_exp_f32 v80, v80\n\t"
  "v_mfma_f32_16x16x32_bf16 a[24:27], v[148:151], v[116:119], a[24:27]\n\t"
  "ds_read_b128 a[212:215], %[ka1] offset:8192\n\t"
  "ds_read_b128 a[216:219], %[ka0] offset:12288\n\t"
  "v_mfma_f32_16x16x32_bf16 a[28:31], v[148:151], v[120:123], a[28:31]\n\t"
  "v_exp_f32 v81, v81\n\t"
  "v_mfma_f32_16x16x32_bf16 a[32:35], v[152:155], v[116:119], a[32:35]\n\t"
  "ds_read_b128 a[220:223], %[ka1] offset:12288\n\t"
  "v_mfma_f32_16x16x32_bf16 a[36:39], v[152:155], v[120:123], a[36:39]\n\t"
  "ds_read_b128 a[224:227], %[ka0] offset:2048\n\t"
  "v_mfma_f32_16x16x32_bf16 a[40:43], v[156:159], v[116:119], a[40:43]\n\t"
  "buffer_load_dwordx4 %[ks1], %[srdk], s40 offen offset:1024 lds\n\t"
  "ds_read_b128 a[228:231], %[ka1] offset:2048\n\t"
  "v_mfma_f32_16x16x32_bf16 a[44:47], v[156:159], v[120:123], a[44:47]\n\t"
  "v_exp_f32 v82, v82\n\t"
  "v_mfma_f32_16x16x32_bf16 a[48:51], v[160:163], v[116:119], a[48:51]\n\t"
  "ds_read_b128 a[232:235], %[ka0] offset:6144\n\t"
  "v_mfma_f32_16x16x32_bf16 a[52:55], v[160:163], v[120:123], a[52:55]\n\t"
  "v_cvt_pk_bf16_f32 v124, v80, v81\n\t"
  "ds_read_b128 a[236:239], %[ka1] offset:6144\n\t"
  "v_mfma_f32_16x16x32_bf16 a[56:59], v[164:167], v[116:119], a[56:59]\n\t"
  "ds_read_b128 a[240:243], %[ka0] offset:10240\n\t"
  "v_mfma_f32_16x16x32_bf16 a[60:63], v[164:167], v[120:123], a[60:63]\n\t"
  "v_exp_f32 v83, v83\n\t"
  "v_mfma_f32_16x16x32_bf16 v[104:107], v[112:115], v[116:119], v[104:107]\n\t"
  "ds_read_b128 a[244:247], %[ka1] offset:10240\n\t"
  "ds_read_b128 a[248:251], %[ka0] offset:14336\n\t"
  "v_mfma_f32_16x16x32_bf16 v[108:111], v[112:115], v[120:123], v[108:111]\n\t"
  "v_exp_f32 v84, v84\n\t"
  "v_mfma_f32_16x16x32_bf16 a[64:67], v[168:171], v[116:119], a[64:67]\n\t"
  "buffer_load_dwordx4 %[ks0], %[srdk], s41 offen offset:2048 lds\n\t"
  "ds_read_b128 a[252:255], %[ka1] offset:14336\n\t"
  "v_mfma_f32_16x16x32_bf16 a[68:71], v[168:171], v[120:123], a[68:71]\n\t"
  "ds_read_b64_tr_b16 v[136:137], %[va0] offset:49152\n\t"
  "v_mfma_f32_16x16x32_bf16 a[72:75], v[172:175], v[116:119], a[72:75]\n\t"
  "v_cvt_pk_bf16_f32 v125, v82, v83\n\t"
  "ds_read_b64_tr_b16 v[138:139], %[va0] offset:51200\n\t"
  "v_mfma_f32_16x16x32_bf16 a[76:79], v[172:175], v[120:123], a[76:79]\n\t"
  "v_exp_f32 v85, v85\n\t"
  "v_mfma_f32_16x16x32_bf16 a[80:83], v[176:179], v[116:119], a[80:83]\n\t"
  "ds_read_b64_tr_b16 v[140:141], %[va1] offset:49152\n\t"
  "v_mfma_f32_16x16x32_bf16 a[84:87], v[176:179], v[120:123], a[84:87]\n\t"
  "ds_read_b64_tr_b16 v[142:143], %[va1] offset:51200\n\t"
  "v_mfma_f32_16x16x32_bf16 a[88:91], v[180:183], v[116:119], a[88:91]\n\t"
  "ds_read_b64_tr_b16 v[144:145], %[va0] offset:49664\n\t"
  "v_mfma_f32_16x16x32_bf16 a[92:95], v[180:183], v[120:123], a[92:95]\n\t"
  "v_exp_f32 v86, v86\n\t"
  "v_mfma_f32_16x16x32_bf16 a[96:99], v[184:187], v[116:119], a[96:99]\n\t"
  "buffer_load_dwordx4 %[ks1], %[srdk], s42 offen offset:3072 lds\n\t"
  "ds_read_b64_tr_b16 v[146:147], %[va0] offset:51712\n\t"
  "v_mfma_f32_16x16x32_bf16 a[100:103], v[184:187], v[120:123], a[100:103]\n\t"
  "v_add_u32 %[ks0], 0x4000, %[ks0]\n\t"
  "ds_read_b64_tr_b16 v[148:149], %[va1] offset:49664\n\t"
  "v_mfma_f32_16x16x32_bf16 a[104:107], v[188:191], v[116:119], a[104:107]\n\t"
  "v_cvt_pk_bf16_f32 v128, v84, v85\n\t"
  "v_add_u32 %[ks1], 0x4000, %[ks1]\n\t"
  "v_mfma_f32_16x16x32_bf16 a[108:111], v[188:191], v[120:123], a[108:111]\n\t"
  "ds_read_b64_tr_b16 v[150:151], %[va1] offset:51712\n\t"
  "ds_read_b64_tr_b16 v[152:153], %[va0] offset:53248\n\t"
  "v_mfma_f32_16x16x32_bf16 a[112:115], v[192:195], v[116:119], a[112:115]\n\t"
  "v_exp_f32 v87, v87\n\t"
  "v_mfma_f32_16x16x32_bf16 a[116:119], v[192:195], v[120:123], a[116:119]\n\t"
  "ds_read_b64_tr_b16 v[154:155], %[va0] offset:55296\n\t"
  "ds_read_b64_tr_b16 v[156:157], %[va1] offset:53248\n\t"
  "v_mfma_f32_16x16x32_bf16 a[120:123], v[196:199], v[116:119], a[120:123]\n\t"
  "v_exp_f32 v88, v88\n\t"
  "v_mfma_f32_16x16x32_bf16 a[124:127], v[196:199], v[120:123], a[124:127]\n\t"
  "s_add_u32 m0, %[m0v], 32768\n\t"
  "ds_read_b64_tr_b16 v[158:159], %[va1] offset:55296\n\t"
  "s_waitcnt lgkmcnt(6)\n\t"
  "v_mfma_f32_16x16x32_bf16 v[64:67], a[192:195], a[128:131], v[96:99]\n\t"
  "buffer_load_dwordx4 %[vs], %[srdv], 0 offen offset:0 lds\n\t"
  "ds_read_b64_tr_b16 v[160:161], %[va0] offset:53760\n\t"
  "v_mfma_f32_16x16x32_bf16 v[68:71], a[192:195], a[160:163], v[100:103]\n\t"
  "v_cvt_pk_bf16_f32 v129, v86, v87\n\t"
  "ds_read_b64_tr_b16 v[162:163], %[va0] offset:55808\n\t"
  "v_mfma_f32_16x16x32_bf16 v[64:67], a[196:199], a[132:135], v[64:67]\n\t"
  "v_exp_f32 v89, v89\n\t"
  "v_mfma_f32_16x16x32_bf16 v[68:71], a[196:199], a[164:167], v[68:71]\n\t"
  "ds_read_b64_tr_b16 v[164:165], %[va1] offset:53760\n\t"
  "ds_read_b64_tr_b16 v[166:167], %[va1] offset:55808\n\t"
  "v_mfma_f32_16x16x32_bf16 v[64:67], a[200:203], a[136:139], v[64:67]\n\t"
  "v_exp_f32 v90, v90\n\t"
  "v_mfma_f32_16x16x32_bf16 v[68:71], a[200:203], a[168:171], v[68:71]\n\t"
  "ds_read_b64_tr_b16 v[168:169], %[va0] offset:57344\n\t"
  "v_mfma_f32_16x16x32_bf16 v[64:67], a[204:207], a[140:143], v[64:67]\n\t"
  "ds_read_b64_tr_b16 v[170:171], %[va0] offset:59392\n\t"
  "v_mfma_f32_16x16x32_bf16 v[68:71], a[204:207], a[172:175], v[68:71]\n\t"
  "v_cvt_pk_bf16_f32 v126, v88, v89\n\t"
  "ds_read_b64_tr_b16 v[172:173], %[va1] offset:57344\n\t"
  "v_mfma_f32_16x16x32_bf16 v[64:67], a[208:211], a[144:147], v[64:67]\n\t"
  "buffer_load_dwordx4 %[vs], %[srdv], s40 offen offset:1024 lds\n\t"
  "ds_read_b64_tr_b16 v[174:175], %[va1] offset:59392\n\t"
  "v_mfma_f32_16x16x32_bf16 v[68:71], a[208:211], a[176:179], v[68:71]\n\t"
  "v_exp_f32 v91, v91\n\t"
  "v_mfma_f32_16x16x32_bf16 v[64:67], a[212:215], a[148:151], v[64:67]\n\t"
  "ds_read_b64_tr_b16 v[176:177], %[va0] offset:57856\n\t"
  "v_mfma_f32_16x16x32_bf16 v[68:71], a[212:215], a[180:183], v[68:71]\n\t"
  "ds_read_b64_tr_b16 v[178:179], %[va0] offset:59904\n\t"
  "v_mfma_f32_16x16x32_bf16 v[64:67], a[216:219], a[152:155], v[64:67]\n\t"
  "v_exp_f32 v92, v92\n\t"
  "v_mfma_f32_16x16x32_bf16 v[68:71], a[216:219], a[184:187], v[68:71]\n\t"
  "ds_read_b64_tr_b16 v[180:181], %[va1] offset:57856\n\t"
  "ds_read_b64_tr_b16 v[182:183], %[va1] offset:59904\n\t"
  "v_mfma_f32_16x16x32_bf16 v[64:67], a[220:223], a[156:159], v[64:67]\n\t"
  "v_cvt_pk_bf16_f32 v127, v90, v91\n\t"
  "v_mfma_f32_16x16x32_bf16 v[68:71], a[220:223], a[188:191], v[68:71]\n\t"
  "ds_read_b64_tr_b16 v[184:185], %[va0] offset:61440\n\t"
  "v_mfma_f32_16x16x32_bf16 v[72:75], a[224:227], a[128:131], v[96:99]\n\t"
  "buffer_load_dwordx4 %[vs], %[srdv], s41 offen offset:2048 lds\n\t"
  "ds_read_b64_tr_b16 v[186:187], %[va0] offset:63488\n\t"
  "v_mfma_f32_16x16x32_bf16 v[76:79], a[224:227], a[160:163], v[100:103]\n\t"
  "v_exp_f32 v93, v93\n\t"
  "v_mfma_f32_16x16x32_bf16 v[72:75], a[228:231], a[132:135], v[72:75]\n\t"
  "ds_read_b64_tr_b16 v[188:189], %[va1] offset:61440\n\t"
  "ds_read_b64_tr_b16 v[190:191], %[va1] offset:63488\n\t"
  "v_mfma_f32_16x16x32_bf16 v[76:79], a[228:231], a[164:167], v[76:79]\n\t"
  "v_exp_f32 v94, v94\n\t"
  "v_mfma_f32_16x16x32_bf16 v[72:75], a[232:235], a[136:139], v[72:75]\n\t"
  "ds_read_b64_tr_b16 v[192:193], %[va0] offset:61952\n\t"
  "v_mfma_f32_16x16x32_bf16 v[76:79], a[232:235], a[168:171], v[76:79]\n\t"
  "ds_read_b64_tr_b16 v[194:195], %[va0] offset:64000\n\t"
  "v_cvt_pk_bf16_f32 v130, v92, v93\n\t"
  "v_mfma_f32_16x16x32_bf16 v[72:75], a[236:239], a[140:143], v[72:75]\n\t"
  "ds_read_b64_tr_b16 v[196:197], %[va1] offset:61952\n\t"
  "v_mfma_f32_16x16x32_bf16 v[76:79], a[236:239], a[172:175], v[76:79]\n\t"
  "v_exp_f32 v95, v95\n\t"
  "v_mfma_f32_16x16x32_bf16 v[72:75], a[240:243], a[144:147], v[72:75]\n\t"
  "buffer_load_dwordx4 %[vs], %[srdv], s42 offen offset:3072 lds\n\t"
  "ds_read_b64_tr_b16 v[198:199], %[va1] offset:64000\n\t"
  "v_mfma_f32_16x16x32_bf16 v[76:79], a[240:243], a[176:179], v[76:79]\n\t"
  "v_add_u32 %[vs], 0x4000, %[vs]\n\t"
  "v_mfma_f32_16x16x32_bf16 v[72:75], a[244:247], a[148:151], v[72:75]\n\t"
  "v_cvt_pk_bf16_f32 v131, v94, v95\n\t"
  "v_mfma_f32_16x16x32_bf16 v[76:79], a[244:247], a[180:183], v[76:79]\n\t"
  "v_mfma_f32_16x16x32_bf16 v[72:75], a[248:251], a[152:155], v[72:75]\n\t"
  "v_mfma_f32_16x16x32_bf16 v[76:79], a[248:251], a[184:187], v[76:79]\n\t"
  "v_mfma_f32_16x16x32_bf16 v[72:75], a[252:255], a[156:159], v[72:75]\n\t"
  "v_mfma_f32_16x16x32_bf16 v[76:79], a[252:255], a[188:191], v[76:79]\n\t"
  "v_max_f32 v200, v104, v108\n\t"
  "v_cmp_nge_f32 vcc, s44, v200\n\t"
  "s_cbranch_vccnz .Lrare0_%=\n\t"
  ".Lret0_%=:\n\t"
  "s_mov_b32 s43, 14\n\t"
  "s_waitcnt vmcnt(16) lgkmcnt(0)\n\t"
  "s_barrier\n\t"
  "s_nop 7\n\t"
  "s_cmp_eq_u32 %[is2], 0\n\t"
  "s_cbranch_scc1 .Lnd_%=\n\t"
  "s_mov_b32 s46, 0x40000\n\t"
  ".Lnc_%=:\n\t"
  "v_xor_b32 v200, %[tag], v33\n\t"
  "v_xor_b32 v201, %[tag], v35\n\t"
  "v_or_b32 v200, v200, v201\n\t"
  "v_xor_b32 v201, %[tag], v37\n\t"
  "v_or_b32 v200, v200, v201\n\t"
  "v_xor_b32 v201, %[tag], v39\n\t"
  "v_or_b32 v200, v200, v201\n\t"
  "v_xor_b32 v201, %[tag], v41\n\t"
  "v_or_b32 v200, v200, v201\n\t"
  "v_xor_b32 v201, %[tag], v43\n\t"
  "v_or_b32 v200, v200, v201\n\t"
  "v_xor_b32 v201, %[tag], v45\n\t"
  "v_or_b32 v200, v200, v201\n\t"
  "v_xor_b32 v201, %[tag], v47\n\t"
  "v_or_b32 v200, v200, v201\n\t"
  "v_cmp_ne_u32 vcc, 0, v200\n\t"
  "s_cbranch_vccz .Lnv_%=\n\t"
  "s_sleep 2\n\t"
  "buffer_load_dwordx4 v[32:35], v248, %[srdws], 0 offen offset:0 sc1\n\t"
  "buffer_load_dwordx4 v[36:39], v49, %[srdws], 0 offen offset:0 sc1\n\t"
  "buffer_load_dwordx4 v[40:43], v248, %[srdws], 0 offen offset:2048 sc1\n\t"
  "buffer_load_dwordx4 v[44:47], v49, %[srdws], 0 offen offset:2048 sc1\n\t"
  "s_waitcnt vmcnt(0)\n\t"
  "s_sub_u32 s46, s46, 1\n\t"
  "s_cmp_lg_u32 s46, 0\n\t"
  "s_cbranch_scc1 .Lnc_%=\n\t"
  ".Lnv_%=:\n\t"
  "s_mov_b32 s46, 0x3a924925\n\t"
  "s_mov_b32 s48, 0x3c000000\n\t"
  "s_mov_b32 s49, 0x43000000\n\t"
  "v_fma_f32 v200, v36, v48, v32\n\t"
  "v_mov_b32 v205, v200\n\t"
  "s_nop 1\n\t"
  "v_permlane32_swap_b32 v200, v205\n\t"
  "s_nop 1\n\t"
  "v_add_f32 v200, v200, v205\n\t"
  "v_mov_b32 v205, v200\n\t"
  "s_nop 1\n\t"
  "v_permlane16_swap_b32 v200, v205\n\t"
  "s_nop 1\n\t"
  "v_add_f32 v200, v200, v205\n\t"
  "v_mul_f32 v200, s46, v200\n\t"
  "v_fma_f32 v202, v32, s48, -v200\n\t"
  "v_mul_f32 v202, v202, v202\n\t"
  "v_fma_f32 v201, v202, s49, v34\n\t"
  "v_fma_f32 v202, v36, s48, -v200\n\t"
  "v_mul_f32 v202, v202, v202\n\t"
  "v_fma_f32 v202, v202, s49, v38\n\t"
  "v_fma_f32 v201, v202, v48, v201\n\t"
  "v_mov_b32 v205, v201\n\t"
  "s_nop 1\n\t"
  "v_permlane32_swap_b32 v201, v205\n\t"
  "s_nop 1\n\t"
  "v_add_f32 v201, v201, v205\n\t"
  "v_mov_b32 v205, v201\n\t"
  "s_nop 1\n\t"
  "v_permlane16_swap_b32 v201, v205\n\t"
  "s_nop 1\n\t"
  "v_add_f32 v201, v201, v205\n\t"
  "v_mul_f32 v201, s46, v201\n\t"
  "v_add_f32 v201, 0x3727c5ac, v201\n\t"
  "v_rsq_f32 v203, v201\n\t"
  "s_nop 0\n\t"
  "v_mul_f32 v204, v200, v203\n\t"
  "v_lshlrev_b32 v205, 16, v216\n\t"
  "v_and_b32 v206, 0xffff0000, v216\n\t"
  "v_fma_f32 v205, v205, v203, -v204\n\t"
  "v_fma_f32 v206, v206, v203, -v204\n\t"
  "v_cvt_pk_bf16_f32 v208, v205, v206\n\t"
  "v_lshlrev_b32 v205, 16, v217\n\t"
  "v_and_b32 v206, 0xffff0000, v217\n\t"
  "v_fma_f32 v205, v205, v203, -v204\n\t"
  "v_fma_f32 v206, v206, v203, -v204\n\t"
  "v_cvt_pk_bf16_f32 v209, v205, v206\n\t"
  "v_lshlrev_b32 v205, 16, v218\n\t"
  "v_and_b32 v206, 0xffff0000, v218\n\t"
  "v_fma_f32 v205, v205, v203, -v204\n\t"
  "v_fma_f32 v206, v206, v203, -v204\n\t"
  "v_cvt_pk_bf16_f32 v210, v205, v206\n\t"
  "v_lshlrev_b32 v205, 16, v219\n\t"
  "v_and_b32 v206, 0xffff0000, v219\n\t"
  "v_fma_f32 v205, v205, v203, -v204\n\t"
  "v_fma_f32 v206, v206, v203, -v204\n\t"
  "v_cvt_pk_bf16_f32 v211, v205, v206\n\t"
  "s_nop 1\n\t"
  "v_permlane32_swap_b32 v208, v210\n\t"
  "v_permlane32_swap_b32 v209, v211\n\t"
  "s_nop 1\n\t"
  "v_permlane16_swap_b32 v208, v210\n\t"
  "v_permlane16_swap_b32 v209, v211\n\t"
  "buffer_store_dwordx4 v[208:211], v250, %[srdws], 0 offen offset:0 sc1\n\t"
  "v_lshlrev_b32 v205, 16, v220\n\t"
  "v_and_b32 v206, 0xffff0000, v220\n\t"
  "v_fma_f32 v205, v205, v203, -v204\n\t"
  "v_fma_f32 v206, v206, v203, -v204\n\t"
  "v_cvt_pk_bf16_f32 v212, v205, v206\n\t"
  "v_lshlrev_b32 v205, 16, v221\n\t"
  "v_and_b32 v206, 0xffff0000, v221\n\t"
  "v_fma_f32 v205, v205, v203, -v204\n\t"
  "v_fma_f32 v206, v206, v203, -v204\n\t"
  "v_cvt_pk_bf16_f32 v213, v205, v206\n\t"
  "v_lshlrev_b32 v205, 16, v222\n\t"
  "v_and_b32 v206, 0xffff0000, v222\n\t"
  "v_fma_f32 v205, v205, v203, -v204\n\t"
  "v_fma_f32 v206, v206, v203, -v204\n\t"
  "v_cvt_pk_bf16_f32 v214, v205, v206\n\t"
  "v_lshlrev_b32 v205, 16, v223\n\t"
  "v_and_b32 v206, 0xffff0000, v223\n\t"
  "v_fma_f32 v205, v205, v203, -v204\n\t"
  "v_fma_f32 v206, v206, v203, -v204\n\t"
  "v_cvt_pk_bf16_f32 v215, v205, v206\n\t"
  "s_nop 1\n\t"
  "v_permlane32_swap_b32 v212, v214\n\t"
  "v_permlane32_swap_b32 v213, v215\n\t"
  "s_nop 1\n\t"
  "v_permlane16_swap_b32 v212, v214\n\t"
  "v_permlane16_swap_b32 v213, v215\n\t"
  "buffer_store_dwordx4 v[212:215], v250, %[srdws], 0 offen offset:512 sc1\n\t"
  "v_lshlrev_b32 v205, 16, v224\n\t"
  "v_and_b32 v206, 0xffff0000, v224\n\t"
  "v_fma_f32 v205, v205, v203, -v204\n\t"
  "v_fma_f32 v206, v206, v203, -v204\n\t"
  "v_cvt_pk_bf16_f32 v208, v205, v206\n\t"
  "v_lshlrev_b32 v205, 16, v225\n\t"
  "v_and_b32 v206, 0xffff0000, v225\n\t"
  "v_fma_f32 v205, v205, v203, -v204\n\t"
  "v_fma_f32 v206, v206, v203, -v204\n\t"
  "v_cvt_pk_bf16_f32 v209, v205, v206\n\t"
  "v_lshlrev_b32 v205, 16, v226\n\t"
  "v_and_b32 v206, 0xffff0000, v226\n\t"
  "v_fma_f32 v205, v205, v203, -v204\n\t"
  "v_fma_f32 v206, v206, v203, -v204\n\t"
  "v_cvt_pk_bf16_f32 v210, v205, v206\n\t"
  "v_lshlrev_b32 v205, 16, v227\n\t"
  "v_and_b32 v206, 0xffff0000, v227\n\t"
  "v_fma_f32 v205, v205, v203, -v204\n\t"
  "v_fma_f32 v206, v206, v203, -v204\n\t"
  "v_cvt_pk_bf16_f32 v211, v205, v206\n\t"
  "s_nop 1\n\t"
  "v_permlane32_swap_b32 v208, v210\n\t"
  "v_permlane32_swap_b32 v209, v211\n\t"
  "s_nop 1\n\t"
  "v_permlane16_swap_b32 v208, v210\n\t"
  "v_permlane16_swap_b32 v209, v211\n\t"
  "buffer_store_dwordx4 v[208:211], v250, %[srdws], %[c4096] offen offset:0 sc1\n\t"
  "v_lshlrev_b32 v205, 16, v228\n\t"
  "v_and_b32 v206, 0xffff0000, v228\n\t"
  "v_fma_f32 v205, v205, v203, -v204\n\t"
  "v_fma_f32 v206, v206, v203, -v204\n\t"
  "v_cvt_pk_bf16_f32 v212, v205, v206\n\t"
  "v_lshlrev_b32 v205, 16, v229\n\t"
  "v_and_b32 v206, 0xffff0000, v229\n\t"
  "v_fma_f32 v205, v205, v203, -v204\n\t"
  "v_fma_f32 v206, v206, v203, -v204\n\t"
  "v_cvt_pk_bf16_f32 v213, v205, v206\n\t"
  "v_lshlrev_b32 v205, 16, v230\n\t"
  "v_and_b32 v206, 0xffff0000, v230\n\t"
  "v_fma_f32 v205, v205, v203, -v204\n\t"
  "v_fma_f32 v206, v206, v203, -v204\n\t"
  "v_cvt_pk_bf16_f32 v214, v205, v206\n\t"
  "v_lshlrev_b32 v205, 16, v231\n\t"
  "v_and_b32 v206, 0xffff0000, v231\n\t"
  "v_fma_f32 v205, v205, v203, -v204\n\t"
  "v_fma_f32 v206, v206, v203, -v204\n\t"
  "v_cvt_pk_bf16_f32 v215, v205, v206\n\t"
  "s_nop 1\n\t"
  "v_permlane32_swap_b32 v212, v214\n\t"
  "v_permlane32_swap_b32 v213, v215\n\t"
  "s_nop 1\n\t"
  "v_permlane16_swap_b32 v212, v214\n\t"
  "v_permlane16_swap_b32 v213, v215\n\t"
  "buffer_store_dwordx4 v[212:215], v250, %[srdws], %[c4096] offen offset:512 sc1\n\t"
  "v_fma_f32 v200, v44, v48, v40\n\t"
  "v_mov_b32 v205, v200\n\t"
  "s_nop 1\n\t"
  "v_permlane32_swap_b32 v200, v205\n\t"
  "s_nop 1\n\t"
  "v_add_f32 v200, v200, v205\n\t"
  "v_mov_b32 v205, v200\n\t"
  "s_nop 1\n\t"
  "v_permlane16_swap_b32 v200, v205\n\t"
  "s_nop 1\n\t"
  "v_add_f32 v200, v200, v205\n\t"
  "v_mul_f32 v200, s46, v200\n\t"
  "v_fma_f32 v202, v40, s48, -v200\n\t"
  "v_mul_f32 v202, v202, v202\n\t"
  "v_fma_f32 v201, v202, s49, v42\n\t"
  "v_fma_f32 v202, v44, s48, -v200\n\t"
  "v_mul_f32 v202, v202, v202\n\t"
  "v_fma_f32 v202, v202, s49, v46\n\t"
  "v_fma_f32 v201, v202, v48, v201\n\t"
  "v_mov_b32 v205, v201\n\t"
  "s_nop 1\n\t"
  "v_permlane32_swap_b32 v201, v205\n\t"
  "s_nop 1\n\t"
  "v_add_f32 v201, v201, v205\n\t"
  "v_mov_b32 v205, v201\n\t"
  "s_nop 1\n\t"
  "v_permlane16_swap_b32 v201, v205\n\t"
  "s_nop 1\n\t"
  "v_add_f32 v201, v201, v205\n\t"
  "v_mul_f32 v201, s46, v201\n\t"
  "v_add_f32 v201, 0x3727c5ac, v201\n\t"
  "v_rsq_f32 v203, v201\n\t"
  "s_nop 0\n\t"
  "v_mul_f32 v204, v200, v203\n\t"
  "v_lshlrev_b32 v205, 16, v232\n\t"
  "v_and_b32 v206, 0xffff0000, v232\n\t"
  "v_fma_f32 v205, v205, v203, -v204\n\t"
  "v_fma_f32 v206, v206, v203, -v204\n\t"
  "v_cvt_pk_bf16_f32 v208, v205, v206\n\t"
  "v_lshlrev_b32 v205, 16, v233\n\t"
  "v_and_b32 v206, 0xffff0000, v233\n\t"
  "v_fma_f32 v205, v205, v203, -v204\n\t"
  "v_fma_f32 v206, v206, v203, -v204\n\t"
  "v_cvt_pk_bf16_f32 v209, v205, v206\n\t"
  "v_lshlrev_b32 v205, 16, v234\n\t"
  "v_and_b32 v206, 0xffff0000, v234\n\t"
  "v_fma_f32 v205, v205, v203, -v204\n\t"
  "v_fma_f32 v206, v206, v203, -v204\n\t"
  "v_cvt_pk_bf16_f32 v210, v205, v206\n\t"
  "v_lshlrev_b32 v205, 16, v235\n\t"
  "v_and_b32 v206, 0xffff0000, v235\n\t"
  "v_fma_f32 v205, v205, v203, -v204\n\t"
  "v_fma_f32 v206, v206, v203, -v204\n\t"
  "v_cvt_pk_bf16_f32 v211, v205, v206\n\t"
  "s_nop 1\n\t"
  "v_permlane32_swap_b32 v208, v210\n\t"
  "v_permlane32_swap_b32 v209, v211\n\t"
  "s_nop 1\n\t"
  "v_permlane16_swap_b32 v208, v210\n\t"
  "v_permlane16_swap_b32 v209, v211\n\t"
  "buffer_store_dwordx4 v[208:211], v251, %[srdws], 0 offen offset:0 sc1\n\t"
  "v_lshlrev_b32 v205, 16, v236\n\t"
  "v_and_b32 v206, 0xffff0000, v236\n\t"
  "v_fma_f32 v205, v205, v203, -v204\n\t"
  "v_fma_f32 v206, v206, v203, -v204\n\t"
  "v_cvt_pk_bf16_f32 v212, v205, v206\n\t"
  "v_lshlrev_b32 v205, 16, v237\n\t"
  "v_and_b32 v206, 0xffff0000, v237\n\t"
  "v_fma_f32 v205, v205, v203, -v204\n\t"
  "v_fma_f32 v206, v206, v203, -v204\n\t"
  "v_cvt_pk_bf16_f32 v213, v205, v206\n\t"
  "v_lshlrev_b32 v205, 16, v238\n\t"
  "v_and_b32 v206, 0xffff0000, v238\n\t"
  "v_fma_f32 v205, v205, v203, -v204\n\t"
  "v_fma_f32 v206, v206, v203, -v204\n\t"
  "v_cvt_pk_bf16_f32 v214, v205, v206\n\t"
  "v_lshlrev_b32 v205, 16, v239\n\t"
  "v_and_b32 v206, 0xffff0000, v239\n\t"
  "v_fma_f32 v205, v205, v203, -v204\n\t"
  "v_fma_f32 v206, v206, v203, -v204\n\t"
  "v_cvt_pk_bf16_f32 v215, v205, v206\n\t"
  "s_nop 1\n\t"
  "v_permlane32_swap_b32 v212, v214\n\t"
  "v_permlane32_swap_b32 v213, v215\n\t"
  "s_nop 1\n\t"
  "v_permlane16_swap_b32 v212, v214\n\t"
  "v_permlane16_swap_b32 v213, v215\n\t"
  "buffer_store_dwordx4 v[212:215], v251, %[srdws], 0 offen offset:512 sc1\n\t"
  "v_lshlrev_b32 v205, 16, v240\n\t"
  "v_and_b32 v206, 0xffff0000, v240\n\t"
  "v_fma_f32 v205, v205, v203, -v204\n\t"
  "v_fma_f32 v206, v206, v203, -v204\n\t"
  "v_cvt_pk_bf16_f32 v208, v205, v206\n\t"
  "v_lshlrev_b32 v205, 16, v241\n\t"
  "v_and_b32 v206, 0xffff0000, v241\n\t"
  "v_fma_f32 v205, v205, v203, -v204\n\t"
  "v_fma_f32 v206, v206, v203, -v204\n\t"
  "v_cvt_pk_bf16_f32 v209, v205, v206\n\t"
  "v_lshlrev_b32 v205, 16, v242\n\t"
  "v_and_b32 v206, 0xffff0000, v242\n\t"
  "v_fma_f32 v205, v205, v203, -v204\n\t"
  "v_fma_f32 v206, v206, v203, -v204\n\t"
  "v_cvt_pk_bf16_f32 v210, v205, v206\n\t"
  "v_lshlrev_b32 v205, 16, v243\n\t"
  "v_and_b32 v206, 0xffff0000, v243\n\t"
  "v_fma_f32 v205, v205, v203, -v204\n\t"
  "v_fma_f32 v206, v206, v203, -v204\n\t"
  "v_cvt_pk_bf16_f32 v211, v205, v206\n\t"
  "s_nop 1\n\t"
  "v_permlane32_swap_b32 v208, v210\n\t"
  "v_permlane32_swap_b32 v209, v211\n\t"
  "s_nop 1\n\t"
  "v_permlane16_swap_b32 v208, v210\n\t"
  "v_permlane16_swap_b32 v209, v211\n\t"
  "buffer_store_dwordx4 v[208:211], v251, %[srdws], %[c4096] offen offset:0 sc1\n\t"
  "v_lshlrev_b32 v205, 16, v244\n\t"
  "v_and_b32 v206, 0xffff0000, v244\n\t"
  "v_fma_f32 v205, v205, v203, -v204\n\t"
  "v_fma_f32 v206, v206, v203, -v204\n\t"
  "v_cvt_pk_bf16_f32 v212, v205, v206\n\t"
  "v_lshlrev_b32 v205, 16, v245\n\t"
  "v_and_b32 v206, 0xffff0000, v245\n\t"
  "v_fma_f32 v205, v205, v203, -v204\n\t"
  "v_fma_f32 v206, v206, v203, -v204\n\t"
  "v_cvt_pk_bf16_f32 v213, v205, v206\n\t"
  "v_lshlrev_b32 v205, 16, v246\n\t"
  "v_and_b32 v206, 0xffff0000, v246\n\t"
  "v_fma_f32 v205, v205, v203, -v204\n\t"
  "v_fma_f32 v206, v206, v203, -v204\n\t"
  "v_cvt_pk_bf16_f32 v214, v205, v206\n\t"
  "v_lshlrev_b32 v205, 16, v247\n\t"
  "v_and_b32 v206, 0xffff0000, v247\n\t"
  "v_fma_f32 v205, v205, v203, -v204\n\t"
  "v_fma_f32 v206, v206, v203, -v204\n\t"
  "v_cvt_pk_bf16_f32 v215, v205, v206\n\t"
  "s_nop 1\n\t"
  "v_permlane32_swap_b32 v212, v214\n\t"
  "v_permlane32_swap_b32 v213, v215\n\t"
  "s_nop 1\n\t"
  "v_permlane16_swap_b32 v212, v214\n\t"
  "v_permlane16_swap_b32 v213, v215\n\t"
  "buffer_store_dwordx4 v[212:215], v251, %[srdws], %[c4096] offen offset:512 sc1\n\t"
  "s_nop 1\n\t"
  ".Lnd_%=:\n\t"
  ".Lloop_%=:\n\t"
  "v_mfma_f32_16x16x32_bf16 a[0:3], v[136:139], v[124:127], a[0:3]\n\t"
  "ds_read_b128 a[192:195], %[ka0] offset:16384\n\t"
  "s_add_u32 m0, %[m0k], 0\n\t"
  "v_mfma_f32_16x16x32_bf16 a[4:7], v[136:139], v[128:131], a[4:7]\n\t"
  "ds_read_b128 a[196:199], %[ka1] offset:16384\n\t"
  "ds_read_b128 a[200:203], %[ka0] offset:20480\n\t"
  "v_mfma_f32_16x16x32_bf16 a[8:11], v[140:143], v[124:127], a[8:11]\n\t"
  "buffer_load_dwordx4 %[ks0], %[srdk], 0 offen offset:0 lds\n\t"
  "v_mfma_f32_16x16x32_bf16 a[12:15], v[140:143], v[128:131], a[12:15]\n\t"
  "ds_read_b128 a[204:207], %[ka1] offset:20480\n\t"
  "v_mfma_f32_16x16x32_bf16 a[16:19], v[144:147], v[124:127], a[16:19]\n\t"
  "ds_read_b128 a[208:211], %[ka0] offset:24576\n\t"
  "v_mfma_f32_16x16x32_bf16 a[20:23], v[144:147], v[128:131], a[20:23]\n\t"
  "v_exp_f32 v64, v64\n\t"
  "v_mfma_f32_16x16x32_bf16 a[24:27], v[148:151], v[124:127], a[24:27]\n\t"
  "ds_read_b128 a[212:215], %[ka1] offset:24576\n\t"
  "ds_read_b128 a[216:219], %[ka0] offset:28672\n\t"
  "v_mfma_f32_16x16x32_bf16 a[28:31], v[148:151], v[128:131], a[28:31]\n\t"
  "v_exp_f32 v65, v65\n\t"
  "v_mfma_f32_16x16x32_bf16 a[32:35], v[152:155], v[124:127], a[32:35]\n\t"
  "ds_read_b128 a[220:223], %[ka1] offset:28672\n\t"
  "v_mfma_f32_16x16x32_bf16 a[36:39], v[152:155], v[128:131], a[36:39]\n\t"
  "ds_read_b128 a[224:227], %[ka0] offset:18432\n\t"
  "v_mfma_f32_16x16x32_bf16 a[40:43], v[156:159], v[124:127], a[40:43]\n\t"
  "buffer_load_dwordx4 %[ks1], %[srdk], s40 offen offset:1024 lds\n\t"
  "ds_read_b128 a[228:231], %[ka1] offset:18432\n\t"
  "v_mfma_f32_16x16x32_bf16 a[44:47], v[156:159], v[128:131], a[44:47]\n\t"
  "v_exp_f32 v66, v66\n\t"
  "v_mfma_f32_16x16x32_bf16 a[48:51], v[160:163], v[124:127], a[48:51]\n\t"
  "ds_read_b128 a[232:235], %[ka0] offset:22528\n\t"
  "v_mfma_f32_16x16x32_bf16 a[52:55], v[160:163], v[128:131], a[52:55]\n\t"
  "v_cvt_pk_bf16_f32 v116, v64, v65\n\t"
  "ds_read_b128 a[236:239], %[ka1] offset:22528\n\t"
  "v_mfma_f32_16x16x32_bf16 a[56:59], v[164:167], v[124:127], a[56:59]\n\t"
  "ds_read_b128 a[240:243], %[ka0] offset:26624\n\t"
  "v_mfma_f32_16x16x32_bf16 a[60:63], v[164:167], v[128:131], a[60:63]\n\t"
  "v_exp_f32 v67, v67\n\t"
  "v_mfma_f32_16x16x32_bf16 v[104:107], v[112:115], v[124:127], v[104:107]\n\t"
  "ds_read_b128 a[244:247], %[ka1] offset:26624\n\t"
  "ds_read_b128 a[248:251], %[ka0] offset:30720\n\t"
  "v_mfma_f32_16x16x32_bf16 v[108:111], v[112:115], v[128:131], v[108:111]\n\t"
  "v_exp_f32 v68, v68\n\t"
  "v_mfma_f32_16x16x32_bf16 a[64:67], v[168:171], v[124:127], a[64:67]\n\t"
  "buffer_load_dwordx4 %[ks0], %[srdk], s41 offen offset:2048 lds\n\t"
  "ds_read_b128 a[252:255], %[ka1] offset:30720\n\t"
  "v_mfma_f32_16x16x32_bf16 a[68:71], v[168:171], v[128:131], a[68:71]\n\t"
  "ds_read_b64_tr_b16 v[136:137], %[va0] offset:0\n\t"
  "v_mfma_f32_16x16x32_bf16 a[72:75], v[172:175], v[124:127], a[72:75]\n\t"
  "v_cvt_pk_bf16_f32 v117, v66, v67\n\t"
  "ds_read_b64_tr_b16 v[138:139], %[va0] offset:2048\n\t"
  "v_mfma_f32_16x16x32_bf16 a[76:79], v[172:175], v[128:131], a[76:79]\n\t"
  "v_exp_f32 v69, v69\n\t"
  "v_mfma_f32_16x16x32_bf16 a[80:83], v[176:179], v[124:127], a[80:83]\n\t"
  "ds_read_b64_tr_b16 v[140:141], %[va1] offset:0\n\t"
  "v_mfma_f32_16x16x32_bf16 a[84:87], v[176:179], v[128:131], a[84:87]\n\t"
  "ds_read_b64_tr_b16 v[142:143], %[va1] offset:2048\n\t"
  "v_mfma_f32_16x16x32_bf16 a[88:91], v[180:183], v[124:127], a[88:91]\n\t"
  "ds_read_b64_tr_b16 v[144:145], %[va0] offset:512\n\t"
  "v_mfma_f32_16x16x32_bf16 a[92:95], v[180:183], v[128:131], a[92:95]\n\t"
  "v_exp_f32 v70, v70\n\t"
  "v_mfma_f32_16x16x32_bf16 a[96:99], v[184:187], v[124:127], a[96:99]\n\t"
  "buffer_load_dwordx4 %[ks1], %[srdk], s42 offen offset:3072 lds\n\t"
  "ds_read_b64_tr_b16 v[146:147], %[va0] offset:2560\n\t"
  "v_mfma_f32_16x16x32_bf16 a[100:103], v[184:187], v[128:131], a[100:103]\n\t"
  "v_add_u32 %[ks0], 0x4000, %[ks0]\n\t"
  "ds_read_b64_tr_b16 v[148:149], %[va1] offset:512\n\t"
  "v_mfma_f32_16x16x32_bf16 a[104:107], v[188:191], v[124:127], a[104:107]\n\t"
  "v_cvt_pk_bf16_f32 v120, v68, v69\n\t"
  "v_add_u32 %[ks1], 0x4000, %[ks1]\n\t"
  "v_mfma_f32_16x16x32_bf16 a[108:111], v[188:191], v[128:131], a[108:111]\n\t"
  "ds_read_b64_tr_b16 v[150:151], %[va1] offset:2560\n\t"
  "ds_read_b64_tr_b16 v[152:153], %[va0] offset:4096\n\t"
  "v_mfma_f32_16x16x32_bf16 a[112:115], v[192:195], v[124:127], a[112:115]\n\t"
  "v_exp_f32 v71, v71\n\t"
  "v_mfma_f32_16x16x32_bf16 a[116:119], v[192:195], v[128:131], a[116:119]\n\t"
  "ds_read_b64_tr_b16 v[154:155], %[va0] offset:6144\n\t"
  "ds_read_b64_tr_b16 v[156:157], %[va1] offset:4096\n\t"
  "v_mfma_f32_16x16x32_bf16 a[120:123], v[196:199], v[124:127], a[120:123]\n\t"
  "v_exp_f32 v72, v72\n\t"
  "v_mfma_f32_16x16x32_bf16 a[124:127], v[196:199], v[128:131], a[124:127]\n\t"
  "s_add_u32 m0, %[m0v], 49152\n\t"
  "ds_read_b64_tr_b16 v[158:159], %[va1] offset:6144\n\t"
  "s_waitcnt lgkmcnt(6)\n\t"
  "v_mfma_f32_16x16x32_bf16 v[80:83], a[192:195], a[128:131], v[96:99]\n\t"
  "buffer_load_dwordx4 %[vs], %[srdv], 0 offen offset:0 lds\n\t"
  "ds_read_b64_tr_b16 v[160:161], %[va0] offset:4608\n\t"
  "v_mfma_f32_16x16x32_bf16 v[84:87], a[192:195], a[160:163], v[100:103]\n\t"
  "v_cvt_pk_bf16_f32 v121, v70, v71\n\t"
  "ds_read_b64_tr_b16 v[162:163], %[va0] offset:6656\n\t"
  "v_mfma_f32_16x16x32_bf16 v[80:83], a[196:199], a[132:135], v[80:83]\n\t"
  "v_exp_f32 v73, v73\n\t"
  "v_mfma_f32_16x16x32_bf16 v[84:87], a[196:199], a[164:167], v[84:87]\n\t"
  "ds_read_b64_tr_b16 v[164:165], %[va1] offset:4608\n\t"
  "ds_read_b64_tr_b16 v[166:167], %[va1] offset:6656\n\t"
  "v_mfma_f32_16x16x32_bf16 v[80:83], a[200:203], a[136:139], v[80:83]\n\t"
  "v_exp_f32 v74, v74\n\t"
  "v_mfma_f32_16x16x32_bf16 v[84:87], a[200:203], a[168:171], v[84:87]\n\t"
  "ds_read_b64_tr_b16 v[168:169], %[va0] offset:8192\n\t"
  "v_mfma_f32_16x16x32_bf16 v[80:83], a[204:207], a[140:143], v[80:83]\n\t"
  "ds_read_b64_tr_b16 v[170:171], %[va0] offset:10240\n\t"
  "v_mfma_f32_16x16x32_bf16 v[84:87], a[204:207], a[172:175], v[84:87]\n\t"
  "v_cvt_pk_bf16_f32 v118, v72, v73\n\t"
  "ds_read_b64_tr_b16 v[172:173], %[va1] offset:8192\n\t"
  "v_mfma_f32_16x16x32_bf16 v[80:83], a[208:211], a[144:147], v[80:83]\n\t"
  "buffer_load_dwordx4 %[vs], %[srdv], s40 offen offset:1024 lds\n\t"
  "ds_read_b64_tr_b16 v[174:175], %[va1] offset:10240\n\t"
  "v_mfma_f32_16x16x32_bf16 v[84:87], a[208:211], a[176:179], v[84:87]\n\t"
  "v_exp_f32 v75, v75\n\t"
  "v_mfma_f32_16x16x32_bf16 v[80:83], a[212:215], a[148:151], v[80:83]\n\t"
  "ds_read_b64_tr_b16 v[176:177], %[va0] offset:8704\n\t"
  "v_mfma_f32_16x16x32_bf16 v[84:87], a[212:215], a[180:183], v[84:87]\n\t"
  "ds_read_b64_tr_b16 v[178:179], %[va0] offset:10752\n\t"
  "v_mfma_f32_16x16x32_bf16 v[80:83], a[216:219], a[152:155], v[80:83]\n\t"
  "v_exp_f32 v76, v76\n\t"
  "v_mfma_f32_16x16x32_bf16 v[84:87], a[216:219], a[184:187], v[84:87]\n\t"
  "ds_read_b64_tr_b16 v[180:181], %[va1] offset:8704\n\t"
  "ds_read_b64_tr_b16 v[182:183], %[va1] offset:10752\n\t"
  "v_mfma_f32_16x16x32_bf16 v[80:83], a[220:223], a[156:159], v[80:83]\n\t"
  "v_cvt_pk_bf16_f32 v119, v74, v75\n\t"
  "v_mfma_f32_16x16x32_bf16 v[84:87], a[220:223], a[188:191], v[84:87]\n\t"
  "ds_read_b64_tr_b16 v[184:185], %[va0] offset:12288\n\t"
  "v_mfma_f32_16x16x32_bf16 v[88:91], a[224:227], a[128:131], v[96:99]\n\t"
  "buffer_load_dwordx4 %[vs], %[srdv], s41 offen offset:2048 lds\n\t"
  "ds_read_b64_tr_b16 v[186:187], %[va0] offset:14336\n\t"
  "v_mfma_f32_16x16x32_bf16 v[92:95], a[224:227], a[160:163], v[100:103]\n\t"
  "v_exp_f32 v77, v77\n\t"
  "v_mfma_f32_16x16x32_bf16 v[88:91], a[228:231], a[132:135], v[88:91]\n\t"
  "ds_read_b64_tr_b16 v[188:189], %[va1] offset:12288\n\t"
  "ds_read_b64_tr_b16 v[190:191], %[va1] offset:14336\n\t"
  "v_mfma_f32_16x16x32_bf16 v[92:95], a[228:231], a[164:167], v[92:95]\n\t"
  "v_exp_f32 v78, v78\n\t"
  "v_mfma_f32_16x16x32_bf16 v[88:91], a[232:235], a[136:139], v[88:91]\n\t"
  "ds_read_b64_tr_b16 v[192:193], %[va0] offset:12800\n\t"
  "v_mfma_f32_16x16x32_bf16 v[92:95], a[232:235], a[168:171], v[92:95]\n\t"
  "ds_read_b64_tr_b16 v[194:195], %[va0] offset:14848\n\t"
  "v_cvt_pk_bf16_f32 v122, v76, v77\n\t"
  "v_mfma_f32_16x16x32_bf16 v[88:91], a[236:239], a[140:143], v[88:91]\n\t"
  "ds_read_b64_tr_b16 v[196:197], %[va1] offset:12800\n\t"
  "v_mfma_f32_16x16x32_bf16 v[92:95], a[236:239], a[172:175], v[92:95]\n\t"
  "v_exp_f32 v79, v79\n\t"
  "v_mfma_f32_16x16x32_bf16 v[88:91], a[240:243], a[144:147], v[88:91]\n\t"
  "buffer_load_dwordx4 %[vs], %[srdv], s42 offen offset:3072 lds\n\t"
  "ds_read_b64_tr_b16 v[198:199], %[va1] offset:14848\n\t"
  "v_mfma_f32_16x16x32_bf16 v[92:95], a[240:243], a[176:179], v[92:95]\n\t"
  "v_add_u32 %[vs], 0x4000, %[vs]\n\t"
  "v_mfma_f32_16x16x32_bf16 v[88:91], a[244:247], a[148:151], v[88:91]\n\t"
  "v_cvt_pk_bf16_f32 v123, v78, v79\n\t"
  "v_mfma_f32_16x16x32_bf16 v[92:95], a[244:247], a[180:183], v[92:95]\n\t"
  "v_mfma_f32_16x16x32_bf16 v[88:91], a[248:251], a[152:155], v[88:91]\n\t"
  "v_mfma_f32_16x16x32_bf16 v[92:95], a[248:251], a[184:187], v[92:95]\n\t"
  "v_mfma_f32_16x16x32_bf16 v[88:91], a[252:255], a[156:159], v[88:91]\n\t"
  "v_mfma_f32_16x16x32_bf16 v[92:95], a[252:255], a[188:191], v[92:95]\n\t"
  "s_waitcnt vmcnt(16) lgkmcnt(0)\n\t"
  "s_barrier\n\t"
  "v_mfma_f32_16x16x32_bf16 a[0:3], v[136:139], v[116:119], a[0:3]\n\t"
  "ds_read_b128 a[192:195], %[ka0] offset:32768\n\t"
  "s_add_u32 m0, %[m0k], 16384\n\t"
  "v_mfma_f32_16x16x32_bf16 a[4:7], v[136:139], v[120:123], a[4:7]\n\t"
  "ds_read_b128 a[196:199], %[ka1] offset:32768\n\t"
  "ds_read_b128 a[200:203], %[ka0] offset:36864\n\t"
  "v_mfma_f32_16x16x32_bf16 a[8:11], v[140:143], v[116:119], a[8:11]\n\t"
  "buffer_load_dwordx4 %[ks0], %[srdk], 0 offen offset:0 lds\n\t"
  "v_mfma_f32_16x16x32_bf16 a[12:15], v[140:143], v[120:123], a[12:15]\n\t"
  "ds_read_b128 a[204:207], %[ka1] offset:36864\n\t"
  "v_mfma_f32_16x16x32_bf16 a[16:19], v[144:147], v[116:119], a[16:19]\n\t"
  "ds_read_b128 a[208:211], %[ka0] offset:40960\n\t"
  "v_mfma_f32_16x16x32_bf16 a[20:23], v[144:147], v[120:123], a[20:23]\n\t"
  "v_exp_f32 v80, v80\n\t"
  "v_mfma_f32_16x16x32_bf16 a[24:27], v[148:151], v[116:119], a[24:27]\n\t"
  "ds_read_b128 a[212:215], %[ka1] offset:40960\n\t"
  "ds_read_b128 a[216:219], %[ka0] offset:45056\n\t"
  "v_mfma_f32_16x16x32_bf16 a[28:31], v[148:151], v[120:123], a[28:31]\n\t"
  "v_exp_f32 v81, v81\n\t"
  "v_mfma_f32_16x16x32_bf16 a[32:35], v[152:155], v[116:119], a[32:35]\n\t"
  "ds_read_b128 a[220:223], %[ka1] offset:45056\n\t"
  "v_mfma_f32_16x16x32_bf16 a[36:39], v[152:155], v[120:123], a[36:39]\n\t"
  "ds_read_b128 a[224:227], %[ka0] offset:34816\n\t"
  "v_mfma_f32_16x16x32_bf16 a[40:43], v[156:159], v[116:119], a[40:43]\n\t"
  "buffer_load_dwordx4 %[ks1], %[srdk], s40 offen offset:1024 lds\n\t"
  "ds_read_b128 a[228:231], %[ka1] offset:34816\n\t"
  "v_mfma_f32_16x16x32_bf16 a[44:47], v[156:159], v[120:123], a[44:47]\n\t"
  "v_exp_f32 v82, v82\n\t"
  "v_mfma_f32_16x16x32_bf16 a[48:51], v[160:163], v[116:119], a[48:51]\n\t"
  "ds_read_b128 a[232:235], %[ka0] offset:38912\n\t"
  "v_mfma_f32_16x16x32_bf16 a[52:55], v[160:163], v[120:123], a[52:55]\n\t"
  "v_cvt_pk_bf16_f32 v124, v80, v81\n\t"
  "ds_read_b128 a[236:239], %[ka1] offset:38912\n\t"
  "v_mfma_f32_16x16x32_bf16 a[56:59], v[164:167], v[116:119], a[56:59]\n\t"
  "ds_read_b128 a[240:243], %[ka0] offset:43008\n\t"
  "v_mfma_f32_16x16x32_bf16 a[60:63], v[164:167], v[120:123], a[60:63]\n\t"
  "v_exp_f32 v83, v83\n\t"
  "v_mfma_f32_16x16x32_bf16 v[104:107], v[112:115], v[116:119], v[104:107]\n\t"
  "ds_read_b128 a[244:247], %[ka1] offset:43008\n\t"
  "ds_read_b128 a[248:251], %[ka0] offset:47104\n\t"
  "v_mfma_f32_16x16x32_bf16 v[108:111], v[112:115], v[120:123], v[108:111]\n\t"
  "v_exp_f32 v84, v84\n\t"
  "v_mfma_f32_16x16x32_bf16 a[64:67], v[168:171], v[116:119], a[64:67]\n\t"
  "buffer_load_dwordx4 %[ks0], %[srdk], s41 offen offset:2048 lds\n\t"
  "ds_read_b128 a[252:255], %[ka1] offset:47104\n\t"
  "v_mfma_f32_16x16x32_bf16 a[68:71], v[168:171], v[120:123], a[68:71]\n\t"
  "ds_read_b64_tr_b16 v[136:137], %[va0] offset:16384\n\t"
  "v_mfma_f32_16x16x32_bf16 a[72:75], v[172:175], v[116:119], a[72:75]\n\t"
  "v_cvt_pk_bf16_f32 v125, v82, v83\n\t"
  "ds_read_b64_tr_b16 v[138:139], %[va0] offset:18432\n\t"
  "v_mfma_f32_16x16x32_bf16 a[76:79], v[172:175], v[120:123], a[76:79]\n\t"
  "v_exp_f32 v85, v85\n\t"
  "v_mfma_f32_16x16x32_bf16 a[80:83], v[176:179], v[116:119], a[80:83]\n\t"
  "ds_read_b64_tr_b16 v[140:141], %[va1] offset:16384\n\t"
  "v_mfma_f32_16x16x32_bf16 a[84:87], v[176:179], v[120:123], a[84:87]\n\t"
  "ds_read_b64_tr_b16 v[142:143], %[va1] offset:18432\n\t"
  "v_mfma_f32_16x16x32_bf16 a[88:91], v[180:183], v[116:119], a[88:91]\n\t"
  "ds_read_b64_tr_b16 v[144:145], %[va0] offset:16896\n\t"
  "v_mfma_f32_16x16x32_bf16 a[92:95], v[180:183], v[120:123], a[92:95]\n\t"
  "v_exp_f32 v86, v86\n\t"
  "v_mfma_f32_16x16x32_bf16 a[96:99], v[184:187], v[116:119], a[96:99]\n\t"
  "buffer_load_dwordx4 %[ks1], %[srdk], s42 offen offset:3072 lds\n\t"
  "ds_read_b64_tr_b16 v[146:147], %[va0] offset:18944\n\t"
  "v_mfma_f32_16x16x32_bf16 a[100:103], v[184:187], v[120:123], a[100:103]\n\t"
  "v_add_u32 %[ks0], 0x4000, %[ks0]\n\t"
  "ds_read_b64_tr_b16 v[148:149], %[va1] offset:16896\n\t"
  "v_mfma_f32_16x16x32_bf16 a[104:107], v[188:191], v[116:119], a[104:107]\n\t"
  "v_cvt_pk_bf16_f32 v128, v84, v85\n\t"
  "v_add_u32 %[ks1], 0x4000, %[ks1]\n\t"
  "v_mfma_f32_16x16x32_bf16 a[108:111], v[188:191], v[120:123], a[108:111]\n\t"
  "ds_read_b64_tr_b16 v[150:151], %[va1] offset:18944\n\t"
  "ds_read_b64_tr_b16 v[152:153], %[va0] offset:20480\n\t"
  "v_mfma_f32_16x16x32_bf16 a[112:115], v[192:195], v[116:119], a[112:115]\n\t"
  "v_exp_f32 v87, v87\n\t"
  "v_mfma_f32_16x16x32_bf16 a[116:119], v[192:195], v[120:123], a[116:119]\n\t"
  "ds_read_b64_tr_b16 v[154:155], %[va0] offset:22528\n\t"
  "ds_read_b64_tr_b16 v[156:157], %[va1] offset:20480\n\t"
  "v_mfma_f32_16x16x32_bf16 a[120:123], v[196:199], v[116:119], a[120:123]\n\t"
  "v_exp_f32 v88, v88\n\t"
  "v_mfma_f32_16x16x32_bf16 a[124:127], v[196:199], v[120:123], a[124:127]\n\t"
  "s_add_u32 m0, %[m0v], 0\n\t"
  "ds_read_b64_tr_b16 v[158:159], %[va1] offset:22528\n\t"
  "s_waitcnt lgkmcnt(6)\n\t"
  "v_mfma_f32_16x16x32_bf16 v[64:67], a[192:195], a[128:131], v[96:99]\n\t"
  "buffer_load_dwordx4 %[vs], %[srdv], 0 offen offset:0 lds\n\t"
  "ds_read_b64_tr_b16 v[160:161], %[va0] offset:20992\n\t"
  "v_mfma_f32_16x16x32_bf16 v[68:71], a[192:195], a[160:163], v[100:103]\n\t"
  "v_cvt_pk_bf16_f32 v129, v86, v87\n\t"
  "ds_read_b64_tr_b16 v[162:163], %[va0] offset:23040\n\t"
  "v_mfma_f32_16x16x32_bf16 v[64:67], a[196:199], a[132:135], v[64:67]\n\t"
  "v_exp_f32 v89, v89\n\t"
  "v_mfma_f32_16x16x32_bf16 v[68:71], a[196:199], a[164:167], v[68:71]\n\t"
  "ds_read_b64_tr_b16 v[164:165], %[va1] offset:20992\n\t"
  "ds_read_b64_tr_b16 v[166:167], %[va1] offset:23040\n\t"
  "v_mfma_f32_16x16x32_bf16 v[64:67], a[200:203], a[136:139], v[64:67]\n\t"
  "v_exp_f32 v90, v90\n\t"
  "v_mfma_f32_16x16x32_bf16 v[68:71], a[200:203], a[168:171], v[68:71]\n\t"
  "ds_read_b64_tr_b16 v[168:169], %[va0] offset:24576\n\t"
  "v_mfma_f32_16x16x32_bf16 v[64:67], a[204:207], a[140:143], v[64:67]\n\t"
  "ds_read_b64_tr_b16 v[170:171], %[va0] offset:26624\n\t"
  "v_mfma_f32_16x16x32_bf16 v[68:71], a[204:207], a[172:175], v[68:71]\n\t"
  "v_cvt_pk_bf16_f32 v126, v88, v89\n\t"
  "ds_read_b64_tr_b16 v[172:173], %[va1] offset:24576\n\t"
  "v_mfma_f32_16x16x32_bf16 v[64:67], a[208:211], a[144:147], v[64:67]\n\t"
  "buffer_load_dwordx4 %[vs], %[srdv], s40 offen offset:1024 lds\n\t"
  "ds_read_b64_tr_b16 v[174:175], %[va1] offset:26624\n\t"
  "v_mfma_f32_16x16x32_bf16 v[68:71], a[208:211], a[176:179], v[68:71]\n\t"
  "v_exp_f32 v91, v91\n\t"
  "v_mfma_f32_16x16x32_bf16 v[64:67], a[212:215], a[148:151], v[64:67]\n\t"
  "ds_read_b64_tr_b16 v[176:177], %[va0] offset:25088\n\t"
  "v_mfma_f32_16x16x32_bf16 v[68:71], a[212:215], a[180:183], v[68:71]\n\t"
  "ds_read_b64_tr_b16 v[178:179], %[va0] offset:27136\n\t"
  "v_mfma_f32_16x16x32_bf16 v[64:67], a[216:219], a[152:155], v[64:67]\n\t"
  "v_exp_f32 v92, v92\n\t"
  "v_mfma_f32_16x16x32_bf16 v[68:71], a[216:219], a[184:187], v[68:71]\n\t"
  "ds_read_b64_tr_b16 v[180:181], %[va1] offset:25088\n\t"
  "ds_read_b64_tr_b16 v[182:183], %[va1] offset:27136\n\t"
  "v_mfma_f32_16x16x32_bf16 v[64:67], a[220:223], a[156:159], v[64:67]\n\t"
  "v_cvt_pk_bf16_f32 v127, v90, v91\n\t"
  "v_mfma_f32_16x16x32_bf16 v[68:71], a[220:223], a[188:191], v[68:71]\n\t"
  "ds_read_b64_tr_b16 v[184:185], %[va0] offset:28672\n\t"
  "v_mfma_f32_16x16x32_bf16 v[72:75], a[224:227], a[128:131], v[96:99]\n\t"
  "buffer_load_dwordx4 %[vs], %[srdv], s41 offen offset:2048 lds\n\t"
  "ds_read_b64_tr_b16 v[186:187], %[va0] offset:30720\n\t"
  "v_mfma_f32_16x16x32_bf16 v[76:79], a[224:227], a[160:163], v[100:103]\n\t"
  "v_exp_f32 v93, v93\n\t"
  "v_mfma_f32_16x16x32_bf16 v[72:75], a[228:231], a[132:135], v[72:75]\n\t"
  "ds_read_b64_tr_b16 v[188:189], %[va1] offset:28672\n\t"
  "ds_read_b64_tr_b16 v[190:191], %[va1] offset:30720\n\t"
  "v_mfma_f32_16x16x32_bf16 v[76:79], a[228:231], a[164:167], v[76:79]\n\t"
  "v_exp_f32 v94, v94\n\t"
  "v_mfma_f32_16x16x32_bf16 v[72:75], a[232:235], a[136:139], v[72:75]\n\t"
  "ds_read_b64_tr_b16 v[192:193], %[va0] offset:29184\n\t"
  "v_mfma_f32_16x16x32_bf16 v[76:79], a[232:235], a[168:171], v[76:79]\n\t"
  "ds_read_b64_tr_b16 v[194:195], %[va0] offset:31232\n\t"
  "v_cvt_pk_bf16_f32 v130, v92, v93\n\t"
  "v_mfma_f32_16x16x32_bf16 v[72:75], a[236:239], a[140:143], v[72:75]\n\t"
  "ds_read_b64_tr_b16 v[196:197], %[va1] offset:29184\n\t"
  "v_mfma_f32_16x16x32_bf16 v[76:79], a[236:239], a[172:175], v[76:79]\n\t"
  "v_exp_f32 v95, v95\n\t"
  "v_mfma_f32_16x16x32_bf16 v[72:75], a[240:243], a[144:147], v[72:75]\n\t"
  "buffer_load_dwordx4 %[vs], %[srdv], s42 offen offset:3072 lds\n\t"
  "ds_read_b64_tr_b16 v[198:199], %[va1] offset:31232\n\t"
  "v_mfma_f32_16x16x32_bf16 v[76:79], a[240:243], a[176:179], v[76:79]\n\t"
  "v_add_u32 %[vs], 0x4000, %[vs]\n\t"
  "v_mfma_f32_16x16x32_bf16 v[72:75], a[244:247], a[148:151], v[72:75]\n\t"
  "v_cvt_pk_bf16_f32 v131, v94, v95\n\t"
  "v_mfma_f32_16x16x32_bf16 v[76:79], a[244:247], a[180:183], v[76:79]\n\t"
  "v_mfma_f32_16x16x32_bf16 v[72:75], a[248:251], a[152:155], v[72:75]\n\t"
  "v_mfma_f32_16x16x32_bf16 v[76:79], a[248:251], a[184:187], v[76:79]\n\t"
  "v_mfma_f32_16x16x32_bf16 v[72:75], a[252:255], a[156:159], v[72:75]\n\t"
  "v_mfma_f32_16x16x32_bf16 v[76:79], a[252:255], a[188:191], v[76:79]\n\t"
  "s_waitcnt vmcnt(16) lgkmcnt(0)\n\t"
  "s_barrier\n\t"
  "v_mfma_f32_16x16x32_bf16 a[0:3], v[136:139], v[124:127], a[0:3]\n\t"
  "ds_read_b128 a[192:195], %[ka0] offset:49152\n\t"
  "s_add_u32 m0, %[m0k], 32768\n\t"
  "v_mfma_f32_16x16x32_bf16 a[4:7], v[136:139], v[128:131], a[4:7]\n\t"
  "ds_read_b128 a[196:199], %[ka1] offset:49152\n\t"
  "ds_read_b128 a[200:203], %[ka0] offset:53248\n\t"
  "v_mfma_f32_16x16x32_bf16 a[8:11], v[140:143], v[124:127], a[8:11]\n\t"
  "buffer_load_dwordx4 %[ks0], %[srdk], 0 offen offset:0 lds\n\t"
  "v_mfma_f32_16x16x32_bf16 a[12:15], v[140:143], v[128:131], a[12:15]\n\t"
  "ds_read_b128 a[204:207], %[ka1] offset:53248\n\t"
  "v_mfma_f32_16x16x32_bf16 a[16:19], v[144:147], v[124:127], a[16:19]\n\t"
  "ds_read_b128 a[208:211], %[ka0] offset:57344\n\t"
  "v_mfma_f32_16x16x32_bf16 a[20:23], v[144:147], v[128:131], a[20:23]\n\t"
  "v_exp_f32 v64, v64\n\t"
  "v_mfma_f32_16x16x32_bf16 a[24:27], v[148:151], v[124:127], a[24:27]\n\t"
  "ds_read_b128 a[212:215], %[ka1] offset:57344\n\t"
  "ds_read_b128 a[216:219], %[ka0] offset:61440\n\t"
  "v_mfma_f32_16x16x32_bf16 a[28:31], v[148:151], v[128:131], a[28:31]\n\t"
  "v_exp_f32 v65, v65\n\t"
  "v_mfma_f32_16x16x32_bf16 a[32:35], v[152:155], v[124:127], a[32:35]\n\t"
  "ds_read_b128 a[220:223], %[ka1] offset:61440\n\t"
  "v_mfma_f32_16x16x32_bf16 a[36:39], v[152:155], v[128:131], a[36:39]\n\t"
  "ds_read_b128 a[224:227], %[ka0] offset:51200\n\t"
  "v_mfma_f32_16x16x32_bf16 a[40:43], v[156:159], v[124:127], a[40:43]\n\t"
  "buffer_load_dwordx4 %[ks1], %[srdk], s40 offen offset:1024 lds\n\t"
  "ds_read_b128 a[228:231], %[ka1] offset:51200\n\t"
  "v_mfma_f32_16x16x32_bf16 a[44:47], v[156:159], v[128:131], a[44:47]\n\t"
  "v_exp_f32 v66, v66\n\t"
  "v_mfma_f32_16x16x32_bf16 a[48:51], v[160:163], v[124:127], a[48:51]\n\t"
  "ds_read_b128 a[232:235], %[ka0] offset:55296\n\t"
  "v_mfma_f32_16x16x32_bf16 a[52:55], v[160:163], v[128:131], a[52:55]\n\t"
  "v_cvt_pk_bf16_f32 v116, v64, v65\n\t"
  "ds_read_b128 a[236:239], %[ka1] offset:55296\n\t"
  "v_mfma_f32_16x16x32_bf16 a[56:59], v[164:167], v[124:127], a[56:59]\n\t"
  "ds_read_b128 a[240:243], %[ka0] offset:59392\n\t"
  "v_mfma_f32_16x16x32_bf16 a[60:63], v[164:167], v[128:131], a[60:63]\n\t"
  "v_exp_f32 v67, v67\n\t"
  "v_mfma_f32_16x16x32_bf16 v[104:107], v[112:115], v[124:127], v[104:107]\n\t"
  "ds_read_b128 a[244:247], %[ka1] offset:59392\n\t"
  "ds_read_b128 a[248:251], %[ka0] offset:63488\n\t"
  "v_mfma_f32_16x16x32_bf16 v[108:111], v[112:115], v[128:131], v[108:111]\n\t"
  "v_exp_f32 v68, v68\n\t"
  "v_mfma_f32_16x16x32_bf16 a[64:67], v[168:171], v[124:127], a[64:67]\n\t"
  "buffer_load_dwordx4 %[ks0], %[srdk], s41 offen offset:2048 lds\n\t"
  "ds_read_b128 a[252:255], %[ka1] offset:63488\n\t"
  "v_mfma_f32_16x16x32_bf16 a[68:71], v[168:171], v[128:131], a[68:71]\n\t"
  "ds_read_b64_tr_b16 v[136:137], %[va0] offset:32768\n\t"
  "v_mfma_f32_16x16x32_bf16 a[72:75], v[172:175], v[124:127], a[72:75]\n\t"
  "v_cvt_pk_bf16_f32 v117, v66, v67\n\t"
  "ds_read_b64_tr_b16 v[138:139], %[va0] offset:34816\n\t"
  "v_mfma_f32_16x16x32_bf16 a[76:79], v[172:175], v[128:131], a[76:79]\n\t"
  "v_exp_f32 v69, v69\n\t"
  "v_mfma_f32_16x16x32_bf16 a[80:83], v[176:179], v[124:127], a[80:83]\n\t"
  "ds_read_b64_tr_b16 v[140:141], %[va1] offset:32768\n\t"
  "v_mfma_f32_16x16x32_bf16 a[84:87], v[176:179], v[128:131], a[84:87]\n\t"
  "ds_read_b64_tr_b16 v[142:143], %[va1] offset:34816\n\t"
  "v_mfma_f32_16x16x32_bf16 a[88:91], v[180:183], v[124:127], a[88:91]\n\t"
  "ds_read_b64_tr_b16 v[144:145], %[va0] offset:33280\n\t"
  "v_mfma_f32_16x16x32_bf16 a[92:95], v[180:183], v[128:131], a[92:95]\n\t"
  "v_exp_f32 v70, v70\n\t"
  "v_mfma_f32_16x16x32_bf16 a[96:99], v[184:187], v[124:127], a[96:99]\n\t"
  "buffer_load_dwordx4 %[ks1], %[srdk], s42 offen offset:3072 lds\n\t"
  "ds_read_b64_tr_b16 v[146:147], %[va0] offset:35328\n\t"
  "v_mfma_f32_16x16x32_bf16 a[100:103], v[184:187], v[128:131], a[100:103]\n\t"
  "v_add_u32 %[ks0], 0x4000, %[ks0]\n\t"
  "ds_read_b64_tr_b16 v[148:149], %[va1] offset:33280\n\t"
  "v_mfma_f32_16x16x32_bf16 a[104:107], v[188:191], v[124:127], a[104:107]\n\t"
  "v_cvt_pk_bf16_f32 v120, v68, v69\n\t"
  "v_add_u32 %[ks1], 0x4000, %[ks1]\n\t"
  "v_mfma_f32_16x16x32_bf16 a[108:111], v[188:191], v[128:131], a[108:111]\n\t"
  "ds_read_b64_tr_b16 v[150:151], %[va1] offset:35328\n\t"
  "ds_read_b64_tr_b16 v[152:153], %[va0] offset:36864\n\t"
  "v_mfma_f32_16x16x32_bf16 a[112:115], v[192:195], v[124:127], a[112:115]\n\t"
  "v_exp_f32 v71, v71\n\t"
  "v_mfma_f32_16x16x32_bf16 a[116:119], v[192:195], v[128:131], a[116:119]\n\t"
  "ds_read_b64_tr_b16 v[154:155], %[va0] offset:38912\n\t"
  "ds_read_b64_tr_b16 v[156:157], %[va1] offset:36864\n\t"
  "v_mfma_f32_16x16x32_bf16 a[120:123], v[196:199], v[124:127], a[120:123]\n\t"
  "v_exp_f32 v72, v72\n\t"
  "v_mfma_f32_16x16x32_bf16 a[124:127], v[196:199], v[128:131], a[124:127]\n\t"
  "s_add_u32 m0, %[m0v], 16384\n\t"
  "ds_read_b64_tr_b16 v[158:159], %[va1] offset:38912\n\t"
  "s_waitcnt lgkmcnt(6)\n\t"
  "v_mfma_f32_16x16x32_bf16 v[80:83], a[192:195], a[128:131], v[96:99]\n\t"
  "buffer_load_dwordx4 %[vs], %[srdv], 0 offen offset:0 lds\n\t"
  "ds_read_b64_tr_b16 v[160:161], %[va0] offset:37376\n\t"
  "v_mfma_f32_16x16x32_bf16 v[84:87], a[192:195], a[160:163], v[100:103]\n\t"
  "v_cvt_pk_bf16_f32 v121, v70, v71\n\t"
  "ds_read_b64_tr_b16 v[162:163], %[va0] offset:39424\n\t"
  "v_mfma_f32_16x16x32_bf16 v[80:83], a[196:199], a[132:135], v[80:83]\n\t"
  "v_exp_f32 v73, v73\n\t"
  "v_mfma_f32_16x16x32_bf16 v[84:87], a[196:199], a[164:167], v[84:87]\n\t"
  "ds_read_b64_tr_b16 v[164:165], %[va1] offset:37376\n\t"
  "ds_read_b64_tr_b16 v[166:167], %[va1] offset:39424\n\t"
  "v_mfma_f32_16x16x32_bf16 v[80:83], a[200:203], a[136:139], v[80:83]\n\t"
  "v_exp_f32 v74, v74\n\t"
  "v_mfma_f32_16x16x32_bf16 v[84:87], a[200:203], a[168:171], v[84:87]\n\t"
  "ds_read_b64_tr_b16 v[168:169], %[va0] offset:40960\n\t"
  "v_mfma_f32_16x16x32_bf16 v[80:83], a[204:207], a[140:143], v[80:83]\n\t"
  "ds_read_b64_tr_b16 v[170:171], %[va0] offset:43008\n\t"
  "v_mfma_f32_16x16x32_bf16 v[84:87], a[204:207], a[172:175], v[84:87]\n\t"
  "v_cvt_pk_bf16_f32 v118, v72, v73\n\t"
  "ds_read_b64_tr_b16 v[172:173], %[va1] offset:40960\n\t"
  "v_mfma_f32_16x16x32_bf16 v[80:83], a[208:211], a[144:147], v[80:83]\n\t"
  "buffer_load_dwordx4 %[vs], %[srdv], s40 offen offset:1024 lds\n\t"
  "ds_read_b64_tr_b16 v[174:175], %[va1] offset:43008\n\t"
  "v_mfma_f32_16x16x32_bf16 v[84:87], a[208:211], a[176:179], v[84:87]\n\t"
  "v_exp_f32 v75, v75\n\t"
  "v_mfma_f32_16x16x32_bf16 v[80:83], a[212:215], a[148:151], v[80:83]\n\t"
  "ds_read_b64_tr_b16 v[176:177], %[va0] offset:41472\n\t"
  "v_mfma_f32_16x16x32_bf16 v[84:87], a[212:215], a[180:183], v[84:87]\n\t"
  "ds_read_b64_tr_b16 v[178:179], %[va0] offset:43520\n\t"
  "v_mfma_f32_16x16x32_bf16 v[80:83], a[216:219], a[152:155], v[80:83]\n\t"
  "v_exp_f32 v76, v76\n\t"
  "v_mfma_f32_16x16x32_bf16 v[84:87], a[216:219], a[184:187], v[84:87]\n\t"
  "ds_read_b64_tr_b16 v[180:181], %[va1] offset:41472\n\t"
  "ds_read_b64_tr_b16 v[182:183], %[va1] offset:43520\n\t"
  "v_mfma_f32_16x16x32_bf16 v[80:83], a[220:223], a[156:159], v[80:83]\n\t"
  "v_cvt_pk_bf16_f32 v119, v74, v75\n\t"
  "v_mfma_f32_16x16x32_bf16 v[84:87], a[220:223], a[188:191], v[84:87]\n\t"
  "ds_read_b64_tr_b16 v[184:185], %[va0] offset:45056\n\t"
  "v_mfma_f32_16x16x32_bf16 v[88:91], a[224:227], a[128:131], v[96:99]\n\t"
  "buffer_load_dwordx4 %[vs], %[srdv], s41 offen offset:2048 lds\n\t"
  "ds_read_b64_tr_b16 v[186:187], %[va0] offset:47104\n\t"
  "v_mfma_f32_16x16x32_bf16 v[92:95], a[224:227], a[160:163], v[100:103]\n\t"
  "v_exp_f32 v77, v77\n\t"
  "v_mfma_f32_16x16x32_bf16 v[88:91], a[228:231], a[132:135], v[88:91]\n\t"
  "ds_read_b64_tr_b16 v[188:189], %[va1] offset:45056\n\t"
  "ds_read_b64_tr_b16 v[190:191], %[va1] offset:47104\n\t"
  "v_mfma_f32_16x16x32_bf16 v[92:95], a[228:231], a[164:167], v[92:95]\n\t"
  "v_exp_f32 v78, v78\n\t"
  "v_mfma_f32_16x16x32_bf16 v[88:91], a[232:235], a[136:139], v[88:91]\n\t"
  "ds_read_b64_tr_b16 v[192:193], %[va0] offset:45568\n\t"
  "v_mfma_f32_16x16x32_bf16 v[92:95], a[232:235], a[168:171], v[92:95]\n\t"
  "ds_read_b64_tr_b16 v[194:195], %[va0] offset:47616\n\t"
  "v_cvt_pk_bf16_f32 v122, v76, v77\n\t"
  "v_mfma_f32_16x16x32_bf16 v[88:91], a[236:239], a[140:143], v[88:91]\n\t"
  "ds_read_b64_tr_b16 v[196:197], %[va1] offset:45568\n\t"
  "v_mfma_f32_16x16x32_bf16 v[92:95], a[236:239], a[172:175], v[92:95]\n\t"
  "v_exp_f32 v79, v79\n\t"
  "v_mfma_f32_16x16x32_bf16 v[88:91], a[240:243], a[144:147], v[88:91]\n\t"
  "buffer_load_dwordx4 %[vs], %[srdv], s42 offen offset:3072 lds\n\t"
  "ds_read_b64_tr_b16 v[198:199], %[va1] offset:47616\n\t"
  "v_mfma_f32_16x16x32_bf16 v[92:95], a[240:243], a[176:179], v[92:95]\n\t"
  "v_add_u32 %[vs], 0x4000, %[vs]\n\t"
  "v_mfma_f32_16x16x32_bf16 v[88:91], a[244:247], a[148:151], v[88:91]\n\t"
  "v_cvt_pk_bf16_f32 v123, v78, v79\n\t"
  "v_mfma_f32_16x16x32_bf16 v[92:95], a[244:247], a[180:183], v[92:95]\n\t"
  "v_mfma_f32_16x16x32_bf16 v[88:91], a[248:251], a[152:155], v[88:91]\n\t"
  "v_mfma_f32_16x16x32_bf16 v[92:95], a[248:251], a[184:187], v[92:95]\n\t"
  "v_mfma_f32_16x16x32_bf16 v[88:91], a[252:255], a[156:159], v[88:91]\n\t"
  "v_mfma_f32_16x16x32_bf16 v[92:95], a[252:255], a[188:191], v[92:95]\n\t"
  "s_waitcnt vmcnt(16) lgkmcnt(0)\n\t"
  "s_barrier\n\t"
  "v_mfma_f32_16x16x32_bf16 a[0:3], v[136:139], v[116:119], a[0:3]\n\t"
  "ds_read_b128 a[192:195], %[ka0] offset:0\n\t"
  "s_add_u32 m0, %[m0k], 49152\n\t"
  "v_mfma_f32_16x16x32_bf16 a[4:7], v[136:139], v[120:123], a[4:7]\n\t"
  "ds_read_b128 a[196:199], %[ka1] offset:0\n\t"
  "ds_read_b128 a[200:203], %[ka0] offset:4096\n\t"
  "v_mfma_f32_16x16x32_bf16 a[8:11], v[140:143], v[116:119], a[8:11]\n\t"
  "buffer_load_dwordx4 %[ks0], %[srdk], 0 offen offset:0 lds\n\t"
  "v_mfma_f32_16x16x32_bf16 a[12:15], v[140:143], v[120:123], a[12:15]\n\t"
  "ds_read_b128 a[204:207], %[ka1] offset:4096\n\t"
  "v_mfma_f32_16x16x32_bf16 a[16:19], v[144:147], v[116:119], a[16:19]\n\t"
  "ds_read_b128 a[208:211], %[ka0] offset:8192\n\t"
  "v_mfma_f32_16x16x32_bf16 a[20:23], v[144:147], v[120:123], a[20:23]\n\t"
  "v_exp_f32 v80, v80\n\t"
  "v_mfma_f32_16x16x32_bf16 a[24:27], v[148:151], v[116:119], a[24:27]\n\t"
  "ds_read_b128 a[212:215], %[ka1] offset:8192\n\t"
  "ds_read_b128 a[216:219], %[ka0] offset:12288\n\t"
  "v_mfma_f32_16x16x32_bf16 a[28:31], v[148:151], v[120:123], a[28:31]\n\t"
  "v_exp_f32 v81, v81\n\t"
  "v_mfma_f32_16x16x32_bf16 a[32:35], v[152:155], v[116:119], a[32:35]\n\t"
  "ds_read_b128 a[220:223], %[ka1] offset:12288\n\t"
  "v_mfma_f32_16x16x32_bf16 a[36:39], v[152:155], v[120:123], a[36:39]\n\t"
  "ds_read_b128 a[224:227], %[ka0] offset:2048\n\t"
  "v_mfma_f32_16x16x32_bf16 a[40:43], v[156:159], v[116:119], a[40:43]\n\t"
  "buffer_load_dwordx4 %[ks1], %[srdk], s40 offen offset:1024 lds\n\t"
  "ds_read_b128 a[228:231], %[ka1] offset:2048\n\t"
  "v_mfma_f32_16x16x32_bf16 a[44:47], v[156:159], v[120:123], a[44:47]\n\t"
  "v_exp_f32 v82, v82\n\t"
  "v_mfma_f32_16x16x32_bf16 a[48:51], v[160:163], v[116:119], a[48:51]\n\t"
  "ds_read_b128 a[232:235], %[ka0] offset:6144\n\t"
  "v_mfma_f32_16x16x32_bf16 a[52:55], v[160:163], v[120:123], a[52:55]\n\t"
  "v_cvt_pk_bf16_f32 v124, v80, v81\n\t"
  "ds_read_b128 a[236:239], %[ka1] offset:6144\n\t"
  "v_mfma_f32_16x16x32_bf16 a[56:59], v[164:167], v[116:119], a[56:59]\n\t"
  "ds_read_b128 a[240:243], %[ka0] offset:10240\n\t"
  "v_mfma_f32_16x16x32_bf16 a[60:63], v[164:167], v[120:123], a[60:63]\n\t"
  "v_exp_f32 v83, v83\n\t"
  "v_mfma_f32_16x16x32_bf16 v[104:107], v[112:115], v[116:119], v[104:107]\n\t"
  "ds_read_b128 a[244:247], %[ka1] offset:10240\n\t"
  "ds_read_b128 a[248:251], %[ka0] offset:14336\n\t"
  "v_mfma_f32_16x16x32_bf16 v[108:111], v[112:115], v[120:123], v[108:111]\n\t"
  "v_exp_f32 v84, v84\n\t"
  "v_mfma_f32_16x16x32_bf16 a[64:67], v[168:171], v[116:119], a[64:67]\n\t"
  "buffer_load_dwordx4 %[ks0], %[srdk], s41 offen offset:2048 lds\n\t"
  "ds_read_b128 a[252:255], %[ka1] offset:14336\n\t"
  "v_mfma_f32_16x16x32_bf16 a[68:71], v[168:171], v[120:123], a[68:71]\n\t"
  "ds_read_b64_tr_b16 v[136:137], %[va0] offset:49152\n\t"
  "v_mfma_f32_16x16x32_bf16 a[72:75], v[172:175], v[116:119], a[72:75]\n\t"
  "v_cvt_pk_bf16_f32 v125, v82, v83\n\t"
  "ds_read_b64_tr_b16 v[138:139], %[va0] offset:51200\n\t"
  "v_mfma_f32_16x16x32_bf16 a[76:79], v[172:175], v[120:123], a[76:79]\n\t"
  "v_exp_f32 v85, v85\n\t"
  "v_mfma_f32_16x16x32_bf16 a[80:83], v[176:179], v[116:119], a[80:83]\n\t"
  "ds_read_b64_tr_b16 v[140:141], %[va1] offset:49152\n\t"
  "v_mfma_f32_16x16x32_bf16 a[84:87], v[176:179], v[120:123], a[84:87]\n\t"
  "ds_read_b64_tr_b16 v[142:143], %[va1] offset:51200\n\t"
  "v_mfma_f32_16x16x32_bf16 a[88:91], v[180:183], v[116:119], a[88:91]\n\t"
  "ds_read_b64_tr_b16 v[144:145], %[va0] offset:49664\n\t"
  "v_mfma_f32_16x16x32_bf16 a[92:95], v[180:183], v[120:123], a[92:95]\n\t"
  "v_exp_f32 v86, v86\n\t"
  "v_mfma_f32_16x16x32_bf16 a[96:99], v[184:187], v[116:119], a[96:99]\n\t"
  "buffer_load_dwordx4 %[ks1], %[srdk], s42 offen offset:3072 lds\n\t"
  "ds_read_b64_tr_b16 v[146:147], %[va0] offset:51712\n\t"
  "v_mfma_f32_16x16x32_bf16 a[100:103], v[184:187], v[120:123], a[100:103]\n\t"
  "v_add_u32 %[ks0], 0x4000, %[ks0]\n\t"
  "ds_read_b64_tr_b16 v[148:149], %[va1] offset:49664\n\t"
  "v_mfma_f32_16x16x32_bf16 a[104:107], v[188:191], v[116:119], a[104:107]\n\t"
  "v_cvt_pk_bf16_f32 v128, v84, v85\n\t"
  "v_add_u32 %[ks1], 0x4000, %[ks1]\n\t"
  "v_mfma_f32_16x16x32_bf16 a[108:111], v[188:191], v[120:123], a[108:111]\n\t"
  "ds_read_b64_tr_b16 v[150:151], %[va1] offset:51712\n\t"
  "ds_read_b64_tr_b16 v[152:153], %[va0] offset:53248\n\t"
  "v_mfma_f32_16x16x32_bf16 a[112:115], v[192:195], v[116:119], a[112:115]\n\t"
  "v_exp_f32 v87, v87\n\t"
  "v_mfma_f32_16x16x32_bf16 a[116:119], v[192:195], v[120:123], a[116:119]\n\t"
  "ds_read_b64_tr_b16 v[154:155], %[va0] offset:55296\n\t"
  "ds_read_b64_tr_b16 v[156:157], %[va1] offset:53248\n\t"
  "v_mfma_f32_16x16x32_bf16 a[120:123], v[196:199], v[116:119], a[120:123]\n\t"
  "v_exp_f32 v88, v88\n\t"
  "v_mfma_f32_16x16x32_bf16 a[124:127], v[196:199], v[120:123], a[124:127]\n\t"
  "s_add_u32 m0, %[m0v], 32768\n\t"
  "ds_read_b64_tr_b16 v[158:159], %[va1] offset:55296\n\t"
  "s_waitcnt lgkmcnt(6)\n\t"
  "v_mfma_f32_16x16x32_bf16 v[64:67], a[192:195], a[128:131], v[96:99]\n\t"
  "buffer_load_dwordx4 %[vs], %[srdv], 0 offen offset:0 lds\n\t"
  "ds_read_b64_tr_b16 v[160:161], %[va0] offset:53760\n\t"
  "v_mfma_f32_16x16x32_bf16 v[68:71], a[192:195], a[160:163], v[100:103]\n\t"
  "v_cvt_pk_bf16_f32 v129, v86, v87\n\t"
  "ds_read_b64_tr_b16 v[162:163], %[va0] offset:55808\n\t"
  "v_mfma_f32_16x16x32_bf16 v[64:67], a[196:199], a[132:135], v[64:67]\n\t"
  "v_exp_f32 v89, v89\n\t"
  "v_mfma_f32_16x16x32_bf16 v[68:71], a[196:199], a[164:167], v[68:71]\n\t"
  "ds_read_b64_tr_b16 v[164:165], %[va1] offset:53760\n\t"
  "ds_read_b64_tr_b16 v[166:167], %[va1] offset:55808\n\t"
  "v_mfma_f32_16x16x32_bf16 v[64:67], a[200:203], a[136:139], v[64:67]\n\t"
  "v_exp_f32 v90, v90\n\t"
  "v_mfma_f32_16x16x32_bf16 v[68:71], a[200:203], a[168:171], v[68:71]\n\t"
  "ds_read_b64_tr_b16 v[168:169], %[va0] offset:57344\n\t"
  "v_mfma_f32_16x16x32_bf16 v[64:67], a[204:207], a[140:143], v[64:67]\n\t"
  "ds_read_b64_tr_b16 v[170:171], %[va0] offset:59392\n\t"
  "v_mfma_f32_16x16x32_bf16 v[68:71], a[204:207], a[172:175], v[68:71]\n\t"
  "v_cvt_pk_bf16_f32 v126, v88, v89\n\t"
  "ds_read_b64_tr_b16 v[172:173], %[va1] offset:57344\n\t"
  "v_mfma_f32_16x16x32_bf16 v[64:67], a[208:211], a[144:147], v[64:67]\n\t"
  "buffer_load_dwordx4 %[vs], %[srdv], s40 offen offset:1024 lds\n\t"
  "ds_read_b64_tr_b16 v[174:175], %[va1] offset:59392\n\t"
  "v_mfma_f32_16x16x32_bf16 v[68:71], a[208:211], a[176:179], v[68:71]\n\t"
  "v_exp_f32 v91, v91\n\t"
  "v_mfma_f32_16x16x32_bf16 v[64:67], a[212:215], a[148:151], v[64:67]\n\t"
  "ds_read_b64_tr_b16 v[176:177], %[va0] offset:57856\n\t"
  "v_mfma_f32_16x16x32_bf16 v[68:71], a[212:215], a[180:183], v[68:71]\n\t"
  "ds_read_b64_tr_b16 v[178:179], %[va0] offset:59904\n\t"
  "v_mfma_f32_16x16x32_bf16 v[64:67], a[216:219], a[152:155], v[64:67]\n\t"
  "v_exp_f32 v92, v92\n\t"
  "v_mfma_f32_16x16x32_bf16 v[68:71], a[216:219], a[184:187], v[68:71]\n\t"
  "ds_read_b64_tr_b16 v[180:181], %[va1] offset:57856\n\t"
  "ds_read_b64_tr_b16 v[182:183], %[va1] offset:59904\n\t"
  "v_mfma_f32_16x16x32_bf16 v[64:67], a[220:223], a[156:159], v[64:67]\n\t"
  "v_cvt_pk_bf16_f32 v127, v90, v91\n\t"
  "v_mfma_f32_16x16x32_bf16 v[68:71], a[220:223], a[188:191], v[68:71]\n\t"
  "ds_read_b64_tr_b16 v[184:185], %[va0] offset:61440\n\t"
  "v_mfma_f32_16x16x32_bf16 v[72:75], a[224:227], a[128:131], v[96:99]\n\t"
  "buffer_load_dwordx4 %[vs], %[srdv], s41 offen offset:2048 lds\n\t"
  "ds_read_b64_tr_b16 v[186:187], %[va0] offset:63488\n\t"
  "v_mfma_f32_16x16x32_bf16 v[76:79], a[224:227], a[160:163], v[100:103]\n\t"
  "v_exp_f32 v93, v93\n\t"
  "v_mfma_f32_16x16x32_bf16 v[72:75], a[228:231], a[132:135], v[72:75]\n\t"
  "ds_read_b64_tr_b16 v[188:189], %[va1] offset:61440\n\t"
  "ds_read_b64_tr_b16 v[190:191], %[va1] offset:63488\n\t"
  "v_mfma_f32_16x16x32_bf16 v[76:79], a[228:231], a[164:167], v[76:79]\n\t"
  "v_exp_f32 v94, v94\n\t"
  "v_mfma_f32_16x16x32_bf16 v[72:75], a[232:235], a[136:139], v[72:75]\n\t"
  "ds_read_b64_tr_b16 v[192:193], %[va0] offset:61952\n\t"
  "v_mfma_f32_16x16x32_bf16 v[76:79], a[232:235], a[168:171], v[76:79]\n\t"
  "ds_read_b64_tr_b16 v[194:195], %[va0] offset:64000\n\t"
  "v_cvt_pk_bf16_f32 v130, v92, v93\n\t"
  "v_mfma_f32_16x16x32_bf16 v[72:75], a[236:239], a[140:143], v[72:75]\n\t"
  "ds_read_b64_tr_b16 v[196:197], %[va1] offset:61952\n\t"
  "v_mfma_f32_16x16x32_bf16 v[76:79], a[236:239], a[172:175], v[76:79]\n\t"
  "v_exp_f32 v95, v95\n\t"
  "v_mfma_f32_16x16x32_bf16 v[72:75], a[240:243], a[144:147], v[72:75]\n\t"
  "buffer_load_dwordx4 %[vs], %[srdv], s42 offen offset:3072 lds\n\t"
  "ds_read_b64_tr_b16 v[198:199], %[va1] offset:64000\n\t"
  "v_mfma_f32_16x16x32_bf16 v[76:79], a[240:243], a[176:179], v[76:79]\n\t"
  "v_add_u32 %[vs], 0x4000, %[vs]\n\t"
  "v_mfma_f32_16x16x32_bf16 v[72:75], a[244:247], a[148:151], v[72:75]\n\t"
  "v_cvt_pk_bf16_f32 v131, v94, v95\n\t"
  "v_mfma_f32_16x16x32_bf16 v[76:79], a[244:247], a[180:183], v[76:79]\n\t"
  "v_mfma_f32_16x16x32_bf16 v[72:75], a[248:251], a[152:155], v[72:75]\n\t"
  "v_mfma_f32_16x16x32_bf16 v[76:79], a[248:251], a[184:187], v[76:79]\n\t"
  "v_mfma_f32_16x16x32_bf16 v[72:75], a[252:255], a[156:159], v[72:75]\n\t"
  "v_mfma_f32_16x16x32_bf16 v[76:79], a[252:255], a[188:191], v[76:79]\n\t"
  "v_max_f32 v200, v104, v108\n\t"
  "v_cmp_nge_f32 vcc, s44, v200\n\t"
  "s_cbranch_vccnz .Lrare1_%=\n\t"
  ".Lret1_%=:\n\t"
  "s_cmp_lg_u32 s43, 13\n\t"
  "s_cbranch_scc1 .Lhk_%=\n\t"
  "s_cmp_lg_u32 s47, 0\n\t"
  "s_cbranch_scc1 .Lhk_%=\n\t"
  "v_mov_b32 v200, 0\n\t"
  "v_mov_b32 v201, %[hs1]\n\t"
  "s_mov_b64 exec, 1\n\t"
  "buffer_atomic_add v201, v200, %[srdws], %[cntb] offen\n\t"
  "s_mov_b64 exec, -1\n\t"
  ".Lhk_%=:\n\t"
  "s_sub_u32 s46, s43, %[wit]\n\t"
  "s_and_b32 s48, s46, 0xfffffff3\n\t"
  "s_cbranch_scc1 .Lhw_%=\n\t"
  "s_cmp_eq_u32 s46, 8\n\t"
  "s_cbranch_scc0 .Lhw1_%=\n\t"
  "buffer_load_dword v254, v252, %[srdp], 0 offen\n\t"
  ".Lhw1_%=:\n\t"
  "s_cmp_eq_u32 s46, 4\n\t"
  "s_cbranch_scc0 .Lhw2_%=\n\t"
  "buffer_load_dword v254, v255, %[srdg], 0 offen\n\t"
  ".Lhw2_%=:\n\t"
  "s_cmp_eq_u32 s46, 0\n\t"
  "s_cbranch_scc0 .Lhw_%=\n\t"
  "buffer_load_dword v254, v253, %[srdo], 0 offen\n\t"
  ".Lhw_%=:\n\t"
  "s_cmp_lg_u32 s43, 3\n\t"
  "s_cbranch_scc1 .Lhc_%=\n\t"
  "s_getpc_b64 s[48:49]\n\t"
  ".Lhpc_%=:\n\t"
  "v_min_u32 v200, .Lkm_code_end-.Lhpc_%=-4, %[cwo]\n\t"
  "global_load_dword v254, v200, s[48:49]\n\t"
  ".Lhc_%=:\n\t"
  "s_cmp_eq_u32 %[hs2], 0\n\t"
  "s_cbranch_scc1 .Lhf_%=\n\t"
  "s_cmp_gt_u32 s43, 2\n\t"
  "s_cbranch_scc1 .Lhf_%=\n\t"
  "s_cmp_eq_u32 s43, 2\n\t"
  "s_cbranch_scc0 .Lhf1_%=\n\t"
  "buffer_load_dword v32, %[cboff], %[srdws], 0 offen sc1\n\t"
  "s_branch .Lhf_%=\n\t"
  ".Lhf1_%=:\n\t"
  "s_mov_b32 s46, 0x40000\n\t"
  ".Lhf2_%=:\n\t"
  "v_cmp_ne_u32 vcc, 0x70, v32\n\t"
  "s_cbranch_vccz .Lhf_%=\n\t"
  "s_sleep 2\n\t"
  "buffer_load_dword v32, %[cboff], %[srdws], 0 offen sc1\n\t"
  "s_waitcnt vmcnt(0)\n\t"
  "s_sub_u32 s46, s46, 1\n\t"
  "s_cmp_lg_u32 s46, 0\n\t"
  "s_cbranch_scc1 .Lhf2_%=\n\t"
  ".Lhf_%=:\n\t"
  "s_sub_u32 s43, s43, 1\n\t"
  "s_cmp_lg_u32 s43, 0\n\t"
  "s_waitcnt vmcnt(16) lgkmcnt(0)\n\t"
  "s_barrier\n\t"
  "s_cbranch_scc1 .Lloop_%=\n\t"
  "v_mfma_f32_16x16x32_bf16 a[0:3], v[136:139], v[124:127], a[0:3]\n\t"
  "ds_read_b128 a[192:195], %[ka0] offset:16384\n\t"
  "s_add_u32 m0, %[m0o], 131072\n\t"
  "v_mfma_f32_16x16x32_bf16 a[4:7], v[136:139], v[128:131], a[4:7]\n\t"
  "ds_read_b128 a[196:199], %[ka1] offset:16384\n\t"
  "ds_read_b128 a[200:203], %[ka0] offset:20480\n\t"
  "v_mfma_f32_16x16x32_bf16 a[8:11], v[140:143], v[124:127], a[8:11]\n\t"
  "v_mfma_f32_16x16x32_bf16 a[12:15], v[140:143], v[128:131], a[12:15]\n\t"
  "ds_read_b128 a[204:207], %[ka1] offset:20480\n\t"
  "buffer_load_dwordx4 %[wos], %[srdo], 0 offen offset:0 lds\n\t"
  "v_mfma_f32_16x16x32_bf16 a[16:19], v[144:147], v[124:127], a[16:19]\n\t"
  "ds_read_b128 a[208:211], %[ka0] offset:24576\n\t"
  "v_mfma_f32_16x16x32_bf16 a[20:23], v[144:147], v[128:131], a[20:23]\n\t"
  "v_exp_f32 v64, v64\n\t"
  "v_mfma_f32_16x16x32_bf16 a[24:27], v[148:151], v[124:127], a[24:27]\n\t"
  "ds_read_b128 a[212:215], %[ka1] offset:24576\n\t"
  "ds_read_b128 a[216:219], %[ka0] offset:28672\n\t"
  "v_mfma_f32_16x16x32_bf16 a[28:31], v[148:151], v[128:131], a[28:31]\n\t"
  "buffer_load_dwordx4 %[wos], %[srdo], %[c6144] offen offset:1024 lds\n\t"
  "v_mfma_f32_16x16x32_bf16 a[32:35], v[152:155], v[124:127], a[32:35]\n\t"
  "v_exp_f32 v65, v65\n\t"
  "v_mfma_f32_16x16x32_bf16 a[36:39], v[152:155], v[128:131], a[36:39]\n\t"
  "ds_read_b128 a[220:223], %[ka1] offset:28672\n\t"
  "ds_read_b128 a[224:227], %[ka0] offset:18432\n\t"
  "v_mfma_f32_16x16x32_bf16 a[40:43], v[156:159], v[124:127], a[40:43]\n\t"
  "v_exp_f32 v66, v66\n\t"
  "v_mfma_f32_16x16x32_bf16 a[44:47], v[156:159], v[128:131], a[44:47]\n\t"
  "buffer_load_dwordx4 %[wos], %[srdo], %[c12288] offen offset:2048 lds\n\t"
  "ds_read_b128 a[228:231], %[ka1] offset:18432\n\t"
  "v_mfma_f32_16x16x32_bf16 a[48:51], v[160:163], v[124:127], a[48:51]\n\t"
  "ds_read_b128 a[232:235], %[ka0] offset:22528\n\t"
  "v_mfma_f32_16x16x32_bf16 a[52:55], v[160:163], v[128:131], a[52:55]\n\t"
  "v_cvt_pk_bf16_f32 v116, v64, v65\n\t"
  "ds_read_b128 a[236:239], %[ka1] offset:22528\n\t"
  "v_mfma_f32_16x16x32_bf16 a[56:59], v[164:167], v[124:127], a[56:59]\n\t"
  "buffer_load_dwordx4 %[wos], %[srdo], %[c18432] offen offset:3072 lds\n\t"
  "ds_read_b128 a[240:243], %[ka0] offset:26624\n\t"
  "v_mfma_f32_16x16x32_bf16 a[60:63], v[164:167], v[128:131], a[60:63]\n\t"
  "v_exp_f32 v67, v67\n\t"
  "v_mfma_f32_16x16x32_bf16 v[104:107], v[112:115], v[124:127], v[104:107]\n\t"
  "ds_read_b128 a[244:247], %[ka1] offset:26624\n\t"
  "ds_read_b128 a[248:251], %[ka0] offset:30720\n\t"
  "v_mfma_f32_16x16x32_bf16 v[108:111], v[112:115], v[128:131], v[108:111]\n\t"
  "s_add_u32 m0, %[m0o], 135168\n\t"
  "v_mfma_f32_16x16x32_bf16 a[64:67], v[168:171], v[124:127], a[64:67]\n\t"
  "v_exp_f32 v68, v68\n\t"
  "v_mfma_f32_16x16x32_bf16 a[68:71], v[168:171], v[128:131], a[68:71]\n\t"
  "ds_read_b128 a[252:255], %[ka1] offset:30720\n\t"
  "ds_read_b64_tr_b16 v[136:137], %[va0] offset:0\n\t"
  "v_mfma_f32_16x16x32_bf16 a[72:75], v[172:175], v[124:127], a[72:75]\n\t"
  "v_cvt_pk_bf16_f32 v117, v66, v67\n\t"
  "buffer_load_dwordx4 %[wos], %[srdo], %[c28672] offen offset:0 lds\n\t"
  "v_mfma_f32_16x16x32_bf16 a[76:79], v[172:175], v[128:131], a[76:79]\n\t"
  "ds_read_b64_tr_b16 v[138:139], %[va0] offset:2048\n\t"
  "ds_read_b64_tr_b16 v[140:141], %[va1] offset:0\n\t"
  "v_mfma_f32_16x16x32_bf16 a[80:83], v[176:179], v[124:127], a[80:83]\n\t"
  "v_exp_f32 v69, v69\n\t"
  "v_mfma_f32_16x16x32_bf16 a[84:87], v[176:179], v[128:131], a[84:87]\n\t"
  "ds_read_b64_tr_b16 v[142:143], %[va1] offset:2048\n\t"
  "buffer_load_dwordx4 %[wos], %[srdo], %[c34816] offen offset:1024 lds\n\t"
  "v_mfma_f32_16x16x32_bf16 a[88:91], v[180:183], v[124:127], a[88:91]\n\t"
  "ds_read_b64_tr_b16 v[144:145], %[va0] offset:512\n\t"
  "v_mfma_f32_16x16x32_bf16 a[92:95], v[180:183], v[128:131], a[92:95]\n\t"
  "v_exp_f32 v70, v70\n\t"
  "v_mfma_f32_16x16x32_bf16 a[96:99], v[184:187], v[124:127], a[96:99]\n\t"
  "ds_read_b64_tr_b16 v[146:147], %[va0] offset:2560\n\t"
  "ds_read_b64_tr_b16 v[148:149], %[va1] offset:512\n\t"
  "v_mfma_f32_16x16x32_bf16 a[100:103], v[184:187], v[128:131], a[100:103]\n\t"
  "buffer_load_dwordx4 %[wos], %[srdo], %[c40960] offen offset:2048 lds\n\t"
  "v_cvt_pk_bf16_f32 v120, v68, v69\n\t"
  "v_mfma_f32_16x16x32_bf16 a[104:107], v[188:191], v[124:127], a[104:107]\n\t"
  "ds_read_b64_tr_b16 v[150:151], %[va1] offset:2560\n\t"
  "v_mfma_f32_16x16x32_bf16 a[108:111], v[188:191], v[128:131], a[108:111]\n\t"
  "v_exp_f32 v71, v71\n\t"
  "v_mfma_f32_16x16x32_bf16 a[112:115], v[192:195], v[124:127], a[112:115]\n\t"
  "ds_read_b64_tr_b16 v[152:153], %[va0] offset:4096\n\t"
  "buffer_load_dwordx4 %[wos], %[srdo], %[c47104] offen offset:3072 lds\n\t"
  "v_mfma_f32_16x16x32_bf16 a[116:119], v[192:195], v[128:131], a[116:119]\n\t"
  "ds_read_b64_tr_b16 v[154:155], %[va0] offset:6144\n\t"
  "ds_read_b64_tr_b16 v[156:157], %[va1] offset:4096\n\t"
  "v_mfma_f32_16x16x32_bf16 a[120:123], v[196:199], v[124:127], a[120:123]\n\t"
  "v_exp_f32 v72, v72\n\t"
  "v_mfma_f32_16x16x32_bf16 a[124:127], v[196:199], v[128:131], a[124:127]\n\t"
  "ds_read_b64_tr_b16 v[158:159], %[va1] offset:6144\n\t"
  "v_add_u32 %[wos], 0x80, %[wos]\n\t"
  "s_waitcnt lgkmcnt(5)\n\t"
  "v_mfma_f32_16x16x32_bf16 v[80:83], a[192:195], a[128:131], v[96:99]\n\t"
  "ds_read_b64_tr_b16 v[160:161], %[va0] offset:4608\n\t"
  "v_cvt_pk_bf16_f32 v121, v70, v71\n\t"
  "v_mfma_f32_16x16x32_bf16 v[84:87], a[192:195], a[160:163], v[100:103]\n\t"
  "ds_read_b64_tr_b16 v[162:163], %[va0] offset:6656\n\t"
  "v_mfma_f32_16x16x32_bf16 v[80:83], a[196:199], a[132:135], v[80:83]\n\t"
  "v_exp_f32 v73, v73\n\t"
  "v_mfma_f32_16x16x32_bf16 v[84:87], a[196:199], a[164:167], v[84:87]\n\t"
  "ds_read_b64_tr_b16 v[164:165], %[va1] offset:4608\n\t"
  "s_add_u32 m0, %[m0v], 49152\n\t"
  "v_mfma_f32_16x16x32_bf16 v[80:83], a[200:203], a[136:139], v[80:83]\n\t"
  "ds_read_b64_tr_b16 v[166:167], %[va1] offset:6656\n\t"
  "v_mfma_f32_16x16x32_bf16 v[84:87], a[200:203], a[168:171], v[84:87]\n\t"
  "v_exp_f32 v74, v74\n\t"
  "v_mfma_f32_16x16x32_bf16 v[80:83], a[204:207], a[140:143], v[80:83]\n\t"
  "ds_read_b64_tr_b16 v[168:169], %[va0] offset:8192\n\t"
  "buffer_load_dwordx4 %[vs], %[srdv], 0 offen offset:0 lds\n\t"
  "v_mfma_f32_16x16x32_bf16 v[84:87], a[204:207], a[172:175], v[84:87]\n\t"
  "ds_read_b64_tr_b16 v[170:171], %[va0] offset:10240\n\t"
  "v_cvt_pk_bf16_f32 v118, v72, v73\n\t"
  "v_mfma_f32_16x16x32_bf16 v[80:83], a[208:211], a[144:147], v[80:83]\n\t"
  "ds_read_b64_tr_b16 v[172:173], %[va1] offset:8192\n\t"
  "ds_read_b64_tr_b16 v[174:175], %[va1] offset:10240\n\t"
  "v_mfma_f32_16x16x32_bf16 v[84:87], a[208:211], a[176:179], v[84:87]\n\t"
  "v_exp_f32 v75, v75\n\t"
  "v_mfma_f32_16x16x32_bf16 v[80:83], a[212:215], a[148:151], v[80:83]\n\t"
  "buffer_load_dwordx4 %[vs], %[srdv], s40 offen offset:1024 lds\n\t"
  "ds_read_b64_tr_b16 v[176:177], %[va0] offset:8704\n\t"
  "v_mfma_f32_16x16x32_bf16 v[84:87], a[212:215], a[180:183], v[84:87]\n\t"
  "ds_read_b64_tr_b16 v[178:179], %[va0] offset:10752\n\t"
  "v_mfma_f32_16x16x32_bf16 v[80:83], a[216:219], a[152:155], v[80:83]\n\t"
  "v_exp_f32 v76, v76\n\t"
  "v_mfma_f32_16x16x32_bf16 v[84:87], a[216:219], a[184:187], v[84:87]\n\t"
  "ds_read_b64_tr_b16 v[180:181], %[va1] offset:8704\n\t"
  "buffer_load_dwordx4 %[vs], %[srdv], s41 offen offset:2048 lds\n\t"
  "v_mfma_f32_16x16x32_bf16 v[80:83], a[220:223], a[156:159], v[80:83]\n\t"
  "ds_read_b64_tr_b16 v[182:183], %[va1] offset:10752\n\t"
  "v_cvt_pk_bf16_f32 v119, v74, v75\n\t"
  "v_mfma_f32_16x16x32_bf16 v[84:87], a[220:223], a[188:191], v[84:87]\n\t"
  "ds_read_b64_tr_b16 v[184:185], %[va0] offset:12288\n\t"
  "buffer_load_dwordx4 %[vs], %[srdv], s42 offen offset:3072 lds\n\t"
  "v_mfma_f32_16x16x32_bf16 v[88:91], a[224:227], a[128:131], v[96:99]\n\t"
  "v_exp_f32 v77, v77\n\t"
  "v_mfma_f32_16x16x32_bf16 v[92:95], a[224:227], a[160:163], v[100:103]\n\t"
  "ds_read_b64_tr_b16 v[186:187], %[va0] offset:14336\n\t"
  "ds_read_b64_tr_b16 v[188:189], %[va1] offset:12288\n\t"
  "v_mfma_f32_16x16x32_bf16 v[88:91], a[228:231], a[132:135], v[88:91]\n\t"
  "ds_read_b64_tr_b16 v[190:191], %[va1] offset:14336\n\t"
  "v_mfma_f32_16x16x32_bf16 v[92:95], a[228:231], a[164:167], v[92:95]\n\t"
  "v_exp_f32 v78, v78\n\t"
  "v_mfma_f32_16x16x32_bf16 v[88:91], a[232:235], a[136:139], v[88:91]\n\t"
  "v_add_u32 %[vs], 0x4000, %[vs]\n\t"
  "ds_read_b64_tr_b16 v[192:193], %[va0] offset:12800\n\t"
  "v_mfma_f32_16x16x32_bf16 v[92:95], a[232:235], a[168:171], v[92:95]\n\t"
  "ds_read_b64_tr_b16 v[194:195], %[va0] offset:14848\n\t"
  "v_cvt_pk_bf16_f32 v122, v76, v77\n\t"
  "v_mfma_f32_16x16x32_bf16 v[88:91], a[236:239], a[140:143], v[88:91]\n\t"
  "ds_read_b64_tr_b16 v[196:197], %[va1] offset:12800\n\t"
  "v_mfma_f32_16x16x32_bf16 v[92:95], a[236:239], a[172:175], v[92:95]\n\t"
  "v_exp_f32 v79, v79\n\t"
  "v_mfma_f32_16x16x32_bf16 v[88:91], a[240:243], a[144:147], v[88:91]\n\t"
  "ds_read_b64_tr_b16 v[198:199], %[va1] offset:14848\n\t"
  "v_mfma_f32_16x16x32_bf16 v[92:95], a[240:243], a[176:179], v[92:95]\n\t"
  "v_mfma_f32_16x16x32_bf16 v[88:91], a[244:247], a[148:151], v[88:91]\n\t"
  "v_cvt_pk_bf16_f32 v123, v78, v79\n\t"
  "v_mfma_f32_16x16x32_bf16 v[92:95], a[244:247], a[180:183], v[92:95]\n\t"
  "v_mfma_f32_16x16x32_bf16 v[88:91], a[248:251], a[152:155], v[88:91]\n\t"
  "v_mfma_f32_16x16x32_bf16 v[92:95], a[248:251], a[184:187], v[92:95]\n\t"
  "v_mfma_f32_16x16x32_bf16 v[88:91], a[252:255], a[156:159], v[88:91]\n\t"
  "v_mfma_f32_16x16x32_bf16 v[92:95], a[252:255], a[188:191], v[92:95]\n\t"
  "s_waitcnt vmcnt(16) lgkmcnt(0)\n\t"
  "s_barrier\n\t"
  "v_mfma_f32_16x16x32_bf16 a[0:3], v[136:139], v[116:119], a[0:3]\n\t"
  "ds_read_b128 a[192:195], %[ka0] offset:32768\n\t"
  "s_add_u32 m0, %[m0o], 0\n\t"
  "v_mfma_f32_16x16x32_bf16 a[4:7], v[136:139], v[120:123], a[4:7]\n\t"
  "ds_read_b128 a[196:199], %[ka1] offset:32768\n\t"
  "ds_read_b128 a[200:203], %[ka0] offset:36864\n\t"
  "v_mfma_f32_16x16x32_bf16 a[8:11], v[140:143], v[116:119], a[8:11]\n\t"
  "v_mfma_f32_16x16x32_bf16 a[12:15], v[140:143], v[120:123], a[12:15]\n\t"
  "ds_read_b128 a[204:207], %[ka1] offset:36864\n\t"
  "v_mfma_f32_16x16x32_bf16 a[16:19], v[144:147], v[116:119], a[16:19]\n\t"
  "ds_read_b128 a[208:211], %[ka0] offset:40960\n\t"
  "v_mfma_f32_16x16x32_bf16 a[20:23], v[144:147], v[120:123], a[20:23]\n\t"
  "v_exp_f32 v80, v80\n\t"
  "v_mfma_f32_16x16x32_bf16 a[24:27], v[148:151], v[116:119], a[24:27]\n\t"
  "buffer_load_dwordx4 %[wos], %[srdo], 0 offen offset:0 lds\n\t"
  "ds_read_b128 a[212:215], %[ka1] offset:40960\n\t"
  "v_mfma_f32_16x16x32_bf16 a[28:31], v[148:151], v[120:123], a[28:31]\n\t"
  "ds_read_b128 a[216:219], %[ka0] offset:45056\n\t"
  "v_mfma_f32_16x16x32_bf16 a[32:35], v[152:155], v[116:119], a[32:35]\n\t"
  "v_exp_f32 v81, v81\n\t"
  "v_mfma_f32_16x16x32_bf16 a[36:39], v[152:155], v[120:123], a[36:39]\n\t"
  "ds_read_b128 a[220:223], %[ka1] offset:45056\n\t"
  "ds_read_b128 a[224:227], %[ka0] offset:34816\n\t"
  "v_mfma_f32_16x16x32_bf16 a[40:43], v[156:159], v[116:119], a[40:43]\n\t"
  "v_exp_f32 v82, v82\n\t"
  "v_mfma_f32_16x16x32_bf16 a[44:47], v[156:159], v[120:123], a[44:47]\n\t"
  "ds_read_b128 a[228:231], %[ka1] offset:34816\n\t"
  "buffer_load_dwordx4 %[wos], %[srdo], %[c6144] offen offset:1024 lds\n\t"
  "v_mfma_f32_16x16x32_bf16 a[48:51], v[160:163], v[116:119], a[48:51]\n\t"
  "ds_read_b128 a[232:235], %[ka0] offset:38912\n\t"
  "v_mfma_f32_16x16x32_bf16 a[52:55], v[160:163], v[120:123], a[52:55]\n\t"
  "v_cvt_pk_bf16_f32 v124, v80, v81\n\t"
  "ds_read_b128 a[236:239], %[ka1] offset:38912\n\t"
  "v_mfma_f32_16x16x32_bf16 a[56:59], v[164:167], v[116:119], a[56:59]\n\t"
  "ds_read_b128 a[240:243], %[ka0] offset:43008\n\t"
  "v_mfma_f32_16x16x32_bf16 a[60:63], v[164:167], v[120:123], a[60:63]\n\t"
  "v_exp_f32 v83, v83\n\t"
  "v_mfma_f32_16x16x32_bf16 v[104:107], v[112:115], v[116:119], v[104:107]\n\t"
  "ds_read_b128 a[244:247], %[ka1] offset:43008\n\t"
  "buffer_load_dwordx4 %[wos], %[srdo], %[c12288] offen offset:2048 lds\n\t"
  "v_mfma_f32_16x16x32_bf16 v[108:111], v[112:115], v[120:123], v[108:111]\n\t"
  "ds_read_b128 a[248:251], %[ka0] offset:47104\n\t"
  "v_mfma_f32_16x16x32_bf16 a[64:67], v[168:171], v[116:119], a[64:67]\n\t"
  "v_exp_f32 v84, v84\n\t"
  "v_mfma_f32_16x16x32_bf16 a[68:71], v[168:171], v[120:123], a[68:71]\n\t"
  "ds_read_b128 a[252:255], %[ka1] offset:47104\n\t"
  "ds_read_b64_tr_b16 v[136:137], %[va0] offset:16384\n\t"
  "v_mfma_f32_16x16x32_bf16 a[72:75], v[172:175], v[116:119], a[72:75]\n\t"
  "v_cvt_pk_bf16_f32 v125, v82, v83\n\t"
  "ds_read_b64_tr_b16 v[138:139], %[va0] offset:18432\n\t"
  "v_mfma_f32_16x16x32_bf16 a[76:79], v[172:175], v[120:123], a[76:79]\n\t"
  "buffer_load_dwordx4 %[wos], %[srdo], %[c18432] offen offset:3072 lds\n\t"
  "ds_read_b64_tr_b16 v[140:141], %[va1] offset:16384\n\t"
  "v_mfma_f32_16x16x32_bf16 a[80:83], v[176:179], v[116:119], a[80:83]\n\t"
  "v_exp_f32 v85, v85\n\t"
  "v_mfma_f32_16x16x32_bf16 a[84:87], v[176:179], v[120:123], a[84:87]\n\t"
  "ds_read_b64_tr_b16 v[142:143], %[va1] offset:18432\n\t"
  "v_mfma_f32_16x16x32_bf16 a[88:91], v[180:183], v[116:119], a[88:91]\n\t"
  "ds_read_b64_tr_b16 v[144:145], %[va0] offset:16896\n\t"
  "v_mfma_f32_16x16x32_bf16 a[92:95], v[180:183], v[120:123], a[92:95]\n\t"
  "v_exp_f32 v86, v86\n\t"
  "v_mfma_f32_16x16x32_bf16 a[96:99], v[184:187], v[116:119], a[96:99]\n\t"
  "ds_read_b64_tr_b16 v[146:147], %[va0] offset:18944\n\t"
  "s_add_u32 m0, %[m0o], 4096\n\t"
  "v_mfma_f32_16x16x32_bf16 a[100:103], v[184:187], v[120:123], a[100:103]\n\t"
  "ds_read_b64_tr_b16 v[148:149], %[va1] offset:16896\n\t"
  "v_cvt_pk_bf16_f32 v128, v84, v85\n\t"
  "v_mfma_f32_16x16x32_bf16 a[104:107], v[188:191], v[116:119], a[104:107]\n\t"
  "ds_read_b64_tr_b16 v[150:151], %[va1] offset:18944\n\t"
  "v_mfma_f32_16x16x32_bf16 a[108:111], v[188:191], v[120:123], a[108:111]\n\t"
  "v_exp_f32 v87, v87\n\t"
  "v_mfma_f32_16x16x32_bf16 a[112:115], v[192:195], v[116:119], a[112:115]\n\t"
  "ds_read_b64_tr_b16 v[152:153], %[va0] offset:20480\n\t"
  "ds_read_b64_tr_b16 v[154:155], %[va0] offset:22528\n\t"
  "v_mfma_f32_16x16x32_bf16 a[116:119], v[192:195], v[120:123], a[116:119]\n\t"
  "buffer_load_dwordx4 %[wos], %[srdo], %[c28672] offen offset:0 lds\n\t"
  "ds_read_b64_tr_b16 v[156:157], %[va1] offset:20480\n\t"
  "v_mfma_f32_16x16x32_bf16 a[120:123], v[196:199], v[116:119], a[120:123]\n\t"
  "v_exp_f32 v88, v88\n\t"
  "v_mfma_f32_16x16x32_bf16 a[124:127], v[196:199], v[120:123], a[124:127]\n\t"
  "ds_read_b64_tr_b16 v[158:159], %[va1] offset:22528\n\t"
  "s_waitcnt lgkmcnt(6)\n\t"
  "v_mfma_f32_16x16x32_bf16 v[64:67], a[192:195], a[128:131], v[96:99]\n\t"
  "ds_read_b64_tr_b16 v[160:161], %[va0] offset:20992\n\t"
  "v_cvt_pk_bf16_f32 v129, v86, v87\n\t"
  "v_mfma_f32_16x16x32_bf16 v[68:71], a[192:195], a[160:163], v[100:103]\n\t"
  "ds_read_b64_tr_b16 v[162:163], %[va0] offset:23040\n\t"
  "v_mfma_f32_16x16x32_bf16 v[64:67], a[196:199], a[132:135], v[64:67]\n\t"
  "v_exp_f32 v89, v89\n\t"
  "v_mfma_f32_16x16x32_bf16 v[68:71], a[196:199], a[164:167], v[68:71]\n\t"
  "buffer_load_dwordx4 %[wos], %[srdo], %[c34816] offen offset:1024 lds\n\t"
  "ds_read_b64_tr_b16 v[164:165], %[va1] offset:20992\n\t"
  "v_mfma_f32_16x16x32_bf16 v[64:67], a[200:203], a[136:139], v[64:67]\n\t"
  "ds_read_b64_tr_b16 v[166:167], %[va1] offset:23040\n\t"
  "v_mfma_f32_16x16x32_bf16 v[68:71], a[200:203], a[168:171], v[68:71]\n\t"
  "v_exp_f32 v90, v90\n\t"
  "v_mfma_f32_16x16x32_bf16 v[64:67], a[204:207], a[140:143], v[64:67]\n\t"
  "ds_read_b64_tr_b16 v[168:169], %[va0] offset:24576\n\t"
  "ds_read_b64_tr_b16 v[170:171], %[va0] offset:26624\n\t"
  "v_mfma_f32_16x16x32_bf16 v[68:71], a[204:207], a[172:175], v[68:71]\n\t"
  "buffer_load_dwordx4 %[wos], %[srdo], %[c40960] offen offset:2048 lds\n\t"
  "v_cvt_pk_bf16_f32 v126, v88, v89\n\t"
  "v_mfma_f32_16x16x32_bf16 v[64:67], a[208:211], a[144:147], v[64:67]\n\t"
  "ds_read_b64_tr_b16 v[172:173], %[va1] offset:24576\n\t"
  "ds_read_b64_tr_b16 v[174:175], %[va1] offset:26624\n\t"
  "v_mfma_f32_16x16x32_bf16 v[68:71], a[208:211], a[176:179], v[68:71]\n\t"
  "v_exp_f32 v91, v91\n\t"
  "v_mfma_f32_16x16x32_bf16 v[64:67], a[212:215], a[148:151], v[64:67]\n\t"
  "ds_read_b64_tr_b16 v[176:177], %[va0] offset:25088\n\t"
  "v_mfma_f32_16x16x32_bf16 v[68:71], a[212:215], a[180:183], v[68:71]\n\t"
  "ds_read_b64_tr_b16 v[178:179], %[va0] offset:27136\n\t"
  "v_mfma_f32_16x16x32_bf16 v[64:67], a[216:219], a[152:155], v[64:67]\n\t"
  "v_exp_f32 v92, v92\n\t"
  "v_mfma_f32_16x16x32_bf16 v[68:71], a[216:219], a[184:187], v[68:71]\n\t"
  "buffer_load_dwordx4 %[wos], %[srdo], %[c47104] offen offset:3072 lds\n\t"
  "ds_read_b64_tr_b16 v[180:181], %[va1] offset:25088\n\t"
  "v_mfma_f32_16x16x32_bf16 v[64:67], a[220:223], a[156:159], v[64:67]\n\t"
  "ds_read_b64_tr_b16 v[182:183], %[va1] offset:27136\n\t"
  "v_cvt_pk_bf16_f32 v127, v90, v91\n\t"
  "v_mfma_f32_16x16x32_bf16 v[68:71], a[220:223], a[188:191], v[68:71]\n\t"
  "ds_read_b64_tr_b16 v[184:185], %[va0] offset:28672\n\t"
  "v_mfma_f32_16x16x32_bf16 v[72:75], a[224:227], a[128:131], v[96:99]\n\t"
  "v_exp_f32 v93, v93\n\t"
  "v_mfma_f32_16x16x32_bf16 v[76:79], a[224:227], a[160:163], v[100:103]\n\t"
  "ds_read_b64_tr_b16 v[186:187], %[va0] offset:30720\n\t"
  "v_add_u32 %[wos], 0x80, %[wos]\n\t"
  "v_mfma_f32_16x16x32_bf16 v[72:75], a[228:231], a[132:135], v[72:75]\n\t"
  "ds_read_b64_tr_b16 v[188:189], %[va1] offset:28672\n\t"
  "ds_read_b64_tr_b16 v[190:191], %[va1] offset:30720\n\t"
  "v_mfma_f32_16x16x32_bf16 v[76:79], a[228:231], a[164:167], v[76:79]\n\t"
  "v_exp_f32 v94, v94\n\t"
  "v_mfma_f32_16x16x32_bf16 v[72:75], a[232:235], a[136:139], v[72:75]\n\t"
  "ds_read_b64_tr_b16 v[192:193], %[va0] offset:29184\n\t"
  "v_mfma_f32_16x16x32_bf16 v[76:79], a[232:235], a[168:171], v[76:79]\n\t"
  "ds_read_b64_tr_b16 v[194:195], %[va0] offset:31232\n\t"
  "v_cvt_pk_bf16_f32 v130, v92, v93\n\t"
  "v_mfma_f32_16x16x32_bf16 v[72:75], a[236:239], a[140:143], v[72:75]\n\t"
  "ds_read_b64_tr_b16 v[196:197], %[va1] offset:29184\n\t"
  "v_mfma_f32_16x16x32_bf16 v[76:79], a[236:239], a[172:175], v[76:79]\n\t"
  "v_exp_f32 v95, v95\n\t"
  "v_mfma_f32_16x16x32_bf16 v[72:75], a[240:243], a[144:147], v[72:75]\n\t"
  "ds_read_b64_tr_b16 v[198:199], %[va1] offset:31232\n\t"
  "v_mfma_f32_16x16x32_bf16 v[76:79], a[240:243], a[176:179], v[76:79]\n\t"
  "v_mfma_f32_16x16x32_bf16 v[72:75], a[244:247], a[148:151], v[72:75]\n\t"
  "v_cvt_pk_bf16_f32 v131, v94, v95\n\t"
  "v_mfma_f32_16x16x32_bf16 v[76:79], a[244:247], a[180:183], v[76:79]\n\t"
  "v_mfma_f32_16x16x32_bf16 v[72:75], a[248:251], a[152:155], v[72:75]\n\t"
  "v_mfma_f32_16x16x32_bf16 v[76:79], a[248:251], a[184:187], v[76:79]\n\t"
  "v_mfma_f32_16x16x32_bf16 v[72:75], a[252:255], a[156:159], v[72:75]\n\t"
  "v_mfma_f32_16x16x32_bf16 v[76:79], a[252:255], a[188:191], v[76:79]\n\t"
  "s_waitcnt vmcnt(16) lgkmcnt(0)\n\t"
  "s_barrier\n\t"
  "v_mfma_f32_16x16x32_bf16 a[0:3], v[136:139], v[124:127], a[0:3]\n\t"
  "ds_read_b128 a[192:195], %[ka0] offset:49152\n\t"
  "ds_read_b128 a[196:199], %[ka1] offset:49152\n\t"
  "v_mfma_f32_16x16x32_bf16 a[4:7], v[136:139], v[128:131], a[4:7]\n\t"
  "ds_read_b128 a[200:203], %[ka0] offset:53248\n\t"
  "v_mfma_f32_16x16x32_bf16 a[8:11], v[140:143], v[124:127], a[8:11]\n\t"
  "v_mfma_f32_16x16x32_bf16 a[12:15], v[140:143], v[128:131], a[12:15]\n\t"
  "ds_read_b128 a[204:207], %[ka1] offset:53248\n\t"
  "v_mfma_f32_16x16x32_bf16 a[16:19], v[144:147], v[124:127], a[16:19]\n\t"
  "ds_read_b128 a[208:211], %[ka0] offset:57344\n\t"
  "v_mfma_f32_16x16x32_bf16 a[20:23], v[144:147], v[128:131], a[20:23]\n\t"
  "v_exp_f32 v64, v64\n\t"
  "v_mfma_f32_16x16x32_bf16 a[24:27], v[148:151], v[124:127], a[24:27]\n\t"
  "ds_read_b128 a[212:215], %[ka1] offset:57344\n\t"
  "ds_read_b128 a[216:219], %[ka0] offset:61440\n\t"
  "v_mfma_f32_16x16x32_bf16 a[28:31], v[148:151], v[128:131], a[28:31]\n\t"
  "v_exp_f32 v65, v65\n\t"
  "v_mfma_f32_16x16x32_bf16 a[32:35], v[152:155], v[124:127], a[32:35]\n\t"
  "ds_read_b128 a[220:223], %[ka1] offset:61440\n\t"
  "v_mfma_f32_16x16x32_bf16 a[36:39], v[152:155], v[128:131], a[36:39]\n\t"
  "ds_read_b128 a[224:227], %[ka0] offset:51200\n\t"
  "v_mfma_f32_16x16x32_bf16 a[40:43], v[156:159], v[124:127], a[40:43]\n\t"
  "v_exp_f32 v66, v66\n\t"
  "v_mfma_f32_16x16x32_bf16 a[44:47], v[156:159], v[128:131], a[44:47]\n\t"
  "ds_read_b128 a[228:231], %[ka1] offset:51200\n\t"
  "ds_read_b128 a[232:235], %[ka0] offset:55296\n\t"
  "v_mfma_f32_16x16x32_bf16 a[48:51], v[160:163], v[124:127], a[48:51]\n\t"
  "v_mfma_f32_16x16x32_bf16 a[52:55], v[160:163], v[128:131], a[52:55]\n\t"
  "v_cvt_pk_bf16_f32 v116, v64, v65\n\t"
  "ds_read_b128 a[236:239], %[ka1] offset:55296\n\t"
  "v_mfma_f32_16x16x32_bf16 a[56:59], v[164:167], v[124:127], a[56:59]\n\t"
  "ds_read_b128 a[240:243], %[ka0] offset:59392\n\t"
  "v_mfma_f32_16x16x32_bf16 a[60:63], v[164:167], v[128:131], a[60:63]\n\t"
  "v_exp_f32 v67, v67\n\t"
  "v_mfma_f32_16x16x32_bf16 v[104:107], v[112:115], v[124:127], v[104:107]\n\t"
  "ds_read_b128 a[244:247], %[ka1] offset:59392\n\t"
  "ds_read_b128 a[248:251], %[ka0] offset:63488\n\t"
  "v_mfma_f32_16x16x32_bf16 v[108:111], v[112:115], v[128:131], v[108:111]\n\t"
  "v_exp_f32 v68, v68\n\t"
  "v_mfma_f32_16x16x32_bf16 a[64:67], v[168:171], v[124:127], a[64:67]\n\t"
  "ds_read_b128 a[252:255], %[ka1] offset:63488\n\t"
  "v_mfma_f32_16x16x32_bf16 a[68:71], v[168:171], v[128:131], a[68:71]\n\t"
  "ds_read_b64_tr_b16 v[136:137], %[va0] offset:32768\n\t"
  "v_mfma_f32_16x16x32_bf16 a[72:75], v[172:175], v[124:127], a[72:75]\n\t"
  "v_cvt_pk_bf16_f32 v117, v66, v67\n\t"
  "ds_read_b64_tr_b16 v[138:139], %[va0] offset:34816\n\t"
  "v_mfma_f32_16x16x32_bf16 a[76:79], v[172:175], v[128:131], a[76:79]\n\t"
  "v_exp_f32 v69, v69\n\t"
  "v_mfma_f32_16x16x32_bf16 a[80:83], v[176:179], v[124:127], a[80:83]\n\t"
  "ds_read_b64_tr_b16 v[140:141], %[va1] offset:32768\n\t"
  "v_mfma_f32_16x16x32_bf16 a[84:87], v[176:179], v[128:131], a[84:87]\n\t"
  "ds_read_b64_tr_b16 v[142:143], %[va1] offset:34816\n\t"
  "v_mfma_f32_16x16x32_bf16 a[88:91], v[180:183], v[124:127], a[88:91]\n\t"
  "ds_read_b64_tr_b16 v[144:145], %[va0] offset:33280\n\t"
  "v_mfma_f32_16x16x32_bf16 a[92:95], v[180:183], v[128:131], a[92:95]\n\t"
  "v_exp_f32 v70, v70\n\t"
  "v_mfma_f32_16x16x32_bf16 a[96:99], v[184:187], v[124:127], a[96:99]\n\t"
  "ds_read_b64_tr_b16 v[146:147], %[va0] offset:35328\n\t"
  "ds_read_b64_tr_b16 v[148:149], %[va1] offset:33280\n\t"
  "v_mfma_f32_16x16x32_bf16 a[100:103], v[184:187], v[128:131], a[100:103]\n\t"
  "v_cvt_pk_bf16_f32 v120, v68, v69\n\t"
  "v_mfma_f32_16x16x32_bf16 a[104:107], v[188:191], v[124:127], a[104:107]\n\t"
  "ds_read_b64_tr_b16 v[150:151], %[va1] offset:35328\n\t"
  "v_mfma_f32_16x16x32_bf16 a[108:111], v[188:191], v[128:131], a[108:111]\n\t"
  "v_exp_f32 v71, v71\n\t"
  "v_mfma_f32_16x16x32_bf16 a[112:115], v[192:195], v[124:127], a[112:115]\n\t"
  "ds_read_b64_tr_b16 v[152:153], %[va0] offset:36864\n\t"
  "ds_read_b64_tr_b16 v[154:155], %[va0] offset:38912\n\t"
  "v_mfma_f32_16x16x32_bf16 a[116:119], v[192:195], v[128:131], a[116:119]\n\t"
  "v_exp_f32 v72, v72\n\t"
  "v_mfma_f32_16x16x32_bf16 a[120:123], v[196:199], v[124:127], a[120:123]\n\t"
  "ds_read_b64_tr_b16 v[156:157], %[va1] offset:36864\n\t"
  "ds_read_b64_tr_b16 v[158:159], %[va1] offset:38912\n\t"
  "v_mfma_f32_16x16x32_bf16 a[124:127], v[196:199], v[128:131], a[124:127]\n\t"
  "s_waitcnt lgkmcnt(5)\n\t"
  "v_mfma_f32_16x16x32_bf16 v[80:83], a[192:195], a[128:131], v[96:99]\n\t"
  "ds_read_b64_tr_b16 v[160:161], %[va0] offset:37376\n\t"
  "v_cvt_pk_bf16_f32 v121, v70, v71\n\t"
  "v_mfma_f32_16x16x32_bf16 v[84:87], a[192:195], a[160:163], v[100:103]\n\t"
  "ds_read_b64_tr_b16 v[162:163], %[va0] offset:39424\n\t"
  "v_mfma_f32_16x16x32_bf16 v[80:83], a[196:199], a[132:135], v[80:83]\n\t"
  "v_exp_f32 v73, v73\n\t"
  "v_mfma_f32_16x16x32_bf16 v[84:87], a[196:199], a[164:167], v[84:87]\n\t"
  "ds_read_b64_tr_b16 v[164:165], %[va1] offset:37376\n\t"
  "ds_read_b64_tr_b16 v[166:167], %[va1] offset:39424\n\t"
  "v_mfma_f32_16x16x32_bf16 v[80:83], a[200:203], a[136:139], v[80:83]\n\t"
  "v_exp_f32 v74, v74\n\t"
  "v_mfma_f32_16x16x32_bf16 v[84:87], a[200:203], a[168:171], v[84:87]\n\t"
  "ds_read_b64_tr_b16 v[168:169], %[va0] offset:40960\n\t"
  "v_mfma_f32_16x16x32_bf16 v[80:83], a[204:207], a[140:143], v[80:83]\n\t"
  "ds_read_b64_tr_b16 v[170:171], %[va0] offset:43008\n\t"
  "v_mfma_f32_16x16x32_bf16 v[84:87], a[204:207], a[172:175], v[84:87]\n\t"
  "v_cvt_pk_bf16_f32 v118, v72, v73\n\t"
  "ds_read_b64_tr_b16 v[172:173], %[va1] offset:40960\n\t"
  "v_mfma_f32_16x16x32_bf16 v[80:83], a[208:211], a[144:147], v[80:83]\n\t"
  "ds_read_b64_tr_b16 v[174:175], %[va1] offset:43008\n\t"
  "v_mfma_f32_16x16x32_bf16 v[84:87], a[208:211], a[176:179], v[84:87]\n\t"
  "v_exp_f32 v75, v75\n\t"
  "v_mfma_f32_16x16x32_bf16 v[80:83], a[212:215], a[148:151], v[80:83]\n\t"
  "ds_read_b64_tr_b16 v[176:177], %[va0] offset:41472\n\t"
  "v_mfma_f32_16x16x32_bf16 v[84:87], a[212:215], a[180:183], v[84:87]\n\t"
  "ds_read_b64_tr_b16 v[178:179], %[va0] offset:43520\n\t"
  "v_mfma_f32_16x16x32_bf16 v[80:83], a[216:219], a[152:155], v[80:83]\n\t"
  "v_exp_f32 v76, v76\n\t"
  "v_mfma_f32_16x16x32_bf16 v[84:87], a[216:219], a[184:187], v[84:87]\n\t"
  "ds_read_b64_tr_b16 v[180:181], %[va1] offset:41472\n\t"
  "ds_read_b64_tr_b16 v[182:183], %[va1] offset:43520\n\t"
  "v_mfma_f32_16x16x32_bf16 v[80:83], a[220:223], a[156:159], v[80:83]\n\t"
  "v_cvt_pk_bf16_f32 v119, v74, v75\n\t"
  "v_mfma_f32_16x16x32_bf16 v[84:87], a[220:223], a[188:191], v[84:87]\n\t"
  "ds_read_b64_tr_b16 v[184:185], %[va0] offset:45056\n\t"
  "v_mfma_f32_16x16x32_bf16 v[88:91], a[224:227], a[128:131], v[96:99]\n\t"
  "v_exp_f32 v77, v77\n\t"
  "v_mfma_f32_16x16x32_bf16 v[92:95], a[224:227], a[160:163], v[100:103]\n\t"
  "ds_read_b64_tr_b16 v[186:187], %[va0] offset:47104\n\t"
  "ds_read_b64_tr_b16 v[188:189], %[va1] offset:45056\n\t"
  "v_mfma_f32_16x16x32_bf16 v[88:91], a[228:231], a[132:135], v[88:91]\n\t"
  "ds_read_b64_tr_b16 v[190:191], %[va1] offset:47104\n\t"
  "v_mfma_f32_16x16x32_bf16 v[92:95], a[228:231], a[164:167], v[92:95]\n\t"
  "v_exp_f32 v78, v78\n\t"
  "v_mfma_f32_16x16x32_bf16 v[88:91], a[232:235], a[136:139], v[88:91]\n\t"
  "ds_read_b64_tr_b16 v[192:193], %[va0] offset:45568\n\t"
  "v_mfma_f32_16x16x32_bf16 v[92:95], a[232:235], a[168:171], v[92:95]\n\t"
  "ds_read_b64_tr_b16 v[194:195], %[va0] offset:47616\n\t"
  "v_cvt_pk_bf16_f32 v122, v76, v77\n\t"
  "v_mfma_f32_16x16x32_bf16 v[88:91], a[236:239], a[140:143], v[88:91]\n\t"
  "ds_read_b64_tr_b16 v[196:197], %[va1] offset:45568\n\t"
  "v_mfma_f32_16x16x32_bf16 v[92:95], a[236:239], a[172:175], v[92:95]\n\t"
  "v_exp_f32 v79, v79\n\t"
  "v_mfma_f32_16x16x32_bf16 v[88:91], a[240:243], a[144:147], v[88:91]\n\t"
  "ds_read_b64_tr_b16 v[198:199], %[va1] offset:47616\n\t"
  "v_mfma_f32_16x16x32_bf16 v[92:95], a[240:243], a[176:179], v[92:95]\n\t"
  "v_mfma_f32_16x16x32_bf16 v[88:91], a[244:247], a[148:151], v[88:91]\n\t"
  "v_cvt_pk_bf16_f32 v123, v78, v79\n\t"
  "v_mfma_f32_16x16x32_bf16 v[92:95], a[244:247], a[180:183], v[92:95]\n\t"
  "v_mfma_f32_16x16x32_bf16 v[88:91], a[248:251], a[152:155], v[88:91]\n\t"
  "v_mfma_f32_16x16x32_bf16 v[92:95], a[248:251], a[184:187], v[92:95]\n\t"
  "v_mfma_f32_16x16x32_bf16 v[88:91], a[252:255], a[156:159], v[88:91]\n\t"
  "v_mfma_f32_16x16x32_bf16 v[92:95], a[252:255], a[188:191], v[92:95]\n\t"
  "s_waitcnt vmcnt(8) lgkmcnt(0)\n\t"
  "s_barrier\n\t"
  "v_mfma_f32_16x16x32_bf16 a[0:3], v[136:139], v[116:119], a[0:3]\n\t"
  "s_add_u32 m0, %[m0o], 32768\n\t"
  "v_mfma_f32_16x16x32_bf16 a[4:7], v[136:139], v[120:123], a[4:7]\n\t"
  "ds_read_b64_tr_b16 v[136:137], %[va0] offset:49152\n\t"
  "ds_read_b64_tr_b16 v[138:139], %[va0] offset:51200\n\t"
  "v_mfma_f32_16x16x32_bf16 a[8:11], v[140:143], v[116:119], a[8:11]\n\t"
  "v_mfma_f32_16x16x32_bf16 a[12:15], v[140:143], v[120:123], a[12:15]\n\t"
  "ds_read_b64_tr_b16 v[140:141], %[va1] offset:49152\n\t"
  "v_mfma_f32_16x16x32_bf16 a[16:19], v[144:147], v[116:119], a[16:19]\n\t"
  "v_mfma_f32_16x16x32_bf16 a[20:23], v[144:147], v[120:123], a[20:23]\n\t"
  "v_exp_f32 v80, v80\n\t"
  "v_mfma_f32_16x16x32_bf16 a[24:27], v[148:151], v[116:119], a[24:27]\n\t"
  "ds_read_b64_tr_b16 v[142:143], %[va1] offset:51200\n\t"
  "buffer_load_dwordx4 %[wos], %[srdo], 0 offen offset:0 lds\n\t"
  "v_mfma_f32_16x16x32_bf16 a[28:31], v[148:151], v[120:123], a[28:31]\n\t"
  "ds_read_b64_tr_b16 v[144:145], %[va0] offset:49664\n\t"
  "v_mfma_f32_16x16x32_bf16 a[32:35], v[152:155], v[116:119], a[32:35]\n\t"
  "v_exp_f32 v81, v81\n\t"
  "v_mfma_f32_16x16x32_bf16 a[36:39], v[152:155], v[120:123], a[36:39]\n\t"
  "ds_read_b64_tr_b16 v[146:147], %[va0] offset:51712\n\t"
  "v_mfma_f32_16x16x32_bf16 a[40:43], v[156:159], v[116:119], a[40:43]\n\t"
  "v_exp_f32 v82, v82\n\t"
  "v_mfma_f32_16x16x32_bf16 a[44:47], v[156:159], v[120:123], a[44:47]\n\t"
  "ds_read_b64_tr_b16 v[148:149], %[va1] offset:49664\n\t"
  "buffer_load_dwordx4 %[wos], %[srdo], %[c6144] offen offset:1024 lds\n\t"
  "v_mfma_f32_16x16x32_bf16 a[48:51], v[160:163], v[116:119], a[48:51]\n\t"
  "ds_read_b64_tr_b16 v[150:151], %[va1] offset:51712\n\t"
  "v_mfma_f32_16x16x32_bf16 a[52:55], v[160:163], v[120:123], a[52:55]\n\t"
  "v_cvt_pk_bf16_f32 v124, v80, v81\n\t"
  "v_mfma_f32_16x16x32_bf16 a[56:59], v[164:167], v[116:119], a[56:59]\n\t"
  "ds_read_b64_tr_b16 v[152:153], %[va0] offset:53248\n\t"
  "v_mfma_f32_16x16x32_bf16 a[60:63], v[164:167], v[120:123], a[60:63]\n\t"
  "v_exp_f32 v83, v83\n\t"
  "v_mfma_f32_16x16x32_bf16 v[104:107], v[112:115], v[116:119], v[104:107]\n\t"
  "buffer_load_dwordx4 %[wos], %[srdo], %[c12288] offen offset:2048 lds\n\t"
  "ds_read_b64_tr_b16 v[154:155], %[va0] offset:55296\n\t"
  "v_mfma_f32_16x16x32_bf16 v[108:111], v[112:115], v[120:123], v[108:111]\n\t"
  "v_exp_f32 v84, v84\n\t"
  "v_mfma_f32_16x16x32_bf16 a[64:67], v[168:171], v[116:119], a[64:67]\n\t"
  "ds_read_b64_tr_b16 v[156:157], %[va1] offset:53248\n\t"
  "v_mfma_f32_16x16x32_bf16 a[68:71], v[168:171], v[120:123], a[68:71]\n\t"
  "ds_read_b64_tr_b16 v[158:159], %[va1] offset:55296\n\t"
  "v_mfma_f32_16x16x32_bf16 a[72:75], v[172:175], v[116:119], a[72:75]\n\t"
  "v_cvt_pk_bf16_f32 v125, v82, v83\n\t"
  "buffer_load_dwordx4 %[wos], %[srdo], %[c18432] offen offset:3072 lds\n\t"
  "v_mfma_f32_16x16x32_bf16 a[76:79], v[172:175], v[120:123], a[76:79]\n\t"
  "v_exp_f32 v85, v85\n\t"
  "v_mfma_f32_16x16x32_bf16 a[80:83], v[176:179], v[116:119], a[80:83]\n\t"
  "ds_read_b64_tr_b16 v[160:161], %[va0] offset:53760\n\t"
  "v_mfma_f32_16x16x32_bf16 a[84:87], v[176:179], v[120:123], a[84:87]\n\t"
  "ds_read_b64_tr_b16 v[162:163], %[va0] offset:55808\n\t"
  "v_mfma_f32_16x16x32_bf16 a[88:91], v[180:183], v[116:119], a[88:91]\n\t"
  "v_exp_f32 v86, v86\n\t"
  "v_mfma_f32_16x16x32_bf16 a[92:95], v[180:183], v[120:123], a[92:95]\n\t"
  "ds_read_b64_tr_b16 v[164:165], %[va1] offset:53760\n\t"
  "s_add_u32 m0, %[m0o], 36864\n\t"
  "v_mfma_f32_16x16x32_bf16 a[96:99], v[184:187], v[116:119], a[96:99]\n\t"
  "v_mfma_f32_16x16x32_bf16 a[100:103], v[184:187], v[120:123], a[100:103]\n\t"
  "v_cvt_pk_bf16_f32 v128, v84, v85\n\t"
  "ds_read_b64_tr_b16 v[166:167], %[va1] offset:55808\n\t"
  "v_mfma_f32_16x16x32_bf16 a[104:107], v[188:191], v[116:119], a[104:107]\n\t"
  "v_exp_f32 v87, v87\n\t"
  "v_mfma_f32_16x16x32_bf16 a[108:111], v[188:191], v[120:123], a[108:111]\n\t"
  "ds_read_b64_tr_b16 v[168:169], %[va0] offset:57344\n\t"
  "v_mfma_f32_16x16x32_bf16 a[112:115], v[192:195], v[116:119], a[112:115]\n\t"
  "v_mfma_f32_16x16x32_bf16 a[116:119], v[192:195], v[120:123], a[116:119]\n\t"
  "buffer_load_dwordx4 %[wos], %[srdo], %[c28672] offen offset:0 lds\n\t"
  "ds_read_b64_tr_b16 v[170:171], %[va0] offset:59392\n\t"
  "v_mfma_f32_16x16x32_bf16 a[120:123], v[196:199], v[116:119], a[120:123]\n\t"
  "v_exp_f32 v88, v88\n\t"
  "v_mfma_f32_16x16x32_bf16 a[124:127], v[196:199], v[120:123], a[124:127]\n\t"
  "ds_read_b64_tr_b16 v[172:173], %[va1] offset:57344\n\t"
  "v_cvt_pk_bf16_f32 v129, v86, v87\n\t"
  "ds_read_b64_tr_b16 v[174:175], %[va1] offset:59392\n\t"
  "v_exp_f32 v89, v89\n\t"
  "buffer_load_dwordx4 %[wos], %[srdo], %[c34816] offen offset:1024 lds\n\t"
  "ds_read_b64_tr_b16 v[176:177], %[va0] offset:57856\n\t"
  "ds_read_b64_tr_b16 v[178:179], %[va0] offset:59904\n\t"
  "v_exp_f32 v90, v90\n\t"
  "ds_read_b64_tr_b16 v[180:181], %[va1] offset:57856\n\t"
  "buffer_load_dwordx4 %[wos], %[srdo], %[c40960] offen offset:2048 lds\n\t"
  "v_cvt_pk_bf16_f32 v126, v88, v89\n\t"
  "ds_read_b64_tr_b16 v[182:183], %[va1] offset:59904\n\t"
  "v_exp_f32 v91, v91\n\t"
  "ds_read_b64_tr_b16 v[184:185], %[va0] offset:61440\n\t"
  "v_exp_f32 v92, v92\n\t"
  "ds_read_b64_tr_b16 v[186:187], %[va0] offset:63488\n\t"
  "buffer_load_dwordx4 %[wos], %[srdo], %[c47104] offen offset:3072 lds\n\t"
  "ds_read_b64_tr_b16 v[188:189], %[va1] offset:61440\n\t"
  "v_cvt_pk_bf16_f32 v127, v90, v91\n\t"
  "ds_read_b64_tr_b16 v[190:191], %[va1] offset:63488\n\t"
  "v_exp_f32 v93, v93\n\t"
  "v_add_u32 %[wos], 0x80, %[wos]\n\t"
  "ds_read_b64_tr_b16 v[192:193], %[va0] offset:61952\n\t"
  "v_exp_f32 v94, v94\n\t"
  "ds_read_b64_tr_b16 v[194:195], %[va0] offset:64000\n\t"
  "ds_read_b64_tr_b16 v[196:197], %[va1] offset:61952\n\t"
  "v_cvt_pk_bf16_f32 v130, v92, v93\n\t"
  "ds_read_b64_tr_b16 v[198:199], %[va1] offset:64000\n\t"
  "v_exp_f32 v95, v95\n\t"
  "s_nop 0\n\t"
  "v_cvt_pk_bf16_f32 v131, v94, v95\n\t"
  "s_waitcnt lgkmcnt(0)\n\t"
  "s_nop 1\n\t"
  "v_mfma_f32_16x16x32_bf16 a[0:3], v[136:139], v[124:127], a[0:3]\n\t"
  "v_mfma_f32_16x16x32_bf16 a[4:7], v[136:139], v[128:131], a[4:7]\n\t"
  "v_mfma_f32_16x16x32_bf16 a[8:11], v[140:143], v[124:127], a[8:11]\n\t"
  "v_mfma_f32_16x16x32_bf16 a[12:15], v[140:143], v[128:131], a[12:15]\n\t"
  "v_mfma_f32_16x16x32_bf16 a[16:19], v[144:147], v[124:127], a[16:19]\n\t"
  "v_mfma_f32_16x16x32_bf16 a[20:23], v[144:147], v[128:131], a[20:23]\n\t"
  "v_mfma_f32_16x16x32_bf16 a[24:27], v[148:151], v[124:127], a[24:27]\n\t"
  "v_mfma_f32_16x16x32_bf16 a[28:31], v[148:151], v[128:131], a[28:31]\n\t"
  "v_mfma_f32_16x16x32_bf16 a[32:35], v[152:155], v[124:127], a[32:35]\n\t"
  "v_mfma_f32_16x16x32_bf16 a[36:39], v[152:155], v[128:131], a[36:39]\n\t"
  "v_mfma_f32_16x16x32_bf16 a[40:43], v[156:159], v[124:127], a[40:43]\n\t"
  "v_mfma_f32_16x16x32_bf16 a[44:47], v[156:159], v[128:131], a[44:47]\n\t"
  "v_mfma_f32_16x16x32_bf16 a[48:51], v[160:163], v[124:127], a[48:51]\n\t"
  "v_mfma_f32_16x16x32_bf16 a[52:55], v[160:163], v[128:131], a[52:55]\n\t"
  "v_mfma_f32_16x16x32_bf16 a[56:59], v[164:167], v[124:127], a[56:59]\n\t"
  "v_mfma_f32_16x16x32_bf16 a[60:63], v[164:167], v[128:131], a[60:63]\n\t"
  "v_mfma_f32_16x16x32_bf16 v[104:107], v[112:115], v[124:127], v[104:107]\n\t"
  "v_mfma_f32_16x16x32_bf16 v[108:111], v[112:115], v[128:131], v[108:111]\n\t"
  "v_mfma_f32_16x16x32_bf16 a[64:67], v[168:171], v[124:127], a[64:67]\n\t"
  "v_mfma_f32_16x16x32_bf16 a[68:71], v[168:171], v[128:131], a[68:71]\n\t"
  "v_mfma_f32_16x16x32_bf16 a[72:75], v[172:175], v[124:127], a[72:75]\n\t"
  "v_mfma_f32_16x16x32_bf16 a[76:79], v[172:175], v[128:131], a[76:79]\n\t"
  "v_mfma_f32_16x16x32_bf16 a[80:83], v[176:179], v[124:127], a[80:83]\n\t"
  "v_mfma_f32_16x16x32_bf16 a[84:87], v[176:179], v[128:131], a[84:87]\n\t"
  "v_mfma_f32_16x16x32_bf16 a[88:91], v[180:183], v[124:127], a[88:91]\n\t"
  "v_mfma_f32_16x16x32_bf16 a[92:95], v[180:183], v[128:131], a[92:95]\n\t"
  "v_mfma_f32_16x16x32_bf16 a[96:99], v[184:187], v[124:127], a[96:99]\n\t"
  "v_mfma_f32_16x16x32_bf16 a[100:103], v[184:187], v[128:131], a[100:103]\n\t"
  "v_mfma_f32_16x16x32_bf16 a[104:107], v[188:191], v[124:127], a[104:107]\n\t"
  "v_mfma_f32_16x16x32_bf16 a[108:111], v[188:191], v[128:131], a[108:111]\n\t"
  "v_mfma_f32_16x16x32_bf16 a[112:115], v[192:195], v[124:127], a[112:115]\n\t"
  "v_mfma_f32_16x16x32_bf16 a[116:119], v[192:195], v[128:131], a[116:119]\n\t"
  "v_mfma_f32_16x16x32_bf16 a[120:123], v[196:199], v[124:127], a[120:123]\n\t"
  "v_mfma_f32_16x16x32_bf16 a[124:127], v[196:199], v[128:131], a[124:127]\n\t"
  "s_nop 15\n\t"
  "v_max_f32 v200, v104, v108\n\t"
  "s_mov_b32 s46, 0x5f800000\n\t"
  "v_cmp_nge_f32 vcc, s46, v200\n\t"
  "v_cmp_u_f32 s[48:49], v104, v108\n\t"
  "s_nop 3\n\t"
  "s_or_b64 vcc, vcc, s[48:49]\n\t"
  "s_cmp_lg_u64 vcc, 0\n\t"
  "s_cselect_b32 s46, 1, 0\n\t"
  "s_or_b32 s46, s46, s50\n\t"
  "v_mov_b32 v200, s46\n\t"
  "ds_write_b32 %[fla], v200\n\t"
  "s_waitcnt lgkmcnt(0)\n\t"
  "s_barrier\n\t"
  "ds_read_b128 v[204:207], %[flb]\n\t"
  "s_waitcnt lgkmcnt(0)\n\t"
  "v_or3_b32 v200, v204, v205, v206\n\t"
  "v_or_b32 v200, v200, v207\n\t"
  "s_nop 0\n\t"
  "v_readfirstlane_b32 s46, v200\n\t"
  "s_nop 3\n\t"
  "s_andn2_b32 s46, s46, s47\n\t"
  "s_cbranch_scc0 .Lend_%=\n\t"
  "s_mov_b32 s47, 1\n\t"
  "s_mov_b32 s46, 0\n\t"
  "s_waitcnt vmcnt(0)\n\t"
  "v_mov_b32 v208, 0xff800000\n\t"
  "v_mov_b32 v209, 0xff800000\n\t"
  ".Lmx_%=:\n\t"
  "buffer_load_dwordx4 v[136:139], %[kmx], %[srdk], s46 offen offset:0\n\t"
  "buffer_load_dwordx4 v[140:143], %[kmx], %[srdk], s46 offen offset:64\n\t"
  "buffer_load_dwordx4 v[144:147], %[kmx], %[srdk], s46 offen offset:128\n\t"
  "buffer_load_dwordx4 v[148:151], %[kmx], %[srdk], s46 offen offset:192\n\t"
  "buffer_load_dwordx4 v[152:155], %[kmx], %[srdk], s46 offen offset:256\n\t"
  "buffer_load_dwordx4 v[156:159], %[kmx], %[srdk], s46 offen offset:320\n\t"
  "buffer_load_dwordx4 v[160:163], %[kmx], %[srdk], s46 offen offset:384\n\t"
  "buffer_load_dwordx4 v[164:167], %[kmx], %[srdk], s46 offen offset:448\n\t"
  "s_waitcnt vmcnt(0)\n\t"
  "v_mfma_f32_16x16x32_bf16 v[64:67], v[136:139], a[128:131], 0\n\t"
  "v_mfma_f32_16x16x32_bf16 v[68:71], v[136:139], a[160:163], 0\n\t"
  "v_mfma_f32_16x16x32_bf16 v[64:67], v[140:143], a[132:135], v[64:67]\n\t"
  "v_mfma_f32_16x16x32_bf16 v[68:71], v[140:143], a[164:167], v[68:71]\n\t"
  "v_mfma_f32_16x16x32_bf16 v[64:67], v[144:147], a[136:139], v[64:67]\n\t"
  "v_mfma_f32_16x16x32_bf16 v[68:71], v[144:147], a[168:171], v[68:71]\n\t"
  "v_mfma_f32_16x16x32_bf16 v[64:67], v[148:151], a[140:143], v[64:67]\n\t"
  "v_mfma_f32_16x16x32_bf16 v[68:71], v[148:151], a[172:175], v[68:71]\n\t"
  "v_mfma_f32_16x16x32_bf16 v[64:67], v[152:155], a[144:147], v[64:67]\n\t"
  "v_mfma_f32_16x16x32_bf16 v[68:71], v[152:155], a[176:179], v[68:71]\n\t"
  "v_mfma_f32_16x16x32_bf16 v[64:67], v[156:159], a[148:151], v[64:67]\n\t"
  "v_mfma_f32_16x16x32_bf16 v[68:71], v[156:159], a[180:183], v[68:71]\n\t"
  "v_mfma_f32_16x16x32_bf16 v[64:67], v[160:163], a[152:155], v[64:67]\n\t"
  "v_mfma_f32_16x16x32_bf16 v[68:71], v[160:163], a[184:187], v[68:71]\n\t"
  "v_mfma_f32_16x16x32_bf16 v[64:67], v[164:167], a[156:159], v[64:67]\n\t"
  "v_mfma_f32_16x16x32_bf16 v[68:71], v[164:167], a[188:191], v[68:71]\n\t"
  "s_nop 15\n\t"
  "v_max3_f32 v208, v208, v64, v65\n\t"
  "v_max3_f32 v208, v208, v66, v67\n\t"
  "v_max3_f32 v209, v209, v68, v69\n\t"
  "v_max3_f32 v209, v209, v70, v71\n\t"
  "s_add_u32 s46, s46, 8192\n\t"
  "s_cmp_lt_u32 s46, 0x100000\n\t"
  "s_cbranch_scc1 .Lmx_%=\n\t"
  "v_mov_b32 %[mi0], v208\n\t"
  "v_mov_b32 %[mi1], v209\n\t"
  "v_subrev_u32 %[ks0], 0x100000, %[ks0]\n\t"
  "v_subrev_u32 %[ks1], 0x100000, %[ks1]\n\t"
  "v_subrev_u32 %[vs], 0x100000, %[vs]\n\t"
  "v_subrev_u32 %[wos], 384, %[wos]\n\t"
  "s_barrier\n\t"
  "s_mov_b32 s40, 3072\n\t"
  "s_mov_b32 s41, 6144\n\t"
  "s_mov_b32 s42, 9216\n\t"
  "s_add_u32 m0, %[m0k], 0\n\t"
  "s_nop 0\n\t"
  "buffer_load_dwordx4 %[ks0], %[srdk], 0 offen offset:0 lds\n\t"
  "buffer_load_dwordx4 %[ks1], %[srdk], s40 offen offset:1024 lds\n\t"
  "buffer_load_dwordx4 %[ks0], %[srdk], s41 offen offset:2048 lds\n\t"
  "buffer_load_dwordx4 %[ks1], %[srdk], s42 offen offset:3072 lds\n\t"
  "v_add_u32 %[ks0], 0x4000, %[ks0]\n\t"
  "v_add_u32 %[ks1], 0x4000, %[ks1]\n\t"
  "s_add_u32 m0, %[m0k], 16384\n\t"
  "s_nop 0\n\t"
  "buffer_load_dwordx4 %[ks0], %[srdk], 0 offen offset:0 lds\n\t"
  "buffer_load_dwordx4 %[ks1], %[srdk], s40 offen offset:1024 lds\n\t"
  "buffer_load_dwordx4 %[ks0], %[srdk], s41 offen offset:2048 lds\n\t"
  "buffer_load_dwordx4 %[ks1], %[srdk], s42 offen offset:3072 lds\n\t"
  "v_add_u32 %[ks0], 0x4000, %[ks0]\n\t"
  "v_add_u32 %[ks1], 0x4000, %[ks1]\n\t"
  "s_add_u32 m0, %[m0k], 32768\n\t"
  "s_nop 0\n\t"
  "buffer_load_dwordx4 %[ks0], %[srdk], 0 offen offset:0 lds\n\t"
  "buffer_load_dwordx4 %[ks1], %[srdk], s40 offen offset:1024 lds\n\t"
  "buffer_load_dwordx4 %[ks0], %[srdk], s41 offen offset:2048 lds\n\t"
  "buffer_load_dwordx4 %[ks1], %[srdk], s42 offen offset:3072 lds\n\t"
  "v_add_u32 %[ks0], 0x4000, %[ks0]\n\t"
  "v_add_u32 %[ks1], 0x4000, %[ks1]\n\t"
  "s_add_u32 m0, %[m0k], 49152\n\t"
  "s_nop 0\n\t"
  "buffer_load_dwordx4 %[ks0], %[srdk], 0 offen offset:0 lds\n\t"
  "buffer_load_dwordx4 %[ks1], %[srdk], s40 offen offset:1024 lds\n\t"
  "buffer_load_dwordx4 %[ks0], %[srdk], s41 offen offset:2048 lds\n\t"
  "buffer_load_dwordx4 %[ks1], %[srdk], s42 offen offset:3072 lds\n\t"
  "v_add_u32 %[ks0], 0x4000, %[ks0]\n\t"
  "v_add_u32 %[ks1], 0x4000, %[ks1]\n\t"
  "s_add_u32 m0, %[m0v], 0\n\t"
  "s_nop 0\n\t"
  "buffer_load_dwordx4 %[vs], %[srdv], 0 offen offset:0 lds\n\t"
  "buffer_load_dwordx4 %[vs], %[srdv], s40 offen offset:1024 lds\n\t"
  "buffer_load_dwordx4 %[vs], %[srdv], s41 offen offset:2048 lds\n\t"
  "buffer_load_dwordx4 %[vs], %[srdv], s42 offen offset:3072 lds\n\t"
  "v_add_u32 %[vs], 0x4000, %[vs]\n\t"
  "s_add_u32 m0, %[m0v], 16384\n\t"
  "s_nop 0\n\t"
  "buffer_load_dwordx4 %[vs], %[srdv], 0 offen offset:0 lds\n\t"
  "buffer_load_dwordx4 %[vs], %[srdv], s40 offen offset:1024 lds\n\t"
  "buffer_load_dwordx4 %[vs], %[srdv], s41 offen offset:2048 lds\n\t"
  "buffer_load_dwordx4 %[vs], %[srdv], s42 offen offset:3072 lds\n\t"
  "v_add_u32 %[vs], 0x4000, %[vs]\n\t"
  "s_waitcnt vmcnt(0)\n\t"
  "s_branch .Lstart_%=\n\t"
  ".Lrare0_%=:\n\t"
  "s_mov_b32 s45, 0\n\t"
  "s_branch .Lrarec_%=\n\t"
  ".Lrare1_%=:\n\t"
  "s_mov_b32 s45, 1\n\t"
  "s_branch .Lrarec_%=\n\t"
  ".Lrarec_%=:\n\t"
  "s_nop 15\n\t"
  "v_max_f32 v206, v104, v108\n\t"
  "s_mov_b32 s46, 0x5f800000\n\t"
  "v_cmp_nge_f32 vcc, s46, v206\n\t"
  "s_nop 3\n\t"
  "s_cmp_lg_u64 vcc, 0\n\t"
  "s_cselect_b32 s46, 1, 0\n\t"
  "s_or_b32 s50, s50, s46\n\t"
  "v_frexp_exp_i32_f32 v206, v104\n\t"
  "v_max_i32 v206, 0, v206\n\t"
  "v_cvt_f32_i32 v204, v206\n\t"
  "v_sub_u32 v206, 0, v206\n\t"
  "v_ldexp_f32 v202, 1.0, v206\n\t"
  "v_sub_f32 v96, v96, v204\n\t"
  "v_mul_f32 v104, v104, v202\n\t"
  "v_sub_f32 v64, v64, v204\n\t"
  "v_sub_f32 v72, v72, v204\n\t"
  "v_sub_f32 v97, v97, v204\n\t"
  "v_mul_f32 v105, v105, v202\n\t"
  "v_sub_f32 v65, v65, v204\n\t"
  "v_sub_f32 v73, v73, v204\n\t"
  "v_sub_f32 v98, v98, v204\n\t"
  "v_mul_f32 v106, v106, v202\n\t"
  "v_sub_f32 v66, v66, v204\n\t"
  "v_sub_f32 v74, v74, v204\n\t"
  "v_sub_f32 v99, v99, v204\n\t"
  "v_mul_f32 v107, v107, v202\n\t"
  "v_sub_f32 v67, v67, v204\n\t"
  "v_sub_f32 v75, v75, v204\n\t"
  "v_lshlrev_b32 v207, 16, v124\n\t"
  "v_and_b32 v208, 0xffff0000, v124\n\t"
  "v_mul_f32 v207, v207, v202\n\t"
  "v_mul_f32 v208, v208, v202\n\t"
  "v_cvt_pk_bf16_f32 v124, v207, v208\n\t"
  "v_lshlrev_b32 v207, 16, v125\n\t"
  "v_and_b32 v208, 0xffff0000, v125\n\t"
  "v_mul_f32 v207, v207, v202\n\t"
  "v_mul_f32 v208, v208, v202\n\t"
  "v_cvt_pk_bf16_f32 v125, v207, v208\n\t"
  "v_lshlrev_b32 v207, 16, v126\n\t"
  "v_and_b32 v208, 0xffff0000, v126\n\t"
  "v_mul_f32 v207, v207, v202\n\t"
  "v_mul_f32 v208, v208, v202\n\t"
  "v_cvt_pk_bf16_f32 v126, v207, v208\n\t"
  "v_lshlrev_b32 v207, 16, v127\n\t"
  "v_and_b32 v208, 0xffff0000, v127\n\t"
  "v_mul_f32 v207, v207, v202\n\t"
  "v_mul_f32 v208, v208, v202\n\t"
  "v_cvt_pk_bf16_f32 v127, v207, v208\n\t"
  "v_frexp_exp_i32_f32 v206, v108\n\t"
  "v_max_i32 v206, 0, v206\n\t"
  "v_cvt_f32_i32 v205, v206\n\t"
  "v_sub_u32 v206, 0, v206\n\t"
  "v_ldexp_f32 v203, 1.0, v206\n\t"
  "v_sub_f32 v100, v100, v205\n\t"
  "v_mul_f32 v108, v108, v203\n\t"
  "v_sub_f32 v68, v68, v205\n\t"
  "v_sub_f32 v76, v76, v205\n\t"
  "v_sub_f32 v101, v101, v205\n\t"
  "v_mul_f32 v109, v109, v203\n\t"
  "v_sub_f32 v69, v69, v205\n\t"
  "v_sub_f32 v77, v77, v205\n\t"
  "v_sub_f32 v102, v102, v205\n\t"
  "v_mul_f32 v110, v110, v203\n\t"
  "v_sub_f32 v70, v70, v205\n\t"
  "v_sub_f32 v78, v78, v205\n\t"
  "v_sub_f32 v103, v103, v205\n\t"
  "v_mul_f32 v111, v111, v203\n\t"
  "v_sub_f32 v71, v71, v205\n\t"
  "v_sub_f32 v79, v79, v205\n\t"
  "v_lshlrev_b32 v207, 16, v128\n\t"
  "v_and_b32 v208, 0xffff0000, v128\n\t"
  "v_mul_f32 v207, v207, v203\n\t"
  "v_mul_f32 v208, v208, v203\n\t"
  "v_cvt_pk_bf16_f32 v128, v207, v208\n\t"
  "v_lshlrev_b32 v207, 16, v129\n\t"
  "v_and_b32 v208, 0xffff0000, v129\n\t"
  "v_mul_f32 v207, v207, v203\n\t"
  "v_mul_f32 v208, v208, v203\n\t"
  "v_cvt_pk_bf16_f32 v129, v207, v208\n\t"
  "v_lshlrev_b32 v207, 16, v130\n\t"
  "v_and_b32 v208, 0xffff0000, v130\n\t"
  "v_mul_f32 v207, v207, v203\n\t"
  "v_mul_f32 v208, v208, v203\n\t"
  "v_cvt_pk_bf16_f32 v130, v207, v208\n\t"
  "v_lshlrev_b32 v207, 16, v131\n\t"
  "v_and_b32 v208, 0xffff0000, v131\n\t"
  "v_mul_f32 v207, v207, v203\n\t"
  "v_mul_f32 v208, v208, v203\n\t"
  "v_cvt_pk_bf16_f32 v131, v207, v208\n\t"
  "v_accvgpr_read_b32 v209, a0\n\t"
  "v_mul_f32 v209, v209, v202\n\t"
  "v_accvgpr_write_b32 a0, v209\n\t"
  "v_accvgpr_read_b32 v210, a1\n\t"
  "v_mul_f32 v210, v210, v202\n\t"
  "v_accvgpr_write_b32 a1, v210\n\t"
  "v_accvgpr_read_b32 v211, a2\n\t"
  "v_mul_f32 v211, v211, v202\n\t"
  "v_accvgpr_write_b32 a2, v211\n\t"
  "v_accvgpr_read_b32 v212, a3\n\t"
  "v_mul_f32 v212, v212, v202\n\t"
  "v_accvgpr_write_b32 a3, v212\n\t"
  "v_accvgpr_read_b32 v209, a4\n\t"
  "v_mul_f32 v209, v209, v203\n\t"
  "v_accvgpr_write_b32 a4, v209\n\t"
  "v_accvgpr_read_b32 v210, a5\n\t"
  "v_mul_f32 v210, v210, v203\n\t"
  "v_accvgpr_write_b32 a5, v210\n\t"
  "v_accvgpr_read_b32 v211, a6\n\t"
  "v_mul_f32 v211, v211, v203\n\t"
  "v_accvgpr_write_b32 a6, v211\n\t"
  "v_accvgpr_read_b32 v212, a7\n\t"
  "v_mul_f32 v212, v212, v203\n\t"
  "v_accvgpr_write_b32 a7, v212\n\t"
  "v_accvgpr_read_b32 v209, a8\n\t"
  "v_mul_f32 v209, v209, v202\n\t"
  "v_accvgpr_write_b32 a8, v209\n\t"
  "v_accvgpr_read_b32 v210, a9\n\t"
  "v_mul_f32 v210, v210, v202\n\t"
  "v_accvgpr_write_b32 a9, v210\n\t"
  "v_accvgpr_read_b32 v211, a10\n\t"
  "v_mul_f32 v211, v211, v202\n\t"
  "v_accvgpr_write_b32 a10, v211\n\t"
  "v_accvgpr_read_b32 v212, a11\n\t"
  "v_mul_f32 v212, v212, v202\n\t"
  "v_accvgpr_write_b32 a11, v212\n\t"
  "v_accvgpr_read_b32 v209, a12\n\t"
  "v_mul_f32 v209, v209, v203\n\t"
  "v_accvgpr_write_b32 a12, v209\n\t"
  "v_accvgpr_read_b32 v210, a13\n\t"
  "v_mul_f32 v210, v210, v203\n\t"
  "v_accvgpr_write_b32 a13, v210\n\t"
  "v_accvgpr_read_b32 v211, a14\n\t"
  "v_mul_f32 v211, v211, v203\n\t"
  "v_accvgpr_write_b32 a14, v211\n\t"
  "v_accvgpr_read_b32 v212, a15\n\t"
  "v_mul_f32 v212, v212, v203\n\t"
  "v_accvgpr_write_b32 a15, v212\n\t"
  "v_accvgpr_read_b32 v209, a16\n\t"
  "v_mul_f32 v209, v209, v202\n\t"
  "v_accvgpr_write_b32 a16, v209\n\t"
  "v_accvgpr_read_b32 v210, a17\n\t"
  "v_mul_f32 v210, v210, v202\n\t"
  "v_accvgpr_write_b32 a17, v210\n\t"
  "v_accvgpr_read_b32 v211, a18\n\t"
  "v_mul_f32 v211, v211, v202\n\t"
  "v_accvgpr_write_b32 a18, v211\n\t"
  "v_accvgpr_read_b32 v212, a19\n\t"
  "v_mul_f32 v212, v212, v202\n\t"
  "v_accvgpr_write_b32 a19, v212\n\t"
  "v_accvgpr_read_b32 v209, a20\n\t"
  "v_mul_f32 v209, v209, v203\n\t"
  "v_accvgpr_write_b32 a20, v209\n\t"
  "v_accvgpr_read_b32 v210, a21\n\t"
  "v_mul_f32 v210, v210, v203\n\t"
  "v_accvgpr_write_b32 a21, v210\n\t"
  "v_accvgpr_read_b32 v211, a22\n\t"
  "v_mul_f32 v211, v211, v203\n\t"
  "v_accvgpr_write_b32 a22, v211\n\t"
  "v_accvgpr_read_b32 v212, a23\n\t"
  "v_mul_f32 v212, v212, v203\n\t"
  "v_accvgpr_write_b32 a23, v212\n\t"
  "v_accvgpr_read_b32 v209, a24\n\t"
  "v_mul_f32 v209, v209, v202\n\t"
  "v_accvgpr_write_b32 a24, v209\n\t"
  "v_accvgpr_read_b32 v210, a25\n\t"
  "v_mul_f32 v210, v210, v202\n\t"
  "v_accvgpr_write_b32 a25, v210\n\t"
  "v_accvgpr_read_b32 v211, a26\n\t"
  "v_mul_f32 v211, v211, v202\n\t"
  "v_accvgpr_write_b32 a26, v211\n\t"
  "v_accvgpr_read_b32 v212, a27\n\t"
  "v_mul_f32 v212, v212, v202\n\t"
  "v_accvgpr_write_b32 a27, v212\n\t"
  "v_accvgpr_read_b32 v209, a28\n\t"
  "v_mul_f32 v209, v209, v203\n\t"
  "v_accvgpr_write_b32 a28, v209\n\t"
  "v_accvgpr_read_b32 v210, a29\n\t"
  "v_mul_f32 v210, v210, v203\n\t"
  "v_accvgpr_write_b32 a29, v210\n\t"
  "v_accvgpr_read_b32 v211, a30\n\t"
  "v_mul_f32 v211, v211, v203\n\t"
  "v_accvgpr_write_b32 a30, v211\n\t"
  "v_accvgpr_read_b32 v212, a31\n\t"
  "v_mul_f32 v212, v212, v203\n\t"
  "v_accvgpr_write_b32 a31, v212\n\t"
  "v_accvgpr_read_b32 v209, a32\n\t"
  "v_mul_f32 v209, v209, v202\n\t"
  "v_accvgpr_write_b32 a32, v209\n\t"
  "v_accvgpr_read_b32 v210, a33\n\t"
  "v_mul_f32 v210, v210, v202\n\t"
  "v_accvgpr_write_b32 a33, v210\n\t"
  "v_accvgpr_read_b32 v211, a34\n\t"
  "v_mul_f32 v211, v211, v202\n\t"
  "v_accvgpr_write_b32 a34, v211\n\t"
  "v_accvgpr_read_b32 v212, a35\n\t"
  "v_mul_f32 v212, v212, v202\n\t"
  "v_accvgpr_write_b32 a35, v212\n\t"
  "v_accvgpr_read_b32 v209, a36\n\t"
  "v_mul_f32 v209, v209, v203\n\t"
  "v_accvgpr_write_b32 a36, v209\n\t"
  "v_accvgpr_read_b32 v210, a37\n\t"
  "v_mul_f32 v210, v210, v203\n\t"
  "v_accvgpr_write_b32 a37, v210\n\t"
  "v_accvgpr_read_b32 v211, a38\n\t"
  "v_mul_f32 v211, v211, v203\n\t"
  "v_accvgpr_write_b32 a38, v211\n\t"
  "v_accvgpr_read_b32 v212, a39\n\t"
  "v_mul_f32 v212, v212, v203\n\t"
  "v_accvgpr_write_b32 a39, v212\n\t"
  "v_accvgpr_read_b32 v209, a40\n\t"
  "v_mul_f32 v209, v209, v202\n\t"
  "v_accvgpr_write_b32 a40, v209\n\t"
  "v_accvgpr_read_b32 v210, a41\n\t"
  "v_mul_f32 v210, v210, v202\n\t"
  "v_accvgpr_write_b32 a41, v210\n\t"
  "v_accvgpr_read_b32 v211, a42\n\t"
  "v_mul_f32 v211, v211, v202\n\t"
  "v_accvgpr_write_b32 a42, v211\n\t"
  "v_accvgpr_read_b32 v212, a43\n\t"
  "v_mul_f32 v212, v212, v202\n\t"
  "v_accvgpr_write_b32 a43, v212\n\t"
  "v_accvgpr_read_b32 v209, a44\n\t"
  "v_mul_f32 v209, v209, v203\n\t"
  "v_accvgpr_write_b32 a44, v209\n\t"
  "v_accvgpr_read_b32 v210, a45\n\t"
  "v_mul_f32 v210, v210, v203\n\t"
  "v_accvgpr_write_b32 a45, v210\n\t"
  "v_accvgpr_read_b32 v211, a46\n\t"
  "v_mul_f32 v211, v211, v203\n\t"
  "v_accvgpr_write_b32 a46, v211\n\t"
  "v_accvgpr_read_b32 v212, a47\n\t"
  "v_mul_f32 v212, v212, v203\n\t"
  "v_accvgpr_write_b32 a47, v212\n\t"
  "v_accvgpr_read_b32 v209, a48\n\t"
  "v_mul_f32 v209, v209, v202\n\t"
  "v_accvgpr_write_b32 a48, v209\n\t"
  "v_accvgpr_read_b32 v210, a49\n\t"
  "v_mul_f32 v210, v210, v202\n\t"
  "v_accvgpr_write_b32 a49, v210\n\t"
  "v_accvgpr_read_b32 v211, a50\n\t"
  "v_mul_f32 v211, v211, v202\n\t"
  "v_accvgpr_write_b32 a50, v211\n\t"
  "v_accvgpr_read_b32 v212, a51\n\t"
  "v_mul_f32 v212, v212, v202\n\t"
  "v_accvgpr_write_b32 a51, v212\n\t"
  "v_accvgpr_read_b32 v209, a52\n\t"
  "v_mul_f32 v209, v209, v203\n\t"
  "v_accvgpr_write_b32 a52, v209\n\t"
  "v_accvgpr_read_b32 v210, a53\n\t"
  "v_mul_f32 v210, v210, v203\n\t"
  "v_accvgpr_write_b32 a53, v210\n\t"
  "v_accvgpr_read_b32 v211, a54\n\t"
  "v_mul_f32 v211, v211, v203\n\t"
  "v_accvgpr_write_b32 a54, v211\n\t"
  "v_accvgpr_read_b32 v212, a55\n\t"
  "v_mul_f32 v212, v212, v203\n\t"
  "v_accvgpr_write_b32 a55, v212\n\t"
  "v_accvgpr_read_b32 v209, a56\n\t"
  "v_mul_f32 v209, v209, v202\n\t"
  "v_accvgpr_write_b32 a56, v209\n\t"
  "v_accvgpr_read_b32 v210, a57\n\t"
  "v_mul_f32 v210, v210, v202\n\t"
  "v_accvgpr_write_b32 a57, v210\n\t"
  "v_accvgpr_read_b32 v211, a58\n\t"
  "v_mul_f32 v211, v211, v202\n\t"
  "v_accvgpr_write_b32 a58, v211\n\t"
  "v_accvgpr_read_b32 v212, a59\n\t"
  "v_mul_f32 v212, v212, v202\n\t"
  "v_accvgpr_write_b32 a59, v212\n\t"
  "v_accvgpr_read_b32 v209, a60\n\t"
  "v_mul_f32 v209, v209, v203\n\t"
  "v_accvgpr_write_b32 a60, v209\n\t"
  "v_accvgpr_read_b32 v210, a61\n\t"
  "v_mul_f32 v210, v210, v203\n\t"
  "v_accvgpr_write_b32 a61, v210\n\t"
  "v_accvgpr_read_b32 v211, a62\n\t"
  "v_mul_f32 v211, v211, v203\n\t"
  "v_accvgpr_write_b32 a62, v211\n\t"
  "v_accvgpr_read_b32 v212, a63\n\t"
  "v_mul_f32 v212, v212, v203\n\t"
  "v_accvgpr_write_b32 a63, v212\n\t"
  "v_accvgpr_read_b32 v209, a64\n\t"
  "v_mul_f32 v209, v209, v202\n\t"
  "v_accvgpr_write_b32 a64, v209\n\t"
  "v_accvgpr_read_b32 v210, a65\n\t"
  "v_mul_f32 v210, v210, v202\n\t"
  "v_accvgpr_write_b32 a65, v210\n\t"
  "v_accvgpr_read_b32 v211, a66\n\t"
  "v_mul_f32 v211, v211, v202\n\t"
  "v_accvgpr_write_b32 a66, v211\n\t"
  "v_accvgpr_read_b32 v212, a67\n\t"
  "v_mul_f32 v212, v212, v202\n\t"
  "v_accvgpr_write_b32 a67, v212\n\t"
  "v_accvgpr_read_b32 v209, a68\n\t"
  "v_mul_f32 v209, v209, v203\n\t"
  "v_accvgpr_write_b32 a68, v209\n\t"
  "v_accvgpr_read_b32 v210, a69\n\t"
  "v_mul_f32 v210, v210, v203\n\t"
  "v_accvgpr_write_b32 a69, v210\n\t"
  "v_accvgpr_read_b32 v211, a70\n\t"
  "v_mul_f32 v211, v211, v203\n\t"
  "v_accvgpr_write_b32 a70, v211\n\t"
  "v_accvgpr_read_b32 v212, a71\n\t"
  "v_mul_f32 v212, v212, v203\n\t"
  "v_accvgpr_write_b32 a71, v212\n\t"
  "v_accvgpr_read_b32 v209, a72\n\t"
  "v_mul_f32 v209, v209, v202\n\t"
  "v_accvgpr_write_b32 a72, v209\n\t"
  "v_accvgpr_read_b32 v210, a73\n\t"
  "v_mul_f32 v210, v210, v202\n\t"
  "v_accvgpr_write_b32 a73, v210\n\t"
  "v_accvgpr_read_b32 v211, a74\n\t"
  "v_mul_f32 v211, v211, v202\n\t"
  "v_accvgpr_write_b32 a74, v211\n\t"
  "v_accvgpr_read_b32 v212, a75\n\t"
  "v_mul_f32 v212, v212, v202\n\t"
  "v_accvgpr_write_b32 a75, v212\n\t"
  "v_accvgpr_read_b32 v209, a76\n\t"
  "v_mul_f32 v209, v209, v203\n\t"
  "v_accvgpr_write_b32 a76, v209\n\t"
  "v_accvgpr_read_b32 v210, a77\n\t"
  "v_mul_f32 v210, v210, v203\n\t"
  "v_accvgpr_write_b32 a77, v210\n\t"
  "v_accvgpr_read_b32 v211, a78\n\t"
  "v_mul_f32 v211, v211, v203\n\t"
  "v_accvgpr_write_b32 a78, v211\n\t"
  "v_accvgpr_read_b32 v212, a79\n\t"
  "v_mul_f32 v212, v212, v203\n\t"
  "v_accvgpr_write_b32 a79, v212\n\t"
  "v_accvgpr_read_b32 v209, a80\n\t"
  "v_mul_f32 v209, v209, v202\n\t"
  "v_accvgpr_write_b32 a80, v209\n\t"
  "v_accvgpr_read_b32 v210, a81\n\t"
  "v_mul_f32 v210, v210, v202\n\t"
  "v_accvgpr_write_b32 a81, v210\n\t"
  "v_accvgpr_read_b32 v211, a82\n\t"
  "v_mul_f32 v211, v211, v202\n\t"
  "v_accvgpr_write_b32 a82, v211\n\t"
  "v_accvgpr_read_b32 v212, a83\n\t"
  "v_mul_f32 v212, v212, v202\n\t"
  "v_accvgpr_write_b32 a83, v212\n\t"
  "v_accvgpr_read_b32 v209, a84\n\t"
  "v_mul_f32 v209, v209, v203\n\t"
  "v_accvgpr_write_b32 a84, v209\n\t"
  "v_accvgpr_read_b32 v210, a85\n\t"
  "v_mul_f32 v210, v210, v203\n\t"
  "v_accvgpr_write_b32 a85, v210\n\t"
  "v_accvgpr_read_b32 v211, a86\n\t"
  "v_mul_f32 v211, v211, v203\n\t"
  "v_accvgpr_write_b32 a86, v211\n\t"
  "v_accvgpr_read_b32 v212, a87\n\t"
  "v_mul_f32 v212, v212, v203\n\t"
  "v_accvgpr_write_b32 a87, v212\n\t"
  "v_accvgpr_read_b32 v209, a88\n\t"
  "v_mul_f32 v209, v209, v202\n\t"
  "v_accvgpr_write_b32 a88, v209\n\t"
  "v_accvgpr_read_b32 v210, a89\n\t"
  "v_mul_f32 v210, v210, v202\n\t"
  "v_accvgpr_write_b32 a89, v210\n\t"
  "v_accvgpr_read_b32 v211, a90\n\t"
  "v_mul_f32 v211, v211, v202\n\t"
  "v_accvgpr_write_b32 a90, v211\n\t"
  "v_accvgpr_read_b32 v212, a91\n\t"
  "v_mul_f32 v212, v212, v202\n\t"
  "v_accvgpr_write_b32 a91, v212\n\t"
  "v_accvgpr_read_b32 v209, a92\n\t"
  "v_mul_f32 v209, v209, v203\n\t"
  "v_accvgpr_write_b32 a92, v209\n\t"
  "v_accvgpr_read_b32 v210, a93\n\t"
  "v_mul_f32 v210, v210, v203\n\t"
  "v_accvgpr_write_b32 a93, v210\n\t"
  "v_accvgpr_read_b32 v211, a94\n\t"
  "v_mul_f32 v211, v211, v203\n\t"
  "v_accvgpr_write_b32 a94, v211\n\t"
  "v_accvgpr_read_b32 v212, a95\n\t"
  "v_mul_f32 v212, v212, v203\n\t"
  "v_accvgpr_write_b32 a95, v212\n\t"
  "v_accvgpr_read_b32 v209, a96\n\t"
  "v_mul_f32 v209, v209, v202\n\t"
  "v_accvgpr_write_b32 a96, v209\n\t"
  "v_accvgpr_read_b32 v210, a97\n\t"
  "v_mul_f32 v210, v210, v202\n\t"
  "v_accvgpr_write_b32 a97, v210\n\t"
  "v_accvgpr_read_b32 v211, a98\n\t"
  "v_mul_f32 v211, v211, v202\n\t"
  "v_accvgpr_write_b32 a98, v211\n\t"
  "v_accvgpr_read_b32 v212, a99\n\t"
  "v_mul_f32 v212, v212, v202\n\t"
  "v_accvgpr_write_b32 a99, v212\n\t"
  "v_accvgpr_read_b32 v209, a100\n\t"
  "v_mul_f32 v209, v209, v203\n\t"
  "v_accvgpr_write_b32 a100, v209\n\t"
  "v_accvgpr_read_b32 v210, a101\n\t"
  "v_mul_f32 v210, v210, v203\n\t"
  "v_accvgpr_write_b32 a101, v210\n\t"
  "v_accvgpr_read_b32 v211, a102\n\t"
  "v_mul_f32 v211, v211, v203\n\t"
  "v_accvgpr_write_b32 a102, v211\n\t"
  "v_accvgpr_read_b32 v212, a103\n\t"
  "v_mul_f32 v212, v212, v203\n\t"
  "v_accvgpr_write_b32 a103, v212\n\t"
  "v_accvgpr_read_b32 v209, a104\n\t"
  "v_mul_f32 v209, v209, v202\n\t"
  "v_accvgpr_write_b32 a104, v209\n\t"
  "v_accvgpr_read_b32 v210, a105\n\t"
  "v_mul_f32 v210, v210, v202\n\t"
  "v_accvgpr_write_b32 a105, v210\n\t"
  "v_accvgpr_read_b32 v211, a106\n\t"
  "v_mul_f32 v211, v211, v202\n\t"
  "v_accvgpr_write_b32 a106, v211\n\t"
  "v_accvgpr_read_b32 v212, a107\n\t"
  "v_mul_f32 v212, v212, v202\n\t"
  "v_accvgpr_write_b32 a107, v212\n\t"
  "v_accvgpr_read_b32 v209, a108\n\t"
  "v_mul_f32 v209, v209, v203\n\t"
  "v_accvgpr_write_b32 a108, v209\n\t"
  "v_accvgpr_read_b32 v210, a109\n\t"
  "v_mul_f32 v210, v210, v203\n\t"
  "v_accvgpr_write_b32 a109, v210\n\t"
  "v_accvgpr_read_b32 v211, a110\n\t"
  "v_mul_f32 v211, v211, v203\n\t"
  "v_accvgpr_write_b32 a110, v211\n\t"
  "v_accvgpr_read_b32 v212, a111\n\t"
  "v_mul_f32 v212, v212, v203\n\t"
  "v_accvgpr_write_b32 a111, v212\n\t"
  "v_accvgpr_read_b32 v209, a112\n\t"
  "v_mul_f32 v209, v209, v202\n\t"
  "v_accvgpr_write_b32 a112, v209\n\t"
  "v_accvgpr_read_b32 v210, a113\n\t"
  "v_mul_f32 v210, v210, v202\n\t"
  "v_accvgpr_write_b32 a113, v210\n\t"
  "v_accvgpr_read_b32 v211, a114\n\t"
  "v_mul_f32 v211, v211, v202\n\t"
  "v_accvgpr_write_b32 a114, v211\n\t"
  "v_accvgpr_read_b32 v212, a115\n\t"
  "v_mul_f32 v212, v212, v202\n\t"
  "v_accvgpr_write_b32 a115, v212\n\t"
  "v_accvgpr_read_b32 v209, a116\n\t"
  "v_mul_f32 v209, v209, v203\n\t"
  "v_accvgpr_write_b32 a116, v209\n\t"
  "v_accvgpr_read_b32 v210, a117\n\t"
  "v_mul_f32 v210, v210, v203\n\t"
  "v_accvgpr_write_b32 a117, v210\n\t"
  "v_accvgpr_read_b32 v211, a118\n\t"
  "v_mul_f32 v211, v211, v203\n\t"
  "v_accvgpr_write_b32 a118, v211\n\t"
  "v_accvgpr_read_b32 v212, a119\n\t"
  "v_mul_f32 v212, v212, v203\n\t"
  "v_accvgpr_write_b32 a119, v212\n\t"
  "v_accvgpr_read_b32 v209, a120\n\t"
  "v_mul_f32 v209, v209, v202\n\t"
  "v_accvgpr_write_b32 a120, v209\n\t"
  "v_accvgpr_read_b32 v210, a121\n\t"
  "v_mul_f32 v210, v210, v202\n\t"
  "v_accvgpr_write_b32 a121, v210\n\t"
  "v_accvgpr_read_b32 v211, a122\n\t"
  "v_mul_f32 v211, v211, v202\n\t"
  "v_accvgpr_write_b32 a122, v211\n\t"
  "v_accvgpr_read_b32 v212, a123\n\t"
  "v_mul_f32 v212, v212, v202\n\t"
  "v_accvgpr_write_b32 a123, v212\n\t"
  "v_accvgpr_read_b32 v209, a124\n\t"
  "v_mul_f32 v209, v209, v203\n\t"
  "v_accvgpr_write_b32 a124, v209\n\t"
  "v_accvgpr_read_b32 v210, a125\n\t"
  "v_mul_f32 v210, v210, v203\n\t"
  "v_accvgpr_write_b32 a125, v210\n\t"
  "v_accvgpr_read_b32 v211, a126\n\t"
  "v_mul_f32 v211, v211, v203\n\t"
  "v_accvgpr_write_b32 a126, v211\n\t"
  "v_accvgpr_read_b32 v212, a127\n\t"
  "v_mul_f32 v212, v212, v203\n\t"
  "v_accvgpr_write_b32 a127, v212\n\t"
  "s_nop 3\n\t"
  "s_cmp_eq_u32 s45, 0\n\t"
  "s_cbranch_scc1 .Lret0_%=\n\t"
  "s_branch .Lret1_%=\n\t"
  ".Lend_%=:\n\t"
  "s_waitcnt vmcnt(0)\n\t"
      : "={a[0:15]}"(o[0]), "={a[16:31]}"(o[1]), "={a[32:47]}"(o[2]), "={a[48:63]}"(o[3]), "={a[64:79]}"(o[4]), "={a[80:95]}"(o[5]), "={a[96:111]}"(o[6]), "={a[112:127]}"(o[7]), "={v104}"(l0), "={v108}"(l1), [ks0] "+v"(ks0), [ks1] "+v"(ks1), [vs] "+v"(vs), [wos] "+v"(wos), [mi0] "+v"(mi0), [mi1] "+v"(mi1),
        "+{v[248:255]}"(hp)
      : "{a[128:143]}"(q[0]), "{a[144:159]}"(q[1]), "{a[160:175]}"(q[2]), "{a[176:191]}"(q[3]), "{v[216:231]}"(zp[0]), "{v[232:247]}"(zp[1]), "{v[48:49]}"(hq),
        [ka0] "v"(ka0), [ka1] "v"(ka1), [va0] "v"(va0), [va1] "v"(va1), [fla] "v"(fla), [flb] "v"(flb), [kmx] "v"(kmx), [cboff] "v"((lane & 7u) * 128u), [cwo] "v"((((idx & 3u) * 256u) + tid) * 128u),
        [srdk] "s"(srdk), [srdv] "s"(srdv), [srdo] "s"(srdo), [srdws] "s"(srdws), [srdp] "s"(srdp), [srdg] "s"(srdg),
        [m0k] "s"(m0k), [m0v] "s"(m0v), [m0o] "s"(m0o), [is2] "s"(is2u), [hs1] "s"(hs1u), [cntb] "s"(cntb), [wit] "s"(wit), [tag] "s"(1u), [hs2] "s"(hs2), [c4096] "s"(4096u), [c6144] "s"(6144u), [c12288] "s"(12288u), [c18432] "s"(18432u), [c28672] "s"(28672u), [c31744] "s"(31744u), [c34816] "s"(34816u), [c40960] "s"(40960u), [c47104] "s"(47104u)
      : "v64", "v65", "v66", "v67", "v68", "v69", "v70", "v71", "v72", "v73", "v74", "v75", "v76", "v77", "v78", "v79", "v80", "v81", "v82", "v83", "v84", "v85", "v86", "v87", "v88", "v89", "v90", "v91", "v92", "v93", "v94", "v95", "v96", "v97", "v98", "v99", "v100", "v101", "v102", "v103", "v105", "v106", "v107", "v109", "v110", "v111", "v112", "v113", "v114", "v115", "v116", "v117", "v118", "v119", "v120", "v121", "v122", "v123", "v124", "v125", "v126", "v127", "v128", "v129", "v130", "v131", "v132", "v133", "v134", "v135", "v136", "v137", "v138", "v139", "v140", "v141", "v142", "v143", "v144", "v145", "v146", "v147", "v148", "v149", "v150", "v151", "v152", "v153", "v154", "v155", "v156", "v157", "v158", "v159", "v160", "v161", "v162", "v163", "v164", "v165", "v166", "v167", "v168", "v169", "v170", "v171", "v172", "v173", "v174", "v175", "v176", "v177", "v178", "v179", "v180", "v181", "v182", "v183", "v184", "v185", "v186", "v187", "v188", "v189", "v190", "v191", "v192", "v193", "v194", "v195", "v196", "v197", "v198", "v199", "v200", "v201", "v202", "v203", "v204", "v205", "v206", "v207", "v208", "v209", "v210", "v211", "v212", "v213", "v214", "v215", "a192", "a193", "a194", "a195", "a196", "a197", "a198", "a199", "a200", "a201", "a202", "a203", "a204", "a205", "a206", "a207", "a208", "a209", "a210", "a211", "a212", "a213", "a214", "a215", "a216", "a217", "a218", "a219", "a220", "a221", "a222", "a223", "a224", "a225", "a226", "a227", "a228", "a229", "a230", "a231", "a232", "a233", "a234", "a235", "a236", "a237", "a238", "a239", "a240", "a241", "a242", "a243", "a244", "a245", "a246", "a247", "a248", "a249", "a250", "a251", "a252", "a253", "a254", "a255", "s40", "s41", "s42", "s43", "s44", "s45", "vcc", "scc", "memory", "s48", "s49", "s50", "s46", "s47", "v32", "v33", "v34", "v35", "v36", "v37", "v38", "v39", "v40", "v41", "v42", "v43", "v44", "v45", "v46", "v47");
  }
  TS(1, 4);
  u32x16 of[4];
  {
    const float inv[2] = {__builtin_amdgcn_rcpf(l0), __builtin_amdgcn_rcpf(l1)};
    #pragma unroll
    for (int qb = 0; qb < 2; ++qb)
      #pragma unroll
      for (int s = 0; s < 8; ++s) {
        const int ra = ((2 * s) * 2 + qb) * 4, rb = ((2 * s + 1) * 2 + qb) * 4, f = qb * 8 + s;
        of[f >> 2][(f & 3) * 4] = cvtpk(o[ra >> 4][ra & 15] * inv[qb], o[ra >> 4][(ra & 15) + 1] * inv[qb]);
        of[f >> 2][(f & 3) * 4 + 1] = cvtpk(o[ra >> 4][(ra & 15) + 2] * inv[qb], o[ra >> 4][(ra & 15) + 3] * inv[qb]);
        of[f >> 2][(f & 3) * 4 + 2] = cvtpk(o[rb >> 4][rb & 15] * inv[qb], o[rb >> 4][(rb & 15) + 1] * inv[qb]);
        of[f >> 2][(f & 3) * 4 + 3] = cvtpk(o[rb >> 4][(rb & 15) + 2] * inv[qb], o[rb >> 4][(rb & 15) + 3] * inv[qb]);
      }
  }
  TS(1, 5);
  unsigned lane_;
  asm volatile("v_mbcnt_lo_u32_b32 %0, -1, 0\n\tv_mbcnt_hi_u32_b32 %0, -1, %0" : "=v"(lane_));
  const unsigned c_ = lane_ & 15u, g_ = lane_ >> 4, r7_ = lane_ >> 3, swz_ = (c_ >> 1) & 7u;
  const unsigned ka0_ = ldsb + (c_ >> 3) * 1024u + (c_ & 7u) * 128u + ((g_ ^ swz_) * 16u);
  const unsigned ka1_ = ldsb + (c_ >> 3) * 1024u + (c_ & 7u) * 128u + (((4u + g_) ^ swz_) * 16u);
  const unsigned vbase_ = ldsb + (g_ >> 1) * 1024u + ((g_ & 1u) * 4u + (c_ >> 2)) * 64u + (c_ & 3u) * 8u;
  const unsigned vk0_ = vbase_ + (g_ & 1u) * 32u, vk1_ = vbase_ + ((g_ & 1u) ^ 1u) * 32u;
  const unsigned va0_ = vk0_ + 65536u, va1_ = vk1_ + 65536u;
  const unsigned t64 = xcd * 4u + (idx % 28u) / 7u, fb = idx % 7u;
  TS(1, 6);
  const unsigned tw = wid & 1u, fw = wid >> 1, k7_ = (lane_ >> 2) & 7u;
  const unsigned vchunk_ = (lane_ >> 5) * 64u + (((lane_ & 3u) ^ ((k7_ >> 2) << 1)) * 16u);
  const unsigned ldsw = rfl(ldsb + wid * 28672u);
  u32x16 pa, pb;
  pa[0] = wos; pa[1] = vk0_; pa[2] = vk1_; pa[3] = va0_; pa[4] = va1_; pa[5] = vk0_ + 131072u; pa[6] = vk1_ + 131072u;
  pa[7] = (unsigned)WS_Z2N + fb * 64u * 8192u + wid * 4096u + lane_ * 16u;
  pa[8] = (t64 * 64u + wid * 16u + r7_) * 4096u + (((lane_ & 7u) ^ (r7_ >> 1)) * 16u);
  pa[9] = (t64 * 64u + wid * 16u + r7_) * 4096u + (((lane_ & 7u) ^ (4u | (r7_ >> 1))) * 16u);
  #pragma unroll
  for (int i = 0; i < 3; ++i) {
    pa[10 + 2 * i] = ka0_ + tw * 4096u + 65536u * i; pa[11 + 2 * i] = ka1_ + tw * 4096u + 65536u * i;
    pb[2 * i] = vk0_ + fw * 4096u + 65536u * i; pb[1 + 2 * i] = vk1_ + fw * 4096u + 65536u * i;
  }
  pb[6] = (unsigned)WS_P + ((head * 128u + (row0 >> 4)) * 14u * 64u + lane_) * 16u;
  pb[7] = (unsigned)WS_Z1 + (((t64 * 4u + tw * 2u) * 28u + fb * 4u + fw * 2u) * 64u + lane_) * 16u;
  pb[8] = (unsigned)WS_U + (((t64 * 28u + fb * 4u + fw * 2u) * 4u + tw * 2u) * 64u + lane_) * 16u;
  pb[9] = (t64 * 64u + tw * 32u + c_) * 2u;
  pb[10] = (unsigned)WS_U + (t64 * 28u * 4u + wid) * 1024u + lane_ * 16u;
  pb[11] = (wid * 224u + k7_) * 896u + fb * 128u + vchunk_;
  pb[12] = (unsigned)WS_P + ((t64 * 4u + wid) * 14u + fb * 2u) * 1024u + lane_ * 16u;
  pb[13] = vk0_ + wid * 28672u; pb[14] = vk1_ + wid * 28672u; pb[15] = 0u;
  __amdgpu_buffer_rsrc_t srdb = __builtin_amdgcn_make_buffer_rsrc((void*)BSP, (short)0, 4096, SRD3);
  const unsigned m0w = rfl(ldsb + wid * 2048u);
  const unsigned cntp = 1024u + qblk * 128u, cntpr = 1024u + (t64 >> 1) * 128u, cntu = 12288u + t64 * 128u;
  u32x4 sel0 = {0u, 0u, 0u, 0u}, sel1 = {0u, 0u, 0u, 0u};
  {
    const unsigned one = (g_ == (c_ >> 2)) ? ((c_ & 1u) ? 0x3f800000u : 0x00003f80u) : 0u;
    sel0[0] = (c_ & 2u) ? 0u : one; sel0[1] = (c_ & 2u) ? one : 0u;
    sel1[2] = sel0[0]; sel1[3] = sel0[1];
  }
  const unsigned tok = t64 * 64u + wid * 16u + c_;
  const size_t ooff = (size_t)tok * 448u + fb * 64u + g_ * 4u;
  u32x2 xv[4];
  #pragma unroll
  for (int nt = 0; nt < 4; ++nt) xv[nt] = *(const u32x2*)(X + ooff + nt * 16u);
  f32x16 acc[2];
  asm volatile(
  "s_mov_b32 s47, 31744\n\t"
  "s_mov_b32 s51, 4096\n\t"
  "s_mov_b32 s52, 14336\n\t"
  "s_mov_b32 s57, 28672\n\t"
  "v_mov_b32 v206, 0\n\t"
  "v_mov_b32 v207, 1\n\t"
  "s_cmp_eq_u32 %[hs2], 0\n\t"
  "s_cbranch_scc1 .Lnos2_%=\n\t"
  "s_barrier\n\t"
  "ds_read_b64_tr_b16 v[128:129], v229 offset:0\n\t"
  "ds_read_b64_tr_b16 v[130:131], v229 offset:2048\n\t"
  "ds_read_b64_tr_b16 v[132:133], v230 offset:0\n\t"
  "ds_read_b64_tr_b16 v[134:135], v230 offset:2048\n\t"
  "ds_read_b64_tr_b16 v[136:137], v229 offset:512\n\t"
  "ds_read_b64_tr_b16 v[138:139], v229 offset:2560\n\t"
  "ds_read_b64_tr_b16 v[140:141], v230 offset:512\n\t"
  "ds_read_b64_tr_b16 v[142:143], v230 offset:2560\n\t"
  "ds_read_b64_tr_b16 v[144:145], v229 offset:4096\n\t"
  "ds_read_b64_tr_b16 v[146:147], v229 offset:6144\n\t"
  "ds_read_b64_tr_b16 v[148:149], v230 offset:4096\n\t"
  "ds_read_b64_tr_b16 v[150:151], v230 offset:6144\n\t"
  "ds_read_b64_tr_b16 v[152:153], v229 offset:4608\n\t"
  "ds_read_b64_tr_b16 v[154:155], v229 offset:6656\n\t"
  "ds_read_b64_tr_b16 v[156:157], v230 offset:4608\n\t"
  "ds_read_b64_tr_b16 v[158:159], v230 offset:6656\n\t"
  "ds_read_b64_tr_b16 v[160:161], v229 offset:8192\n\t"
  "ds_read_b64_tr_b16 v[162:163], v229 offset:10240\n\t"
  "ds_read_b64_tr_b16 v[164:165], v230 offset:8192\n\t"
  "ds_read_b64_tr_b16 v[166:167], v230 offset:10240\n\t"
  "s_waitcnt lgkmcnt(14)\n\t"
  "v_mfma_f32_16x16x32_bf16 a[0:3], v[128:131], v[64:67], 0\n\t"
  "ds_read_b64_tr_b16 v[168:169], v229 offset:8704\n\t"
  "ds_read_b64_tr_b16 v[170:171], v229 offset:10752\n\t"
  "v_mfma_f32_16x16x32_bf16 a[4:7], v[128:131], v[96:99], 0\n\t"
  "s_add_u32 m0, %[m0o], 65536\n\t"
  "s_nop 0\n\t"
  "buffer_load_dwordx4 v224, %[srdo], 0 offen offset:0 lds\n\t"
  "v_mfma_f32_16x16x32_bf16 a[8:11], v[132:135], v[64:67], 0\n\t"
  "ds_read_b64_tr_b16 v[172:173], v230 offset:8704\n\t"
  "ds_read_b64_tr_b16 v[174:175], v230 offset:10752\n\t"
  "v_mfma_f32_16x16x32_bf16 a[12:15], v[132:135], v[96:99], 0\n\t"
  "buffer_load_dwordx4 v224, %[srdo], %[c6144] offen offset:1024 lds\n\t"
  "v_mfma_f32_16x16x32_bf16 a[16:19], v[136:139], v[64:67], 0\n\t"
  "ds_read_b64_tr_b16 v[176:177], v229 offset:12288\n\t"
  "ds_read_b64_tr_b16 v[178:179], v229 offset:14336\n\t"
  "v_mfma_f32_16x16x32_bf16 a[20:23], v[136:139], v[96:99], 0\n\t"
  "buffer_load_dwordx4 v224, %[srdo], %[c12288] offen offset:2048 lds\n\t"
  "buffer_load_dwordx4 v224, %[srdo], %[c18432] offen offset:3072 lds\n\t"
  "s_add_u32 m0, %[m0o], 69632\n\t"
  "s_waitcnt lgkmcnt(14)\n\t"
  "v_mfma_f32_16x16x32_bf16 a[24:27], v[140:143], v[64:67], 0\n\t"
  "ds_read_b64_tr_b16 v[180:181], v230 offset:12288\n\t"
  "ds_read_b64_tr_b16 v[182:183], v230 offset:14336\n\t"
  "v_mfma_f32_16x16x32_bf16 a[28:31], v[140:143], v[96:99], 0\n\t"
  "buffer_load_dwordx4 v224, %[srdo], %[c28672] offen offset:0 lds\n\t"
  "v_mfma_f32_16x16x32_bf16 a[0:3], v[144:147], v[68:71], a[0:3]\n\t"
  "ds_read_b64_tr_b16 v[184:185], v229 offset:12800\n\t"
  "ds_read_b64_tr_b16 v[186:187], v229 offset:14848\n\t"
  "v_mfma_f32_16x16x32_bf16 a[4:7], v[144:147], v[100:103], a[4:7]\n\t"
  "buffer_load_dwordx4 v224, %[srdo], %[c34816] offen offset:1024 lds\n\t"
  "buffer_load_dwordx4 v224, %[srdo], %[c40960] offen offset:2048 lds\n\t"
  "v_mfma_f32_16x16x32_bf16 a[8:11], v[148:151], v[68:71], a[8:11]\n\t"
  "ds_read_b64_tr_b16 v[188:189], v230 offset:12800\n\t"
  "ds_read_b64_tr_b16 v[190:191], v230 offset:14848\n\t"
  "v_mfma_f32_16x16x32_bf16 a[12:15], v[148:151], v[100:103], a[12:15]\n\t"
  "buffer_load_dwordx4 v224, %[srdo], %[c47104] offen offset:3072 lds\n\t"
  "v_add_u32 v224, 0x80, v224\n\t"
  "s_add_u32 m0, %[m0o], 98304\n\t"
  "s_waitcnt lgkmcnt(14)\n\t"
  "v_mfma_f32_16x16x32_bf16 a[16:19], v[152:155], v[68:71], a[16:19]\n\t"
  "ds_read_b64_tr_b16 v[128:129], v229 offset:16384\n\t"
  "ds_read_b64_tr_b16 v[130:131], v229 offset:18432\n\t"
  "v_mfma_f32_16x16x32_bf16 a[20:23], v[152:155], v[100:103], a[20:23]\n\t"
  "buffer_load_dwordx4 v224, %[srdo], 0 offen offset:0 lds\n\t"
  "buffer_load_dwordx4 v224, %[srdo], %[c6144] offen offset:1024 lds\n\t"
  "v_mfma_f32_16x16x32_bf16 a[24:27], v[156:159], v[68:71], a[24:27]\n\t"
  "ds_read_b64_tr_b16 v[132:133], v230 offset:16384\n\t"
  "ds_read_b64_tr_b16 v[134:135], v230 offset:18432\n\t"
  "v_mfma_f32_16x16x32_bf16 a[28:31], v[156:159], v[100:103], a[28:31]\n\t"
  "buffer_load_dwordx4 v224, %[srdo], %[c12288] offen offset:2048 lds\n\t"
  "v_mfma_f32_16x16x32_bf16 a[0:3], v[160:163], v[72:75], a[0:3]\n\t"
  "ds_read_b64_tr_b16 v[136:137], v229 offset:16896\n\t"
  "ds_read_b64_tr_b16 v[138:139], v229 offset:18944\n\t"
  "v_mfma_f32_16x16x32_bf16 a[4:7], v[160:163], v[104:107], a[4:7]\n\t"
  "buffer_load_dwordx4 v224, %[srdo], %[c18432] offen offset:3072 lds\n\t"
  "s_add_u32 m0, %[m0o], 102400\n\t"
  "s_nop 0\n\t"
  "buffer_load_dwordx4 v224, %[srdo], %[c28672] offen offset:0 lds\n\t"
  "s_waitcnt lgkmcnt(14)\n\t"
  "v_mfma_f32_16x16x32_bf16 a[8:11], v[164:167], v[72:75], a[8:11]\n\t"
  "ds_read_b64_tr_b16 v[140:141], v230 offset:16896\n\t"
  "ds_read_b64_tr_b16 v[142:143], v230 offset:18944\n\t"
  "v_mfma_f32_16x16x32_bf16 a[12:15], v[164:167], v[104:107], a[12:15]\n\t"
  "buffer_load_dwordx4 v224, %[srdo], %[c34816] offen offset:1024 lds\n\t"
  "v_mfma_f32_16x16x32_bf16 a[16:19], v[168:171], v[72:75], a[16:19]\n\t"
  "ds_read_b64_tr_b16 v[144:145], v229 offset:20480\n\t"
  "ds_read_b64_tr_b16 v[146:147], v229 offset:22528\n\t"
  "v_mfma_f32_16x16x32_bf16 a[20:23], v[168:171], v[104:107], a[20:23]\n\t"
  "buffer_load_dwordx4 v224, %[srdo], %[c40960] offen offset:2048 lds\n\t"
  "buffer_load_dwordx4 v224, %[srdo], %[c47104] offen offset:3072 lds\n\t"
  "v_add_u32 v224, 0x80, v224\n\t"
  "v_mfma_f32_16x16x32_bf16 a[24:27], v[172:175], v[72:75], a[24:27]\n\t"
  "ds_read_b64_tr_b16 v[148:149], v230 offset:20480\n\t"
  "ds_read_b64_tr_b16 v[150:151], v230 offset:22528\n\t"
  "v_mfma_f32_16x16x32_bf16 a[28:31], v[172:175], v[104:107], a[28:31]\n\t"
  "s_waitcnt lgkmcnt(14)\n\t"
  "v_mfma_f32_16x16x32_bf16 a[0:3], v[176:179], v[76:79], a[0:3]\n\t"
  "ds_read_b64_tr_b16 v[152:153], v229 offset:20992\n\t"
  "ds_read_b64_tr_b16 v[154:155], v229 offset:23040\n\t"
  "v_mfma_f32_16x16x32_bf16 a[4:7], v[176:179], v[108:111], a[4:7]\n\t"
  "v_mfma_f32_16x16x32_bf16 a[8:11], v[180:183], v[76:79], a[8:11]\n\t"
  "ds_read_b64_tr_b16 v[156:157], v230 offset:20992\n\t"
  "ds_read_b64_tr_b16 v[158:159], v230 offset:23040\n\t"
  "v_mfma_f32_16x16x32_bf16 a[12:15], v[180:183], v[108:111], a[12:15]\n\t"
  "v_mfma_f32_16x16x32_bf16 a[16:19], v[184:187], v[76:79], a[16:19]\n\t"
  "ds_read_b64_tr_b16 v[160:161], v229 offset:24576\n\t"
  "ds_read_b64_tr_b16 v[162:163], v229 offset:26624\n\t"
  "v_mfma_f32_16x16x32_bf16 a[20:23], v[184:187], v[108:111], a[20:23]\n\t"
  "s_waitcnt lgkmcnt(14)\n\t"
  "v_mfma_f32_16x16x32_bf16 a[24:27], v[188:191], v[76:79], a[24:27]\n\t"
  "ds_read_b64_tr_b16 v[164:165], v230 offset:24576\n\t"
  "ds_read_b64_tr_b16 v[166:167], v230 offset:26624\n\t"
  "v_mfma_f32_16x16x32_bf16 a[28:31], v[188:191], v[108:111], a[28:31]\n\t"
  "v_mfma_f32_16x16x32_bf16 a[0:3], v[128:131], v[80:83], a[0:3]\n\t"
  "ds_read_b64_tr_b16 v[168:169], v229 offset:25088\n\t"
  "ds_read_b64_tr_b16 v[170:171], v229 offset:27136\n\t"
  "v_mfma_f32_16x16x32_bf16 a[4:7], v[128:131], v[112:115], a[4:7]\n\t"
  "v_mfma_f32_16x16x32_bf16 a[8:11], v[132:135], v[80:83], a[8:11]\n\t"
  "ds_read_b64_tr_b16 v[172:173], v230 offset:25088\n\t"
  "ds_read_b64_tr_b16 v[174:175], v230 offset:27136\n\t"
  "v_mfma_f32_16x16x32_bf16 a[12:15], v[132:135], v[112:115], a[12:15]\n\t"
  "s_waitcnt lgkmcnt(14)\n\t"
  "v_mfma_f32_16x16x32_bf16 a[16:19], v[136:139], v[80:83], a[16:19]\n\t"
  "ds_read_b64_tr_b16 v[176:177], v229 offset:28672\n\t"
  "ds_read_b64_tr_b16 v[178:179], v229 offset:30720\n\t"
  "v_mfma_f32_16x16x32_bf16 a[20:23], v[136:139], v[112:115], a[20:23]\n\t"
  "v_mfma_f32_16x16x32_bf16 a[24:27], v[140:143], v[80:83], a[24:27]\n\t"
  "ds_read_b64_tr_b16 v[180:181], v230 offset:28672\n\t"
  "ds_read_b64_tr_b16 v[182:183], v230 offset:30720\n\t"
  "v_mfma_f32_16x16x32_bf16 a[28:31], v[140:143], v[112:115], a[28:31]\n\t"
  "v_mfma_f32_16x16x32_bf16 a[0:3], v[144:147], v[84:87], a[0:3]\n\t"
  "ds_read_b64_tr_b16 v[184:185], v229 offset:29184\n\t"
  "ds_read_b64_tr_b16 v[186:187], v229 offset:31232\n\t"
  "v_mfma_f32_16x16x32_bf16 a[4:7], v[144:147], v[116:119], a[4:7]\n\t"
  "s_waitcnt lgkmcnt(14)\n\t"
  "v_mfma_f32_16x16x32_bf16 a[8:11], v[148:151], v[84:87], a[8:11]\n\t"
  "ds_read_b64_tr_b16 v[188:189], v230 offset:29184\n\t"
  "ds_read_b64_tr_b16 v[190:191], v230 offset:31232\n\t"
  "v_mfma_f32_16x16x32_bf16 a[12:15], v[148:151], v[116:119], a[12:15]\n\t"
  "v_mfma_f32_16x16x32_bf16 a[16:19], v[152:155], v[84:87], a[16:19]\n\t"
  "s_barrier\n\t"
  "ds_read_b64_tr_b16 v[128:129], v225 offset:0\n\t"
  "ds_read_b64_tr_b16 v[130:131], v225 offset:2048\n\t"
  "v_mfma_f32_16x16x32_bf16 a[20:23], v[152:155], v[116:119], a[20:23]\n\t"
  "v_mfma_f32_16x16x32_bf16 a[24:27], v[156:159], v[84:87], a[24:27]\n\t"
  "ds_read_b64_tr_b16 v[132:133], v226 offset:0\n\t"
  "ds_read_b64_tr_b16 v[134:135], v226 offset:2048\n\t"
  "v_mfma_f32_16x16x32_bf16 a[28:31], v[156:159], v[116:119], a[28:31]\n\t"
  "s_waitcnt lgkmcnt(14)\n\t"
  "v_mfma_f32_16x16x32_bf16 a[0:3], v[160:163], v[88:91], a[0:3]\n\t"
  "ds_read_b64_tr_b16 v[136:137], v225 offset:512\n\t"
  "ds_read_b64_tr_b16 v[138:139], v225 offset:2560\n\t"
  "v_mfma_f32_16x16x32_bf16 a[4:7], v[160:163], v[120:123], a[4:7]\n\t"
  "v_mfma_f32_16x16x32_bf16 a[8:11], v[164:167], v[88:91], a[8:11]\n\t"
  "ds_read_b64_tr_b16 v[140:141], v226 offset:512\n\t"
  "ds_read_b64_tr_b16 v[142:143], v226 offset:2560\n\t"
  "v_mfma_f32_16x16x32_bf16 a[12:15], v[164:167], v[120:123], a[12:15]\n\t"
  "v_mfma_f32_16x16x32_bf16 a[16:19], v[168:171], v[88:91], a[16:19]\n\t"
  "ds_read_b64_tr_b16 v[144:145], v225 offset:4096\n\t"
  "ds_read_b64_tr_b16 v[146:147], v225 offset:6144\n\t"
  "v_mfma_f32_16x16x32_bf16 a[20:23], v[168:171], v[120:123], a[20:23]\n\t"
  "s_waitcnt lgkmcnt(14)\n\t"
  "v_mfma_f32_16x16x32_bf16 a[24:27], v[172:175], v[88:91], a[24:27]\n\t"
  "ds_read_b64_tr_b16 v[148:149], v226 offset:4096\n\t"
  "ds_read_b64_tr_b16 v[150:151], v226 offset:6144\n\t"
  "v_mfma_f32_16x16x32_bf16 a[28:31], v[172:175], v[120:123], a[28:31]\n\t"
  "v_mfma_f32_16x16x32_bf16 a[0:3], v[176:179], v[92:95], a[0:3]\n\t"
  "ds_read_b64_tr_b16 v[152:153], v225 offset:4608\n\t"
  "ds_read_b64_tr_b16 v[154:155], v225 offset:6656\n\t"
  "v_mfma_f32_16x16x32_bf16 a[4:7], v[176:179], v[124:127], a[4:7]\n\t"
  "v_mfma_f32_16x16x32_bf16 a[8:11], v[180:183], v[92:95], a[8:11]\n\t"
  "ds_read_b64_tr_b16 v[156:157], v226 offset:4608\n\t"
  "ds_read_b64_tr_b16 v[158:159], v226 offset:6656\n\t"
  "v_mfma_f32_16x16x32_bf16 a[12:15], v[180:183], v[124:127], a[12:15]\n\t"
  "s_waitcnt lgkmcnt(14)\n\t"
  "v_mfma_f32_16x16x32_bf16 a[16:19], v[184:187], v[92:95], a[16:19]\n\t"
  "ds_read_b64_tr_b16 v[160:161], v225 offset:8192\n\t"
  "ds_read_b64_tr_b16 v[162:163], v225 offset:10240\n\t"
  "v_mfma_f32_16x16x32_bf16 a[20:23], v[184:187], v[124:127], a[20:23]\n\t"
  "v_mfma_f32_16x16x32_bf16 a[24:27], v[188:191], v[92:95], a[24:27]\n\t"
  "ds_read_b64_tr_b16 v[164:165], v226 offset:8192\n\t"
  "ds_read_b64_tr_b16 v[166:167], v226 offset:10240\n\t"
  "v_mfma_f32_16x16x32_bf16 a[28:31], v[188:191], v[124:127], a[28:31]\n\t"
  "v_mfma_f32_16x16x32_bf16 a[32:35], v[128:131], v[64:67], 0\n\t"
  "ds_read_b64_tr_b16 v[168:169], v225 offset:8704\n\t"
  "ds_read_b64_tr_b16 v[170:171], v225 offset:10752\n\t"
  "v_mfma_f32_16x16x32_bf16 a[36:39], v[128:131], v[96:99], 0\n\t"
  "s_waitcnt lgkmcnt(14)\n\t"
  "v_mfma_f32_16x16x32_bf16 a[40:43], v[132:135], v[64:67], 0\n\t"
  "ds_read_b64_tr_b16 v[172:173], v226 offset:8704\n\t"
  "ds_read_b64_tr_b16 v[174:175], v226 offset:10752\n\t"
  "v_mfma_f32_16x16x32_bf16 a[44:47], v[132:135], v[96:99], 0\n\t"
  "v_mfma_f32_16x16x32_bf16 a[48:51], v[136:139], v[64:67], 0\n\t"
  "ds_read_b64_tr_b16 v[176:177], v225 offset:12288\n\t"
  "ds_read_b64_tr_b16 v[178:179], v225 offset:14336\n\t"
  "v_mfma_f32_16x16x32_bf16 a[52:55], v[136:139], v[96:99], 0\n\t"
  "v_mfma_f32_16x16x32_bf16 a[56:59], v[140:143], v[64:67], 0\n\t"
  "ds_read_b64_tr_b16 v[180:181], v226 offset:12288\n\t"
  "ds_read_b64_tr_b16 v[182:183], v226 offset:14336\n\t"
  "v_mfma_f32_16x16x32_bf16 a[60:63], v[140:143], v[96:99], 0\n\t"
  "s_waitcnt lgkmcnt(14)\n\t"
  "v_mfma_f32_16x16x32_bf16 a[32:35], v[144:147], v[68:71], a[32:35]\n\t"
  "ds_read_b64_tr_b16 v[184:185], v225 offset:12800\n\t"
  "ds_read_b64_tr_b16 v[186:187], v225 offset:14848\n\t"
  "v_mfma_f32_16x16x32_bf16 a[36:39], v[144:147], v[100:103], a[36:39]\n\t"
  "v_accvgpr_read_b32 v200, a0\n\t"
  "v_accvgpr_read_b32 v201, a1\n\t"
  "v_accvgpr_read_b32 v202, a2\n\t"
  "v_mfma_f32_16x16x32_bf16 a[40:43], v[148:151], v[68:71], a[40:43]\n\t"
  "ds_read_b64_tr_b16 v[188:189], v226 offset:12800\n\t"
  "ds_read_b64_tr_b16 v[190:191], v226 offset:14848\n\t"
  "v_mfma_f32_16x16x32_bf16 a[44:47], v[148:151], v[100:103], a[44:47]\n\t"
  "v_accvgpr_read_b32 v203, a3\n\t"
  "v_cvt_pk_bf16_f32 v192, v200, v201\n\t"
  "v_cvt_pk_bf16_f32 v193, v202, v203\n\t"
  "v_mfma_f32_16x16x32_bf16 a[48:51], v[152:155], v[68:71], a[48:51]\n\t"
  "ds_read_b64_tr_b16 v[128:129], v225 offset:16384\n\t"
  "ds_read_b64_tr_b16 v[130:131], v225 offset:18432\n\t"
  "v_mfma_f32_16x16x32_bf16 a[52:55], v[152:155], v[100:103], a[52:55]\n\t"
  "v_accvgpr_read_b32 v200, a8\n\t"
  "v_accvgpr_read_b32 v201, a9\n\t"
  "s_waitcnt lgkmcnt(14)\n\t"
  "v_mfma_f32_16x16x32_bf16 a[56:59], v[156:159], v[68:71], a[56:59]\n\t"
  "ds_read_b64_tr_b16 v[132:133], v226 offset:16384\n\t"
  "ds_read_b64_tr_b16 v[134:135], v226 offset:18432\n\t"
  "v_mfma_f32_16x16x32_bf16 a[60:63], v[156:159], v[100:103], a[60:63]\n\t"
  "v_accvgpr_read_b32 v202, a10\n\t"
  "v_accvgpr_read_b32 v203, a11\n\t"
  "v_cvt_pk_bf16_f32 v194, v200, v201\n\t"
  "v_mfma_f32_16x16x32_bf16 a[32:35], v[160:163], v[72:75], a[32:35]\n\t"
  "ds_read_b64_tr_b16 v[136:137], v225 offset:16896\n\t"
  "ds_read_b64_tr_b16 v[138:139], v225 offset:18944\n\t"
  "v_mfma_f32_16x16x32_bf16 a[36:39], v[160:163], v[104:107], a[36:39]\n\t"
  "v_cvt_pk_bf16_f32 v195, v202, v203\n\t"
  "buffer_store_dwordx4 v[192:195], v246, %[srdws], 0 offen offset:0 sc1\n\t"
  "v_accvgpr_read_b32 v200, a16\n\t"
  "v_mfma_f32_16x16x32_bf16 a[40:43], v[164:167], v[72:75], a[40:43]\n\t"
  "ds_read_b64_tr_b16 v[140:141], v226 offset:16896\n\t"
  "ds_read_b64_tr_b16 v[142:143], v226 offset:18944\n\t"
  "v_mfma_f32_16x16x32_bf16 a[44:47], v[164:167], v[104:107], a[44:47]\n\t"
  "v_accvgpr_read_b32 v201, a17\n\t"
  "v_accvgpr_read_b32 v202, a18\n\t"
  "s_waitcnt lgkmcnt(14)\n\t"
  "v_mfma_f32_16x16x32_bf16 a[48:51], v[168:171], v[72:75], a[48:51]\n\t"
  "ds_read_b64_tr_b16 v[144:145], v225 offset:20480\n\t"
  "ds_read_b64_tr_b16 v[146:147], v225 offset:22528\n\t"
  "v_mfma_f32_16x16x32_bf16 a[52:55], v[168:171], v[104:107], a[52:55]\n\t"
  "v_accvgpr_read_b32 v203, a19\n\t"
  "v_cvt_pk_bf16_f32 v196, v200, v201\n\t"
  "v_cvt_pk_bf16_f32 v197, v202, v203\n\t"
  "v_mfma_f32_16x16x32_bf16 a[56:59], v[172:175], v[72:75], a[56:59]\n\t"
  "ds_read_b64_tr_b16 v[148:149], v226 offset:20480\n\t"
  "ds_read_b64_tr_b16 v[150:151], v226 offset:22528\n\t"
  "v_mfma_f32_16x16x32_bf16 a[60:63], v[172:175], v[104:107], a[60:63]\n\t"
  "v_accvgpr_read_b32 v200, a24\n\t"
  "v_accvgpr_read_b32 v201, a25\n\t"
  "v_accvgpr_read_b32 v202, a26\n\t"
  "v_mfma_f32_16x16x32_bf16 a[32:35], v[176:179], v[76:79], a[32:35]\n\t"
  "ds_read_b64_tr_b16 v[152:153], v225 offset:20992\n\t"
  "ds_read_b64_tr_b16 v[154:155], v225 offset:23040\n\t"
  "v_mfma_f32_16x16x32_bf16 a[36:39], v[176:179], v[108:111], a[36:39]\n\t"
  "v_accvgpr_read_b32 v203, a27\n\t"
  "v_cvt_pk_bf16_f32 v198, v200, v201\n\t"
  "s_waitcnt lgkmcnt(14)\n\t"
  "v_mfma_f32_16x16x32_bf16 a[40:43], v[180:183], v[76:79], a[40:43]\n\t"
  "ds_read_b64_tr_b16 v[156:157], v226 offset:20992\n\t"
  "ds_read_b64_tr_b16 v[158:159], v226 offset:23040\n\t"
  "v_mfma_f32_16x16x32_bf16 a[44:47], v[180:183], v[108:111], a[44:47]\n\t"
  "v_cvt_pk_bf16_f32 v199, v202, v203\n\t"
  "buffer_store_dwordx4 v[196:199], v246, %[srdws], 0 offen offset:1024 sc1\n\t"
  "v_accvgpr_read_b32 v200, a4\n\t"
  "v_mfma_f32_16x16x32_bf16 a[48:51], v[184:187], v[76:79], a[48:51]\n\t"
  "ds_read_b64_tr_b16 v[160:161], v225 offset:24576\n\t"
  "ds_read_b64_tr_b16 v[162:163], v225 offset:26624\n\t"
  "v_mfma_f32_16x16x32_bf16 a[52:55], v[184:187], v[108:111], a[52:55]\n\t"
  "v_accvgpr_read_b32 v201, a5\n\t"
  "v_accvgpr_read_b32 v202, a6\n\t"
  "v_accvgpr_read_b32 v203, a7\n\t"
  "v_mfma_f32_16x16x32_bf16 a[56:59], v[188:191], v[76:79], a[56:59]\n\t"
  "ds_read_b64_tr_b16 v[164:165], v226 offset:24576\n\t"
  "ds_read_b64_tr_b16 v[166:167], v226 offset:26624\n\t"
  "v_mfma_f32_16x16x32_bf16 a[60:63], v[188:191], v[108:111], a[60:63]\n\t"
  "v_cvt_pk_bf16_f32 v192, v200, v201\n\t"
  "v_cvt_pk_bf16_f32 v193, v202, v203\n\t"
  "s_waitcnt lgkmcnt(14)\n\t"
  "v_mfma_f32_16x16x32_bf16 a[32:35], v[128:131], v[80:83], a[32:35]\n\t"
  "ds_read_b64_tr_b16 v[168:169], v225 offset:25088\n\t"
  "ds_read_b64_tr_b16 v[170:171], v225 offset:27136\n\t"
  "v_mfma_f32_16x16x32_bf16 a[36:39], v[128:131], v[112:115], a[36:39]\n\t"
  "v_accvgpr_read_b32 v200, a12\n\t"
  "v_accvgpr_read_b32 v201, a13\n\t"
  "v_accvgpr_read_b32 v202, a14\n\t"
  "v_mfma_f32_16x16x32_bf16 a[40:43], v[132:135], v[80:83], a[40:43]\n\t"
  "ds_read_b64_tr_b16 v[172:173], v226 offset:25088\n\t"
  "ds_read_b64_tr_b16 v[174:175], v226 offset:27136\n\t"
  "v_mfma_f32_16x16x32_bf16 a[44:47], v[132:135], v[112:115], a[44:47]\n\t"
  "v_accvgpr_read_b32 v203, a15\n\t"
  "v_cvt_pk_bf16_f32 v194, v200, v201\n\t"
  "v_cvt_pk_bf16_f32 v195, v202, v203\n\t"
  "v_mfma_f32_16x16x32_bf16 a[48:51], v[136:139], v[80:83], a[48:51]\n\t"
  "ds_read_b64_tr_b16 v[176:177], v225 offset:28672\n\t"
  "ds_read_b64_tr_b16 v[178:179], v225 offset:30720\n\t"
  "v_mfma_f32_16x16x32_bf16 a[52:55], v[136:139], v[112:115], a[52:55]\n\t"
  "buffer_store_dwordx4 v[192:195], v246, %[srdws], s52 offen offset:0 sc1\n\t"
  "v_accvgpr_read_b32 v200, a20\n\t"
  "s_waitcnt lgkmcnt(14)\n\t"
  "v_mfma_f32_16x16x32_bf16 a[56:59], v[140:143], v[80:83], a[56:59]\n\t"
  "ds_read_b64_tr_b16 v[180:181], v226 offset:28672\n\t"
  "ds_read_b64_tr_b16 v[182:183], v226 offset:30720\n\t"
  "v_mfma_f32_16x16x32_bf16 a[60:63], v[140:143], v[112:115], a[60:63]\n\t"
  "v_accvgpr_read_b32 v201, a21\n\t"
  "v_accvgpr_read_b32 v202, a22\n\t"
  "v_accvgpr_read_b32 v203, a23\n\t"
  "v_mfma_f32_16x16x32_bf16 a[32:35], v[144:147], v[84:87], a[32:35]\n\t"
  "ds_read_b64_tr_b16 v[184:185], v225 offset:29184\n\t"
  "ds_read_b64_tr_b16 v[186:187], v225 offset:31232\n\t"
  "v_mfma_f32_16x16x32_bf16 a[36:39], v[144:147], v[116:119], a[36:39]\n\t"
  "v_cvt_pk_bf16_f32 v196, v200, v201\n\t"
  "v_cvt_pk_bf16_f32 v197, v202, v203\n\t"
  "v_accvgpr_read_b32 v200, a28\n\t"
  "v_mfma_f32_16x16x32_bf16 a[40:43], v[148:151], v[84:87], a[40:43]\n\t"
  "ds_read_b64_tr_b16 v[188:189], v226 offset:29184\n\t"
  "ds_read_b64_tr_b16 v[190:191], v226 offset:31232\n\t"
  "v_mfma_f32_16x16x32_bf16 a[44:47], v[148:151], v[116:119], a[44:47]\n\t"
  "v_accvgpr_read_b32 v201, a29\n\t"
  "v_accvgpr_read_b32 v202, a30\n\t"
  "s_waitcnt lgkmcnt(14)\n\t"
  "v_mfma_f32_16x16x32_bf16 a[48:51], v[152:155], v[84:87], a[48:51]\n\t"
  "s_barrier\n\t"
  "ds_read_b64_tr_b16 v[128:129], v225 offset:32768\n\t"
  "ds_read_b64_tr_b16 v[130:131], v225 offset:34816\n\t"
  "v_mfma_f32_16x16x32_bf16 a[52:55], v[152:155], v[116:119], a[52:55]\n\t"
  "s_add_u32 m0, %[m0o], 131072\n\t"
  "s_nop 0\n\t"
  "buffer_load_dwordx4 v224, %[srdo], 0 offen offset:0 lds\n\t"
  "v_accvgpr_read_b32 v203, a31\n\t"
  "v_cvt_pk_bf16_f32 v198, v200, v201\n\t"
  "v_cvt_pk_bf16_f32 v199, v202, v203\n\t"
  "v_mfma_f32_16x16x32_bf16 a[56:59], v[156:159], v[84:87], a[56:59]\n\t"
  "ds_read_b64_tr_b16 v[132:133], v226 offset:32768\n\t"
  "ds_read_b64_tr_b16 v[134:135], v226 offset:34816\n\t"
  "v_mfma_f32_16x16x32_bf16 a[60:63], v[156:159], v[116:119], a[60:63]\n\t"
  "buffer_store_dwordx4 v[196:199], v246, %[srdws], s52 offen offset:1024 sc1\n\t"
  "v_add_u32 v246, 0x800, v246\n\t"
  "v_mfma_f32_16x16x32_bf16 a[32:35], v[160:163], v[88:91], a[32:35]\n\t"
  "ds_read_b64_tr_b16 v[136:137], v225 offset:33280\n\t"
  "ds_read_b64_tr_b16 v[138:139], v225 offset:35328\n\t"
  "v_mfma_f32_16x16x32_bf16 a[36:39], v[160:163], v[120:123], a[36:39]\n\t"
  "buffer_load_dwordx4 v224, %[srdo], %[c6144] offen offset:1024 lds\n\t"
  "s_waitcnt lgkmcnt(14)\n\t"
  "v_mfma_f32_16x16x32_bf16 a[40:43], v[164:167], v[88:91], a[40:43]\n\t"
  "ds_read_b64_tr_b16 v[140:141], v226 offset:33280\n\t"
  "ds_read_b64_tr_b16 v[142:143], v226 offset:35328\n\t"
  "v_mfma_f32_16x16x32_bf16 a[44:47], v[164:167], v[120:123], a[44:47]\n\t"
  "v_mfma_f32_16x16x32_bf16 a[48:51], v[168:171], v[88:91], a[48:51]\n\t"
  "ds_read_b64_tr_b16 v[144:145], v225 offset:36864\n\t"
  "ds_read_b64_tr_b16 v[146:147], v225 offset:38912\n\t"
  "v_mfma_f32_16x16x32_bf16 a[52:55], v[168:171], v[120:123], a[52:55]\n\t"
  "buffer_load_dwordx4 v224, %[srdo], %[c12288] offen offset:2048 lds\n\t"
  "v_mfma_f32_16x16x32_bf16 a[56:59], v[172:175], v[88:91], a[56:59]\n\t"
  "ds_read_b64_tr_b16 v[148:149], v226 offset:36864\n\t"
  "ds_read_b64_tr_b16 v[150:151], v226 offset:38912\n\t"
  "v_mfma_f32_16x16x32_bf16 a[60:63], v[172:175], v[120:123], a[60:63]\n\t"
  "s_waitcnt lgkmcnt(14)\n\t"
  "v_mfma_f32_16x16x32_bf16 a[32:35], v[176:179], v[92:95], a[32:35]\n\t"
  "ds_read_b64_tr_b16 v[152:153], v225 offset:37376\n\t"
  "ds_read_b64_tr_b16 v[154:155], v225 offset:39424\n\t"
  "v_mfma_f32_16x16x32_bf16 a[36:39], v[176:179], v[124:127], a[36:39]\n\t"
  "buffer_load_dwordx4 v224, %[srdo], %[c18432] offen offset:3072 lds\n\t"
  "s_add_u32 m0, %[m0o], 135168\n\t"
  "v_mfma_f32_16x16x32_bf16 a[40:43], v[180:183], v[92:95], a[40:43]\n\t"
  "ds_read_b64_tr_b16 v[156:157], v226 offset:37376\n\t"
  "ds_read_b64_tr_b16 v[158:159], v226 offset:39424\n\t"
  "v_mfma_f32_16x16x32_bf16 a[44:47], v[180:183], v[124:127], a[44:47]\n\t"
  "v_mfma_f32_16x16x32_bf16 a[48:51], v[184:187], v[92:95], a[48:51]\n\t"
  "ds_read_b64_tr_b16 v[160:161], v225 offset:40960\n\t"
  "ds_read_b64_tr_b16 v[162:163], v225 offset:43008\n\t"
  "v_mfma_f32_16x16x32_bf16 a[52:55], v[184:187], v[124:127], a[52:55]\n\t"
  "buffer_load_dwordx4 v224, %[srdo], %[c28672] offen offset:0 lds\n\t"
  "s_waitcnt lgkmcnt(14)\n\t"
  "v_mfma_f32_16x16x32_bf16 a[56:59], v[188:191], v[92:95], a[56:59]\n\t"
  "ds_read_b64_tr_b16 v[164:165], v226 offset:40960\n\t"
  "ds_read_b64_tr_b16 v[166:167], v226 offset:43008\n\t"
  "v_mfma_f32_16x16x32_bf16 a[60:63], v[188:191], v[124:127], a[60:63]\n\t"
  "v_mfma_f32_16x16x32_bf16 a[0:3], v[128:131], v[64:67], 0\n\t"
  "ds_read_b64_tr_b16 v[168:169], v225 offset:41472\n\t"
  "ds_read_b64_tr_b16 v[170:171], v225 offset:43520\n\t"
  "v_mfma_f32_16x16x32_bf16 a[4:7], v[128:131], v[96:99], 0\n\t"
  "buffer_load_dwordx4 v224, %[srdo], %[c34816] offen offset:1024 lds\n\t"
  "v_mfma_f32_16x16x32_bf16 a[8:11], v[132:135], v[64:67], 0\n\t"
  "ds_read_b64_tr_b16 v[172:173], v226 offset:41472\n\t"
  "ds_read_b64_tr_b16 v[174:175], v226 offset:43520\n\t"
  "v_mfma_f32_16x16x32_bf16 a[12:15], v[132:135], v[96:99], 0\n\t"
  "s_waitcnt lgkmcnt(14)\n\t"
  "v_mfma_f32_16x16x32_bf16 a[16:19], v[136:139], v[64:67], 0\n\t"
  "ds_read_b64_tr_b16 v[176:177], v225 offset:45056\n\t"
  "ds_read_b64_tr_b16 v[178:179], v225 offset:47104\n\t"
  "v_mfma_f32_16x16x32_bf16 a[20:23], v[136:139], v[96:99], 0\n\t"
  "buffer_load_dwordx4 v224, %[srdo], %[c40960] offen offset:2048 lds\n\t"
  "v_mfma_f32_16x16x32_bf16 a[24:27], v[140:143], v[64:67], 0\n\t"
  "ds_read_b64_tr_b16 v[180:181], v226 offset:45056\n\t"
  "ds_read_b64_tr_b16 v[182:183], v226 offset:47104\n\t"
  "v_mfma_f32_16x16x32_bf16 a[28:31], v[140:143], v[96:99], 0\n\t"
  "v_mfma_f32_16x16x32_bf16 a[0:3], v[144:147], v[68:71], a[0:3]\n\t"
  "ds_read_b64_tr_b16 v[184:185], v225 offset:45568\n\t"
  "ds_read_b64_tr_b16 v[186:187], v225 offset:47616\n\t"
  "v_mfma_f32_16x16x32_bf16 a[4:7], v[144:147], v[100:103], a[4:7]\n\t"
  "buffer_load_dwordx4 v224, %[srdo], %[c47104] offen offset:3072 lds\n\t"
  "v_add_u32 v224, 0x80, v224\n\t"
  "v_accvgpr_read_b32 v200, a32\n\t"
  "v_accvgpr_read_b32 v201, a33\n\t"
  "v_accvgpr_read_b32 v202, a34\n\t"
  "s_waitcnt lgkmcnt(14)\n\t"
  "v_mfma_f32_16x16x32_bf16 a[8:11], v[148:151], v[68:71], a[8:11]\n\t"
  "ds_read_b64_tr_b16 v[188:189], v226 offset:45568\n\t"
  "ds_read_b64_tr_b16 v[190:191], v226 offset:47616\n\t"
  "v_mfma_f32_16x16x32_bf16 a[12:15], v[148:151], v[100:103], a[12:15]\n\t"
  "v_accvgpr_read_b32 v203, a35\n\t"
  "v_cvt_pk_bf16_f32 v192, v200, v201\n\t"
  "v_cvt_pk_bf16_f32 v193, v202, v203\n\t"
  "v_mfma_f32_16x16x32_bf16 a[16:19], v[152:155], v[68:71], a[16:19]\n\t"
  "ds_read_b64_tr_b16 v[128:129], v225 offset:49152\n\t"
  "ds_read_b64_tr_b16 v[130:131], v225 offset:51200\n\t"
  "v_mfma_f32_16x16x32_bf16 a[20:23], v[152:155], v[100:103], a[20:23]\n\t"
  "v_accvgpr_read_b32 v200, a40\n\t"
  "v_accvgpr_read_b32 v201, a41\n\t"
  "v_mfma_f32_16x16x32_bf16 a[24:27], v[156:159], v[68:71], a[24:27]\n\t"
  "ds_read_b64_tr_b16 v[132:133], v226 offset:49152\n\t"
  "ds_read_b64_tr_b16 v[134:135], v226 offset:51200\n\t"
  "v_mfma_f32_16x16x32_bf16 a[28:31], v[156:159], v[100:103], a[28:31]\n\t"
  "v_accvgpr_read_b32 v202, a42\n\t"
  "v_accvgpr_read_b32 v203, a43\n\t"
  "v_cvt_pk_bf16_f32 v194, v200, v201\n\t"
  "s_waitcnt lgkmcnt(14)\n\t"
  "v_mfma_f32_16x16x32_bf16 a[0:3], v[160:163], v[72:75], a[0:3]\n\t"
  "ds_read_b64_tr_b16 v[136:137], v225 offset:49664\n\t"
  "ds_read_b64_tr_b16 v[138:139], v225 offset:51712\n\t"
  "v_mfma_f32_16x16x32_bf16 a[4:7], v[160:163], v[104:107], a[4:7]\n\t"
  "v_cvt_pk_bf16_f32 v195, v202, v203\n\t"
  "buffer_store_dwordx4 v[192:195], v246, %[srdws], 0 offen offset:0 sc1\n\t"
  "v_accvgpr_read_b32 v200, a48\n\t"
  "v_mfma_f32_16x16x32_bf16 a[8:11], v[164:167], v[72:75], a[8:11]\n\t"
  "ds_read_b64_tr_b16 v[140:141], v226 offset:49664\n\t"
  "ds_read_b64_tr_b16 v[142:143], v226 offset:51712\n\t"
  "v_mfma_f32_16x16x32_bf16 a[12:15], v[164:167], v[104:107], a[12:15]\n\t"
  "v_accvgpr_read_b32 v201, a49\n\t"
  "v_accvgpr_read_b32 v202, a50\n\t"
  "v_mfma_f32_16x16x32_bf16 a[16:19], v[168:171], v[72:75], a[16:19]\n\t"
  "ds_read_b64_tr_b16 v[144:145], v225 offset:53248\n\t"
  "ds_read_b64_tr_b16 v[146:147], v225 offset:55296\n\t"
  "v_mfma_f32_16x16x32_bf16 a[20:23], v[168:171], v[104:107], a[20:23]\n\t"
  "v_accvgpr_read_b32 v203, a51\n\t"
  "v_cvt_pk_bf16_f32 v196, v200, v201\n\t"
  "v_cvt_pk_bf16_f32 v197, v202, v203\n\t"
  "s_waitcnt lgkmcnt(14)\n\t"
  "v_mfma_f32_16x16x32_bf16 a[24:27], v[172:175], v[72:75], a[24:27]\n\t"
  "ds_read_b64_tr_b16 v[148:149], v226 offset:53248\n\t"
  "ds_read_b64_tr_b16 v[150:151], v226 offset:55296\n\t"
  "v_mfma_f32_16x16x32_bf16 a[28:31], v[172:175], v[104:107], a[28:31]\n\t"
  "v_accvgpr_read_b32 v200, a56\n\t"
  "v_accvgpr_read_b32 v201, a57\n\t"
  "v_accvgpr_read_b32 v202, a58\n\t"
  "v_mfma_f32_16x16x32_bf16 a[0:3], v[176:179], v[76:79], a[0:3]\n\t"
  "ds_read_b64_tr_b16 v[152:153], v225 offset:53760\n\t"
  "ds_read_b64_tr_b16 v[154:155], v225 offset:55808\n\t"
  "v_mfma_f32_16x16x32_bf16 a[4:7], v[176:179], v[108:111], a[4:7]\n\t"
  "v_accvgpr_read_b32 v203, a59\n\t"
  "v_cvt_pk_bf16_f32 v198, v200, v201\n\t"
  "v_mfma_f32_16x16x32_bf16 a[8:11], v[180:183], v[76:79], a[8:11]\n\t"
  "ds_read_b64_tr_b16 v[156:157], v226 offset:53760\n\t"
  "ds_read_b64_tr_b16 v[158:159], v226 offset:55808\n\t"
  "v_mfma_f32_16x16x32_bf16 a[12:15], v[180:183], v[108:111], a[12:15]\n\t"
  "v_cvt_pk_bf16_f32 v199, v202, v203\n\t"
  "buffer_store_dwordx4 v[196:199], v246, %[srdws], 0 offen offset:1024 sc1\n\t"
  "v_accvgpr_read_b32 v200, a36\n\t"
  "s_waitcnt lgkmcnt(14)\n\t"
  "v_mfma_f32_16x16x32_bf16 a[16:19], v[184:187], v[76:79], a[16:19]\n\t"
  "ds_read_b64_tr_b16 v[160:161], v225 offset:57344\n\t"
  "ds_read_b64_tr_b16 v[162:163], v225 offset:59392\n\t"
  "v_mfma_f32_16x16x32_bf16 a[20:23], v[184:187], v[108:111], a[20:23]\n\t"
  "v_accvgpr_read_b32 v201, a37\n\t"
  "v_accvgpr_read_b32 v202, a38\n\t"
  "v_accvgpr_read_b32 v203, a39\n\t"
  "v_mfma_f32_16x16x32_bf16 a[24:27], v[188:191], v[76:79], a[24:27]\n\t"
  "ds_read_b64_tr_b16 v[164:165], v226 offset:57344\n\t"
  "ds_read_b64_tr_b16 v[166:167], v226 offset:59392\n\t"
  "v_mfma_f32_16x16x32_bf16 a[28:31], v[188:191], v[108:111], a[28:31]\n\t"
  "v_cvt_pk_bf16_f32 v192, v200, v201\n\t"
  "v_cvt_pk_bf16_f32 v193, v202, v203\n\t"
  "v_mfma_f32_16x16x32_bf16 a[0:3], v[128:131], v[80:83], a[0:3]\n\t"
  "ds_read_b64_tr_b16 v[168:169], v225 offset:57856\n\t"
  "ds_read_b64_tr_b16 v[170:171], v225 offset:59904\n\t"
  "v_mfma_f32_16x16x32_bf16 a[4:7], v[128:131], v[112:115], a[4:7]\n\t"
  "v_accvgpr_read_b32 v200, a44\n\t"
  "v_accvgpr_read_b32 v201, a45\n\t"
  "v_accvgpr_read_b32 v202, a46\n\t"
  "s_waitcnt lgkmcnt(14)\n\t"
  "v_mfma_f32_16x16x32_bf16 a[8:11], v[132:135], v[80:83], a[8:11]\n\t"
  "ds_read_b64_tr_b16 v[172:173], v226 offset:57856\n\t"
  "ds_read_b64_tr_b16 v[174:175], v226 offset:59904\n\t"
  "v_mfma_f32_16x16x32_bf16 a[12:15], v[132:135], v[112:115], a[12:15]\n\t"
  "v_accvgpr_read_b32 v203, a47\n\t"
  "v_cvt_pk_bf16_f32 v194, v200, v201\n\t"
  "v_cvt_pk_bf16_f32 v195, v202, v203\n\t"
  "v_mfma_f32_16x16x32_bf16 a[16:19], v[136:139], v[80:83], a[16:19]\n\t"
  "ds_read_b64_tr_b16 v[176:177], v225 offset:61440\n\t"
  "ds_read_b64_tr_b16 v[178:179], v225 offset:63488\n\t"
  "v_mfma_f32_16x16x32_bf16 a[20:23], v[136:139], v[112:115], a[20:23]\n\t"
  "buffer_store_dwordx4 v[192:195], v246, %[srdws], s52 offen offset:0 sc1\n\t"
  "v_accvgpr_read_b32 v200, a52\n\t"
  "v_mfma_f32_16x16x32_bf16 a[24:27], v[140:143], v[80:83], a[24:27]\n\t"
  "ds_read_b64_tr_b16 v[180:181], v226 offset:61440\n\t"
  "ds_read_b64_tr_b16 v[182:183], v226 offset:63488\n\t"
  "v_mfma_f32_16x16x32_bf16 a[28:31], v[140:143], v[112:115], a[28:31]\n\t"
  "v_accvgpr_read_b32 v201, a53\n\t"
  "v_accvgpr_read_b32 v202, a54\n\t"
  "v_accvgpr_read_b32 v203, a55\n\t"
  "s_waitcnt lgkmcnt(14)\n\t"
  "v_mfma_f32_16x16x32_bf16 a[0:3], v[144:147], v[84:87], a[0:3]\n\t"
  "ds_read_b64_tr_b16 v[184:185], v225 offset:61952\n\t"
  "ds_read_b64_tr_b16 v[186:187], v225 offset:64000\n\t"
  "v_mfma_f32_16x16x32_bf16 a[4:7], v[144:147], v[116:119], a[4:7]\n\t"
  "v_cvt_pk_bf16_f32 v196, v200, v201\n\t"
  "v_cvt_pk_bf16_f32 v197, v202, v203\n\t"
  "v_accvgpr_read_b32 v200, a60\n\t"
  "v_mfma_f32_16x16x32_bf16 a[8:11], v[148:151], v[84:87], a[8:11]\n\t"
  "ds_read_b64_tr_b16 v[188:189], v226 offset:61952\n\t"
  "ds_read_b64_tr_b16 v[190:191], v226 offset:64000\n\t"
  "v_mfma_f32_16x16x32_bf16 a[12:15], v[148:151], v[116:119], a[12:15]\n\t"
  "v_accvgpr_read_b32 v201, a61\n\t"
  "v_accvgpr_read_b32 v202, a62\n\t"
  "v_mfma_f32_16x16x32_bf16 a[16:19], v[152:155], v[84:87], a[16:19]\n\t"
  "s_waitcnt vmcnt(23)\n\t"
  "s_barrier\n\t"
  "ds_read_b64_tr_b16 v[128:129], v227 offset:0\n\t"
  "ds_read_b64_tr_b16 v[130:131], v227 offset:2048\n\t"
  "v_mfma_f32_16x16x32_bf16 a[20:23], v[152:155], v[116:119], a[20:23]\n\t"
  "s_add_u32 m0, %[m0o], 0\n\t"
  "s_nop 0\n\t"
  "buffer_load_dwordx4 v224, %[srdo], 0 offen offset:0 lds\n\t"
  "v_accvgpr_read_b32 v203, a63\n\t"
  "v_cvt_pk_bf16_f32 v198, v200, v201\n\t"
  "v_cvt_pk_bf16_f32 v199, v202, v203\n\t"
  "s_waitcnt lgkmcnt(14)\n\t"
  "v_mfma_f32_16x16x32_bf16 a[24:27], v[156:159], v[84:87], a[24:27]\n\t"
  "ds_read_b64_tr_b16 v[132:133], v228 offset:0\n\t"
  "ds_read_b64_tr_b16 v[134:135], v228 offset:2048\n\t"
  "v_mfma_f32_16x16x32_bf16 a[28:31], v[156:159], v[116:119], a[28:31]\n\t"
  "buffer_store_dwordx4 v[196:199], v246, %[srdws], s52 offen offset:1024 sc1\n\t"
  "v_add_u32 v246, 0x800, v246\n\t"
  "v_mfma_f32_16x16x32_bf16 a[0:3], v[160:163], v[88:91], a[0:3]\n\t"
  "ds_read_b64_tr_b16 v[136:137], v227 offset:512\n\t"
  "ds_read_b64_tr_b16 v[138:139], v227 offset:2560\n\t"
  "v_mfma_f32_16x16x32_bf16 a[4:7], v[160:163], v[120:123], a[4:7]\n\t"
  "buffer_load_dwordx4 v224, %[srdo], %[c6144] offen offset:1024 lds\n\t"
  "v_mfma_f32_16x16x32_bf16 a[8:11], v[164:167], v[88:91], a[8:11]\n\t"
  "ds_read_b64_tr_b16 v[140:141], v228 offset:512\n\t"
  "ds_read_b64_tr_b16 v[142:143], v228 offset:2560\n\t"
  "v_mfma_f32_16x16x32_bf16 a[12:15], v[164:167], v[120:123], a[12:15]\n\t"
  "s_waitcnt lgkmcnt(14)\n\t"
  "v_mfma_f32_16x16x32_bf16 a[16:19], v[168:171], v[88:91], a[16:19]\n\t"
  "ds_read_b64_tr_b16 v[144:145], v227 offset:4096\n\t"
  "ds_read_b64_tr_b16 v[146:147], v227 offset:6144\n\t"
  "v_mfma_f32_16x16x32_bf16 a[20:23], v[168:171], v[120:123], a[20:23]\n\t"
  "buffer_load_dwordx4 v224, %[srdo], %[c12288] offen offset:2048 lds\n\t"
  "v_mfma_f32_16x16x32_bf16 a[24:27], v[172:175], v[88:91], a[24:27]\n\t"
  "ds_read_b64_tr_b16 v[148:149], v228 offset:4096\n\t"
  "ds_read_b64_tr_b16 v[150:151], v228 offset:6144\n\t"
  "v_mfma_f32_16x16x32_bf16 a[28:31], v[172:175], v[120:123], a[28:31]\n\t"
  "v_mfma_f32_16x16x32_bf16 a[0:3], v[176:179], v[92:95], a[0:3]\n\t"
  "ds_read_b64_tr_b16 v[152:153], v227 offset:4608\n\t"
  "ds_read_b64_tr_b16 v[154:155], v227 offset:6656\n\t"
  "v_mfma_f32_16x16x32_bf16 a[4:7], v[176:179], v[124:127], a[4:7]\n\t"
  "buffer_load_dwordx4 v224, %[srdo], %[c18432] offen offset:3072 lds\n\t"
  "s_add_u32 m0, %[m0o], 4096\n\t"
  "s_waitcnt lgkmcnt(14)\n\t"
  "v_mfma_f32_16x16x32_bf16 a[8:11], v[180:183], v[92:95], a[8:11]\n\t"
  "ds_read_b64_tr_b16 v[156:157], v228 offset:4608\n\t"
  "ds_read_b64_tr_b16 v[158:159], v228 offset:6656\n\t"
  "v_mfma_f32_16x16x32_bf16 a[12:15], v[180:183], v[124:127], a[12:15]\n\t"
  "v_mfma_f32_16x16x32_bf16 a[16:19], v[184:187], v[92:95], a[16:19]\n\t"
  "ds_read_b64_tr_b16 v[160:161], v227 offset:8192\n\t"
  "ds_read_b64_tr_b16 v[162:163], v227 offset:10240\n\t"
  "v_mfma_f32_16x16x32_bf16 a[20:23], v[184:187], v[124:127], a[20:23]\n\t"
  "buffer_load_dwordx4 v224, %[srdo], %[c28672] offen offset:0 lds\n\t"
  "v_mfma_f32_16x16x32_bf16 a[24:27], v[188:191], v[92:95], a[24:27]\n\t"
  "ds_read_b64_tr_b16 v[164:165], v228 offset:8192\n\t"
  "ds_read_b64_tr_b16 v[166:167], v228 offset:10240\n\t"
  "v_mfma_f32_16x16x32_bf16 a[28:31], v[188:191], v[124:127], a[28:31]\n\t"
  "s_waitcnt lgkmcnt(14)\n\t"
  "v_mfma_f32_16x16x32_bf16 a[32:35], v[128:131], v[64:67], 0\n\t"
  "ds_read_b64_tr_b16 v[168:169], v227 offset:8704\n\t"
  "ds_read_b64_tr_b16 v[170:171], v227 offset:10752\n\t"
  "v_mfma_f32_16x16x32_bf16 a[36:39], v[128:131], v[96:99], 0\n\t"
  "buffer_load_dwordx4 v224, %[srdo], %[c34816] offen offset:1024 lds\n\t"
  "v_mfma_f32_16x16x32_bf16 a[40:43], v[132:135], v[64:67], 0\n\t"
  "ds_read_b64_tr_b16 v[172:173], v228 offset:8704\n\t"
  "ds_read_b64_tr_b16 v[174:175], v228 offset:10752\n\t"
  "v_mfma_f32_16x16x32_bf16 a[44:47], v[132:135], v[96:99], 0\n\t"
  "v_mfma_f32_16x16x32_bf16 a[48:51], v[136:139], v[64:67], 0\n\t"
  "ds_read_b64_tr_b16 v[176:177], v227 offset:12288\n\t"
  "ds_read_b64_tr_b16 v[178:179], v227 offset:14336\n\t"
  "v_mfma_f32_16x16x32_bf16 a[52:55], v[136:139], v[96:99], 0\n\t"
  "buffer_load_dwordx4 v224, %[srdo], %[c40960] offen offset:2048 lds\n\t"
  "s_waitcnt lgkmcnt(14)\n\t"
  "v_mfma_f32_16x16x32_bf16 a[56:59], v[140:143], v[64:67], 0\n\t"
  "ds_read_b64_tr_b16 v[180:181], v228 offset:12288\n\t"
  "ds_read_b64_tr_b16 v[182:183], v228 offset:14336\n\t"
  "v_mfma_f32_16x16x32_bf16 a[60:63], v[140:143], v[96:99], 0\n\t"
  "v_mfma_f32_16x16x32_bf16 a[32:35], v[144:147], v[68:71], a[32:35]\n\t"
  "ds_read_b64_tr_b16 v[184:185], v227 offset:12800\n\t"
  "ds_read_b64_tr_b16 v[186:187], v227 offset:14848\n\t"
  "v_mfma_f32_16x16x32_bf16 a[36:39], v[144:147], v[100:103], a[36:39]\n\t"
  "buffer_load_dwordx4 v224, %[srdo], %[c47104] offen offset:3072 lds\n\t"
  "v_add_u32 v224, 0x80, v224\n\t"
  "v_accvgpr_read_b32 v200, a0\n\t"
  "v_accvgpr_read_b32 v201, a1\n\t"
  "v_accvgpr_read_b32 v202, a2\n\t"
  "v_mfma_f32_16x16x32_bf16 a[40:43], v[148:151], v[68:71], a[40:43]\n\t"
  "ds_read_b64_tr_b16 v[188:189], v228 offset:12800\n\t"
  "ds_read_b64_tr_b16 v[190:191], v228 offset:14848\n\t"
  "v_mfma_f32_16x16x32_bf16 a[44:47], v[148:151], v[100:103], a[44:47]\n\t"
  "v_accvgpr_read_b32 v203, a3\n\t"
  "v_cvt_pk_bf16_f32 v192, v200, v201\n\t"
  "v_cvt_pk_bf16_f32 v193, v202, v203\n\t"
  "s_waitcnt lgkmcnt(14)\n\t"
  "v_mfma_f32_16x16x32_bf16 a[48:51], v[152:155], v[68:71], a[48:51]\n\t"
  "ds_read_b64_tr_b16 v[128:129], v227 offset:16384\n\t"
  "ds_read_b64_tr_b16 v[130:131], v227 offset:18432\n\t"
  "v_mfma_f32_16x16x32_bf16 a[52:55], v[152:155], v[100:103], a[52:55]\n\t"
  "v_accvgpr_read_b32 v200, a8\n\t"
  "v_accvgpr_read_b32 v201, a9\n\t"
  "v_mfma_f32_16x16x32_bf16 a[56:59], v[156:159], v[68:71], a[56:59]\n\t"
  "ds_read_b64_tr_b16 v[132:133], v228 offset:16384\n\t"
  "ds_read_b64_tr_b16 v[134:135], v228 offset:18432\n\t"
  "v_mfma_f32_16x16x32_bf16 a[60:63], v[156:159], v[100:103], a[60:63]\n\t"
  "v_accvgpr_read_b32 v202, a10\n\t"
  "v_accvgpr_read_b32 v203, a11\n\t"
  "v_cvt_pk_bf16_f32 v194, v200, v201\n\t"
  "v_mfma_f32_16x16x32_bf16 a[32:35], v[160:163], v[72:75], a[32:35]\n\t"
  "ds_read_b64_tr_b16 v[136:137], v227 offset:16896\n\t"
  "ds_read_b64_tr_b16 v[138:139], v227 offset:18944\n\t"
  "v_mfma_f32_16x16x32_bf16 a[36:39], v[160:163], v[104:107], a[36:39]\n\t"
  "v_cvt_pk_bf16_f32 v195, v202, v203\n\t"
  "buffer_store_dwordx4 v[192:195], v246, %[srdws], 0 offen offset:0 sc1\n\t"
  "v_accvgpr_read_b32 v200, a16\n\t"
  "s_waitcnt lgkmcnt(14)\n\t"
  "v_mfma_f32_16x16x32_bf16 a[40:43], v[164:167], v[72:75], a[40:43]\n\t"
  "ds_read_b64_tr_b16 v[140:141], v228 offset:16896\n\t"
  "ds_read_b64_tr_b16 v[142:143], v228 offset:18944\n\t"
  "v_mfma_f32_16x16x32_bf16 a[44:47], v[164:167], v[104:107], a[44:47]\n\t"
  "v_accvgpr_read_b32 v201, a17\n\t"
  "v_accvgpr_read_b32 v202, a18\n\t"
  "v_mfma_f32_16x16x32_bf16 a[48:51], v[168:171], v[72:75], a[48:51]\n\t"
  "ds_read_b64_tr_b16 v[144:145], v227 offset:20480\n\t"
  "ds_read_b64_tr_b16 v[146:147], v227 offset:22528\n\t"
  "v_mfma_f32_16x16x32_bf16 a[52:55], v[168:171], v[104:107], a[52:55]\n\t"
  "v_accvgpr_read_b32 v203, a19\n\t"
  "v_cvt_pk_bf16_f32 v196, v200, v201\n\t"
  "v_cvt_pk_bf16_f32 v197, v202, v203\n\t"
  "v_mfma_f32_16x16x32_bf16 a[56:59], v[172:175], v[72:75], a[56:59]\n\t"
  "ds_read_b64_tr_b16 v[148:149], v228 offset:20480\n\t"
  "ds_read_b64_tr_b16 v[150:151], v228 offset:22528\n\t"
  "v_mfma_f32_16x16x32_bf16 a[60:63], v[172:175], v[104:107], a[60:63]\n\t"
  "v_accvgpr_read_b32 v200, a24\n\t"
  "v_accvgpr_read_b32 v201, a25\n\t"
  "v_accvgpr_read_b32 v202, a26\n\t"
  "s_waitcnt lgkmcnt(14)\n\t"
  "v_mfma_f32_16x16x32_bf16 a[32:35], v[176:179], v[76:79], a[32:35]\n\t"
  "ds_read_b64_tr_b16 v[152:153], v227 offset:20992\n\t"
  "ds_read_b64_tr_b16 v[154:155], v227 offset:23040\n\t"
  "v_mfma_f32_16x16x32_bf16 a[36:39], v[176:179], v[108:111], a[36:39]\n\t"
  "v_accvgpr_read_b32 v203, a27\n\t"
  "v_cvt_pk_bf16_f32 v198, v200, v201\n\t"
  "v_mfma_f32_16x16x32_bf16 a[40:43], v[180:183], v[76:79], a[40:43]\n\t"
  "ds_read_b64_tr_b16 v[156:157], v228 offset:20992\n\t"
  "ds_read_b64_tr_b16 v[158:159], v228 offset:23040\n\t"
  "v_mfma_f32_16x16x32_bf16 a[44:47], v[180:183], v[108:111], a[44:47]\n\t"
  "v_cvt_pk_bf16_f32 v199, v202, v203\n\t"
  "buffer_store_dwordx4 v[196:199], v246, %[srdws], 0 offen offset:1024 sc1\n\t"
  "v_accvgpr_read_b32 v200, a4\n\t"
  "v_mfma_f32_16x16x32_bf16 a[48:51], v[184:187], v[76:79], a[48:51]\n\t"
  "ds_read_b64_tr_b16 v[160:161], v227 offset:24576\n\t"
  "ds_read_b64_tr_b16 v[162:163], v227 offset:26624\n\t"
  "v_mfma_f32_16x16x32_bf16 a[52:55], v[184:187], v[108:111], a[52:55]\n\t"
  "v_accvgpr_read_b32 v201, a5\n\t"
  "v_accvgpr_read_b32 v202, a6\n\t"
  "v_accvgpr_read_b32 v203, a7\n\t"
  "s_waitcnt lgkmcnt(14)\n\t"
  "v_mfma_f32_16x16x32_bf16 a[56:59], v[188:191], v[76:79], a[56:59]\n\t"
  "ds_read_b64_tr_b16 v[164:165], v228 offset:24576\n\t"
  "ds_read_b64_tr_b16 v[166:167], v228 offset:26624\n\t"
  "v_mfma_f32_16x16x32_bf16 a[60:63], v[188:191], v[108:111], a[60:63]\n\t"
  "v_cvt_pk_bf16_f32 v192, v200, v201\n\t"
  "v_cvt_pk_bf16_f32 v193, v202, v203\n\t"
  "v_mfma_f32_16x16x32_bf16 a[32:35], v[128:131], v[80:83], a[32:35]\n\t"
  "ds_read_b64_tr_b16 v[168:169], v227 offset:25088\n\t"
  "ds_read_b64_tr_b16 v[170:171], v227 offset:27136\n\t"
  "v_mfma_f32_16x16x32_bf16 a[36:39], v[128:131], v[112:115], a[36:39]\n\t"
  "v_accvgpr_read_b32 v200, a12\n\t"
  "v_accvgpr_read_b32 v201, a13\n\t"
  "v_accvgpr_read_b32 v202, a14\n\t"
  "v_mfma_f32_16x16x32_bf16 a[40:43], v[132:135], v[80:83], a[40:43]\n\t"
  "ds_read_b64_tr_b16 v[172:173], v228 offset:25088\n\t"
  "ds_read_b64_tr_b16 v[174:175], v228 offset:27136\n\t"
  "v_mfma_f32_16x16x32_bf16 a[44:47], v[132:135], v[112:115], a[44:47]\n\t"
  "v_accvgpr_read_b32 v203, a15\n\t"
  "v_cvt_pk_bf16_f32 v194, v200, v201\n\t"
  "v_cvt_pk_bf16_f32 v195, v202, v203\n\t"
  "s_waitcnt lgkmcnt(14)\n\t"
  "v_mfma_f32_16x16x32_bf16 a[48:51], v[136:139], v[80:83], a[48:51]\n\t"
  "ds_read_b64_tr_b16 v[176:177], v227 offset:28672\n\t"
  "ds_read_b64_tr_b16 v[178:179], v227 offset:30720\n\t"
  "v_mfma_f32_16x16x32_bf16 a[52:55], v[136:139], v[112:115], a[52:55]\n\t"
  "buffer_store_dwordx4 v[192:195], v246, %[srdws], s52 offen offset:0 sc1\n\t"
  "v_accvgpr_read_b32 v200, a20\n\t"
  "v_mfma_f32_16x16x32_bf16 a[56:59], v[140:143], v[80:83], a[56:59]\n\t"
  "ds_read_b64_tr_b16 v[180:181], v228 offset:28672\n\t"
  "ds_read_b64_tr_b16 v[182:183], v228 offset:30720\n\t"
  "v_mfma_f32_16x16x32_bf16 a[60:63], v[140:143], v[112:115], a[60:63]\n\t"
  "v_accvgpr_read_b32 v201, a21\n\t"
  "v_accvgpr_read_b32 v202, a22\n\t"
  "v_accvgpr_read_b32 v203, a23\n\t"
  "v_mfma_f32_16x16x32_bf16 a[32:35], v[144:147], v[84:87], a[32:35]\n\t"
  "ds_read_b64_tr_b16 v[184:185], v227 offset:29184\n\t"
  "ds_read_b64_tr_b16 v[186:187], v227 offset:31232\n\t"
  "v_mfma_f32_16x16x32_bf16 a[36:39], v[144:147], v[116:119], a[36:39]\n\t"
  "v_cvt_pk_bf16_f32 v196, v200, v201\n\t"
  "v_cvt_pk_bf16_f32 v197, v202, v203\n\t"
  "v_accvgpr_read_b32 v200, a28\n\t"
  "s_waitcnt lgkmcnt(14)\n\t"
  "v_mfma_f32_16x16x32_bf16 a[40:43], v[148:151], v[84:87], a[40:43]\n\t"
  "ds_read_b64_tr_b16 v[188:189], v228 offset:29184\n\t"
  "ds_read_b64_tr_b16 v[190:191], v228 offset:31232\n\t"
  "v_mfma_f32_16x16x32_bf16 a[44:47], v[148:151], v[116:119], a[44:47]\n\t"
  "v_accvgpr_read_b32 v201, a29\n\t"
  "v_accvgpr_read_b32 v202, a30\n\t"
  "v_mfma_f32_16x16x32_bf16 a[48:51], v[152:155], v[84:87], a[48:51]\n\t"
  "s_waitcnt vmcnt(27)\n\t"
  "s_barrier\n\t"
  "ds_read_b64_tr_b16 v[128:129], v227 offset:32768\n\t"
  "ds_read_b64_tr_b16 v[130:131], v227 offset:34816\n\t"
  "v_mfma_f32_16x16x32_bf16 a[52:55], v[152:155], v[116:119], a[52:55]\n\t"
  "s_add_u32 m0, %[m0k], 32768\n\t"
  "s_nop 0\n\t"
  "buffer_load_dwordx4 v231, %[srdws], 0 offen offset:0 sc1 lds\n\t"
  "v_accvgpr_read_b32 v203, a31\n\t"
  "v_cvt_pk_bf16_f32 v198, v200, v201\n\t"
  "v_cvt_pk_bf16_f32 v199, v202, v203\n\t"
  "v_mfma_f32_16x16x32_bf16 a[56:59], v[156:159], v[84:87], a[56:59]\n\t"
  "ds_read_b64_tr_b16 v[132:133], v228 offset:32768\n\t"
  "ds_read_b64_tr_b16 v[134:135], v228 offset:34816\n\t"
  "v_mfma_f32_16x16x32_bf16 a[60:63], v[156:159], v[116:119], a[60:63]\n\t"
  "buffer_store_dwordx4 v[196:199], v246, %[srdws], s52 offen offset:1024 sc1\n\t"
  "v_add_u32 v246, 0x800, v246\n\t"
  "s_waitcnt lgkmcnt(14)\n\t"
  "v_mfma_f32_16x16x32_bf16 a[32:35], v[160:163], v[88:91], a[32:35]\n\t"
  "ds_read_b64_tr_b16 v[136:137], v227 offset:33280\n\t"
  "ds_read_b64_tr_b16 v[138:139], v227 offset:35328\n\t"
  "v_mfma_f32_16x16x32_bf16 a[36:39], v[160:163], v[120:123], a[36:39]\n\t"
  "v_mfma_f32_16x16x32_bf16 a[40:43], v[164:167], v[88:91], a[40:43]\n\t"
  "ds_read_b64_tr_b16 v[140:141], v228 offset:33280\n\t"
  "ds_read_b64_tr_b16 v[142:143], v228 offset:35328\n\t"
  "v_mfma_f32_16x16x32_bf16 a[44:47], v[164:167], v[120:123], a[44:47]\n\t"
  "buffer_load_dwordx4 v231, %[srdws], 0 offen offset:1024 sc1 lds\n\t"
  "v_mfma_f32_16x16x32_bf16 a[48:51], v[168:171], v[88:91], a[48:51]\n\t"
  "ds_read_b64_tr_b16 v[144:145], v227 offset:36864\n\t"
  "ds_read_b64_tr_b16 v[146:147], v227 offset:38912\n\t"
  "v_mfma_f32_16x16x32_bf16 a[52:55], v[168:171], v[120:123], a[52:55]\n\t"
  "s_waitcnt lgkmcnt(14)\n\t"
  "v_mfma_f32_16x16x32_bf16 a[56:59], v[172:175], v[88:91], a[56:59]\n\t"
  "ds_read_b64_tr_b16 v[148:149], v228 offset:36864\n\t"
  "ds_read_b64_tr_b16 v[150:151], v228 offset:38912\n\t"
  "v_mfma_f32_16x16x32_bf16 a[60:63], v[172:175], v[120:123], a[60:63]\n\t"
  "v_mfma_f32_16x16x32_bf16 a[32:35], v[176:179], v[92:95], a[32:35]\n\t"
  "ds_read_b64_tr_b16 v[152:153], v227 offset:37376\n\t"
  "ds_read_b64_tr_b16 v[154:155], v227 offset:39424\n\t"
  "v_mfma_f32_16x16x32_bf16 a[36:39], v[176:179], v[124:127], a[36:39]\n\t"
  "buffer_load_dwordx4 v231, %[srdws], 0 offen offset:2048 sc1 lds\n\t"
  "v_mfma_f32_16x16x32_bf16 a[40:43], v[180:183], v[92:95], a[40:43]\n\t"
  "ds_read_b64_tr_b16 v[156:157], v228 offset:37376\n\t"
  "ds_read_b64_tr_b16 v[158:159], v228 offset:39424\n\t"
  "v_mfma_f32_16x16x32_bf16 a[44:47], v[180:183], v[124:127], a[44:47]\n\t"
  "s_waitcnt lgkmcnt(14)\n\t"
  "v_mfma_f32_16x16x32_bf16 a[48:51], v[184:187], v[92:95], a[48:51]\n\t"
  "ds_read_b64_tr_b16 v[160:161], v227 offset:40960\n\t"
  "ds_read_b64_tr_b16 v[162:163], v227 offset:43008\n\t"
  "v_mfma_f32_16x16x32_bf16 a[52:55], v[184:187], v[124:127], a[52:55]\n\t"
  "buffer_load_dwordx4 v231, %[srdws], 0 offen offset:3072 sc1 lds\n\t"
  "v_add_u32 v231, 0x4000, v231\n\t"
  "s_add_u32 m0, %[m0w], 49152\n\t"
  "v_mfma_f32_16x16x32_bf16 a[56:59], v[188:191], v[92:95], a[56:59]\n\t"
  "ds_read_b64_tr_b16 v[164:165], v228 offset:40960\n\t"
  "ds_read_b64_tr_b16 v[166:167], v228 offset:43008\n\t"
  "v_mfma_f32_16x16x32_bf16 a[60:63], v[188:191], v[124:127], a[60:63]\n\t"
  "v_mfma_f32_16x16x32_bf16 a[0:3], v[128:131], v[64:67], 0\n\t"
  "ds_read_b64_tr_b16 v[168:169], v227 offset:41472\n\t"
  "ds_read_b64_tr_b16 v[170:171], v227 offset:43520\n\t"
  "v_mfma_f32_16x16x32_bf16 a[4:7], v[128:131], v[96:99], 0\n\t"
  "s_waitcnt lgkmcnt(14)\n\t"
  "v_mfma_f32_16x16x32_bf16 a[8:11], v[132:135], v[64:67], 0\n\t"
  "ds_read_b64_tr_b16 v[172:173], v228 offset:41472\n\t"
  "ds_read_b64_tr_b16 v[174:175], v228 offset:43520\n\t"
  "v_mfma_f32_16x16x32_bf16 a[12:15], v[132:135], v[96:99], 0\n\t"
  "buffer_load_dwordx4 v232, %[srdp], 0 offen offset:0 lds\n\t"
  "v_add_u32 v232, 0x80, v232\n\t"
  "v_mfma_f32_16x16x32_bf16 a[16:19], v[136:139], v[64:67], 0\n\t"
  "ds_read_b64_tr_b16 v[176:177], v227 offset:45056\n\t"
  "ds_read_b64_tr_b16 v[178:179], v227 offset:47104\n\t"
  "v_mfma_f32_16x16x32_bf16 a[20:23], v[136:139], v[96:99], 0\n\t"
  "v_mfma_f32_16x16x32_bf16 a[24:27], v[140:143], v[64:67], 0\n\t"
  "ds_read_b64_tr_b16 v[180:181], v228 offset:45056\n\t"
  "ds_read_b64_tr_b16 v[182:183], v228 offset:47104\n\t"
  "v_mfma_f32_16x16x32_bf16 a[28:31], v[140:143], v[96:99], 0\n\t"
  "buffer_load_dwordx4 v233, %[srdp], %[c31744] offen offset:1024 lds\n\t"
  "v_add_u32 v233, 0x80, v233\n\t"
  "s_waitcnt lgkmcnt(14)\n\t"
  "v_mfma_f32_16x16x32_bf16 a[0:3], v[144:147], v[68:71], a[0:3]\n\t"
  "ds_read_b64_tr_b16 v[184:185], v227 offset:45568\n\t"
  "ds_read_b64_tr_b16 v[186:187], v227 offset:47616\n\t"
  "v_mfma_f32_16x16x32_bf16 a[4:7], v[144:147], v[100:103], a[4:7]\n\t"
  "v_accvgpr_read_b32 v200, a32\n\t"
  "v_accvgpr_read_b32 v201, a33\n\t"
  "v_accvgpr_read_b32 v202, a34\n\t"
  "v_mfma_f32_16x16x32_bf16 a[8:11], v[148:151], v[68:71], a[8:11]\n\t"
  "ds_read_b64_tr_b16 v[188:189], v228 offset:45568\n\t"
  "ds_read_b64_tr_b16 v[190:191], v228 offset:47616\n\t"
  "v_mfma_f32_16x16x32_bf16 a[12:15], v[148:151], v[100:103], a[12:15]\n\t"
  "v_accvgpr_read_b32 v203, a35\n\t"
  "v_cvt_pk_bf16_f32 v192, v200, v201\n\t"
  "v_cvt_pk_bf16_f32 v193, v202, v203\n\t"
  "v_mfma_f32_16x16x32_bf16 a[16:19], v[152:155], v[68:71], a[16:19]\n\t"
  "ds_read_b64_tr_b16 v[128:129], v227 offset:49152\n\t"
  "ds_read_b64_tr_b16 v[130:131], v227 offset:51200\n\t"
  "v_mfma_f32_16x16x32_bf16 a[20:23], v[152:155], v[100:103], a[20:23]\n\t"
  "v_accvgpr_read_b32 v200, a40\n\t"
  "v_accvgpr_read_b32 v201, a41\n\t"
  "s_waitcnt lgkmcnt(14)\n\t"
  "v_mfma_f32_16x16x32_bf16 a[24:27], v[156:159], v[68:71], a[24:27]\n\t"
  "ds_read_b64_tr_b16 v[132:133], v228 offset:49152\n\t"
  "ds_read_b64_tr_b16 v[134:135], v228 offset:51200\n\t"
  "v_mfma_f32_16x16x32_bf16 a[28:31], v[156:159], v[100:103], a[28:31]\n\t"
  "v_accvgpr_read_b32 v202, a42\n\t"
  "v_accvgpr_read_b32 v203, a43\n\t"
  "v_cvt_pk_bf16_f32 v194, v200, v201\n\t"
  "v_mfma_f32_16x16x32_bf16 a[0:3], v[160:163], v[72:75], a[0:3]\n\t"
  "ds_read_b64_tr_b16 v[136:137], v227 offset:49664\n\t"
  "ds_read_b64_tr_b16 v[138:139], v227 offset:51712\n\t"
  "v_mfma_f32_16x16x32_bf16 a[4:7], v[160:163], v[104:107], a[4:7]\n\t"
  "v_cvt_pk_bf16_f32 v195, v202, v203\n\t"
  "buffer_store_dwordx4 v[192:195], v246, %[srdws], 0 offen offset:0 sc1\n\t"
  "v_accvgpr_read_b32 v200, a48\n\t"
  "v_mfma_f32_16x16x32_bf16 a[8:11], v[164:167], v[72:75], a[8:11]\n\t"
  "ds_read_b64_tr_b16 v[140:141], v228 offset:49664\n\t"
  "ds_read_b64_tr_b16 v[142:143], v228 offset:51712\n\t"
  "v_mfma_f32_16x16x32_bf16 a[12:15], v[164:167], v[104:107], a[12:15]\n\t"
  "v_accvgpr_read_b32 v201, a49\n\t"
  "v_accvgpr_read_b32 v202, a50\n\t"
  "s_waitcnt lgkmcnt(14)\n\t"
  "v_mfma_f32_16x16x32_bf16 a[16:19], v[168:171], v[72:75], a[16:19]\n\t"
  "ds_read_b64_tr_b16 v[144:145], v227 offset:53248\n\t"
  "ds_read_b64_tr_b16 v[146:147], v227 offset:55296\n\t"
  "v_mfma_f32_16x16x32_bf16 a[20:23], v[168:171], v[104:107], a[20:23]\n\t"
  "v_accvgpr_read_b32 v203, a51\n\t"
  "v_cvt_pk_bf16_f32 v196, v200, v201\n\t"
  "v_cvt_pk_bf16_f32 v197, v202, v203\n\t"
  "v_mfma_f32_16x16x32_bf16 a[24:27], v[172:175], v[72:75], a[24:27]\n\t"
  "ds_read_b64_tr_b16 v[148:149], v228 offset:53248\n\t"
  "ds_read_b64_tr_b16 v[150:151], v228 offset:55296\n\t"
  "v_mfma_f32_16x16x32_bf16 a[28:31], v[172:175], v[104:107], a[28:31]\n\t"
  "v_accvgpr_read_b32 v200, a56\n\t"
  "v_accvgpr_read_b32 v201, a57\n\t"
  "v_accvgpr_read_b32 v202, a58\n\t"
  "v_mfma_f32_16x16x32_bf16 a[0:3], v[176:179], v[76:79], a[0:3]\n\t"
  "ds_read_b64_tr_b16 v[152:153], v227 offset:53760\n\t"
  "ds_read_b64_tr_b16 v[154:155], v227 offset:55808\n\t"
  "v_mfma_f32_16x16x32_bf16 a[4:7], v[176:179], v[108:111], a[4:7]\n\t"
  "v_accvgpr_read_b32 v203, a59\n\t"
  "v_cvt_pk_bf16_f32 v198, v200, v201\n\t"
  "s_waitcnt lgkmcnt(14)\n\t"
  "v_mfma_f32_16x16x32_bf16 a[8:11], v[180:183], v[76:79], a[8:11]\n\t"
  "ds_read_b64_tr_b16 v[156:157], v228 offset:53760\n\t"
  "ds_read_b64_tr_b16 v[158:159], v228 offset:55808\n\t"
  "v_mfma_f32_16x16x32_bf16 a[12:15], v[180:183], v[108:111], a[12:15]\n\t"
  "v_cvt_pk_bf16_f32 v199, v202, v203\n\t"
  "buffer_store_dwordx4 v[196:199], v246, %[srdws], 0 offen offset:1024 sc1\n\t"
  "v_accvgpr_read_b32 v200, a36\n\t"
  "v_mfma_f32_16x16x32_bf16 a[16:19], v[184:187], v[76:79], a[16:19]\n\t"
  "ds_read_b64_tr_b16 v[160:161], v227 offset:57344\n\t"
  "ds_read_b64_tr_b16 v[162:163], v227 offset:59392\n\t"
  "v_mfma_f32_16x16x32_bf16 a[20:23], v[184:187], v[108:111], a[20:23]\n\t"
  "v_accvgpr_read_b32 v201, a37\n\t"
  "v_accvgpr_read_b32 v202, a38\n\t"
  "v_accvgpr_read_b32 v203, a39\n\t"
  "v_mfma_f32_16x16x32_bf16 a[24:27], v[188:191], v[76:79], a[24:27]\n\t"
  "ds_read_b64_tr_b16 v[164:165], v228 offset:57344\n\t"
  "ds_read_b64_tr_b16 v[166:167], v228 offset:59392\n\t"
  "v_mfma_f32_16x16x32_bf16 a[28:31], v[188:191], v[108:111], a[28:31]\n\t"
  "v_cvt_pk_bf16_f32 v192, v200, v201\n\t"
  "v_cvt_pk_bf16_f32 v193, v202, v203\n\t"
  "s_waitcnt lgkmcnt(14)\n\t"
  "v_mfma_f32_16x16x32_bf16 a[0:3], v[128:131], v[80:83], a[0:3]\n\t"
  "ds_read_b64_tr_b16 v[168:169], v227 offset:57856\n\t"
  "ds_read_b64_tr_b16 v[170:171], v227 offset:59904\n\t"
  "v_mfma_f32_16x16x32_bf16 a[4:7], v[128:131], v[112:115], a[4:7]\n\t"
  "v_accvgpr_read_b32 v200, a44\n\t"
  "v_accvgpr_read_b32 v201, a45\n\t"
  "v_accvgpr_read_b32 v202, a46\n\t"
  "v_mfma_f32_16x16x32_bf16 a[8:11], v[132:135], v[80:83], a[8:11]\n\t"
  "ds_read_b64_tr_b16 v[172:173], v228 offset:57856\n\t"
  "ds_read_b64_tr_b16 v[174:175], v228 offset:59904\n\t"
  "v_mfma_f32_16x16x32_bf16 a[12:15], v[132:135], v[112:115], a[12:15]\n\t"
  "v_accvgpr_read_b32 v203, a47\n\t"
  "v_cvt_pk_bf16_f32 v194, v200, v201\n\t"
  "v_cvt_pk_bf16_f32 v195, v202, v203\n\t"
  "v_mfma_f32_16x16x32_bf16 a[16:19], v[136:139], v[80:83], a[16:19]\n\t"
  "ds_read_b64_tr_b16 v[176:177], v227 offset:61440\n\t"
  "ds_read_b64_tr_b16 v[178:179], v227 offset:63488\n\t"
  "v_mfma_f32_16x16x32_bf16 a[20:23], v[136:139], v[112:115], a[20:23]\n\t"
  "buffer_store_dwordx4 v[192:195], v246, %[srdws], s52 offen offset:0 sc1\n\t"
  "v_accvgpr_read_b32 v200, a52\n\t"
  "s_waitcnt lgkmcnt(14)\n\t"
  "v_mfma_f32_16x16x32_bf16 a[24:27], v[140:143], v[80:83], a[24:27]\n\t"
  "ds_read_b64_tr_b16 v[180:181], v228 offset:61440\n\t"
  "ds_read_b64_tr_b16 v[182:183], v228 offset:63488\n\t"
  "v_mfma_f32_16x16x32_bf16 a[28:31], v[140:143], v[112:115], a[28:31]\n\t"
  "v_accvgpr_read_b32 v201, a53\n\t"
  "v_accvgpr_read_b32 v202, a54\n\t"
  "v_accvgpr_read_b32 v203, a55\n\t"
  "v_mfma_f32_16x16x32_bf16 a[0:3], v[144:147], v[84:87], a[0:3]\n\t"
  "ds_read_b64_tr_b16 v[184:185], v227 offset:61952\n\t"
  "ds_read_b64_tr_b16 v[186:187], v227 offset:64000\n\t"
  "v_mfma_f32_16x16x32_bf16 a[4:7], v[144:147], v[116:119], a[4:7]\n\t"
  "v_cvt_pk_bf16_f32 v196, v200, v201\n\t"
  "v_cvt_pk_bf16_f32 v197, v202, v203\n\t"
  "v_accvgpr_read_b32 v200, a60\n\t"
  "v_mfma_f32_16x16x32_bf16 a[8:11], v[148:151], v[84:87], a[8:11]\n\t"
  "ds_read_b64_tr_b16 v[188:189], v228 offset:61952\n\t"
  "ds_read_b64_tr_b16 v[190:191], v228 offset:64000\n\t"
  "v_mfma_f32_16x16x32_bf16 a[12:15], v[148:151], v[116:119], a[12:15]\n\t"
  "v_accvgpr_read_b32 v201, a61\n\t"
  "v_accvgpr_read_b32 v202, a62\n\t"
  "s_waitcnt lgkmcnt(14)\n\t"
  "v_mfma_f32_16x16x32_bf16 a[16:19], v[152:155], v[84:87], a[16:19]\n\t"
  "s_waitcnt vmcnt(25)\n\t"
  "s_barrier\n\t"
  "ds_read_b64_tr_b16 v[128:129], v229 offset:0\n\t"
  "ds_read_b64_tr_b16 v[130:131], v229 offset:2048\n\t"
  "v_mfma_f32_16x16x32_bf16 a[20:23], v[152:155], v[116:119], a[20:23]\n\t"
  "s_add_u32 m0, %[m0k], 65536\n\t"
  "s_nop 0\n\t"
  "buffer_load_dwordx4 v231, %[srdws], 0 offen offset:0 sc1 lds\n\t"
  "v_accvgpr_read_b32 v203, a63\n\t"
  "v_cvt_pk_bf16_f32 v198, v200, v201\n\t"
  "v_cvt_pk_bf16_f32 v199, v202, v203\n\t"
  "v_mfma_f32_16x16x32_bf16 a[24:27], v[156:159], v[84:87], a[24:27]\n\t"
  "ds_read_b64_tr_b16 v[132:133], v230 offset:0\n\t"
  "ds_read_b64_tr_b16 v[134:135], v230 offset:2048\n\t"
  "v_mfma_f32_16x16x32_bf16 a[28:31], v[156:159], v[116:119], a[28:31]\n\t"
  "buffer_store_dwordx4 v[196:199], v246, %[srdws], s52 offen offset:1024 sc1\n\t"
  "v_add_u32 v246, 0x800, v246\n\t"
  "v_mfma_f32_16x16x32_bf16 a[0:3], v[160:163], v[88:91], a[0:3]\n\t"
  "ds_read_b64_tr_b16 v[136:137], v229 offset:512\n\t"
  "ds_read_b64_tr_b16 v[138:139], v229 offset:2560\n\t"
  "v_mfma_f32_16x16x32_bf16 a[4:7], v[160:163], v[120:123], a[4:7]\n\t"
  "s_waitcnt lgkmcnt(14)\n\t"
  "v_mfma_f32_16x16x32_bf16 a[8:11], v[164:167], v[88:91], a[8:11]\n\t"
  "ds_read_b64_tr_b16 v[140:141], v230 offset:512\n\t"
  "ds_read_b64_tr_b16 v[142:143], v230 offset:2560\n\t"
  "v_mfma_f32_16x16x32_bf16 a[12:15], v[164:167], v[120:123], a[12:15]\n\t"
  "buffer_load_dwordx4 v231, %[srdws], 0 offen offset:1024 sc1 lds\n\t"
  "v_mfma_f32_16x16x32_bf16 a[16:19], v[168:171], v[88:91], a[16:19]\n\t"
  "ds_read_b64_tr_b16 v[144:145], v229 offset:4096\n\t"
  "ds_read_b64_tr_b16 v[146:147], v229 offset:6144\n\t"
  "v_mfma_f32_16x16x32_bf16 a[20:23], v[168:171], v[120:123], a[20:23]\n\t"
  "v_mfma_f32_16x16x32_bf16 a[24:27], v[172:175], v[88:91], a[24:27]\n\t"
  "ds_read_b64_tr_b16 v[148:149], v230 offset:4096\n\t"
  "ds_read_b64_tr_b16 v[150:151], v230 offset:6144\n\t"
  "v_mfma_f32_16x16x32_bf16 a[28:31], v[172:175], v[120:123], a[28:31]\n\t"
  "s_waitcnt lgkmcnt(14)\n\t"
  "v_mfma_f32_16x16x32_bf16 a[0:3], v[176:179], v[92:95], a[0:3]\n\t"
  "ds_read_b64_tr_b16 v[152:153], v229 offset:4608\n\t"
  "ds_read_b64_tr_b16 v[154:155], v229 offset:6656\n\t"
  "v_mfma_f32_16x16x32_bf16 a[4:7], v[176:179], v[124:127], a[4:7]\n\t"
  "buffer_load_dwordx4 v231, %[srdws], 0 offen offset:2048 sc1 lds\n\t"
  "v_mfma_f32_16x16x32_bf16 a[8:11], v[180:183], v[92:95], a[8:11]\n\t"
  "ds_read_b64_tr_b16 v[156:157], v230 offset:4608\n\t"
  "ds_read_b64_tr_b16 v[158:159], v230 offset:6656\n\t"
  "v_mfma_f32_16x16x32_bf16 a[12:15], v[180:183], v[124:127], a[12:15]\n\t"
  "v_mfma_f32_16x16x32_bf16 a[16:19], v[184:187], v[92:95], a[16:19]\n\t"
  "ds_read_b64_tr_b16 v[160:161], v229 offset:8192\n\t"
  "ds_read_b64_tr_b16 v[162:163], v229 offset:10240\n\t"
  "v_mfma_f32_16x16x32_bf16 a[20:23], v[184:187], v[124:127], a[20:23]\n\t"
  "buffer_load_dwordx4 v231, %[srdws], 0 offen offset:3072 sc1 lds\n\t"
  "v_add_u32 v231, 0x4000, v231\n\t"
  "s_add_u32 m0, %[m0w], 81920\n\t"
  "s_waitcnt lgkmcnt(14)\n\t"
  "v_mfma_f32_16x16x32_bf16 a[24:27], v[188:191], v[92:95], a[24:27]\n\t"
  "ds_read_b64_tr_b16 v[164:165], v230 offset:8192\n\t"
  "ds_read_b64_tr_b16 v[166:167], v230 offset:10240\n\t"
  "v_mfma_f32_16x16x32_bf16 a[28:31], v[188:191], v[124:127], a[28:31]\n\t"
  "v_mfma_f32_16x16x32_bf16 a[32:35], v[128:131], v[64:67], 0\n\t"
  "ds_read_b64_tr_b16 v[168:169], v229 offset:8704\n\t"
  "ds_read_b64_tr_b16 v[170:171], v229 offset:10752\n\t"
  "v_mfma_f32_16x16x32_bf16 a[36:39], v[128:131], v[96:99], 0\n\t"
  "v_mfma_f32_16x16x32_bf16 a[40:43], v[132:135], v[64:67], 0\n\t"
  "ds_read_b64_tr_b16 v[172:173], v230 offset:8704\n\t"
  "ds_read_b64_tr_b16 v[174:175], v230 offset:10752\n\t"
  "v_mfma_f32_16x16x32_bf16 a[44:47], v[132:135], v[96:99], 0\n\t"
  "buffer_load_dwordx4 v232, %[srdp], 0 offen offset:0 lds\n\t"
  "v_add_u32 v232, 0x80, v232\n\t"
  "s_waitcnt lgkmcnt(14)\n\t"
  "v_mfma_f32_16x16x32_bf16 a[48:51], v[136:139], v[64:67], 0\n\t"
  "ds_read_b64_tr_b16 v[176:177], v229 offset:12288\n\t"
  "ds_read_b64_tr_b16 v[178:179], v229 offset:14336\n\t"
  "v_mfma_f32_16x16x32_bf16 a[52:55], v[136:139], v[96:99], 0\n\t"
  "v_mfma_f32_16x16x32_bf16 a[56:59], v[140:143], v[64:67], 0\n\t"
  "ds_read_b64_tr_b16 v[180:181], v230 offset:12288\n\t"
  "ds_read_b64_tr_b16 v[182:183], v230 offset:14336\n\t"
  "v_mfma_f32_16x16x32_bf16 a[60:63], v[140:143], v[96:99], 0\n\t"
  "buffer_load_dwordx4 v233, %[srdp], %[c31744] offen offset:1024 lds\n\t"
  "v_add_u32 v233, 0x80, v233\n\t"
  "v_mfma_f32_16x16x32_bf16 a[32:35], v[144:147], v[68:71], a[32:35]\n\t"
  "ds_read_b64_tr_b16 v[184:185], v229 offset:12800\n\t"
  "ds_read_b64_tr_b16 v[186:187], v229 offset:14848\n\t"
  "v_mfma_f32_16x16x32_bf16 a[36:39], v[144:147], v[100:103], a[36:39]\n\t"
  "v_accvgpr_read_b32 v200, a0\n\t"
  "v_accvgpr_read_b32 v201, a1\n\t"
  "v_accvgpr_read_b32 v202, a2\n\t"
  "s_waitcnt lgkmcnt(14)\n\t"
  "v_mfma_f32_16x16x32_bf16 a[40:43], v[148:151], v[68:71], a[40:43]\n\t"
  "ds_read_b64_tr_b16 v[188:189], v230 offset:12800\n\t"
  "ds_read_b64_tr_b16 v[190:191], v230 offset:14848\n\t"
  "v_mfma_f32_16x16x32_bf16 a[44:47], v[148:151], v[100:103], a[44:47]\n\t"
  "v_accvgpr_read_b32 v203, a3\n\t"
  "v_cvt_pk_bf16_f32 v192, v200, v201\n\t"
  "v_cvt_pk_bf16_f32 v193, v202, v203\n\t"
  "v_mfma_f32_16x16x32_bf16 a[48:51], v[152:155], v[68:71], a[48:51]\n\t"
  "ds_read_b64_tr_b16 v[128:129], v229 offset:16384\n\t"
  "ds_read_b64_tr_b16 v[130:131], v229 offset:18432\n\t"
  "v_mfma_f32_16x16x32_bf16 a[52:55], v[152:155], v[100:103], a[52:55]\n\t"
  "v_accvgpr_read_b32 v200, a8\n\t"
  "v_accvgpr_read_b32 v201, a9\n\t"
  "v_mfma_f32_16x16x32_bf16 a[56:59], v[156:159], v[68:71], a[56:59]\n\t"
  "ds_read_b64_tr_b16 v[132:133], v230 offset:16384\n\t"
  "ds_read_b64_tr_b16 v[134:135], v230 offset:18432\n\t"
  "v_mfma_f32_16x16x32_bf16 a[60:63], v[156:159], v[100:103], a[60:63]\n\t"
  "v_accvgpr_read_b32 v202, a10\n\t"
  "v_accvgpr_read_b32 v203, a11\n\t"
  "v_cvt_pk_bf16_f32 v194, v200, v201\n\t"
  "s_waitcnt lgkmcnt(14)\n\t"
  "v_mfma_f32_16x16x32_bf16 a[32:35], v[160:163], v[72:75], a[32:35]\n\t"
  "ds_read_b64_tr_b16 v[136:137], v229 offset:16896\n\t"
  "ds_read_b64_tr_b16 v[138:139], v229 offset:18944\n\t"
  "v_mfma_f32_16x16x32_bf16 a[36:39], v[160:163], v[104:107], a[36:39]\n\t"
  "v_cvt_pk_bf16_f32 v195, v202, v203\n\t"
  "buffer_store_dwordx4 v[192:195], v246, %[srdws], 0 offen offset:0 sc1\n\t"
  "v_accvgpr_read_b32 v200, a16\n\t"
  "v_mfma_f32_16x16x32_bf16 a[40:43], v[164:167], v[72:75], a[40:43]\n\t"
  "ds_read_b64_tr_b16 v[140:141], v230 offset:16896\n\t"
  "ds_read_b64_tr_b16 v[142:143], v230 offset:18944\n\t"
  "v_mfma_f32_16x16x32_bf16 a[44:47], v[164:167], v[104:107], a[44:47]\n\t"
  "v_accvgpr_read_b32 v201, a17\n\t"
  "v_accvgpr_read_b32 v202, a18\n\t"
  "v_mfma_f32_16x16x32_bf16 a[48:51], v[168:171], v[72:75], a[48:51]\n\t"
  "ds_read_b64_tr_b16 v[144:145], v229 offset:20480\n\t"
  "ds_read_b64_tr_b16 v[146:147], v229 offset:22528\n\t"
  "v_mfma_f32_16x16x32_bf16 a[52:55], v[168:171], v[104:107], a[52:55]\n\t"
  "v_accvgpr_read_b32 v203, a19\n\t"
  "v_cvt_pk_bf16_f32 v196, v200, v201\n\t"
  "v_cvt_pk_bf16_f32 v197, v202, v203\n\t"
  "s_waitcnt lgkmcnt(14)\n\t"
  "v_mfma_f32_16x16x32_bf16 a[56:59], v[172:175], v[72:75], a[56:59]\n\t"
  "ds_read_b64_tr_b16 v[148:149], v230 offset:20480\n\t"
  "ds_read_b64_tr_b16 v[150:151], v230 offset:22528\n\t"
  "v_mfma_f32_16x16x32_bf16 a[60:63], v[172:175], v[104:107], a[60:63]\n\t"
  "v_accvgpr_read_b32 v200, a24\n\t"
  "v_accvgpr_read_b32 v201, a25\n\t"
  "v_accvgpr_read_b32 v202, a26\n\t"
  "v_mfma_f32_16x16x32_bf16 a[32:35], v[176:179], v[76:79], a[32:35]\n\t"
  "ds_read_b64_tr_b16 v[152:153], v229 offset:20992\n\t"
  "ds_read_b64_tr_b16 v[154:155], v229 offset:23040\n\t"
  "v_mfma_f32_16x16x32_bf16 a[36:39], v[176:179], v[108:111], a[36:39]\n\t"
  "v_accvgpr_read_b32 v203, a27\n\t"
  "v_cvt_pk_bf16_f32 v198, v200, v201\n\t"
  "v_mfma_f32_16x16x32_bf16 a[40:43], v[180:183], v[76:79], a[40:43]\n\t"
  "ds_read_b64_tr_b16 v[156:157], v230 offset:20992\n\t"
  "ds_read_b64_tr_b16 v[158:159], v230 offset:23040\n\t"
  "v_mfma_f32_16x16x32_bf16 a[44:47], v[180:183], v[108:111], a[44:47]\n\t"
  "v_cvt_pk_bf16_f32 v199, v202, v203\n\t"
  "buffer_store_dwordx4 v[196:199], v246, %[srdws], 0 offen offset:1024 sc1\n\t"
  "v_accvgpr_read_b32 v200, a4\n\t"
  "s_waitcnt lgkmcnt(14)\n\t"
  "v_mfma_f32_16x16x32_bf16 a[48:51], v[184:187], v[76:79], a[48:51]\n\t"
  "ds_read_b64_tr_b16 v[160:161], v229 offset:24576\n\t"
  "ds_read_b64_tr_b16 v[162:163], v229 offset:26624\n\t"
  "v_mfma_f32_16x16x32_bf16 a[52:55], v[184:187], v[108:111], a[52:55]\n\t"
  "v_accvgpr_read_b32 v201, a5\n\t"
  "v_accvgpr_read_b32 v202, a6\n\t"
  "v_accvgpr_read_b32 v203, a7\n\t"
  "v_mfma_f32_16x16x32_bf16 a[56:59], v[188:191], v[76:79], a[56:59]\n\t"
  "ds_read_b64_tr_b16 v[164:165], v230 offset:24576\n\t"
  "ds_read_b64_tr_b16 v[166:167], v230 offset:26624\n\t"
  "v_mfma_f32_16x16x32_bf16 a[60:63], v[188:191], v[108:111], a[60:63]\n\t"
  "v_cvt_pk_bf16_f32 v192, v200, v201\n\t"
  "v_cvt_pk_bf16_f32 v193, v202, v203\n\t"
  "v_mfma_f32_16x16x32_bf16 a[32:35], v[128:131], v[80:83], a[32:35]\n\t"
  "ds_read_b64_tr_b16 v[168:169], v229 offset:25088\n\t"
  "ds_read_b64_tr_b16 v[170:171], v229 offset:27136\n\t"
  "v_mfma_f32_16x16x32_bf16 a[36:39], v[128:131], v[112:115], a[36:39]\n\t"
  "v_accvgpr_read_b32 v200, a12\n\t"
  "v_accvgpr_read_b32 v201, a13\n\t"
  "v_accvgpr_read_b32 v202, a14\n\t"
  "s_waitcnt lgkmcnt(14)\n\t"
  "v_mfma_f32_16x16x32_bf16 a[40:43], v[132:135], v[80:83], a[40:43]\n\t"
  "ds_read_b64_tr_b16 v[172:173], v230 offset:25088\n\t"
  "ds_read_b64_tr_b16 v[174:175], v230 offset:27136\n\t"
  "v_mfma_f32_16x16x32_bf16 a[44:47], v[132:135], v[112:115], a[44:47]\n\t"
  "v_accvgpr_read_b32 v203, a15\n\t"
  "v_cvt_pk_bf16_f32 v194, v200, v201\n\t"
  "v_cvt_pk_bf16_f32 v195, v202, v203\n\t"
  "v_mfma_f32_16x16x32_bf16 a[48:51], v[136:139], v[80:83], a[48:51]\n\t"
  "ds_read_b64_tr_b16 v[176:177], v229 offset:28672\n\t"
  "ds_read_b64_tr_b16 v[178:179], v229 offset:30720\n\t"
  "v_mfma_f32_16x16x32_bf16 a[52:55], v[136:139], v[112:115], a[52:55]\n\t"
  "buffer_store_dwordx4 v[192:195], v246, %[srdws], s52 offen offset:0 sc1\n\t"
  "v_accvgpr_read_b32 v200, a20\n\t"
  "v_mfma_f32_16x16x32_bf16 a[56:59], v[140:143], v[80:83], a[56:59]\n\t"
  "ds_read_b64_tr_b16 v[180:181], v230 offset:28672\n\t"
  "ds_read_b64_tr_b16 v[182:183], v230 offset:30720\n\t"
  "v_mfma_f32_16x16x32_bf16 a[60:63], v[140:143], v[112:115], a[60:63]\n\t"
  "v_accvgpr_read_b32 v201, a21\n\t"
  "v_accvgpr_read_b32 v202, a22\n\t"
  "v_accvgpr_read_b32 v203, a23\n\t"
  "s_waitcnt lgkmcnt(14)\n\t"
  "v_mfma_f32_16x16x32_bf16 a[32:35], v[144:147], v[84:87], a[32:35]\n\t"
  "ds_read_b64_tr_b16 v[184:185], v229 offset:29184\n\t"
  "ds_read_b64_tr_b16 v[186:187], v229 offset:31232\n\t"
  "v_mfma_f32_16x16x32_bf16 a[36:39], v[144:147], v[116:119], a[36:39]\n\t"
  "v_cvt_pk_bf16_f32 v196, v200, v201\n\t"
  "v_cvt_pk_bf16_f32 v197, v202, v203\n\t"
  "v_accvgpr_read_b32 v200, a28\n\t"
  "v_mfma_f32_16x16x32_bf16 a[40:43], v[148:151], v[84:87], a[40:43]\n\t"
  "ds_read_b64_tr_b16 v[188:189], v230 offset:29184\n\t"
  "ds_read_b64_tr_b16 v[190:191], v230 offset:31232\n\t"
  "v_mfma_f32_16x16x32_bf16 a[44:47], v[148:151], v[116:119], a[44:47]\n\t"
  "v_accvgpr_read_b32 v201, a29\n\t"
  "v_accvgpr_read_b32 v202, a30\n\t"
  "v_mfma_f32_16x16x32_bf16 a[48:51], v[152:155], v[84:87], a[48:51]\n\t"
  "s_waitcnt vmcnt(23)\n\t"
  "s_barrier\n\t"
  "ds_read_b64_tr_b16 v[128:129], v225 offset:0\n\t"
  "ds_read_b64_tr_b16 v[130:131], v225 offset:2048\n\t"
  "v_mfma_f32_16x16x32_bf16 a[52:55], v[152:155], v[116:119], a[52:55]\n\t"
  "s_add_u32 m0, %[m0k], 98304\n\t"
  "s_nop 0\n\t"
  "buffer_load_dwordx4 v231, %[srdws], 0 offen offset:0 sc1 lds\n\t"
  "v_accvgpr_read_b32 v203, a31\n\t"
  "v_cvt_pk_bf16_f32 v198, v200, v201\n\t"
  "v_cvt_pk_bf16_f32 v199, v202, v203\n\t"
  "s_waitcnt lgkmcnt(14)\n\t"
  "v_mfma_f32_16x16x32_bf16 a[56:59], v[156:159], v[84:87], a[56:59]\n\t"
  "ds_read_b64_tr_b16 v[132:133], v226 offset:0\n\t"
  "ds_read_b64_tr_b16 v[134:135], v226 offset:2048\n\t"
  "v_mfma_f32_16x16x32_bf16 a[60:63], v[156:159], v[116:119], a[60:63]\n\t"
  "buffer_store_dwordx4 v[196:199], v246, %[srdws], s52 offen offset:1024 sc1\n\t"
  "v_add_u32 v246, 0x800, v246\n\t"
  "v_mfma_f32_16x16x32_bf16 a[32:35], v[160:163], v[88:91], a[32:35]\n\t"
  "ds_read_b64_tr_b16 v[136:137], v225 offset:512\n\t"
  "ds_read_b64_tr_b16 v[138:139], v225 offset:2560\n\t"
  "v_mfma_f32_16x16x32_bf16 a[36:39], v[160:163], v[120:123], a[36:39]\n\t"
  "v_mfma_f32_16x16x32_bf16 a[40:43], v[164:167], v[88:91], a[40:43]\n\t"
  "ds_read_b64_tr_b16 v[140:141], v226 offset:512\n\t"
  "ds_read_b64_tr_b16 v[142:143], v226 offset:2560\n\t"
  "v_mfma_f32_16x16x32_bf16 a[44:47], v[164:167], v[120:123], a[44:47]\n\t"
  "buffer_load_dwordx4 v231, %[srdws], 0 offen offset:1024 sc1 lds\n\t"
  "s_waitcnt lgkmcnt(14)\n\t"
  "v_mfma_f32_16x16x32_bf16 a[48:51], v[168:171], v[88:91], a[48:51]\n\t"
  "ds_read_b64_tr_b16 v[144:145], v225 offset:4096\n\t"
  "ds_read_b64_tr_b16 v[146:147], v225 offset:6144\n\t"
  "v_mfma_f32_16x16x32_bf16 a[52:55], v[168:171], v[120:123], a[52:55]\n\t"
  "v_mfma_f32_16x16x32_bf16 a[56:59], v[172:175], v[88:91], a[56:59]\n\t"
  "ds_read_b64_tr_b16 v[148:149], v226 offset:4096\n\t"
  "ds_read_b64_tr_b16 v[150:151], v226 offset:6144\n\t"
  "v_mfma_f32_16x16x32_bf16 a[60:63], v[172:175], v[120:123], a[60:63]\n\t"
  "v_mfma_f32_16x16x32_bf16 a[32:35], v[176:179], v[92:95], a[32:35]\n\t"
  "ds_read_b64_tr_b16 v[152:153], v225 offset:4608\n\t"
  "ds_read_b64_tr_b16 v[154:155], v225 offset:6656\n\t"
  "v_mfma_f32_16x16x32_bf16 a[36:39], v[176:179], v[124:127], a[36:39]\n\t"
  "buffer_load_dwordx4 v231, %[srdws], 0 offen offset:2048 sc1 lds\n\t"
  "s_waitcnt lgkmcnt(14)\n\t"
  "v_mfma_f32_16x16x32_bf16 a[40:43], v[180:183], v[92:95], a[40:43]\n\t"
  "ds_read_b64_tr_b16 v[156:157], v226 offset:4608\n\t"
  "ds_read_b64_tr_b16 v[158:159], v226 offset:6656\n\t"
  "v_mfma_f32_16x16x32_bf16 a[44:47], v[180:183], v[124:127], a[44:47]\n\t"
  "v_mfma_f32_16x16x32_bf16 a[48:51], v[184:187], v[92:95], a[48:51]\n\t"
  "ds_read_b64_tr_b16 v[160:161], v225 offset:8192\n\t"
  "ds_read_b64_tr_b16 v[162:163], v225 offset:10240\n\t"
  "v_mfma_f32_16x16x32_bf16 a[52:55], v[184:187], v[124:127], a[52:55]\n\t"
  "buffer_load_dwordx4 v231, %[srdws], 0 offen offset:3072 sc1 lds\n\t"
  "v_add_u32 v231, 0x4000, v231\n\t"
  "s_add_u32 m0, %[m0w], 114688\n\t"
  "v_mfma_f32_16x16x32_bf16 a[56:59], v[188:191], v[92:95], a[56:59]\n\t"
  "ds_read_b64_tr_b16 v[164:165], v226 offset:8192\n\t"
  "ds_read_b64_tr_b16 v[166:167], v226 offset:10240\n\t"
  "v_mfma_f32_16x16x32_bf16 a[60:63], v[188:191], v[124:127], a[60:63]\n\t"
  "s_waitcnt lgkmcnt(14)\n\t"
  "v_mfma_f32_16x16x32_bf16 a[0:3], v[128:131], v[64:67], 0\n\t"
  "ds_read_b64_tr_b16 v[168:169], v225 offset:8704\n\t"
  "ds_read_b64_tr_b16 v[170:171], v225 offset:10752\n\t"
  "v_mfma_f32_16x16x32_bf16 a[4:7], v[128:131], v[96:99], 0\n\t"
  "v_mfma_f32_16x16x32_bf16 a[8:11], v[132:135], v[64:67], 0\n\t"
  "ds_read_b64_tr_b16 v[172:173], v226 offset:8704\n\t"
  "ds_read_b64_tr_b16 v[174:175], v226 offset:10752\n\t"
  "v_mfma_f32_16x16x32_bf16 a[12:15], v[132:135], v[96:99], 0\n\t"
  "buffer_load_dwordx4 v232, %[srdp], 0 offen offset:0 lds\n\t"
  "v_add_u32 v232, 0x80, v232\n\t"
  "v_mfma_f32_16x16x32_bf16 a[16:19], v[136:139], v[64:67], 0\n\t"
  "ds_read_b64_tr_b16 v[176:177], v225 offset:12288\n\t"
  "ds_read_b64_tr_b16 v[178:179], v225 offset:14336\n\t"
  "v_mfma_f32_16x16x32_bf16 a[20:23], v[136:139], v[96:99], 0\n\t"
  "s_waitcnt lgkmcnt(14)\n\t"
  "v_mfma_f32_16x16x32_bf16 a[24:27], v[140:143], v[64:67], 0\n\t"
  "ds_read_b64_tr_b16 v[180:181], v226 offset:12288\n\t"
  "ds_read_b64_tr_b16 v[182:183], v226 offset:14336\n\t"
  "v_mfma_f32_16x16x32_bf16 a[28:31], v[140:143], v[96:99], 0\n\t"
  "buffer_load_dwordx4 v233, %[srdp], %[c31744] offen offset:1024 lds\n\t"
  "v_add_u32 v233, 0x80, v233\n\t"
  "v_mfma_f32_16x16x32_bf16 a[0:3], v[144:147], v[68:71], a[0:3]\n\t"
  "ds_read_b64_tr_b16 v[184:185], v225 offset:12800\n\t"
  "ds_read_b64_tr_b16 v[186:187], v225 offset:14848\n\t"
  "v_mfma_f32_16x16x32_bf16 a[4:7], v[144:147], v[100:103], a[4:7]\n\t"
  "v_accvgpr_read_b32 v200, a32\n\t"
  "v_accvgpr_read_b32 v201, a33\n\t"
  "v_accvgpr_read_b32 v202, a34\n\t"
  "v_mfma_f32_16x16x32_bf16 a[8:11], v[148:151], v[68:71], a[8:11]\n\t"
  "ds_read_b64_tr_b16 v[188:189], v226 offset:12800\n\t"
  "ds_read_b64_tr_b16 v[190:191], v226 offset:14848\n\t"
  "v_mfma_f32_16x16x32_bf16 a[12:15], v[148:151], v[100:103], a[12:15]\n\t"
  "v_accvgpr_read_b32 v203, a35\n\t"
  "v_cvt_pk_bf16_f32 v192, v200, v201\n\t"
  "v_cvt_pk_bf16_f32 v193, v202, v203\n\t"
  "s_waitcnt lgkmcnt(14)\n\t"
  "v_mfma_f32_16x16x32_bf16 a[16:19], v[152:155], v[68:71], a[16:19]\n\t"
  "ds_read_b64_tr_b16 v[128:129], v225 offset:16384\n\t"
  "ds_read_b64_tr_b16 v[130:131], v225 offset:18432\n\t"
  "v_mfma_f32_16x16x32_bf16 a[20:23], v[152:155], v[100:103], a[20:23]\n\t"
  "v_accvgpr_read_b32 v200, a40\n\t"
  "v_accvgpr_read_b32 v201, a41\n\t"
  "v_mfma_f32_16x16x32_bf16 a[24:27], v[156:159], v[68:71], a[24:27]\n\t"
  "ds_read_b64_tr_b16 v[132:133], v226 offset:16384\n\t"
  "ds_read_b64_tr_b16 v[134:135], v226 offset:18432\n\t"
  "v_mfma_f32_16x16x32_bf16 a[28:31], v[156:159], v[100:103], a[28:31]\n\t"
  "v_accvgpr_read_b32 v202, a42\n\t"
  "v_accvgpr_read_b32 v203, a43\n\t"
  "v_cvt_pk_bf16_f32 v194, v200, v201\n\t"
  "v_mfma_f32_16x16x32_bf16 a[0:3], v[160:163], v[72:75], a[0:3]\n\t"
  "ds_read_b64_tr_b16 v[136:137], v225 offset:16896\n\t"
  "ds_read_b64_tr_b16 v[138:139], v225 offset:18944\n\t"
  "v_mfma_f32_16x16x32_bf16 a[4:7], v[160:163], v[104:107], a[4:7]\n\t"
  "v_cvt_pk_bf16_f32 v195, v202, v203\n\t"
  "buffer_store_dwordx4 v[192:195], v246, %[srdws], 0 offen offset:0 sc1\n\t"
  "v_accvgpr_read_b32 v200, a48\n\t"
  "s_waitcnt lgkmcnt(14)\n\t"
  "v_mfma_f32_16x16x32_bf16 a[8:11], v[164:167], v[72:75], a[8:11]\n\t"
  "ds_read_b64_tr_b16 v[140:141], v226 offset:16896\n\t"
  "ds_read_b64_tr_b16 v[142:143], v226 offset:18944\n\t"
  "v_mfma_f32_16x16x32_bf16 a[12:15], v[164:167], v[104:107], a[12:15]\n\t"
  "v_accvgpr_read_b32 v201, a49\n\t"
  "v_accvgpr_read_b32 v202, a50\n\t"
  "v_mfma_f32_16x16x32_bf16 a[16:19], v[168:171], v[72:75], a[16:19]\n\t"
  "ds_read_b64_tr_b16 v[144:145], v225 offset:20480\n\t"
  "ds_read_b64_tr_b16 v[146:147], v225 offset:22528\n\t"
  "v_mfma_f32_16x16x32_bf16 a[20:23], v[168:171], v[104:107], a[20:23]\n\t"
  "v_accvgpr_read_b32 v203, a51\n\t"
  "v_cvt_pk_bf16_f32 v196, v200, v201\n\t"
  "v_cvt_pk_bf16_f32 v197, v202, v203\n\t"
  "v_mfma_f32_16x16x32_bf16 a[24:27], v[172:175], v[72:75], a[24:27]\n\t"
  "ds_read_b64_tr_b16 v[148:149], v226 offset:20480\n\t"
  "ds_read_b64_tr_b16 v[150:151], v226 offset:22528\n\t"
  "v_mfma_f32_16x16x32_bf16 a[28:31], v[172:175], v[104:107], a[28:31]\n\t"
  "v_accvgpr_read_b32 v200, a56\n\t"
  "v_accvgpr_read_b32 v201, a57\n\t"
  "v_accvgpr_read_b32 v202, a58\n\t"
  "s_waitcnt lgkmcnt(14)\n\t"
  "v_mfma_f32_16x16x32_bf16 a[0:3], v[176:179], v[76:79], a[0:3]\n\t"
  "ds_read_b64_tr_b16 v[152:153], v225 offset:20992\n\t"
  "ds_read_b64_tr_b16 v[154:155], v225 offset:23040\n\t"
  "v_mfma_f32_16x16x32_bf16 a[4:7], v[176:179], v[108:111], a[4:7]\n\t"
  "v_accvgpr_read_b32 v203, a59\n\t"
  "v_cvt_pk_bf16_f32 v198, v200, v201\n\t"
  "v_mfma_f32_16x16x32_bf16 a[8:11], v[180:183], v[76:79], a[8:11]\n\t"
  "ds_read_b64_tr_b16 v[156:157], v226 offset:20992\n\t"
  "ds_read_b64_tr_b16 v[158:159], v226 offset:23040\n\t"
  "v_mfma_f32_16x16x32_bf16 a[12:15], v[180:183], v[108:111], a[12:15]\n\t"
  "v_cvt_pk_bf16_f32 v199, v202, v203\n\t"
  "buffer_store_dwordx4 v[196:199], v246, %[srdws], 0 offen offset:1024 sc1\n\t"
  "v_accvgpr_read_b32 v200, a36\n\t"
  "v_mfma_f32_16x16x32_bf16 a[16:19], v[184:187], v[76:79], a[16:19]\n\t"
  "ds_read_b64_tr_b16 v[160:161], v225 offset:24576\n\t"
  "ds_read_b64_tr_b16 v[162:163], v225 offset:26624\n\t"
  "v_mfma_f32_16x16x32_bf16 a[20:23], v[184:187], v[108:111], a[20:23]\n\t"
  "v_accvgpr_read_b32 v201, a37\n\t"
  "v_accvgpr_read_b32 v202, a38\n\t"
  "v_accvgpr_read_b32 v203, a39\n\t"
  "s_waitcnt lgkmcnt(14)\n\t"
  "v_mfma_f32_16x16x32_bf16 a[24:27], v[188:191], v[76:79], a[24:27]\n\t"
  "ds_read_b64_tr_b16 v[164:165], v226 offset:24576\n\t"
  "ds_read_b64_tr_b16 v[166:167], v226 offset:26624\n\t"
  "v_mfma_f32_16x16x32_bf16 a[28:31], v[188:191], v[108:111], a[28:31]\n\t"
  "v_cvt_pk_bf16_f32 v192, v200, v201\n\t"
  "v_cvt_pk_bf16_f32 v193, v202, v203\n\t"
  "v_mfma_f32_16x16x32_bf16 a[0:3], v[128:131], v[80:83], a[0:3]\n\t"
  "ds_read_b64_tr_b16 v[168:169], v225 offset:25088\n\t"
  "ds_read_b64_tr_b16 v[170:171], v225 offset:27136\n\t"
  "v_mfma_f32_16x16x32_bf16 a[4:7], v[128:131], v[112:115], a[4:7]\n\t"
  "v_accvgpr_read_b32 v200, a44\n\t"
  "v_accvgpr_read_b32 v201, a45\n\t"
  "v_accvgpr_read_b32 v202, a46\n\t"
  "v_mfma_f32_16x16x32_bf16 a[8:11], v[132:135], v[80:83], a[8:11]\n\t"
  "ds_read_b64_tr_b16 v[172:173], v226 offset:25088\n\t"
  "ds_read_b64_tr_b16 v[174:175], v226 offset:27136\n\t"
  "v_mfma_f32_16x16x32_bf16 a[12:15], v[132:135], v[112:115], a[12:15]\n\t"
  "v_accvgpr_read_b32 v203, a47\n\t"
  "v_cvt_pk_bf16_f32 v194, v200, v201\n\t"
  "v_cvt_pk_bf16_f32 v195, v202, v203\n\t"
  "s_waitcnt lgkmcnt(14)\n\t"
  "v_mfma_f32_16x16x32_bf16 a[16:19], v[136:139], v[80:83], a[16:19]\n\t"
  "ds_read_b64_tr_b16 v[176:177], v225 offset:28672\n\t"
  "ds_read_b64_tr_b16 v[178:179], v225 offset:30720\n\t"
  "v_mfma_f32_16x16x32_bf16 a[20:23], v[136:139], v[112:115], a[20:23]\n\t"
  "buffer_store_dwordx4 v[192:195], v246, %[srdws], s52 offen offset:0 sc1\n\t"
  "v_accvgpr_read_b32 v200, a52\n\t"
  "v_mfma_f32_16x16x32_bf16 a[24:27], v[140:143], v[80:83], a[24:27]\n\t"
  "ds_read_b64_tr_b16 v[180:181], v226 offset:28672\n\t"
  "ds_read_b64_tr_b16 v[182:183], v226 offset:30720\n\t"
  "v_mfma_f32_16x16x32_bf16 a[28:31], v[140:143], v[112:115], a[28:31]\n\t"
  "v_accvgpr_read_b32 v201, a53\n\t"
  "v_accvgpr_read_b32 v202, a54\n\t"
  "v_accvgpr_read_b32 v203, a55\n\t"
  "v_mfma_f32_16x16x32_bf16 a[0:3], v[144:147], v[84:87], a[0:3]\n\t"
  "ds_read_b64_tr_b16 v[184:185], v225 offset:29184\n\t"
  "ds_read_b64_tr_b16 v[186:187], v225 offset:31232\n\t"
  "v_mfma_f32_16x16x32_bf16 a[4:7], v[144:147], v[116:119], a[4:7]\n\t"
  "v_cvt_pk_bf16_f32 v196, v200, v201\n\t"
  "v_cvt_pk_bf16_f32 v197, v202, v203\n\t"
  "v_accvgpr_read_b32 v200, a60\n\t"
  "s_waitcnt lgkmcnt(14)\n\t"
  "v_mfma_f32_16x16x32_bf16 a[8:11], v[148:151], v[84:87], a[8:11]\n\t"
  "ds_read_b64_tr_b16 v[188:189], v226 offset:29184\n\t"
  "ds_read_b64_tr_b16 v[190:191], v226 offset:31232\n\t"
  "v_mfma_f32_16x16x32_bf16 a[12:15], v[148:151], v[116:119], a[12:15]\n\t"
  "v_accvgpr_read_b32 v201, a61\n\t"
  "v_accvgpr_read_b32 v202, a62\n\t"
  "v_mfma_f32_16x16x32_bf16 a[16:19], v[152:155], v[84:87], a[16:19]\n\t"
  "s_waitcnt vmcnt(23)\n\t"
  "s_barrier\n\t"
  "ds_read_b128 v[128:131], v234 offset:49152\n\t"
  "v_mfma_f32_16x16x32_bf16 a[20:23], v[152:155], v[116:119], a[20:23]\n\t"
  "s_add_u32 m0, %[m0k], 131072\n\t"
  "s_nop 0\n\t"
  "buffer_load_dwordx4 v231, %[srdws], 0 offen offset:0 sc1 lds\n\t"
  "v_accvgpr_read_b32 v203, a63\n\t"
  "v_cvt_pk_bf16_f32 v198, v200, v201\n\t"
  "v_cvt_pk_bf16_f32 v199, v202, v203\n\t"
  "v_mfma_f32_16x16x32_bf16 a[24:27], v[156:159], v[84:87], a[24:27]\n\t"
  "ds_read_b128 v[132:135], v234 offset:51200\n\t"
  "v_mfma_f32_16x16x32_bf16 a[28:31], v[156:159], v[116:119], a[28:31]\n\t"
  "buffer_load_dwordx4 v231, %[srdws], 0 offen offset:1024 sc1 lds\n\t"
  "buffer_store_dwordx4 v[196:199], v246, %[srdws], s52 offen offset:1024 sc1\n\t"
  "v_add_u32 v246, 0x800, v246\n\t"
  "s_waitcnt lgkmcnt(12)\n\t"
  "v_mfma_f32_16x16x32_bf16 a[0:3], v[160:163], v[88:91], a[0:3]\n\t"
  "ds_read_b64_tr_b16 v[136:137], v240 offset:32768\n\t"
  "ds_read_b64_tr_b16 v[138:139], v240 offset:34816\n\t"
  "v_mfma_f32_16x16x32_bf16 a[4:7], v[160:163], v[120:123], a[4:7]\n\t"
  "buffer_load_dwordx4 v231, %[srdws], 0 offen offset:2048 sc1 lds\n\t"
  "v_mfma_f32_16x16x32_bf16 a[8:11], v[164:167], v[88:91], a[8:11]\n\t"
  "ds_read_b64_tr_b16 v[140:141], v241 offset:32768\n\t"
  "ds_read_b64_tr_b16 v[142:143], v241 offset:34816\n\t"
  "v_mfma_f32_16x16x32_bf16 a[12:15], v[164:167], v[120:123], a[12:15]\n\t"
  "buffer_load_dwordx4 v231, %[srdws], 0 offen offset:3072 sc1 lds\n\t"
  "v_add_u32 v231, 0x4000, v231\n\t"
  "s_add_u32 m0, %[m0w], 147456\n\t"
  "v_mfma_f32_16x16x32_bf16 a[16:19], v[168:171], v[88:91], a[16:19]\n\t"
  "ds_read_b64_tr_b16 v[144:145], v240 offset:33280\n\t"
  "ds_read_b64_tr_b16 v[146:147], v240 offset:35328\n\t"
  "v_mfma_f32_16x16x32_bf16 a[20:23], v[168:171], v[120:123], a[20:23]\n\t"
  "buffer_load_dwordx4 v232, %[srdp], 0 offen offset:0 lds\n\t"
  "v_add_u32 v232, 0x80, v232\n\t"
  "s_waitcnt lgkmcnt(12)\n\t"
  "v_mfma_f32_16x16x32_bf16 a[24:27], v[172:175], v[88:91], a[24:27]\n\t"
  "ds_read_b64_tr_b16 v[148:149], v241 offset:33280\n\t"
  "ds_read_b64_tr_b16 v[150:151], v241 offset:35328\n\t"
  "v_mfma_f32_16x16x32_bf16 a[28:31], v[172:175], v[120:123], a[28:31]\n\t"
  "buffer_load_dwordx4 v233, %[srdp], %[c31744] offen offset:1024 lds\n\t"
  "v_add_u32 v233, 0x80, v233\n\t"
  "v_mfma_f32_16x16x32_bf16 a[0:3], v[176:179], v[92:95], a[0:3]\n\t"
  "ds_read_b128 v[152:155], v235 offset:49152\n\t"
  "v_mfma_f32_16x16x32_bf16 a[4:7], v[176:179], v[124:127], a[4:7]\n\t"
  "v_mfma_f32_16x16x32_bf16 a[8:11], v[180:183], v[92:95], a[8:11]\n\t"
  "ds_read_b128 v[156:159], v235 offset:51200\n\t"
  "v_mfma_f32_16x16x32_bf16 a[12:15], v[180:183], v[124:127], a[12:15]\n\t"
  "s_waitcnt lgkmcnt(11)\n\t"
  "v_mfma_f32_16x16x32_bf16 a[16:19], v[184:187], v[92:95], a[16:19]\n\t"
  "ds_read_b64_tr_b16 v[160:161], v240 offset:40960\n\t"
  "ds_read_b64_tr_b16 v[162:163], v240 offset:43008\n\t"
  "v_mfma_f32_16x16x32_bf16 a[20:23], v[184:187], v[124:127], a[20:23]\n\t"
  "v_mfma_f32_16x16x32_bf16 a[24:27], v[188:191], v[92:95], a[24:27]\n\t"
  "ds_read_b64_tr_b16 v[164:165], v241 offset:40960\n\t"
  "ds_read_b64_tr_b16 v[166:167], v241 offset:43008\n\t"
  "v_mfma_f32_16x16x32_bf16 a[28:31], v[188:191], v[124:127], a[28:31]\n\t"
  "ds_read_b64_tr_b16 v[168:169], v240 offset:41472\n\t"
  "ds_read_b64_tr_b16 v[170:171], v240 offset:43520\n\t"
  "s_waitcnt lgkmcnt(12)\n\t"
  "ds_read_b64_tr_b16 v[172:173], v241 offset:41472\n\t"
  "ds_read_b64_tr_b16 v[174:175], v241 offset:43520\n\t"
  "v_mfma_f32_16x16x32_bf16 a[64:67], v[136:139], v[128:131], 0\n\t"
  "s_waitcnt vmcnt(20)\n\t"
  "s_barrier\n\t"
  "ds_read_b128 v[176:179], v236 offset:16384\n\t"
  "v_mfma_f32_16x16x32_bf16 a[68:71], v[136:139], v[132:135], 0\n\t"
  "s_add_u32 m0, %[m0k], 0\n\t"
  "s_nop 0\n\t"
  "buffer_load_dwordx4 v231, %[srdws], 0 offen offset:0 sc1 lds\n\t"
  "v_mfma_f32_16x16x32_bf16 a[72:75], v[140:143], v[128:131], 0\n\t"
  "ds_read_b128 v[180:183], v236 offset:18432\n\t"
  "v_mfma_f32_16x16x32_bf16 a[76:79], v[140:143], v[132:135], 0\n\t"
  "buffer_load_dwordx4 v231, %[srdws], 0 offen offset:1024 sc1 lds\n\t"
  "s_waitcnt lgkmcnt(11)\n\t"
  "v_mfma_f32_16x16x32_bf16 a[80:83], v[144:147], v[128:131], 0\n\t"
  "ds_read_b64_tr_b16 v[184:185], v242 offset:0\n\t"
  "ds_read_b64_tr_b16 v[186:187], v242 offset:2048\n\t"
  "v_mfma_f32_16x16x32_bf16 a[84:87], v[144:147], v[132:135], 0\n\t"
  "buffer_load_dwordx4 v231, %[srdws], 0 offen offset:2048 sc1 lds\n\t"
  "v_accvgpr_read_b32 v200, a0\n\t"
  "v_accvgpr_read_b32 v201, a1\n\t"
  "v_accvgpr_read_b32 v202, a2\n\t"
  "v_mfma_f32_16x16x32_bf16 a[88:91], v[148:151], v[128:131], 0\n\t"
  "ds_read_b64_tr_b16 v[188:189], v243 offset:0\n\t"
  "ds_read_b64_tr_b16 v[190:191], v243 offset:2048\n\t"
  "v_mfma_f32_16x16x32_bf16 a[92:95], v[148:151], v[132:135], 0\n\t"
  "buffer_load_dwordx4 v231, %[srdws], 0 offen offset:3072 sc1 lds\n\t"
  "v_add_u32 v231, 0x4000, v231\n\t"
  "s_add_u32 m0, %[m0w], 16384\n\t"
  "v_accvgpr_read_b32 v203, a3\n\t"
  "v_cvt_pk_bf16_f32 v192, v200, v201\n\t"
  "v_cvt_pk_bf16_f32 v193, v202, v203\n\t"
  "ds_read_b64_tr_b16 v[128:129], v242 offset:512\n\t"
  "ds_read_b64_tr_b16 v[130:131], v242 offset:2560\n\t"
  "buffer_load_dwordx4 v232, %[srdp], 0 offen offset:0 lds\n\t"
  "v_add_u32 v232, 0x80, v232\n\t"
  "v_accvgpr_read_b32 v200, a8\n\t"
  "v_accvgpr_read_b32 v201, a9\n\t"
  "s_waitcnt lgkmcnt(12)\n\t"
  "ds_read_b64_tr_b16 v[132:133], v243 offset:512\n\t"
  "ds_read_b64_tr_b16 v[134:135], v243 offset:2560\n\t"
  "buffer_load_dwordx4 v233, %[srdp], %[c31744] offen offset:1024 lds\n\t"
  "v_add_u32 v233, 0x80, v233\n\t"
  "v_accvgpr_read_b32 v202, a10\n\t"
  "v_accvgpr_read_b32 v203, a11\n\t"
  "v_cvt_pk_bf16_f32 v194, v200, v201\n\t"
  "v_mfma_f32_16x16x32_bf16 a[64:67], v[160:163], v[152:155], a[64:67]\n\t"
  "ds_read_b128 v[136:139], v237 offset:16384\n\t"
  "v_mfma_f32_16x16x32_bf16 a[68:71], v[160:163], v[156:159], a[68:71]\n\t"
  "v_cvt_pk_bf16_f32 v195, v202, v203\n\t"
  "buffer_store_dwordx4 v[192:195], v246, %[srdws], 0 offen offset:0 sc1\n\t"
  "v_accvgpr_read_b32 v200, a16\n\t"
  "v_mfma_f32_16x16x32_bf16 a[72:75], v[164:167], v[152:155], a[72:75]\n\t"
  "ds_read_b128 v[140:143], v237 offset:18432\n\t"
  "v_mfma_f32_16x16x32_bf16 a[76:79], v[164:167], v[156:159], a[76:79]\n\t"
  "v_accvgpr_read_b32 v201, a17\n\t"
  "v_accvgpr_read_b32 v202, a18\n\t"
  "s_waitcnt lgkmcnt(11)\n\t"
  "v_mfma_f32_16x16x32_bf16 a[80:83], v[168:171], v[152:155], a[80:83]\n\t"
  "ds_read_b64_tr_b16 v[144:145], v242 offset:8192\n\t"
  "ds_read_b64_tr_b16 v[146:147], v242 offset:10240\n\t"
  "v_mfma_f32_16x16x32_bf16 a[84:87], v[168:171], v[156:159], a[84:87]\n\t"
  "v_accvgpr_read_b32 v203, a19\n\t"
  "v_cvt_pk_bf16_f32 v196, v200, v201\n\t"
  "v_cvt_pk_bf16_f32 v197, v202, v203\n\t"
  "v_mfma_f32_16x16x32_bf16 a[88:91], v[172:175], v[152:155], a[88:91]\n\t"
  "ds_read_b64_tr_b16 v[148:149], v243 offset:8192\n\t"
  "ds_read_b64_tr_b16 v[150:151], v243 offset:10240\n\t"
  "v_mfma_f32_16x16x32_bf16 a[92:95], v[172:175], v[156:159], a[92:95]\n\t"
  "v_accvgpr_read_b32 v200, a24\n\t"
  "v_accvgpr_read_b32 v201, a25\n\t"
  "v_accvgpr_read_b32 v202, a26\n\t"
  "ds_read_b64_tr_b16 v[152:153], v242 offset:8704\n\t"
  "ds_read_b64_tr_b16 v[154:155], v242 offset:10752\n\t"
  "v_accvgpr_read_b32 v203, a27\n\t"
  "v_cvt_pk_bf16_f32 v198, v200, v201\n\t"
  "s_waitcnt lgkmcnt(12)\n\t"
  "ds_read_b64_tr_b16 v[156:157], v243 offset:8704\n\t"
  "ds_read_b64_tr_b16 v[158:159], v243 offset:10752\n\t"
  "v_cvt_pk_bf16_f32 v199, v202, v203\n\t"
  "buffer_store_dwordx4 v[196:199], v246, %[srdws], 0 offen offset:1024 sc1\n\t"
  "v_accvgpr_read_b32 v200, a4\n\t"
  "v_mfma_f32_16x16x32_bf16 a[64:67], v[184:187], v[176:179], a[64:67]\n\t"
  "s_waitcnt vmcnt(18)\n\t"
  "s_barrier\n\t"
  "ds_read_b128 v[160:163], v236 offset:49152\n\t"
  "v_mfma_f32_16x16x32_bf16 a[68:71], v[184:187], v[180:183], a[68:71]\n\t"
  "s_add_u32 m0, %[m0k], 32768\n\t"
  "s_nop 0\n\t"
  "buffer_load_dwordx4 v231, %[srdws], 0 offen offset:0 sc1 lds\n\t"
  "v_accvgpr_read_b32 v201, a5\n\t"
  "v_accvgpr_read_b32 v202, a6\n\t"
  "v_accvgpr_read_b32 v203, a7\n\t"
  "v_mfma_f32_16x16x32_bf16 a[72:75], v[188:191], v[176:179], a[72:75]\n\t"
  "ds_read_b128 v[164:167], v236 offset:51200\n\t"
  "v_mfma_f32_16x16x32_bf16 a[76:79], v[188:191], v[180:183], a[76:79]\n\t"
  "buffer_load_dwordx4 v231, %[srdws], 0 offen offset:1024 sc1 lds\n\t"
  "v_cvt_pk_bf16_f32 v192, v200, v201\n\t"
  "v_cvt_pk_bf16_f32 v193, v202, v203\n\t"
  "s_waitcnt lgkmcnt(11)\n\t"
  "v_mfma_f32_16x16x32_bf16 a[80:83], v[128:131], v[176:179], a[80:83]\n\t"
  "ds_read_b64_tr_b16 v[168:169], v242 offset:32768\n\t"
  "ds_read_b64_tr_b16 v[170:171], v242 offset:34816\n\t"
  "v_mfma_f32_16x16x32_bf16 a[84:87], v[128:131], v[180:183], a[84:87]\n\t"
  "buffer_load_dwordx4 v231, %[srdws], 0 offen offset:2048 sc1 lds\n\t"
  "v_accvgpr_read_b32 v200, a12\n\t"
  "v_accvgpr_read_b32 v201, a13\n\t"
  "v_accvgpr_read_b32 v202, a14\n\t"
  "v_mfma_f32_16x16x32_bf16 a[88:91], v[132:135], v[176:179], a[88:91]\n\t"
  "ds_read_b64_tr_b16 v[172:173], v243 offset:32768\n\t"
  "ds_read_b64_tr_b16 v[174:175], v243 offset:34816\n\t"
  "v_mfma_f32_16x16x32_bf16 a[92:95], v[132:135], v[180:183], a[92:95]\n\t"
  "buffer_load_dwordx4 v231, %[srdws], 0 offen offset:3072 sc1 lds\n\t"
  "v_add_u32 v231, 0x4000, v231\n\t"
  "s_add_u32 m0, %[m0w], 49152\n\t"
  "v_accvgpr_read_b32 v203, a15\n\t"
  "v_cvt_pk_bf16_f32 v194, v200, v201\n\t"
  "v_cvt_pk_bf16_f32 v195, v202, v203\n\t"
  "ds_read_b64_tr_b16 v[176:177], v242 offset:33280\n\t"
  "ds_read_b64_tr_b16 v[178:179], v242 offset:35328\n\t"
  "buffer_load_dwordx4 v232, %[srdp], 0 offen offset:0 lds\n\t"
  "v_add_u32 v232, 0x80, v232\n\t"
  "buffer_store_dwordx4 v[192:195], v246, %[srdws], s52 offen offset:0 sc1\n\t"
  "v_accvgpr_read_b32 v200, a20\n\t"
  "s_waitcnt lgkmcnt(12)\n\t"
  "ds_read_b64_tr_b16 v[180:181], v243 offset:33280\n\t"
  "ds_read_b64_tr_b16 v[182:183], v243 offset:35328\n\t"
  "buffer_load_dwordx4 v233, %[srdp], %[c31744] offen offset:1024 lds\n\t"
  "v_add_u32 v233, 0x80, v233\n\t"
  "v_accvgpr_read_b32 v201, a21\n\t"
  "v_accvgpr_read_b32 v202, a22\n\t"
  "v_accvgpr_read_b32 v203, a23\n\t"
  "v_mfma_f32_16x16x32_bf16 a[64:67], v[144:147], v[136:139], a[64:67]\n\t"
  "ds_read_b128 v[184:187], v237 offset:49152\n\t"
  "v_mfma_f32_16x16x32_bf16 a[68:71], v[144:147], v[140:143], a[68:71]\n\t"
  "v_cvt_pk_bf16_f32 v196, v200, v201\n\t"
  "v_cvt_pk_bf16_f32 v197, v202, v203\n\t"
  "v_accvgpr_read_b32 v200, a28\n\t"
  "v_mfma_f32_16x16x32_bf16 a[72:75], v[148:151], v[136:139], a[72:75]\n\t"
  "ds_read_b128 v[188:191], v237 offset:51200\n\t"
  "v_mfma_f32_16x16x32_bf16 a[76:79], v[148:151], v[140:143], a[76:79]\n\t"
  "v_accvgpr_read_b32 v201, a29\n\t"
  "v_accvgpr_read_b32 v202, a30\n\t"
  "s_waitcnt lgkmcnt(11)\n\t"
  "v_mfma_f32_16x16x32_bf16 a[80:83], v[152:155], v[136:139], a[80:83]\n\t"
  "ds_read_b64_tr_b16 v[128:129], v242 offset:40960\n\t"
  "ds_read_b64_tr_b16 v[130:131], v242 offset:43008\n\t"
  "v_mfma_f32_16x16x32_bf16 a[84:87], v[152:155], v[140:143], a[84:87]\n\t"
  "v_accvgpr_read_b32 v203, a31\n\t"
  "v_cvt_pk_bf16_f32 v198, v200, v201\n\t"
  "v_cvt_pk_bf16_f32 v199, v202, v203\n\t"
  "v_mfma_f32_16x16x32_bf16 a[88:91], v[156:159], v[136:139], a[88:91]\n\t"
  "ds_read_b64_tr_b16 v[132:133], v243 offset:40960\n\t"
  "ds_read_b64_tr_b16 v[134:135], v243 offset:43008\n\t"
  "v_mfma_f32_16x16x32_bf16 a[92:95], v[156:159], v[140:143], a[92:95]\n\t"
  "buffer_store_dwordx4 v[196:199], v246, %[srdws], s52 offen offset:1024 sc1\n\t"
  "v_add_u32 v246, 0x800, v246\n\t"
  "ds_read_b64_tr_b16 v[136:137], v242 offset:41472\n\t"
  "ds_read_b64_tr_b16 v[138:139], v242 offset:43520\n\t"
  "s_waitcnt lgkmcnt(12)\n\t"
  "ds_read_b64_tr_b16 v[140:141], v243 offset:41472\n\t"
  "ds_read_b64_tr_b16 v[142:143], v243 offset:43520\n\t"
  "v_mfma_f32_16x16x32_bf16 a[64:67], v[168:171], v[160:163], a[64:67]\n\t"
  "s_waitcnt vmcnt(16)\n\t"
  "s_barrier\n\t"
  "ds_read_b128 v[144:147], v238 offset:16384\n\t"
  "v_mfma_f32_16x16x32_bf16 a[68:71], v[168:171], v[164:167], a[68:71]\n\t"
  "s_add_u32 m0, %[m0k], 65536\n\t"
  "s_nop 0\n\t"
  "buffer_load_dwordx4 v231, %[srdws], 0 offen offset:0 sc1 lds\n\t"
  "v_mfma_f32_16x16x32_bf16 a[72:75], v[172:175], v[160:163], a[72:75]\n\t"
  "ds_read_b128 v[148:151], v238 offset:18432\n\t"
  "v_mfma_f32_16x16x32_bf16 a[76:79], v[172:175], v[164:167], a[76:79]\n\t"
  "buffer_load_dwordx4 v231, %[srdws], 0 offen offset:1024 sc1 lds\n\t"
  "s_waitcnt lgkmcnt(11)\n\t"
  "v_mfma_f32_16x16x32_bf16 a[80:83], v[176:179], v[160:163], a[80:83]\n\t"
  "ds_read_b64_tr_b16 v[152:153], v244 offset:0\n\t"
  "ds_read_b64_tr_b16 v[154:155], v244 offset:2048\n\t"
  "v_mfma_f32_16x16x32_bf16 a[84:87], v[176:179], v[164:167], a[84:87]\n\t"
  "buffer_load_dwordx4 v231, %[srdws], 0 offen offset:2048 sc1 lds\n\t"
  "v_mfma_f32_16x16x32_bf16 a[88:91], v[180:183], v[160:163], a[88:91]\n\t"
  "ds_read_b64_tr_b16 v[156:157], v245 offset:0\n\t"
  "ds_read_b64_tr_b16 v[158:159], v245 offset:2048\n\t"
  "v_mfma_f32_16x16x32_bf16 a[92:95], v[180:183], v[164:167], a[92:95]\n\t"
  "buffer_load_dwordx4 v231, %[srdws], 0 offen offset:3072 sc1 lds\n\t"
  "v_add_u32 v231, 0x4000, v231\n\t"
  "s_add_u32 m0, %[m0w], 81920\n\t"
  "ds_read_b64_tr_b16 v[160:161], v244 offset:512\n\t"
  "ds_read_b64_tr_b16 v[162:163], v244 offset:2560\n\t"
  "buffer_load_dwordx4 v232, %[srdp], 0 offen offset:0 lds\n\t"
  "v_add_u32 v232, 0x80, v232\n\t"
  "s_waitcnt lgkmcnt(12)\n\t"
  "ds_read_b64_tr_b16 v[164:165], v245 offset:512\n\t"
  "ds_read_b64_tr_b16 v[166:167], v245 offset:2560\n\t"
  "buffer_load_dwordx4 v233, %[srdp], %[c31744] offen offset:1024 lds\n\t"
  "v_add_u32 v233, 0x80, v233\n\t"
  "v_mfma_f32_16x16x32_bf16 a[64:67], v[128:131], v[184:187], a[64:67]\n\t"
  "ds_read_b128 v[168:171], v239 offset:16384\n\t"
  "v_mfma_f32_16x16x32_bf16 a[68:71], v[128:131], v[188:191], a[68:71]\n\t"
  "v_mfma_f32_16x16x32_bf16 a[72:75], v[132:135], v[184:187], a[72:75]\n\t"
  "ds_read_b128 v[172:175], v239 offset:18432\n\t"
  "v_mfma_f32_16x16x32_bf16 a[76:79], v[132:135], v[188:191], a[76:79]\n\t"
  "s_waitcnt lgkmcnt(11)\n\t"
  "v_mfma_f32_16x16x32_bf16 a[80:83], v[136:139], v[184:187], a[80:83]\n\t"
  "ds_read_b64_tr_b16 v[176:177], v244 offset:8192\n\t"
  "ds_read_b64_tr_b16 v[178:179], v244 offset:10240\n\t"
  "v_mfma_f32_16x16x32_bf16 a[84:87], v[136:139], v[188:191], a[84:87]\n\t"
  "v_mfma_f32_16x16x32_bf16 a[88:91], v[140:143], v[184:187], a[88:91]\n\t"
  "ds_read_b64_tr_b16 v[180:181], v245 offset:8192\n\t"
  "ds_read_b64_tr_b16 v[182:183], v245 offset:10240\n\t"
  "v_mfma_f32_16x16x32_bf16 a[92:95], v[140:143], v[188:191], a[92:95]\n\t"
  "ds_read_b64_tr_b16 v[184:185], v244 offset:8704\n\t"
  "ds_read_b64_tr_b16 v[186:187], v244 offset:10752\n\t"
  "s_waitcnt lgkmcnt(12)\n\t"
  "ds_read_b64_tr_b16 v[188:189], v245 offset:8704\n\t"
  "ds_read_b64_tr_b16 v[190:191], v245 offset:10752\n\t"
  "v_mfma_f32_16x16x32_bf16 a[64:67], v[152:155], v[144:147], a[64:67]\n\t"
  "s_waitcnt vmcnt(16)\n\t"
  "s_barrier\n\t"
  "ds_read_b128 v[128:131], v234 offset:16384\n\t"
  "v_mfma_f32_16x16x32_bf16 a[68:71], v[152:155], v[148:151], a[68:71]\n\t"
  "s_add_u32 m0, %[m0k], 98304\n\t"
  "s_nop 0\n\t"
  "buffer_load_dwordx4 v231, %[srdws], 0 offen offset:0 sc1 lds\n\t"
  "v_mfma_f32_16x16x32_bf16 a[72:75], v[156:159], v[144:147], a[72:75]\n\t"
  "ds_read_b128 v[132:135], v234 offset:18432\n\t"
  "v_mfma_f32_16x16x32_bf16 a[76:79], v[156:159], v[148:151], a[76:79]\n\t"
  "buffer_load_dwordx4 v231, %[srdws], 0 offen offset:1024 sc1 lds\n\t"
  "s_waitcnt lgkmcnt(11)\n\t"
  "v_mfma_f32_16x16x32_bf16 a[80:83], v[160:163], v[144:147], a[80:83]\n\t"
  "ds_read_b64_tr_b16 v[136:137], v240 offset:0\n\t"
  "ds_read_b64_tr_b16 v[138:139], v240 offset:2048\n\t"
  "v_mfma_f32_16x16x32_bf16 a[84:87], v[160:163], v[148:151], a[84:87]\n\t"
  "buffer_load_dwordx4 v231, %[srdws], 0 offen offset:2048 sc1 lds\n\t"
  "v_mfma_f32_16x16x32_bf16 a[88:91], v[164:167], v[144:147], a[88:91]\n\t"
  "ds_read_b64_tr_b16 v[140:141], v241 offset:0\n\t"
  "ds_read_b64_tr_b16 v[142:143], v241 offset:2048\n\t"
  "v_mfma_f32_16x16x32_bf16 a[92:95], v[164:167], v[148:151], a[92:95]\n\t"
  "buffer_load_dwordx4 v231, %[srdws], 0 offen offset:3072 sc1 lds\n\t"
  "v_add_u32 v231, 0x4000, v231\n\t"
  "s_add_u32 m0, %[m0w], 114688\n\t"
  "ds_read_b64_tr_b16 v[144:145], v240 offset:512\n\t"
  "ds_read_b64_tr_b16 v[146:147], v240 offset:2560\n\t"
  "buffer_load_dwordx4 v232, %[srdp], 0 offen offset:0 lds\n\t"
  "v_add_u32 v232, 0x80, v232\n\t"
  "s_waitcnt lgkmcnt(12)\n\t"
  "ds_read_b64_tr_b16 v[148:149], v241 offset:512\n\t"
  "ds_read_b64_tr_b16 v[150:151], v241 offset:2560\n\t"
  "buffer_load_dwordx4 v233, %[srdp], %[c31744] offen offset:1024 lds\n\t"
  "v_add_u32 v233, 0x80, v233\n\t"
  "v_mfma_f32_16x16x32_bf16 a[64:67], v[176:179], v[168:171], a[64:67]\n\t"
  "ds_read_b128 v[152:155], v235 offset:16384\n\t"
  "v_mfma_f32_16x16x32_bf16 a[68:71], v[176:179], v[172:175], a[68:71]\n\t"
  "v_mfma_f32_16x16x32_bf16 a[72:75], v[180:183], v[168:171], a[72:75]\n\t"
  "ds_read_b128 v[156:159], v235 offset:18432\n\t"
  "v_mfma_f32_16x16x32_bf16 a[76:79], v[180:183], v[172:175], a[76:79]\n\t"
  "s_waitcnt lgkmcnt(11)\n\t"
  "v_mfma_f32_16x16x32_bf16 a[80:83], v[184:187], v[168:171], a[80:83]\n\t"
  "ds_read_b64_tr_b16 v[160:161], v240 offset:8192\n\t"
  "ds_read_b64_tr_b16 v[162:163], v240 offset:10240\n\t"
  "v_mfma_f32_16x16x32_bf16 a[84:87], v[184:187], v[172:175], a[84:87]\n\t"
  "v_mfma_f32_16x16x32_bf16 a[88:91], v[188:191], v[168:171], a[88:91]\n\t"
  "ds_read_b64_tr_b16 v[164:165], v241 offset:8192\n\t"
  "ds_read_b64_tr_b16 v[166:167], v241 offset:10240\n\t"
  "v_mfma_f32_16x16x32_bf16 a[92:95], v[188:191], v[172:175], a[92:95]\n\t"
  "ds_read_b64_tr_b16 v[168:169], v240 offset:8704\n\t"
  "ds_read_b64_tr_b16 v[170:171], v240 offset:10752\n\t"
  "s_waitcnt lgkmcnt(12)\n\t"
  "ds_read_b64_tr_b16 v[172:173], v241 offset:8704\n\t"
  "ds_read_b64_tr_b16 v[174:175], v241 offset:10752\n\t"
  "v_mfma_f32_16x16x32_bf16 a[64:67], v[136:139], v[128:131], a[64:67]\n\t"
  "s_waitcnt vmcnt(13)\n\t"
  "s_barrier\n\t"
  "ds_read_b128 v[176:179], v234 offset:49152\n\t"
  "v_mfma_f32_16x16x32_bf16 a[68:71], v[136:139], v[132:135], a[68:71]\n\t"
  "s_add_u32 m0, %[m0k], 131072\n\t"
  "s_nop 0\n\t"
  "buffer_load_dwordx4 v231, %[srdws], 0 offen offset:0 sc1 lds\n\t"
  "v_mfma_f32_16x16x32_bf16 a[72:75], v[140:143], v[128:131], a[72:75]\n\t"
  "ds_read_b128 v[180:183], v234 offset:51200\n\t"
  "v_mfma_f32_16x16x32_bf16 a[76:79], v[140:143], v[132:135], a[76:79]\n\t"
  "buffer_load_dwordx4 v231, %[srdws], 0 offen offset:1024 sc1 lds\n\t"
  "s_waitcnt lgkmcnt(11)\n\t"
  "v_mfma_f32_16x16x32_bf16 a[80:83], v[144:147], v[128:131], a[80:83]\n\t"
  "ds_read_b64_tr_b16 v[184:185], v240 offset:32768\n\t"
  "ds_read_b64_tr_b16 v[186:187], v240 offset:34816\n\t"
  "v_mfma_f32_16x16x32_bf16 a[84:87], v[144:147], v[132:135], a[84:87]\n\t"
  "buffer_load_dwordx4 v231, %[srdws], 0 offen offset:2048 sc1 lds\n\t"
  "v_mfma_f32_16x16x32_bf16 a[88:91], v[148:151], v[128:131], a[88:91]\n\t"
  "ds_read_b64_tr_b16 v[188:189], v241 offset:32768\n\t"
  "ds_read_b64_tr_b16 v[190:191], v241 offset:34816\n\t"
  "v_mfma_f32_16x16x32_bf16 a[92:95], v[148:151], v[132:135], a[92:95]\n\t"
  "buffer_load_dwordx4 v231, %[srdws], 0 offen offset:3072 sc1 lds\n\t"
  "v_add_u32 v231, 0x4000, v231\n\t"
  "s_add_u32 m0, %[m0w], 147456\n\t"
  "ds_read_b64_tr_b16 v[128:129], v240 offset:33280\n\t"
  "ds_read_b64_tr_b16 v[130:131], v240 offset:35328\n\t"
  "buffer_load_dwordx4 v232, %[srdp], 0 offen offset:0 lds\n\t"
  "v_add_u32 v232, 0x80, v232\n\t"
  "s_waitcnt lgkmcnt(12)\n\t"
  "ds_read_b64_tr_b16 v[132:133], v241 offset:33280\n\t"
  "ds_read_b64_tr_b16 v[134:135], v241 offset:35328\n\t"
  "buffer_load_dwordx4 v233, %[srdp], %[c31744] offen offset:1024 lds\n\t"
  "v_add_u32 v233, 0x80, v233\n\t"
  "v_mfma_f32_16x16x32_bf16 a[64:67], v[160:163], v[152:155], a[64:67]\n\t"
  "ds_read_b128 v[136:139], v235 offset:49152\n\t"
  "v_mfma_f32_16x16x32_bf16 a[68:71], v[160:163], v[156:159], a[68:71]\n\t"
  "v_mfma_f32_16x16x32_bf16 a[72:75], v[164:167], v[152:155], a[72:75]\n\t"
  "ds_read_b128 v[140:143], v235 offset:51200\n\t"
  "v_mfma_f32_16x16x32_bf16 a[76:79], v[164:167], v[156:159], a[76:79]\n\t"
  "s_waitcnt lgkmcnt(11)\n\t"
  "v_mfma_f32_16x16x32_bf16 a[80:83], v[168:171], v[152:155], a[80:83]\n\t"
  "ds_read_b64_tr_b16 v[144:145], v240 offset:40960\n\t"
  "ds_read_b64_tr_b16 v[146:147], v240 offset:43008\n\t"
  "v_mfma_f32_16x16x32_bf16 a[84:87], v[168:171], v[156:159], a[84:87]\n\t"
  "v_mfma_f32_16x16x32_bf16 a[88:91], v[172:175], v[152:155], a[88:91]\n\t"
  "ds_read_b64_tr_b16 v[148:149], v241 offset:40960\n\t"
  "ds_read_b64_tr_b16 v[150:151], v241 offset:43008\n\t"
  "v_mfma_f32_16x16x32_bf16 a[92:95], v[172:175], v[156:159], a[92:95]\n\t"
  "ds_read_b64_tr_b16 v[152:153], v240 offset:41472\n\t"
  "ds_read_b64_tr_b16 v[154:155], v240 offset:43520\n\t"
  "s_waitcnt lgkmcnt(12)\n\t"
  "ds_read_b64_tr_b16 v[156:157], v241 offset:41472\n\t"
  "ds_read_b64_tr_b16 v[158:159], v241 offset:43520\n\t"
  "v_mfma_f32_16x16x32_bf16 a[64:67], v[184:187], v[176:179], a[64:67]\n\t"
  "s_waitcnt vmcnt(12)\n\t"
  "s_barrier\n\t"
  "s_mov_b64 exec, 1\n\t"
  "buffer_atomic_add v207, v206, %[srdws], %[cntp] offen\n\t"
  "s_mov_b64 exec, -1\n\t"
  "ds_read_b128 v[160:163], v236 offset:16384\n\t"
  "v_mfma_f32_16x16x32_bf16 a[68:71], v[184:187], v[180:183], a[68:71]\n\t"
  "s_add_u32 m0, %[m0k], 0\n\t"
  "s_nop 0\n\t"
  "buffer_load_dwordx4 v231, %[srdws], 0 offen offset:0 sc1 lds\n\t"
  "v_mfma_f32_16x16x32_bf16 a[72:75], v[188:191], v[176:179], a[72:75]\n\t"
  "ds_read_b128 v[164:167], v236 offset:18432\n\t"
  "v_mfma_f32_16x16x32_bf16 a[76:79], v[188:191], v[180:183], a[76:79]\n\t"
  "buffer_load_dwordx4 v231, %[srdws], 0 offen offset:1024 sc1 lds\n\t"
  "s_waitcnt lgkmcnt(11)\n\t"
  "v_mfma_f32_16x16x32_bf16 a[80:83], v[128:131], v[176:179], a[80:83]\n\t"
  "ds_read_b64_tr_b16 v[168:169], v242 offset:0\n\t"
  "ds_read_b64_tr_b16 v[170:171], v242 offset:2048\n\t"
  "v_mfma_f32_16x16x32_bf16 a[84:87], v[128:131], v[180:183], a[84:87]\n\t"
  "buffer_load_dwordx4 v231, %[srdws], 0 offen offset:2048 sc1 lds\n\t"
  "v_mfma_f32_16x16x32_bf16 a[88:91], v[132:135], v[176:179], a[88:91]\n\t"
  "ds_read_b64_tr_b16 v[172:173], v243 offset:0\n\t"
  "ds_read_b64_tr_b16 v[174:175], v243 offset:2048\n\t"
  "v_mfma_f32_16x16x32_bf16 a[92:95], v[132:135], v[180:183], a[92:95]\n\t"
  "buffer_load_dwordx4 v231, %[srdws], 0 offen offset:3072 sc1 lds\n\t"
  "v_add_u32 v231, 0x4000, v231\n\t"
  "s_add_u32 m0, %[m0w], 16384\n\t"
  "ds_read_b64_tr_b16 v[176:177], v242 offset:512\n\t"
  "ds_read_b64_tr_b16 v[178:179], v242 offset:2560\n\t"
  "buffer_load_dwordx4 v232, %[srdp], 0 offen offset:0 lds\n\t"
  "v_add_u32 v232, 0x80, v232\n\t"
  "s_waitcnt lgkmcnt(12)\n\t"
  "ds_read_b64_tr_b16 v[180:181], v243 offset:512\n\t"
  "ds_read_b64_tr_b16 v[182:183], v243 offset:2560\n\t"
  "buffer_load_dwordx4 v233, %[srdp], %[c31744] offen offset:1024 lds\n\t"
  "v_add_u32 v233, 0x80, v233\n\t"
  "v_mfma_f32_16x16x32_bf16 a[64:67], v[144:147], v[136:139], a[64:67]\n\t"
  "ds_read_b128 v[184:187], v237 offset:16384\n\t"
  "v_mfma_f32_16x16x32_bf16 a[68:71], v[144:147], v[140:143], a[68:71]\n\t"
  "v_mfma_f32_16x16x32_bf16 a[72:75], v[148:151], v[136:139], a[72:75]\n\t"
  "ds_read_b128 v[188:191], v237 offset:18432\n\t"
  "v_mfma_f32_16x16x32_bf16 a[76:79], v[148:151], v[140:143], a[76:79]\n\t"
  "s_waitcnt lgkmcnt(11)\n\t"
  "v_mfma_f32_16x16x32_bf16 a[80:83], v[152:155], v[136:139], a[80:83]\n\t"
  "ds_read_b64_tr_b16 v[128:129], v242 offset:8192\n\t"
  "ds_read_b64_tr_b16 v[130:131], v242 offset:10240\n\t"
  "v_mfma_f32_16x16x32_bf16 a[84:87], v[152:155], v[140:143], a[84:87]\n\t"
  "v_mfma_f32_16x16x32_bf16 a[88:91], v[156:159], v[136:139], a[88:91]\n\t"
  "ds_read_b64_tr_b16 v[132:133], v243 offset:8192\n\t"
  "ds_read_b64_tr_b16 v[134:135], v243 offset:10240\n\t"
  "v_mfma_f32_16x16x32_bf16 a[92:95], v[156:159], v[140:143], a[92:95]\n\t"
  "ds_read_b64_tr_b16 v[136:137], v242 offset:8704\n\t"
  "ds_read_b64_tr_b16 v[138:139], v242 offset:10752\n\t"
  "s_waitcnt lgkmcnt(12)\n\t"
  "ds_read_b64_tr_b16 v[140:141], v243 offset:8704\n\t"
  "ds_read_b64_tr_b16 v[142:143], v243 offset:10752\n\t"
  "v_mfma_f32_16x16x32_bf16 a[64:67], v[168:171], v[160:163], a[64:67]\n\t"
  "s_waitcnt vmcnt(13)\n\t"
  "s_barrier\n\t"
  "ds_read_b128 v[144:147], v236 offset:49152\n\t"
  "v_mfma_f32_16x16x32_bf16 a[68:71], v[168:171], v[164:167], a[68:71]\n\t"
  "s_add_u32 m0, %[m0k], 32768\n\t"
  "s_nop 0\n\t"
  "buffer_load_dwordx4 v231, %[srdws], 0 offen offset:0 sc1 lds\n\t"
  "v_mfma_f32_16x16x32_bf16 a[72:75], v[172:175], v[160:163], a[72:75]\n\t"
  "ds_read_b128 v[148:151], v236 offset:51200\n\t"
  "v_mfma_f32_16x16x32_bf16 a[76:79], v[172:175], v[164:167], a[76:79]\n\t"
  "buffer_load_dwordx4 v231, %[srdws], 0 offen offset:1024 sc1 lds\n\t"
  "s_waitcnt lgkmcnt(11)\n\t"
  "v_mfma_f32_16x16x32_bf16 a[80:83], v[176:179], v[160:163], a[80:83]\n\t"
  "ds_read_b64_tr_b16 v[152:153], v242 offset:32768\n\t"
  "ds_read_b64_tr_b16 v[154:155], v242 offset:34816\n\t"
  "v_mfma_f32_16x16x32_bf16 a[84:87], v[176:179], v[164:167], a[84:87]\n\t"
  "buffer_load_dwordx4 v231, %[srdws], 0 offen offset:2048 sc1 lds\n\t"
  "v_mfma_f32_16x16x32_bf16 a[88:91], v[180:183], v[160:163], a[88:91]\n\t"
  "ds_read_b64_tr_b16 v[156:157], v243 offset:32768\n\t"
  "ds_read_b64_tr_b16 v[158:159], v243 offset:34816\n\t"
  "v_mfma_f32_16x16x32_bf16 a[92:95], v[180:183], v[164:167], a[92:95]\n\t"
  "buffer_load_dwordx4 v231, %[srdws], 0 offen offset:3072 sc1 lds\n\t"
  "v_add_u32 v231, 0x4000, v231\n\t"
  "s_add_u32 m0, %[m0w], 49152\n\t"
  "ds_read_b64_tr_b16 v[160:161], v242 offset:33280\n\t"
  "ds_read_b64_tr_b16 v[162:163], v242 offset:35328\n\t"
  "buffer_load_dwordx4 v232, %[srdp], 0 offen offset:0 lds\n\t"
  "v_add_u32 v232, 0x80, v232\n\t"
  "s_waitcnt lgkmcnt(12)\n\t"
  "ds_read_b64_tr_b16 v[164:165], v243 offset:33280\n\t"
  "ds_read_b64_tr_b16 v[166:167], v243 offset:35328\n\t"
  "buffer_load_dwordx4 v233, %[srdp], %[c31744] offen offset:1024 lds\n\t"
  "v_add_u32 v233, 0x80, v233\n\t"
  "v_mfma_f32_16x16x32_bf16 a[64:67], v[128:131], v[184:187], a[64:67]\n\t"
  "ds_read_b128 v[168:171], v237 offset:49152\n\t"
  "v_mfma_f32_16x16x32_bf16 a[68:71], v[128:131], v[188:191], a[68:71]\n\t"
  "v_mfma_f32_16x16x32_bf16 a[72:75], v[132:135], v[184:187], a[72:75]\n\t"
  "ds_read_b128 v[172:175], v237 offset:51200\n\t"
  "v_mfma_f32_16x16x32_bf16 a[76:79], v[132:135], v[188:191], a[76:79]\n\t"
  "s_waitcnt lgkmcnt(11)\n\t"
  "v_mfma_f32_16x16x32_bf16 a[80:83], v[136:139], v[184:187], a[80:83]\n\t"
  "ds_read_b64_tr_b16 v[176:177], v242 offset:40960\n\t"
  "ds_read_b64_tr_b16 v[178:179], v242 offset:43008\n\t"
  "v_mfma_f32_16x16x32_bf16 a[84:87], v[136:139], v[188:191], a[84:87]\n\t"
  "v_mfma_f32_16x16x32_bf16 a[88:91], v[140:143], v[184:187], a[88:91]\n\t"
  "ds_read_b64_tr_b16 v[180:181], v243 offset:40960\n\t"
  "ds_read_b64_tr_b16 v[182:183], v243 offset:43008\n\t"
  "v_mfma_f32_16x16x32_bf16 a[92:95], v[140:143], v[188:191], a[92:95]\n\t"
  "ds_read_b64_tr_b16 v[184:185], v242 offset:41472\n\t"
  "ds_read_b64_tr_b16 v[186:187], v242 offset:43520\n\t"
  "s_waitcnt lgkmcnt(12)\n\t"
  "ds_read_b64_tr_b16 v[188:189], v243 offset:41472\n\t"
  "ds_read_b64_tr_b16 v[190:191], v243 offset:43520\n\t"
  "v_mfma_f32_16x16x32_bf16 a[64:67], v[152:155], v[144:147], a[64:67]\n\t"
  "s_waitcnt vmcnt(13)\n\t"
  "s_barrier\n\t"
  "ds_read_b128 v[128:131], v238 offset:16384\n\t"
  "v_mfma_f32_16x16x32_bf16 a[68:71], v[152:155], v[148:151], a[68:71]\n\t"
  "s_add_u32 m0, %[m0k], 65536\n\t"
  "s_nop 0\n\t"
  "buffer_load_dwordx4 v231, %[srdws], 0 offen offset:0 sc1 lds\n\t"
  "v_mfma_f32_16x16x32_bf16 a[72:75], v[156:159], v[144:147], a[72:75]\n\t"
  "ds_read_b128 v[132:135], v238 offset:18432\n\t"
  "v_mfma_f32_16x16x32_bf16 a[76:79], v[156:159], v[148:151], a[76:79]\n\t"
  "buffer_load_dwordx4 v231, %[srdws], 0 offen offset:1024 sc1 lds\n\t"
  "s_waitcnt lgkmcnt(11)\n\t"
  "v_mfma_f32_16x16x32_bf16 a[80:83], v[160:163], v[144:147], a[80:83]\n\t"
  "ds_read_b64_tr_b16 v[136:137], v244 offset:0\n\t"
  "ds_read_b64_tr_b16 v[138:139], v244 offset:2048\n\t"
  "v_mfma_f32_16x16x32_bf16 a[84:87], v[160:163], v[148:151], a[84:87]\n\t"
  "buffer_load_dwordx4 v231, %[srdws], 0 offen offset:2048 sc1 lds\n\t"
  "v_mfma_f32_16x16x32_bf16 a[88:91], v[164:167], v[144:147], a[88:91]\n\t"
  "ds_read_b64_tr_b16 v[140:141], v245 offset:0\n\t"
  "ds_read_b64_tr_b16 v[142:143], v245 offset:2048\n\t"
  "v_mfma_f32_16x16x32_bf16 a[92:95], v[164:167], v[148:151], a[92:95]\n\t"
  "buffer_load_dwordx4 v231, %[srdws], 0 offen offset:3072 sc1 lds\n\t"
  "v_add_u32 v231, 0x4000, v231\n\t"
  "s_add_u32 m0, %[m0w], 81920\n\t"
  "ds_read_b64_tr_b16 v[144:145], v244 offset:512\n\t"
  "ds_read_b64_tr_b16 v[146:147], v244 offset:2560\n\t"
  "buffer_load_dwordx4 v232, %[srdp], 0 offen offset:0 lds\n\t"
  "v_add_u32 v232, 0x80, v232\n\t"
  "s_waitcnt lgkmcnt(12)\n\t"
  "ds_read_b64_tr_b16 v[148:149], v245 offset:512\n\t"
  "ds_read_b64_tr_b16 v[150:151], v245 offset:2560\n\t"
  "buffer_load_dwordx4 v233, %[srdp], %[c31744] offen offset:1024 lds\n\t"
  "v_add_u32 v233, 0x80, v233\n\t"
  "v_mfma_f32_16x16x32_bf16 a[64:67], v[176:179], v[168:171], a[64:67]\n\t"
  "ds_read_b128 v[152:155], v239 offset:16384\n\t"
  "v_mfma_f32_16x16x32_bf16 a[68:71], v[176:179], v[172:175], a[68:71]\n\t"
  "v_mfma_f32_16x16x32_bf16 a[72:75], v[180:183], v[168:171], a[72:75]\n\t"
  "ds_read_b128 v[156:159], v239 offset:18432\n\t"
  "v_mfma_f32_16x16x32_bf16 a[76:79], v[180:183], v[172:175], a[76:79]\n\t"
  "s_waitcnt lgkmcnt(11)\n\t"
  "v_mfma_f32_16x16x32_bf16 a[80:83], v[184:187], v[168:171], a[80:83]\n\t"
  "ds_read_b64_tr_b16 v[160:161], v244 offset:8192\n\t"
  "ds_read_b64_tr_b16 v[162:163], v244 offset:10240\n\t"
  "v_mfma_f32_16x16x32_bf16 a[84:87], v[184:187], v[172:175], a[84:87]\n\t"
  "v_mfma_f32_16x16x32_bf16 a[88:91], v[188:191], v[168:171], a[88:91]\n\t"
  "ds_read_b64_tr_b16 v[164:165], v245 offset:8192\n\t"
  "ds_read_b64_tr_b16 v[166:167], v245 offset:10240\n\t"
  "v_mfma_f32_16x16x32_bf16 a[92:95], v[188:191], v[172:175], a[92:95]\n\t"
  "ds_read_b64_tr_b16 v[168:169], v244 offset:8704\n\t"
  "ds_read_b64_tr_b16 v[170:171], v244 offset:10752\n\t"
  "s_waitcnt lgkmcnt(12)\n\t"
  "ds_read_b64_tr_b16 v[172:173], v245 offset:8704\n\t"
  "ds_read_b64_tr_b16 v[174:175], v245 offset:10752\n\t"
  "v_mfma_f32_16x16x32_bf16 a[64:67], v[136:139], v[128:131], a[64:67]\n\t"
  "s_waitcnt vmcnt(12)\n\t"
  "s_barrier\n\t"
  "ds_read_b128 v[176:179], v234 offset:16384\n\t"
  "v_mfma_f32_16x16x32_bf16 a[68:71], v[136:139], v[132:135], a[68:71]\n\t"
  "s_add_u32 m0, %[m0k], 98304\n\t"
  "s_nop 0\n\t"
  "buffer_load_dwordx4 v231, %[srdws], 0 offen offset:0 sc1 lds\n\t"
  "v_mfma_f32_16x16x32_bf16 a[72:75], v[140:143], v[128:131], a[72:75]\n\t"
  "ds_read_b128 v[180:183], v234 offset:18432\n\t"
  "v_mfma_f32_16x16x32_bf16 a[76:79], v[140:143], v[132:135], a[76:79]\n\t"
  "buffer_load_dwordx4 v231, %[srdws], 0 offen offset:1024 sc1 lds\n\t"
  "s_waitcnt lgkmcnt(11)\n\t"
  "v_mfma_f32_16x16x32_bf16 a[80:83], v[144:147], v[128:131], a[80:83]\n\t"
  "ds_read_b64_tr_b16 v[184:185], v240 offset:0\n\t"
  "ds_read_b64_tr_b16 v[186:187], v240 offset:2048\n\t"
  "v_mfma_f32_16x16x32_bf16 a[84:87], v[144:147], v[132:135], a[84:87]\n\t"
  "buffer_load_dwordx4 v231, %[srdws], 0 offen offset:2048 sc1 lds\n\t"
  "v_mfma_f32_16x16x32_bf16 a[88:91], v[148:151], v[128:131], a[88:91]\n\t"
  "ds_read_b64_tr_b16 v[188:189], v241 offset:0\n\t"
  "ds_read_b64_tr_b16 v[190:191], v241 offset:2048\n\t"
  "v_mfma_f32_16x16x32_bf16 a[92:95], v[148:151], v[132:135], a[92:95]\n\t"
  "buffer_load_dwordx4 v231, %[srdws], 0 offen offset:3072 sc1 lds\n\t"
  "v_add_u32 v231, 0x4000, v231\n\t"
  "s_add_u32 m0, %[m0w], 114688\n\t"
  "ds_read_b64_tr_b16 v[128:129], v240 offset:512\n\t"
  "ds_read_b64_tr_b16 v[130:131], v240 offset:2560\n\t"
  "buffer_load_dwordx4 v232, %[srdp], 0 offen offset:0 lds\n\t"
  "v_add_u32 v232, 0x80, v232\n\t"
  "s_waitcnt lgkmcnt(12)\n\t"
  "ds_read_b64_tr_b16 v[132:133], v241 offset:512\n\t"
  "ds_read_b64_tr_b16 v[134:135], v241 offset:2560\n\t"
  "buffer_load_dwordx4 v233, %[srdp], %[c31744] offen offset:1024 lds\n\t"
  "v_add_u32 v233, 0x80, v233\n\t"
  "v_mfma_f32_16x16x32_bf16 a[64:67], v[160:163], v[152:155], a[64:67]\n\t"
  "ds_read_b128 v[136:139], v235 offset:16384\n\t"
  "v_mfma_f32_16x16x32_bf16 a[68:71], v[160:163], v[156:159], a[68:71]\n\t"
  "v_mfma_f32_16x16x32_bf16 a[72:75], v[164:167], v[152:155], a[72:75]\n\t"
  "ds_read_b128 v[140:143], v235 offset:18432\n\t"
  "v_mfma_f32_16x16x32_bf16 a[76:79], v[164:167], v[156:159], a[76:79]\n\t"
  "s_waitcnt lgkmcnt(11)\n\t"
  "v_mfma_f32_16x16x32_bf16 a[80:83], v[168:171], v[152:155], a[80:83]\n\t"
  "ds_read_b64_tr_b16 v[144:145], v240 offset:8192\n\t"
  "ds_read_b64_tr_b16 v[146:147], v240 offset:10240\n\t"
  "v_mfma_f32_16x16x32_bf16 a[84:87], v[168:171], v[156:159], a[84:87]\n\t"
  "v_mfma_f32_16x16x32_bf16 a[88:91], v[172:175], v[152:155], a[88:91]\n\t"
  "ds_read_b64_tr_b16 v[148:149], v241 offset:8192\n\t"
  "ds_read_b64_tr_b16 v[150:151], v241 offset:10240\n\t"
  "v_mfma_f32_16x16x32_bf16 a[92:95], v[172:175], v[156:159], a[92:95]\n\t"
  "ds_read_b64_tr_b16 v[152:153], v240 offset:8704\n\t"
  "ds_read_b64_tr_b16 v[154:155], v240 offset:10752\n\t"
  "s_waitcnt lgkmcnt(12)\n\t"
  "ds_read_b64_tr_b16 v[156:157], v241 offset:8704\n\t"
  "ds_read_b64_tr_b16 v[158:159], v241 offset:10752\n\t"
  "v_mfma_f32_16x16x32_bf16 a[64:67], v[184:187], v[176:179], a[64:67]\n\t"
  "s_waitcnt vmcnt(12)\n\t"
  "s_barrier\n\t"
  "ds_read_b128 v[160:163], v234 offset:49152\n\t"
  "v_mfma_f32_16x16x32_bf16 a[68:71], v[184:187], v[180:183], a[68:71]\n\t"
  "s_add_u32 m0, %[m0k], 131072\n\t"
  "s_nop 0\n\t"
  "buffer_load_dwordx4 v231, %[srdws], 0 offen offset:0 sc1 lds\n\t"
  "v_mfma_f32_16x16x32_bf16 a[72:75], v[188:191], v[176:179], a[72:75]\n\t"
  "ds_read_b128 v[164:167], v234 offset:51200\n\t"
  "v_mfma_f32_16x16x32_bf16 a[76:79], v[188:191], v[180:183], a[76:79]\n\t"
  "buffer_load_dwordx4 v231, %[srdws], 0 offen offset:1024 sc1 lds\n\t"
  "s_waitcnt lgkmcnt(11)\n\t"
  "v_mfma_f32_16x16x32_bf16 a[80:83], v[128:131], v[176:179], a[80:83]\n\t"
  "ds_read_b64_tr_b16 v[168:169], v240 offset:32768\n\t"
  "ds_read_b64_tr_b16 v[170:171], v240 offset:34816\n\t"
  "v_mfma_f32_16x16x32_bf16 a[84:87], v[128:131], v[180:183], a[84:87]\n\t"
  "buffer_load_dwordx4 v231, %[srdws], 0 offen offset:2048 sc1 lds\n\t"
  "v_mfma_f32_16x16x32_bf16 a[88:91], v[132:135], v[176:179], a[88:91]\n\t"
  "ds_read_b64_tr_b16 v[172:173], v241 offset:32768\n\t"
  "ds_read_b64_tr_b16 v[174:175], v241 offset:34816\n\t"
  "v_mfma_f32_16x16x32_bf16 a[92:95], v[132:135], v[180:183], a[92:95]\n\t"
  "buffer_load_dwordx4 v231, %[srdws], 0 offen offset:3072 sc1 lds\n\t"
  "v_add_u32 v231, 0x4000, v231\n\t"
  "s_add_u32 m0, %[m0w], 147456\n\t"
  "ds_read_b64_tr_b16 v[176:177], v240 offset:33280\n\t"
  "ds_read_b64_tr_b16 v[178:179], v240 offset:35328\n\t"
  "buffer_load_dwordx4 v232, %[srdp], 0 offen offset:0 lds\n\t"
  "v_add_u32 v232, 0x80, v232\n\t"
  "s_waitcnt lgkmcnt(12)\n\t"
  "ds_read_b64_tr_b16 v[180:181], v241 offset:33280\n\t"
  "ds_read_b64_tr_b16 v[182:183], v241 offset:35328\n\t"
  "buffer_load_dwordx4 v233, %[srdp], %[c31744] offen offset:1024 lds\n\t"
  "v_add_u32 v233, 0x80, v233\n\t"
  "v_mfma_f32_16x16x32_bf16 a[64:67], v[144:147], v[136:139], a[64:67]\n\t"
  "ds_read_b128 v[184:187], v235 offset:49152\n\t"
  "v_mfma_f32_16x16x32_bf16 a[68:71], v[144:147], v[140:143], a[68:71]\n\t"
  "v_mfma_f32_16x16x32_bf16 a[72:75], v[148:151], v[136:139], a[72:75]\n\t"
  "ds_read_b128 v[188:191], v235 offset:51200\n\t"
  "v_mfma_f32_16x16x32_bf16 a[76:79], v[148:151], v[140:143], a[76:79]\n\t"
  "s_waitcnt lgkmcnt(11)\n\t"
  "v_mfma_f32_16x16x32_bf16 a[80:83], v[152:155], v[136:139], a[80:83]\n\t"
  "ds_read_b64_tr_b16 v[128:129], v240 offset:40960\n\t"
  "ds_read_b64_tr_b16 v[130:131], v240 offset:43008\n\t"
  "v_mfma_f32_16x16x32_bf16 a[84:87], v[152:155], v[140:143], a[84:87]\n\t"
  "v_mfma_f32_16x16x32_bf16 a[88:91], v[156:159], v[136:139], a[88:91]\n\t"
  "ds_read_b64_tr_b16 v[132:133], v241 offset:40960\n\t"
  "ds_read_b64_tr_b16 v[134:135], v241 offset:43008\n\t"
  "v_mfma_f32_16x16x32_bf16 a[92:95], v[156:159], v[140:143], a[92:95]\n\t"
  "ds_read_b64_tr_b16 v[136:137], v240 offset:41472\n\t"
  "ds_read_b64_tr_b16 v[138:139], v240 offset:43520\n\t"
  "s_waitcnt lgkmcnt(12)\n\t"
  "ds_read_b64_tr_b16 v[140:141], v241 offset:41472\n\t"
  "ds_read_b64_tr_b16 v[142:143], v241 offset:43520\n\t"
  "v_mfma_f32_16x16x32_bf16 a[64:67], v[168:171], v[160:163], a[64:67]\n\t"
  "s_waitcnt vmcnt(12)\n\t"
  "s_barrier\n\t"
  "ds_read_b128 v[144:147], v236 offset:16384\n\t"
  "v_mfma_f32_16x16x32_bf16 a[68:71], v[168:171], v[164:167], a[68:71]\n\t"
  "s_add_u32 m0, %[m0k], 0\n\t"
  "s_nop 0\n\t"
  "buffer_load_dwordx4 v231, %[srdws], 0 offen offset:0 sc1 lds\n\t"
  "v_mfma_f32_16x16x32_bf16 a[72:75], v[172:175], v[160:163], a[72:75]\n\t"
  "ds_read_b128 v[148:151], v236 offset:18432\n\t"
  "v_mfma_f32_16x16x32_bf16 a[76:79], v[172:175], v[164:167], a[76:79]\n\t"
  "buffer_load_dwordx4 v231, %[srdws], 0 offen offset:1024 sc1 lds\n\t"
  "s_waitcnt lgkmcnt(11)\n\t"
  "v_mfma_f32_16x16x32_bf16 a[80:83], v[176:179], v[160:163], a[80:83]\n\t"
  "ds_read_b64_tr_b16 v[152:153], v242 offset:0\n\t"
  "ds_read_b64_tr_b16 v[154:155], v242 offset:2048\n\t"
  "v_mfma_f32_16x16x32_bf16 a[84:87], v[176:179], v[164:167], a[84:87]\n\t"
  "buffer_load_dwordx4 v231, %[srdws], 0 offen offset:2048 sc1 lds\n\t"
  "v_mfma_f32_16x16x32_bf16 a[88:91], v[180:183], v[160:163], a[88:91]\n\t"
  "ds_read_b64_tr_b16 v[156:157], v243 offset:0\n\t"
  "ds_read_b64_tr_b16 v[158:159], v243 offset:2048\n\t"
  "v_mfma_f32_16x16x32_bf16 a[92:95], v[180:183], v[164:167], a[92:95]\n\t"
  "buffer_load_dwordx4 v231, %[srdws], 0 offen offset:3072 sc1 lds\n\t"
  "v_add_u32 v231, 0x4000, v231\n\t"
  "s_add_u32 m0, %[m0w], 16384\n\t"
  "ds_read_b64_tr_b16 v[160:161], v242 offset:512\n\t"
  "ds_read_b64_tr_b16 v[162:163], v242 offset:2560\n\t"
  "buffer_load_dwordx4 v232, %[srdp], 0 offen offset:0 lds\n\t"
  "v_add_u32 v232, 0x80, v232\n\t"
  "s_waitcnt lgkmcnt(12)\n\t"
  "ds_read_b64_tr_b16 v[164:165], v243 offset:512\n\t"
  "ds_read_b64_tr_b16 v[166:167], v243 offset:2560\n\t"
  "buffer_load_dwordx4 v233, %[srdp], %[c31744] offen offset:1024 lds\n\t"
  "v_add_u32 v233, 0x80, v233\n\t"
  "v_mfma_f32_16x16x32_bf16 a[64:67], v[128:131], v[184:187], a[64:67]\n\t"
  "ds_read_b128 v[168:171], v237 offset:16384\n\t"
  "v_mfma_f32_16x16x32_bf16 a[68:71], v[128:131], v[188:191], a[68:71]\n\t"
  "v_mfma_f32_16x16x32_bf16 a[72:75], v[132:135], v[184:187], a[72:75]\n\t"
  "ds_read_b128 v[172:175], v237 offset:18432\n\t"
  "v_mfma_f32_16x16x32_bf16 a[76:79], v[132:135], v[188:191], a[76:79]\n\t"
  "s_waitcnt lgkmcnt(11)\n\t"
  "v_mfma_f32_16x16x32_bf16 a[80:83], v[136:139], v[184:187], a[80:83]\n\t"
  "ds_read_b64_tr_b16 v[176:177], v242 offset:8192\n\t"
  "ds_read_b64_tr_b16 v[178:179], v242 offset:10240\n\t"
  "v_mfma_f32_16x16x32_bf16 a[84:87], v[136:139], v[188:191], a[84:87]\n\t"
  "v_mfma_f32_16x16x32_bf16 a[88:91], v[140:143], v[184:187], a[88:91]\n\t"
  "ds_read_b64_tr_b16 v[180:181], v243 offset:8192\n\t"
  "ds_read_b64_tr_b16 v[182:183], v243 offset:10240\n\t"
  "v_mfma_f32_16x16x32_bf16 a[92:95], v[140:143], v[188:191], a[92:95]\n\t"
  "ds_read_b64_tr_b16 v[184:185], v242 offset:8704\n\t"
  "ds_read_b64_tr_b16 v[186:187], v242 offset:10752\n\t"
  "s_waitcnt lgkmcnt(12)\n\t"
  "ds_read_b64_tr_b16 v[188:189], v243 offset:8704\n\t"
  "ds_read_b64_tr_b16 v[190:191], v243 offset:10752\n\t"
  "v_mfma_f32_16x16x32_bf16 a[64:67], v[152:155], v[144:147], a[64:67]\n\t"
  "s_waitcnt vmcnt(12)\n\t"
  "s_barrier\n\t"
  "ds_read_b128 v[128:131], v236 offset:49152\n\t"
  "v_mfma_f32_16x16x32_bf16 a[68:71], v[152:155], v[148:151], a[68:71]\n\t"
  "s_add_u32 m0, %[m0k], 32768\n\t"
  "s_nop 0\n\t"
  "buffer_load_dwordx4 v231, %[srdws], 0 offen offset:0 sc1 lds\n\t"
  "v_mfma_f32_16x16x32_bf16 a[72:75], v[156:159], v[144:147], a[72:75]\n\t"
  "ds_read_b128 v[132:135], v236 offset:51200\n\t"
  "v_mfma_f32_16x16x32_bf16 a[76:79], v[156:159], v[148:151], a[76:79]\n\t"
  "buffer_load_dwordx4 v231, %[srdws], 0 offen offset:1024 sc1 lds\n\t"
  "s_waitcnt lgkmcnt(11)\n\t"
  "v_mfma_f32_16x16x32_bf16 a[80:83], v[160:163], v[144:147], a[80:83]\n\t"
  "ds_read_b64_tr_b16 v[136:137], v242 offset:32768\n\t"
  "ds_read_b64_tr_b16 v[138:139], v242 offset:34816\n\t"
  "v_mfma_f32_16x16x32_bf16 a[84:87], v[160:163], v[148:151], a[84:87]\n\t"
  "buffer_load_dwordx4 v231, %[srdws], 0 offen offset:2048 sc1 lds\n\t"
  "v_mfma_f32_16x16x32_bf16 a[88:91], v[164:167], v[144:147], a[88:91]\n\t"
  "ds_read_b64_tr_b16 v[140:141], v243 offset:32768\n\t"
  "ds_read_b64_tr_b16 v[142:143], v243 offset:34816\n\t"
  "v_mfma_f32_16x16x32_bf16 a[92:95], v[164:167], v[148:151], a[92:95]\n\t"
  "buffer_load_dwordx4 v231, %[srdws], 0 offen offset:3072 sc1 lds\n\t"
  "v_add_u32 v231, 0x4000, v231\n\t"
  "s_add_u32 m0, %[m0w], 49152\n\t"
  "ds_read_b64_tr_b16 v[144:145], v242 offset:33280\n\t"
  "ds_read_b64_tr_b16 v[146:147], v242 offset:35328\n\t"
  "buffer_load_dwordx4 v232, %[srdp], 0 offen offset:0 lds\n\t"
  "v_add_u32 v232, 0x80, v232\n\t"
  "s_waitcnt lgkmcnt(12)\n\t"
  "ds_read_b64_tr_b16 v[148:149], v243 offset:33280\n\t"
  "ds_read_b64_tr_b16 v[150:151], v243 offset:35328\n\t"
  "buffer_load_dwordx4 v233, %[srdp], %[c31744] offen offset:1024 lds\n\t"
  "v_add_u32 v233, 0x80, v233\n\t"
  "v_mfma_f32_16x16x32_bf16 a[64:67], v[176:179], v[168:171], a[64:67]\n\t"
  "ds_read_b128 v[152:155], v237 offset:49152\n\t"
  "v_mfma_f32_16x16x32_bf16 a[68:71], v[176:179], v[172:175], a[68:71]\n\t"
  "v_mfma_f32_16x16x32_bf16 a[72:75], v[180:183], v[168:171], a[72:75]\n\t"
  "ds_read_b128 v[156:159], v237 offset:51200\n\t"
  "v_mfma_f32_16x16x32_bf16 a[76:79], v[180:183], v[172:175], a[76:79]\n\t"
  "s_waitcnt lgkmcnt(11)\n\t"
  "v_mfma_f32_16x16x32_bf16 a[80:83], v[184:187], v[168:171], a[80:83]\n\t"
  "ds_read_b64_tr_b16 v[160:161], v242 offset:40960\n\t"
  "ds_read_b64_tr_b16 v[162:163], v242 offset:43008\n\t"
  "v_mfma_f32_16x16x32_bf16 a[84:87], v[184:187], v[172:175], a[84:87]\n\t"
  "v_mfma_f32_16x16x32_bf16 a[88:91], v[188:191], v[168:171], a[88:91]\n\t"
  "ds_read_b64_tr_b16 v[164:165], v243 offset:40960\n\t"
  "ds_read_b64_tr_b16 v[166:167], v243 offset:43008\n\t"
  "v_mfma_f32_16x16x32_bf16 a[92:95], v[188:191], v[172:175], a[92:95]\n\t"
  "ds_read_b64_tr_b16 v[168:169], v242 offset:41472\n\t"
  "ds_read_b64_tr_b16 v[170:171], v242 offset:43520\n\t"
  "s_waitcnt lgkmcnt(12)\n\t"
  "ds_read_b64_tr_b16 v[172:173], v243 offset:41472\n\t"
  "ds_read_b64_tr_b16 v[174:175], v243 offset:43520\n\t"
  "v_mfma_f32_16x16x32_bf16 a[64:67], v[136:139], v[128:131], a[64:67]\n\t"
  "s_waitcnt vmcnt(12)\n\t"
  "s_barrier\n\t"
  "ds_read_b128 v[176:179], v238 offset:16384\n\t"
  "v_mfma_f32_16x16x32_bf16 a[68:71], v[136:139], v[132:135], a[68:71]\n\t"
  "s_add_u32 m0, %[m0k], 65536\n\t"
  "s_nop 0\n\t"
  "buffer_load_dwordx4 v231, %[srdws], 0 offen offset:0 sc1 lds\n\t"
  "v_mfma_f32_16x16x32_bf16 a[72:75], v[140:143], v[128:131], a[72:75]\n\t"
  "ds_read_b128 v[180:183], v238 offset:18432\n\t"
  "v_mfma_f32_16x16x32_bf16 a[76:79], v[140:143], v[132:135], a[76:79]\n\t"
  "buffer_load_dwordx4 v231, %[srdws], 0 offen offset:1024 sc1 lds\n\t"
  "s_waitcnt lgkmcnt(11)\n\t"
  "v_mfma_f32_16x16x32_bf16 a[80:83], v[144:147], v[128:131], a[80:83]\n\t"
  "ds_read_b64_tr_b16 v[184:185], v244 offset:0\n\t"
  "ds_read_b64_tr_b16 v[186:187], v244 offset:2048\n\t"
  "v_mfma_f32_16x16x32_bf16 a[84:87], v[144:147], v[132:135], a[84:87]\n\t"
  "buffer_load_dwordx4 v231, %[srdws], 0 offen offset:2048 sc1 lds\n\t"
  "v_mfma_f32_16x16x32_bf16 a[88:91], v[148:151], v[128:131], a[88:91]\n\t"
  "ds_read_b64_tr_b16 v[188:189], v245 offset:0\n\t"
  "ds_read_b64_tr_b16 v[190:191], v245 offset:2048\n\t"
  "v_mfma_f32_16x16x32_bf16 a[92:95], v[148:151], v[132:135], a[92:95]\n\t"
  "buffer_load_dwordx4 v231, %[srdws], 0 offen offset:3072 sc1 lds\n\t"
  "v_add_u32 v231, 0x4000, v231\n\t"
  "s_add_u32 m0, %[m0w], 81920\n\t"
  "ds_read_b64_tr_b16 v[128:129], v244 offset:512\n\t"
  "ds_read_b64_tr_b16 v[130:131], v244 offset:2560\n\t"
  "buffer_load_dwordx4 v232, %[srdp], 0 offen offset:0 lds\n\t"
  "v_add_u32 v232, 0x80, v232\n\t"
  "s_waitcnt lgkmcnt(12)\n\t"
  "ds_read_b64_tr_b16 v[132:133], v245 offset:512\n\t"
  "ds_read_b64_tr_b16 v[134:135], v245 offset:2560\n\t"
  "buffer_load_dwordx4 v233, %[srdp], %[c31744] offen offset:1024 lds\n\t"
  "v_add_u32 v233, 0x80, v233\n\t"
  "v_mfma_f32_16x16x32_bf16 a[64:67], v[160:163], v[152:155], a[64:67]\n\t"
  "ds_read_b128 v[136:139], v239 offset:16384\n\t"
  "v_mfma_f32_16x16x32_bf16 a[68:71], v[160:163], v[156:159], a[68:71]\n\t"
  "v_mfma_f32_16x16x32_bf16 a[72:75], v[164:167], v[152:155], a[72:75]\n\t"
  "ds_read_b128 v[140:143], v239 offset:18432\n\t"
  "v_mfma_f32_16x16x32_bf16 a[76:79], v[164:167], v[156:159], a[76:79]\n\t"
  "s_waitcnt lgkmcnt(11)\n\t"
  "v_mfma_f32_16x16x32_bf16 a[80:83], v[168:171], v[152:155], a[80:83]\n\t"
  "ds_read_b64_tr_b16 v[144:145], v244 offset:8192\n\t"
  "ds_read_b64_tr_b16 v[146:147], v244 offset:10240\n\t"
  "v_mfma_f32_16x16x32_bf16 a[84:87], v[168:171], v[156:159], a[84:87]\n\t"
  "v_mfma_f32_16x16x32_bf16 a[88:91], v[172:175], v[152:155], a[88:91]\n\t"
  "ds_read_b64_tr_b16 v[148:149], v245 offset:8192\n\t"
  "ds_read_b64_tr_b16 v[150:151], v245 offset:10240\n\t"
  "v_mfma_f32_16x16x32_bf16 a[92:95], v[172:175], v[156:159], a[92:95]\n\t"
  "ds_read_b64_tr_b16 v[152:153], v244 offset:8704\n\t"
  "ds_read_b64_tr_b16 v[154:155], v244 offset:10752\n\t"
  "s_waitcnt lgkmcnt(12)\n\t"
  "ds_read_b64_tr_b16 v[156:157], v245 offset:8704\n\t"
  "ds_read_b64_tr_b16 v[158:159], v245 offset:10752\n\t"
  "v_mfma_f32_16x16x32_bf16 a[64:67], v[184:187], v[176:179], a[64:67]\n\t"
  "s_waitcnt vmcnt(12)\n\t"
  "s_barrier\n\t"
  "ds_read_b128 v[160:163], v234 offset:16384\n\t"
  "v_mfma_f32_16x16x32_bf16 a[68:71], v[184:187], v[180:183], a[68:71]\n\t"
  "s_add_u32 m0, %[m0k], 98304\n\t"
  "s_nop 0\n\t"
  "buffer_load_dwordx4 v231, %[srdws], 0 offen offset:0 sc1 lds\n\t"
  "v_mfma_f32_16x16x32_bf16 a[72:75], v[188:191], v[176:179], a[72:75]\n\t"
  "ds_read_b128 v[164:167], v234 offset:18432\n\t"
  "v_mfma_f32_16x16x32_bf16 a[76:79], v[188:191], v[180:183], a[76:79]\n\t"
  "buffer_load_dwordx4 v231, %[srdws], 0 offen offset:1024 sc1 lds\n\t"
  "s_waitcnt lgkmcnt(11)\n\t"
  "v_mfma_f32_16x16x32_bf16 a[80:83], v[128:131], v[176:179], a[80:83]\n\t"
  "ds_read_b64_tr_b16 v[168:169], v240 offset:0\n\t"
  "ds_read_b64_tr_b16 v[170:171], v240 offset:2048\n\t"
  "v_mfma_f32_16x16x32_bf16 a[84:87], v[128:131], v[180:183], a[84:87]\n\t"
  "buffer_load_dwordx4 v231, %[srdws], 0 offen offset:2048 sc1 lds\n\t"
  "v_mfma_f32_16x16x32_bf16 a[88:91], v[132:135], v[176:179], a[88:91]\n\t"
  "ds_read_b64_tr_b16 v[172:173], v241 offset:0\n\t"
  "ds_read_b64_tr_b16 v[174:175], v241 offset:2048\n\t"
  "v_mfma_f32_16x16x32_bf16 a[92:95], v[132:135], v[180:183], a[92:95]\n\t"
  "buffer_load_dwordx4 v231, %[srdws], 0 offen offset:3072 sc1 lds\n\t"
  "v_add_u32 v231, 0x4000, v231\n\t"
  "s_add_u32 m0, %[m0w], 114688\n\t"
  "ds_read_b64_tr_b16 v[176:177], v240 offset:512\n\t"
  "ds_read_b64_tr_b16 v[178:179], v240 offset:2560\n\t"
  "buffer_load_dwordx4 v232, %[srdp], 0 offen offset:0 lds\n\t"
  "v_add_u32 v232, 0x80, v232\n\t"
  "s_waitcnt lgkmcnt(12)\n\t"
  "ds_read_b64_tr_b16 v[180:181], v241 offset:512\n\t"
  "ds_read_b64_tr_b16 v[182:183], v241 offset:2560\n\t"
  "buffer_load_dwordx4 v233, %[srdp], %[c31744] offen offset:1024 lds\n\t"
  "v_add_u32 v233, 0x80, v233\n\t"
  "v_mfma_f32_16x16x32_bf16 a[64:67], v[144:147], v[136:139], a[64:67]\n\t"
  "ds_read_b128 v[184:187], v235 offset:16384\n\t"
  "v_mfma_f32_16x16x32_bf16 a[68:71], v[144:147], v[140:143], a[68:71]\n\t"
  "v_mfma_f32_16x16x32_bf16 a[72:75], v[148:151], v[136:139], a[72:75]\n\t"
  "ds_read_b128 v[188:191], v235 offset:18432\n\t"
  "v_mfma_f32_16x16x32_bf16 a[76:79], v[148:151], v[140:143], a[76:79]\n\t"
  "s_waitcnt lgkmcnt(11)\n\t"
  "v_mfma_f32_16x16x32_bf16 a[80:83], v[152:155], v[136:139], a[80:83]\n\t"
  "ds_read_b64_tr_b16 v[128:129], v240 offset:8192\n\t"
  "ds_read_b64_tr_b16 v[130:131], v240 offset:10240\n\t"
  "v_mfma_f32_16x16x32_bf16 a[84:87], v[152:155], v[140:143], a[84:87]\n\t"
  "v_mfma_f32_16x16x32_bf16 a[88:91], v[156:159], v[136:139], a[88:91]\n\t"
  "ds_read_b64_tr_b16 v[132:133], v241 offset:8192\n\t"
  "ds_read_b64_tr_b16 v[134:135], v241 offset:10240\n\t"
  "v_mfma_f32_16x16x32_bf16 a[92:95], v[156:159], v[140:143], a[92:95]\n\t"
  "ds_read_b64_tr_b16 v[136:137], v240 offset:8704\n\t"
  "ds_read_b64_tr_b16 v[138:139], v240 offset:10752\n\t"
  "s_waitcnt lgkmcnt(12)\n\t"
  "ds_read_b64_tr_b16 v[140:141], v241 offset:8704\n\t"
  "ds_read_b64_tr_b16 v[142:143], v241 offset:10752\n\t"
  "v_mfma_f32_16x16x32_bf16 a[64:67], v[168:171], v[160:163], a[64:67]\n\t"
  "s_waitcnt vmcnt(12)\n\t"
  "s_barrier\n\t"
  "ds_read_b128 v[144:147], v234 offset:49152\n\t"
  "v_mfma_f32_16x16x32_bf16 a[68:71], v[168:171], v[164:167], a[68:71]\n\t"
  "s_add_u32 m0, %[m0k], 131072\n\t"
  "s_nop 0\n\t"
  "buffer_load_dwordx4 v231, %[srdws], 0 offen offset:0 sc1 lds\n\t"
  "v_mfma_f32_16x16x32_bf16 a[72:75], v[172:175], v[160:163], a[72:75]\n\t"
  "ds_read_b128 v[148:151], v234 offset:51200\n\t"
  "v_mfma_f32_16x16x32_bf16 a[76:79], v[172:175], v[164:167], a[76:79]\n\t"
  "buffer_load_dwordx4 v231, %[srdws], 0 offen offset:1024 sc1 lds\n\t"
  "s_waitcnt lgkmcnt(11)\n\t"
  "v_mfma_f32_16x16x32_bf16 a[80:83], v[176:179], v[160:163], a[80:83]\n\t"
  "ds_read_b64_tr_b16 v[152:153], v240 offset:32768\n\t"
  "ds_read_b64_tr_b16 v[154:155], v240 offset:34816\n\t"
  "v_mfma_f32_16x16x32_bf16 a[84:87], v[176:179], v[164:167], a[84:87]\n\t"
  "buffer_load_dwordx4 v231, %[srdws], 0 offen offset:2048 sc1 lds\n\t"
  "v_mfma_f32_16x16x32_bf16 a[88:91], v[180:183], v[160:163], a[88:91]\n\t"
  "ds_read_b64_tr_b16 v[156:157], v241 offset:32768\n\t"
  "ds_read_b64_tr_b16 v[158:159], v241 offset:34816\n\t"
  "v_mfma_f32_16x16x32_bf16 a[92:95], v[180:183], v[164:167], a[92:95]\n\t"
  "buffer_load_dwordx4 v231, %[srdws], 0 offen offset:3072 sc1 lds\n\t"
  "v_add_u32 v231, 0x4000, v231\n\t"
  "s_add_u32 m0, %[m0w], 147456\n\t"
  "ds_read_b64_tr_b16 v[160:161], v240 offset:33280\n\t"
  "ds_read_b64_tr_b16 v[162:163], v240 offset:35328\n\t"
  "buffer_load_dwordx4 v232, %[srdp], 0 offen offset:0 lds\n\t"
  "v_add_u32 v232, 0x80, v232\n\t"
  "s_waitcnt lgkmcnt(12)\n\t"
  "ds_read_b64_tr_b16 v[164:165], v241 offset:33280\n\t"
  "ds_read_b64_tr_b16 v[166:167], v241 offset:35328\n\t"
  "buffer_load_dwordx4 v233, %[srdp], %[c31744] offen offset:1024 lds\n\t"
  "v_add_u32 v233, 0x80, v233\n\t"
  "v_mfma_f32_16x16x32_bf16 a[64:67], v[128:131], v[184:187], a[64:67]\n\t"
  "ds_read_b128 v[168:171], v235 offset:49152\n\t"
  "v_mfma_f32_16x16x32_bf16 a[68:71], v[128:131], v[188:191], a[68:71]\n\t"
  "v_mfma_f32_16x16x32_bf16 a[72:75], v[132:135], v[184:187], a[72:75]\n\t"
  "ds_read_b128 v[172:175], v235 offset:51200\n\t"
  "v_mfma_f32_16x16x32_bf16 a[76:79], v[132:135], v[188:191], a[76:79]\n\t"
  "s_waitcnt lgkmcnt(11)\n\t"
  "v_mfma_f32_16x16x32_bf16 a[80:83], v[136:139], v[184:187], a[80:83]\n\t"
  "ds_read_b64_tr_b16 v[176:177], v240 offset:40960\n\t"
  "ds_read_b64_tr_b16 v[178:179], v240 offset:43008\n\t"
  "v_mfma_f32_16x16x32_bf16 a[84:87], v[136:139], v[188:191], a[84:87]\n\t"
  "v_mfma_f32_16x16x32_bf16 a[88:91], v[140:143], v[184:187], a[88:91]\n\t"
  "ds_read_b64_tr_b16 v[180:181], v241 offset:40960\n\t"
  "ds_read_b64_tr_b16 v[182:183], v241 offset:43008\n\t"
  "v_mfma_f32_16x16x32_bf16 a[92:95], v[140:143], v[188:191], a[92:95]\n\t"
  "ds_read_b64_tr_b16 v[184:185], v240 offset:41472\n\t"
  "ds_read_b64_tr_b16 v[186:187], v240 offset:43520\n\t"
  "s_waitcnt lgkmcnt(12)\n\t"
  "ds_read_b64_tr_b16 v[188:189], v241 offset:41472\n\t"
  "ds_read_b64_tr_b16 v[190:191], v241 offset:43520\n\t"
  "v_mfma_f32_16x16x32_bf16 a[64:67], v[152:155], v[144:147], a[64:67]\n\t"
  "s_waitcnt vmcnt(12)\n\t"
  "s_barrier\n\t"
  "ds_read_b128 v[128:131], v236 offset:16384\n\t"
  "v_mfma_f32_16x16x32_bf16 a[68:71], v[152:155], v[148:151], a[68:71]\n\t"
  "s_add_u32 m0, %[m0k], 0\n\t"
  "s_nop 0\n\t"
  "buffer_load_dwordx4 v231, %[srdws], 0 offen offset:0 sc1 lds\n\t"
  "v_mfma_f32_16x16x32_bf16 a[72:75], v[156:159], v[144:147], a[72:75]\n\t"
  "ds_read_b128 v[132:135], v236 offset:18432\n\t"
  "v_mfma_f32_16x16x32_bf16 a[76:79], v[156:159], v[148:151], a[76:79]\n\t"
  "buffer_load_dwordx4 v231, %[srdws], 0 offen offset:1024 sc1 lds\n\t"
  "s_waitcnt lgkmcnt(11)\n\t"
  "v_mfma_f32_16x16x32_bf16 a[80:83], v[160:163], v[144:147], a[80:83]\n\t"
  "ds_read_b64_tr_b16 v[136:137], v242 offset:0\n\t"
  "ds_read_b64_tr_b16 v[138:139], v242 offset:2048\n\t"
  "v_mfma_f32_16x16x32_bf16 a[84:87], v[160:163], v[148:151], a[84:87]\n\t"
  "buffer_load_dwordx4 v231, %[srdws], 0 offen offset:2048 sc1 lds\n\t"
  "v_mfma_f32_16x16x32_bf16 a[88:91], v[164:167], v[144:147], a[88:91]\n\t"
  "ds_read_b64_tr_b16 v[140:141], v243 offset:0\n\t"
  "ds_read_b64_tr_b16 v[142:143], v243 offset:2048\n\t"
  "v_mfma_f32_16x16x32_bf16 a[92:95], v[164:167], v[148:151], a[92:95]\n\t"
  "buffer_load_dwordx4 v231, %[srdws], 0 offen offset:3072 sc1 lds\n\t"
  "v_add_u32 v231, 0x4000, v231\n\t"
  "s_add_u32 m0, %[m0w], 16384\n\t"
  "ds_read_b64_tr_b16 v[144:145], v242 offset:512\n\t"
  "ds_read_b64_tr_b16 v[146:147], v242 offset:2560\n\t"
  "buffer_load_dwordx4 v232, %[srdp], 0 offen offset:0 lds\n\t"
  "v_add_u32 v232, 0x80, v232\n\t"
  "s_waitcnt lgkmcnt(12)\n\t"
  "ds_read_b64_tr_b16 v[148:149], v243 offset:512\n\t"
  "ds_read_b64_tr_b16 v[150:151], v243 offset:2560\n\t"
  "buffer_load_dwordx4 v233, %[srdp], %[c31744] offen offset:1024 lds\n\t"
  "v_add_u32 v233, 0x80, v233\n\t"
  "v_mfma_f32_16x16x32_bf16 a[64:67], v[176:179], v[168:171], a[64:67]\n\t"
  "ds_read_b128 v[152:155], v237 offset:16384\n\t"
  "v_mfma_f32_16x16x32_bf16 a[68:71], v[176:179], v[172:175], a[68:71]\n\t"
  "v_mfma_f32_16x16x32_bf16 a[72:75], v[180:183], v[168:171], a[72:75]\n\t"
  "ds_read_b128 v[156:159], v237 offset:18432\n\t"
  "v_mfma_f32_16x16x32_bf16 a[76:79], v[180:183], v[172:175], a[76:79]\n\t"
  "s_waitcnt lgkmcnt(11)\n\t"
  "v_mfma_f32_16x16x32_bf16 a[80:83], v[184:187], v[168:171], a[80:83]\n\t"
  "ds_read_b64_tr_b16 v[160:161], v242 offset:8192\n\t"
  "ds_read_b64_tr_b16 v[162:163], v242 offset:10240\n\t"
  "v_mfma_f32_16x16x32_bf16 a[84:87], v[184:187], v[172:175], a[84:87]\n\t"
  "v_mfma_f32_16x16x32_bf16 a[88:91], v[188:191], v[168:171], a[88:91]\n\t"
  "ds_read_b64_tr_b16 v[164:165], v243 offset:8192\n\t"
  "ds_read_b64_tr_b16 v[166:167], v243 offset:10240\n\t"
  "v_mfma_f32_16x16x32_bf16 a[92:95], v[188:191], v[172:175], a[92:95]\n\t"
  "ds_read_b64_tr_b16 v[168:169], v242 offset:8704\n\t"
  "ds_read_b64_tr_b16 v[170:171], v242 offset:10752\n\t"
  "s_waitcnt lgkmcnt(12)\n\t"
  "ds_read_b64_tr_b16 v[172:173], v243 offset:8704\n\t"
  "ds_read_b64_tr_b16 v[174:175], v243 offset:10752\n\t"
  "v_mfma_f32_16x16x32_bf16 a[64:67], v[136:139], v[128:131], a[64:67]\n\t"
  "s_waitcnt vmcnt(12)\n\t"
  "s_barrier\n\t"
  "ds_read_b128 v[176:179], v236 offset:49152\n\t"
  "v_mfma_f32_16x16x32_bf16 a[68:71], v[136:139], v[132:135], a[68:71]\n\t"
  "s_add_u32 m0, %[m0k], 32768\n\t"
  "s_nop 0\n\t"
  "buffer_load_dwordx4 v231, %[srdws], 0 offen offset:0 sc1 lds\n\t"
  "v_mfma_f32_16x16x32_bf16 a[72:75], v[140:143], v[128:131], a[72:75]\n\t"
  "ds_read_b128 v[180:183], v236 offset:51200\n\t"
  "v_mfma_f32_16x16x32_bf16 a[76:79], v[140:143], v[132:135], a[76:79]\n\t"
  "buffer_load_dwordx4 v231, %[srdws], 0 offen offset:1024 sc1 lds\n\t"
  "s_waitcnt lgkmcnt(11)\n\t"
  "v_mfma_f32_16x16x32_bf16 a[80:83], v[144:147], v[128:131], a[80:83]\n\t"
  "ds_read_b64_tr_b16 v[184:185], v242 offset:32768\n\t"
  "ds_read_b64_tr_b16 v[186:187], v242 offset:34816\n\t"
  "v_mfma_f32_16x16x32_bf16 a[84:87], v[144:147], v[132:135], a[84:87]\n\t"
  "buffer_load_dwordx4 v231, %[srdws], 0 offen offset:2048 sc1 lds\n\t"
  "v_mfma_f32_16x16x32_bf16 a[88:91], v[148:151], v[128:131], a[88:91]\n\t"
  "ds_read_b64_tr_b16 v[188:189], v243 offset:32768\n\t"
  "ds_read_b64_tr_b16 v[190:191], v243 offset:34816\n\t"
  "v_mfma_f32_16x16x32_bf16 a[92:95], v[148:151], v[132:135], a[92:95]\n\t"
  "buffer_load_dwordx4 v231, %[srdws], 0 offen offset:3072 sc1 lds\n\t"
  "v_add_u32 v231, 0x4000, v231\n\t"
  "s_add_u32 m0, %[m0w], 49152\n\t"
  "ds_read_b64_tr_b16 v[128:129], v242 offset:33280\n\t"
  "ds_read_b64_tr_b16 v[130:131], v242 offset:35328\n\t"
  "buffer_load_dwordx4 v232, %[srdp], 0 offen offset:0 lds\n\t"
  "v_add_u32 v232, 0x80, v232\n\t"
  "s_waitcnt lgkmcnt(12)\n\t"
  "ds_read_b64_tr_b16 v[132:133], v243 offset:33280\n\t"
  "ds_read_b64_tr_b16 v[134:135], v243 offset:35328\n\t"
  "buffer_load_dwordx4 v233, %[srdp], %[c31744] offen offset:1024 lds\n\t"
  "v_add_u32 v233, 0x80, v233\n\t"
  "v_mfma_f32_16x16x32_bf16 a[64:67], v[160:163], v[152:155], a[64:67]\n\t"
  "ds_read_b128 v[136:139], v237 offset:49152\n\t"
  "v_mfma_f32_16x16x32_bf16 a[68:71], v[160:163], v[156:159], a[68:71]\n\t"
  "v_mfma_f32_16x16x32_bf16 a[72:75], v[164:167], v[152:155], a[72:75]\n\t"
  "ds_read_b128 v[140:143], v237 offset:51200\n\t"
  "v_mfma_f32_16x16x32_bf16 a[76:79], v[164:167], v[156:159], a[76:79]\n\t"
  "s_waitcnt lgkmcnt(11)\n\t"
  "v_mfma_f32_16x16x32_bf16 a[80:83], v[168:171], v[152:155], a[80:83]\n\t"
  "ds_read_b64_tr_b16 v[144:145], v242 offset:40960\n\t"
  "ds_read_b64_tr_b16 v[146:147], v242 offset:43008\n\t"
  "v_mfma_f32_16x16x32_bf16 a[84:87], v[168:171], v[156:159], a[84:87]\n\t"
  "v_mfma_f32_16x16x32_bf16 a[88:91], v[172:175], v[152:155], a[88:91]\n\t"
  "ds_read_b64_tr_b16 v[148:149], v243 offset:40960\n\t"
  "ds_read_b64_tr_b16 v[150:151], v243 offset:43008\n\t"
  "v_mfma_f32_16x16x32_bf16 a[92:95], v[172:175], v[156:159], a[92:95]\n\t"
  "ds_read_b64_tr_b16 v[152:153], v242 offset:41472\n\t"
  "ds_read_b64_tr_b16 v[154:155], v242 offset:43520\n\t"
  "s_waitcnt lgkmcnt(12)\n\t"
  "ds_read_b64_tr_b16 v[156:157], v243 offset:41472\n\t"
  "ds_read_b64_tr_b16 v[158:159], v243 offset:43520\n\t"
  "v_mfma_f32_16x16x32_bf16 a[64:67], v[184:187], v[176:179], a[64:67]\n\t"
  "s_waitcnt vmcnt(12)\n\t"
  "s_barrier\n\t"
  "ds_read_b128 v[160:163], v238 offset:16384\n\t"
  "v_mfma_f32_16x16x32_bf16 a[68:71], v[184:187], v[180:183], a[68:71]\n\t"
  "s_add_u32 m0, %[m0k], 65536\n\t"
  "s_nop 0\n\t"
  "buffer_load_dwordx4 v231, %[srdws], 0 offen offset:0 sc1 lds\n\t"
  "v_mfma_f32_16x16x32_bf16 a[72:75], v[188:191], v[176:179], a[72:75]\n\t"
  "ds_read_b128 v[164:167], v238 offset:18432\n\t"
  "v_mfma_f32_16x16x32_bf16 a[76:79], v[188:191], v[180:183], a[76:79]\n\t"
  "buffer_load_dwordx4 v231, %[srdws], 0 offen offset:1024 sc1 lds\n\t"
  "s_waitcnt lgkmcnt(11)\n\t"
  "v_mfma_f32_16x16x32_bf16 a[80:83], v[128:131], v[176:179], a[80:83]\n\t"
  "ds_read_b64_tr_b16 v[168:169], v244 offset:0\n\t"
  "ds_read_b64_tr_b16 v[170:171], v244 offset:2048\n\t"
  "v_mfma_f32_16x16x32_bf16 a[84:87], v[128:131], v[180:183], a[84:87]\n\t"
  "buffer_load_dwordx4 v231, %[srdws], 0 offen offset:2048 sc1 lds\n\t"
  "v_mfma_f32_16x16x32_bf16 a[88:91], v[132:135], v[176:179], a[88:91]\n\t"
  "ds_read_b64_tr_b16 v[172:173], v245 offset:0\n\t"
  "ds_read_b64_tr_b16 v[174:175], v245 offset:2048\n\t"
  "v_mfma_f32_16x16x32_bf16 a[92:95], v[132:135], v[180:183], a[92:95]\n\t"
  "buffer_load_dwordx4 v231, %[srdws], 0 offen offset:3072 sc1 lds\n\t"
  "v_add_u32 v231, 0x4000, v231\n\t"
  "s_add_u32 m0, %[m0w], 81920\n\t"
  "ds_read_b64_tr_b16 v[176:177], v244 offset:512\n\t"
  "ds_read_b64_tr_b16 v[178:179], v244 offset:2560\n\t"
  "buffer_load_dwordx4 v232, %[srdp], 0 offen offset:0 lds\n\t"
  "v_add_u32 v232, 0x80, v232\n\t"
  "s_waitcnt lgkmcnt(12)\n\t"
  "ds_read_b64_tr_b16 v[180:181], v245 offset:512\n\t"
  "ds_read_b64_tr_b16 v[182:183], v245 offset:2560\n\t"
  "buffer_load_dwordx4 v233, %[srdp], %[c31744] offen offset:1024 lds\n\t"
  "v_add_u32 v233, 0x80, v233\n\t"
  "v_mfma_f32_16x16x32_bf16 a[64:67], v[144:147], v[136:139], a[64:67]\n\t"
  "ds_read_b128 v[184:187], v239 offset:16384\n\t"
  "v_mfma_f32_16x16x32_bf16 a[68:71], v[144:147], v[140:143], a[68:71]\n\t"
  "v_mfma_f32_16x16x32_bf16 a[72:75], v[148:151], v[136:139], a[72:75]\n\t"
  "ds_read_b128 v[188:191], v239 offset:18432\n\t"
  "v_mfma_f32_16x16x32_bf16 a[76:79], v[148:151], v[140:143], a[76:79]\n\t"
  "s_waitcnt lgkmcnt(11)\n\t"
  "v_mfma_f32_16x16x32_bf16 a[80:83], v[152:155], v[136:139], a[80:83]\n\t"
  "ds_read_b64_tr_b16 v[128:129], v244 offset:8192\n\t"
  "ds_read_b64_tr_b16 v[130:131], v244 offset:10240\n\t"
  "v_mfma_f32_16x16x32_bf16 a[84:87], v[152:155], v[140:143], a[84:87]\n\t"
  "v_mfma_f32_16x16x32_bf16 a[88:91], v[156:159], v[136:139], a[88:91]\n\t"
  "ds_read_b64_tr_b16 v[132:133], v245 offset:8192\n\t"
  "ds_read_b64_tr_b16 v[134:135], v245 offset:10240\n\t"
  "v_mfma_f32_16x16x32_bf16 a[92:95], v[156:159], v[140:143], a[92:95]\n\t"
  "ds_read_b64_tr_b16 v[136:137], v244 offset:8704\n\t"
  "ds_read_b64_tr_b16 v[138:139], v244 offset:10752\n\t"
  "s_waitcnt lgkmcnt(12)\n\t"
  "ds_read_b64_tr_b16 v[140:141], v245 offset:8704\n\t"
  "ds_read_b64_tr_b16 v[142:143], v245 offset:10752\n\t"
  "v_mfma_f32_16x16x32_bf16 a[64:67], v[168:171], v[160:163], a[64:67]\n\t"
  "s_waitcnt vmcnt(12)\n\t"
  "s_barrier\n\t"
  "ds_read_b128 v[144:147], v234 offset:16384\n\t"
  "v_mfma_f32_16x16x32_bf16 a[68:71], v[168:171], v[164:167], a[68:71]\n\t"
  "s_add_u32 m0, %[m0k], 98304\n\t"
  "s_nop 0\n\t"
  "buffer_load_dwordx4 v231, %[srdws], 0 offen offset:0 sc1 lds\n\t"
  "v_mfma_f32_16x16x32_bf16 a[72:75], v[172:175], v[160:163], a[72:75]\n\t"
  "ds_read_b128 v[148:151], v234 offset:18432\n\t"
  "v_mfma_f32_16x16x32_bf16 a[76:79], v[172:175], v[164:167], a[76:79]\n\t"
  "buffer_load_dwordx4 v231, %[srdws], 0 offen offset:1024 sc1 lds\n\t"
  "s_waitcnt lgkmcnt(11)\n\t"
  "v_mfma_f32_16x16x32_bf16 a[80:83], v[176:179], v[160:163], a[80:83]\n\t"
  "ds_read_b64_tr_b16 v[152:153], v240 offset:0\n\t"
  "ds_read_b64_tr_b16 v[154:155], v240 offset:2048\n\t"
  "v_mfma_f32_16x16x32_bf16 a[84:87], v[176:179], v[164:167], a[84:87]\n\t"
  "buffer_load_dwordx4 v231, %[srdws], 0 offen offset:2048 sc1 lds\n\t"
  "v_mfma_f32_16x16x32_bf16 a[88:91], v[180:183], v[160:163], a[88:91]\n\t"
  "ds_read_b64_tr_b16 v[156:157], v241 offset:0\n\t"
  "ds_read_b64_tr_b16 v[158:159], v241 offset:2048\n\t"
  "v_mfma_f32_16x16x32_bf16 a[92:95], v[180:183], v[164:167], a[92:95]\n\t"
  "buffer_load_dwordx4 v231, %[srdws], 0 offen offset:3072 sc1 lds\n\t"
  "v_add_u32 v231, 0x4000, v231\n\t"
  "s_add_u32 m0, %[m0w], 114688\n\t"
  "ds_read_b64_tr_b16 v[160:161], v240 offset:512\n\t"
  "ds_read_b64_tr_b16 v[162:163], v240 offset:2560\n\t"
  "buffer_load_dwordx4 v232, %[srdp], 0 offen offset:0 lds\n\t"
  "v_add_u32 v232, 0x80, v232\n\t"
  "s_waitcnt lgkmcnt(12)\n\t"
  "ds_read_b64_tr_b16 v[164:165], v241 offset:512\n\t"
  "ds_read_b64_tr_b16 v[166:167], v241 offset:2560\n\t"
  "buffer_load_dwordx4 v233, %[srdp], %[c31744] offen offset:1024 lds\n\t"
  "v_add_u32 v233, 0x80, v233\n\t"
  "v_mfma_f32_16x16x32_bf16 a[64:67], v[128:131], v[184:187], a[64:67]\n\t"
  "ds_read_b128 v[168:171], v235 offset:16384\n\t"
  "v_mfma_f32_16x16x32_bf16 a[68:71], v[128:131], v[188:191], a[68:71]\n\t"
  "v_mfma_f32_16x16x32_bf16 a[72:75], v[132:135], v[184:187], a[72:75]\n\t"
  "ds_read_b128 v[172:175], v235 offset:18432\n\t"
  "v_mfma_f32_16x16x32_bf16 a[76:79], v[132:135], v[188:191], a[76:79]\n\t"
  "s_waitcnt lgkmcnt(11)\n\t"
  "v_mfma_f32_16x16x32_bf16 a[80:83], v[136:139], v[184:187], a[80:83]\n\t"
  "ds_read_b64_tr_b16 v[176:177], v240 offset:8192\n\t"
  "ds_read_b64_tr_b16 v[178:179], v240 offset:10240\n\t"
  "v_mfma_f32_16x16x32_bf16 a[84:87], v[136:139], v[188:191], a[84:87]\n\t"
  "v_mfma_f32_16x16x32_bf16 a[88:91], v[140:143], v[184:187], a[88:91]\n\t"
  "ds_read_b64_tr_b16 v[180:181], v241 offset:8192\n\t"
  "ds_read_b64_tr_b16 v[182:183], v241 offset:10240\n\t"
  "v_mfma_f32_16x16x32_bf16 a[92:95], v[140:143], v[188:191], a[92:95]\n\t"
  "ds_read_b64_tr_b16 v[184:185], v240 offset:8704\n\t"
  "ds_read_b64_tr_b16 v[186:187], v240 offset:10752\n\t"
  "s_waitcnt lgkmcnt(12)\n\t"
  "ds_read_b64_tr_b16 v[188:189], v241 offset:8704\n\t"
  "ds_read_b64_tr_b16 v[190:191], v241 offset:10752\n\t"
  "v_mfma_f32_16x16x32_bf16 a[64:67], v[152:155], v[144:147], a[64:67]\n\t"
  "s_waitcnt vmcnt(12)\n\t"
  "s_barrier\n\t"
  "ds_read_b128 v[128:131], v234 offset:49152\n\t"
  "v_mfma_f32_16x16x32_bf16 a[68:71], v[152:155], v[148:151], a[68:71]\n\t"
  "s_add_u32 m0, %[m0k], 131072\n\t"
  "s_nop 0\n\t"
  "buffer_load_dwordx4 v231, %[srdws], 0 offen offset:0 sc1 lds\n\t"
  "v_mfma_f32_16x16x32_bf16 a[72:75], v[156:159], v[144:147], a[72:75]\n\t"
  "ds_read_b128 v[132:135], v234 offset:51200\n\t"
  "v_mfma_f32_16x16x32_bf16 a[76:79], v[156:159], v[148:151], a[76:79]\n\t"
  "buffer_load_dwordx4 v231, %[srdws], 0 offen offset:1024 sc1 lds\n\t"
  "s_waitcnt lgkmcnt(11)\n\t"
  "v_mfma_f32_16x16x32_bf16 a[80:83], v[160:163], v[144:147], a[80:83]\n\t"
  "ds_read_b64_tr_b16 v[136:137], v240 offset:32768\n\t"
  "ds_read_b64_tr_b16 v[138:139], v240 offset:34816\n\t"
  "v_mfma_f32_16x16x32_bf16 a[84:87], v[160:163], v[148:151], a[84:87]\n\t"
  "buffer_load_dwordx4 v231, %[srdws], 0 offen offset:2048 sc1 lds\n\t"
  "v_mfma_f32_16x16x32_bf16 a[88:91], v[164:167], v[144:147], a[88:91]\n\t"
  "ds_read_b64_tr_b16 v[140:141], v241 offset:32768\n\t"
  "ds_read_b64_tr_b16 v[142:143], v241 offset:34816\n\t"
  "v_mfma_f32_16x16x32_bf16 a[92:95], v[164:167], v[148:151], a[92:95]\n\t"
  "buffer_load_dwordx4 v231, %[srdws], 0 offen offset:3072 sc1 lds\n\t"
  "v_add_u32 v231, 0x4000, v231\n\t"
  "s_add_u32 m0, %[m0w], 147456\n\t"
  "ds_read_b64_tr_b16 v[144:145], v240 offset:33280\n\t"
  "ds_read_b64_tr_b16 v[146:147], v240 offset:35328\n\t"
  "buffer_load_dwordx4 v232, %[srdp], 0 offen offset:0 lds\n\t"
  "v_add_u32 v232, 0x80, v232\n\t"
  "s_waitcnt lgkmcnt(12)\n\t"
  "ds_read_b64_tr_b16 v[148:149], v241 offset:33280\n\t"
  "ds_read_b64_tr_b16 v[150:151], v241 offset:35328\n\t"
  "buffer_load_dwordx4 v233, %[srdp], %[c31744] offen offset:1024 lds\n\t"
  "v_add_u32 v233, 0x80, v233\n\t"
  "v_mfma_f32_16x16x32_bf16 a[64:67], v[176:179], v[168:171], a[64:67]\n\t"
  "ds_read_b128 v[152:155], v235 offset:49152\n\t"
  "v_mfma_f32_16x16x32_bf16 a[68:71], v[176:179], v[172:175], a[68:71]\n\t"
  "v_mfma_f32_16x16x32_bf16 a[72:75], v[180:183], v[168:171], a[72:75]\n\t"
  "ds_read_b128 v[156:159], v235 offset:51200\n\t"
  "v_mfma_f32_16x16x32_bf16 a[76:79], v[180:183], v[172:175], a[76:79]\n\t"
  "s_waitcnt lgkmcnt(11)\n\t"
  "v_mfma_f32_16x16x32_bf16 a[80:83], v[184:187], v[168:171], a[80:83]\n\t"
  "ds_read_b64_tr_b16 v[160:161], v240 offset:40960\n\t"
  "ds_read_b64_tr_b16 v[162:163], v240 offset:43008\n\t"
  "v_mfma_f32_16x16x32_bf16 a[84:87], v[184:187], v[172:175], a[84:87]\n\t"
  "v_mfma_f32_16x16x32_bf16 a[88:91], v[188:191], v[168:171], a[88:91]\n\t"
  "ds_read_b64_tr_b16 v[164:165], v241 offset:40960\n\t"
  "ds_read_b64_tr_b16 v[166:167], v241 offset:43008\n\t"
  "v_mfma_f32_16x16x32_bf16 a[92:95], v[188:191], v[172:175], a[92:95]\n\t"
  "ds_read_b64_tr_b16 v[168:169], v240 offset:41472\n\t"
  "ds_read_b64_tr_b16 v[170:171], v240 offset:43520\n\t"
  "s_waitcnt lgkmcnt(12)\n\t"
  "ds_read_b64_tr_b16 v[172:173], v241 offset:41472\n\t"
  "ds_read_b64_tr_b16 v[174:175], v241 offset:43520\n\t"
  "v_mfma_f32_16x16x32_bf16 a[64:67], v[136:139], v[128:131], a[64:67]\n\t"
  "s_waitcnt vmcnt(12)\n\t"
  "s_barrier\n\t"
  "ds_read_b128 v[176:179], v236 offset:16384\n\t"
  "v_mfma_f32_16x16x32_bf16 a[68:71], v[136:139], v[132:135], a[68:71]\n\t"
  "s_add_u32 m0, %[m0k], 0\n\t"
  "s_nop 0\n\t"
  "buffer_load_dwordx4 v231, %[srdws], 0 offen offset:0 sc1 lds\n\t"
  "v_mfma_f32_16x16x32_bf16 a[72:75], v[140:143], v[128:131], a[72:75]\n\t"
  "ds_read_b128 v[180:183], v236 offset:18432\n\t"
  "v_mfma_f32_16x16x32_bf16 a[76:79], v[140:143], v[132:135], a[76:79]\n\t"
  "buffer_load_dwordx4 v231, %[srdws], 0 offen offset:1024 sc1 lds\n\t"
  "s_waitcnt lgkmcnt(11)\n\t"
  "v_mfma_f32_16x16x32_bf16 a[80:83], v[144:147], v[128:131], a[80:83]\n\t"
  "ds_read_b64_tr_b16 v[184:185], v242 offset:0\n\t"
  "ds_read_b64_tr_b16 v[186:187], v242 offset:2048\n\t"
  "v_mfma_f32_16x16x32_bf16 a[84:87], v[144:147], v[132:135], a[84:87]\n\t"
  "buffer_load_dwordx4 v231, %[srdws], 0 offen offset:2048 sc1 lds\n\t"
  "v_mfma_f32_16x16x32_bf16 a[88:91], v[148:151], v[128:131], a[88:91]\n\t"
  "ds_read_b64_tr_b16 v[188:189], v243 offset:0\n\t"
  "ds_read_b64_tr_b16 v[190:191], v243 offset:2048\n\t"
  "v_mfma_f32_16x16x32_bf16 a[92:95], v[148:151], v[132:135], a[92:95]\n\t"
  "buffer_load_dwordx4 v231, %[srdws], 0 offen offset:3072 sc1 lds\n\t"
  "v_add_u32 v231, 0x4000, v231\n\t"
  "s_add_u32 m0, %[m0w], 16384\n\t"
  "ds_read_b64_tr_b16 v[128:129], v242 offset:512\n\t"
  "ds_read_b64_tr_b16 v[130:131], v242 offset:2560\n\t"
  "buffer_load_dwordx4 v232, %[srdp], 0 offen offset:0 lds\n\t"
  "v_add_u32 v232, 0x80, v232\n\t"
  "s_waitcnt lgkmcnt(12)\n\t"
  "ds_read_b64_tr_b16 v[132:133], v243 offset:512\n\t"
  "ds_read_b64_tr_b16 v[134:135], v243 offset:2560\n\t"
  "buffer_load_dwordx4 v233, %[srdp], %[c31744] offen offset:1024 lds\n\t"
  "v_add_u32 v233, 0x80, v233\n\t"
  "v_mfma_f32_16x16x32_bf16 a[64:67], v[160:163], v[152:155], a[64:67]\n\t"
  "ds_read_b128 v[136:139], v237 offset:16384\n\t"
  "v_mfma_f32_16x16x32_bf16 a[68:71], v[160:163], v[156:159], a[68:71]\n\t"
  "v_mfma_f32_16x16x32_bf16 a[72:75], v[164:167], v[152:155], a[72:75]\n\t"
  "ds_read_b128 v[140:143], v237 offset:18432\n\t"
  "v_mfma_f32_16x16x32_bf16 a[76:79], v[164:167], v[156:159], a[76:79]\n\t"
  "s_waitcnt lgkmcnt(11)\n\t"
  "v_mfma_f32_16x16x32_bf16 a[80:83], v[168:171], v[152:155], a[80:83]\n\t"
  "ds_read_b64_tr_b16 v[144:145], v242 offset:8192\n\t"
  "ds_read_b64_tr_b16 v[146:147], v242 offset:10240\n\t"
  "v_mfma_f32_16x16x32_bf16 a[84:87], v[168:171], v[156:159], a[84:87]\n\t"
  "v_mfma_f32_16x16x32_bf16 a[88:91], v[172:175], v[152:155], a[88:91]\n\t"
  "ds_read_b64_tr_b16 v[148:149], v243 offset:8192\n\t"
  "ds_read_b64_tr_b16 v[150:151], v243 offset:10240\n\t"
  "v_mfma_f32_16x16x32_bf16 a[92:95], v[172:175], v[156:159], a[92:95]\n\t"
  "ds_read_b64_tr_b16 v[152:153], v242 offset:8704\n\t"
  "ds_read_b64_tr_b16 v[154:155], v242 offset:10752\n\t"
  "s_waitcnt lgkmcnt(12)\n\t"
  "ds_read_b64_tr_b16 v[156:157], v243 offset:8704\n\t"
  "ds_read_b64_tr_b16 v[158:159], v243 offset:10752\n\t"
  "v_mfma_f32_16x16x32_bf16 a[64:67], v[184:187], v[176:179], a[64:67]\n\t"
  "s_waitcnt vmcnt(12)\n\t"
  "s_barrier\n\t"
  "ds_read_b128 v[160:163], v236 offset:49152\n\t"
  "v_mfma_f32_16x16x32_bf16 a[68:71], v[184:187], v[180:183], a[68:71]\n\t"
  "s_add_u32 m0, %[m0k], 32768\n\t"
  "s_nop 0\n\t"
  "buffer_load_dwordx4 v231, %[srdws], 0 offen offset:0 sc1 lds\n\t"
  "v_mfma_f32_16x16x32_bf16 a[72:75], v[188:191], v[176:179], a[72:75]\n\t"
  "ds_read_b128 v[164:167], v236 offset:51200\n\t"
  "v_mfma_f32_16x16x32_bf16 a[76:79], v[188:191], v[180:183], a[76:79]\n\t"
  "buffer_load_dwordx4 v231, %[srdws], 0 offen offset:1024 sc1 lds\n\t"
  "s_waitcnt lgkmcnt(11)\n\t"
  "v_mfma_f32_16x16x32_bf16 a[80:83], v[128:131], v[176:179], a[80:83]\n\t"
  "ds_read_b64_tr_b16 v[168:169], v242 offset:32768\n\t"
  "ds_read_b64_tr_b16 v[170:171], v242 offset:34816\n\t"
  "v_mfma_f32_16x16x32_bf16 a[84:87], v[128:131], v[180:183], a[84:87]\n\t"
  "buffer_load_dwordx4 v231, %[srdws], 0 offen offset:2048 sc1 lds\n\t"
  "v_mfma_f32_16x16x32_bf16 a[88:91], v[132:135], v[176:179], a[88:91]\n\t"
  "ds_read_b64_tr_b16 v[172:173], v243 offset:32768\n\t"
  "ds_read_b64_tr_b16 v[174:175], v243 offset:34816\n\t"
  "v_mfma_f32_16x16x32_bf16 a[92:95], v[132:135], v[180:183], a[92:95]\n\t"
  "buffer_load_dwordx4 v231, %[srdws], 0 offen offset:3072 sc1 lds\n\t"
  "v_add_u32 v231, 0x4000, v231\n\t"
  "s_add_u32 m0, %[m0w], 49152\n\t"
  "ds_read_b64_tr_b16 v[176:177], v242 offset:33280\n\t"
  "ds_read_b64_tr_b16 v[178:179], v242 offset:35328\n\t"
  "buffer_load_dwordx4 v232, %[srdp], 0 offen offset:0 lds\n\t"
  "v_add_u32 v232, 0x80, v232\n\t"
  "s_waitcnt lgkmcnt(12)\n\t"
  "ds_read_b64_tr_b16 v[180:181], v243 offset:33280\n\t"
  "ds_read_b64_tr_b16 v[182:183], v243 offset:35328\n\t"
  "buffer_load_dwordx4 v233, %[srdp], %[c31744] offen offset:1024 lds\n\t"
  "v_add_u32 v233, 0x80, v233\n\t"
  "v_mfma_f32_16x16x32_bf16 a[64:67], v[144:147], v[136:139], a[64:67]\n\t"
  "ds_read_b128 v[184:187], v237 offset:49152\n\t"
  "v_mfma_f32_16x16x32_bf16 a[68:71], v[144:147], v[140:143], a[68:71]\n\t"
  "v_mfma_f32_16x16x32_bf16 a[72:75], v[148:151], v[136:139], a[72:75]\n\t"
  "ds_read_b128 v[188:191], v237 offset:51200\n\t"
  "v_mfma_f32_16x16x32_bf16 a[76:79], v[148:151], v[140:143], a[76:79]\n\t"
  "s_waitcnt lgkmcnt(11)\n\t"
  "v_mfma_f32_16x16x32_bf16 a[80:83], v[152:155], v[136:139], a[80:83]\n\t"
  "ds_read_b64_tr_b16 v[128:129], v242 offset:40960\n\t"
  "ds_read_b64_tr_b16 v[130:131], v242 offset:43008\n\t"
  "v_mfma_f32_16x16x32_bf16 a[84:87], v[152:155], v[140:143], a[84:87]\n\t"
  "v_mfma_f32_16x16x32_bf16 a[88:91], v[156:159], v[136:139], a[88:91]\n\t"
  "ds_read_b64_tr_b16 v[132:133], v243 offset:40960\n\t"
  "ds_read_b64_tr_b16 v[134:135], v243 offset:43008\n\t"
  "v_mfma_f32_16x16x32_bf16 a[92:95], v[156:159], v[140:143], a[92:95]\n\t"
  "ds_read_b64_tr_b16 v[136:137], v242 offset:41472\n\t"
  "ds_read_b64_tr_b16 v[138:139], v242 offset:43520\n\t"
  "s_waitcnt lgkmcnt(12)\n\t"
  "ds_read_b64_tr_b16 v[140:141], v243 offset:41472\n\t"
  "ds_read_b64_tr_b16 v[142:143], v243 offset:43520\n\t"
  "v_mfma_f32_16x16x32_bf16 a[64:67], v[168:171], v[160:163], a[64:67]\n\t"
  "s_waitcnt vmcnt(12)\n\t"
  "s_barrier\n\t"
  "ds_read_b128 v[144:147], v238 offset:16384\n\t"
  "v_mfma_f32_16x16x32_bf16 a[68:71], v[168:171], v[164:167], a[68:71]\n\t"
  "s_add_u32 m0, %[m0k], 65536\n\t"
  "s_nop 0\n\t"
  "buffer_load_dwordx4 v231, %[srdws], 0 offen offset:0 sc1 lds\n\t"
  "v_mfma_f32_16x16x32_bf16 a[72:75], v[172:175], v[160:163], a[72:75]\n\t"
  "ds_read_b128 v[148:151], v238 offset:18432\n\t"
  "v_mfma_f32_16x16x32_bf16 a[76:79], v[172:175], v[164:167], a[76:79]\n\t"
  "buffer_load_dwordx4 v231, %[srdws], 0 offen offset:1024 sc1 lds\n\t"
  "s_waitcnt lgkmcnt(11)\n\t"
  "v_mfma_f32_16x16x32_bf16 a[80:83], v[176:179], v[160:163], a[80:83]\n\t"
  "ds_read_b64_tr_b16 v[152:153], v244 offset:0\n\t"
  "ds_read_b64_tr_b16 v[154:155], v244 offset:2048\n\t"
  "v_mfma_f32_16x16x32_bf16 a[84:87], v[176:179], v[164:167], a[84:87]\n\t"
  "buffer_load_dwordx4 v231, %[srdws], 0 offen offset:2048 sc1 lds\n\t"
  "v_mfma_f32_16x16x32_bf16 a[88:91], v[180:183], v[160:163], a[88:91]\n\t"
  "ds_read_b64_tr_b16 v[156:157], v245 offset:0\n\t"
  "ds_read_b64_tr_b16 v[158:159], v245 offset:2048\n\t"
  "v_mfma_f32_16x16x32_bf16 a[92:95], v[180:183], v[164:167], a[92:95]\n\t"
  "buffer_load_dwordx4 v231, %[srdws], 0 offen offset:3072 sc1 lds\n\t"
  "v_add_u32 v231, 0x4000, v231\n\t"
  "s_add_u32 m0, %[m0w], 81920\n\t"
  "ds_read_b64_tr_b16 v[160:161], v244 offset:512\n\t"
  "ds_read_b64_tr_b16 v[162:163], v244 offset:2560\n\t"
  "buffer_load_dwordx4 v232, %[srdp], 0 offen offset:0 lds\n\t"
  "v_add_u32 v232, 0x80, v232\n\t"
  "s_waitcnt lgkmcnt(12)\n\t"
  "ds_read_b64_tr_b16 v[164:165], v245 offset:512\n\t"
  "ds_read_b64_tr_b16 v[166:167], v245 offset:2560\n\t"
  "buffer_load_dwordx4 v233, %[srdp], %[c31744] offen offset:1024 lds\n\t"
  "v_add_u32 v233, 0x80, v233\n\t"
  "v_mfma_f32_16x16x32_bf16 a[64:67], v[128:131], v[184:187], a[64:67]\n\t"
  "ds_read_b128 v[168:171], v239 offset:16384\n\t"
  "v_mfma_f32_16x16x32_bf16 a[68:71], v[128:131], v[188:191], a[68:71]\n\t"
  "v_mfma_f32_16x16x32_bf16 a[72:75], v[132:135], v[184:187], a[72:75]\n\t"
  "ds_read_b128 v[172:175], v239 offset:18432\n\t"
  "v_mfma_f32_16x16x32_bf16 a[76:79], v[132:135], v[188:191], a[76:79]\n\t"
  "s_waitcnt lgkmcnt(11)\n\t"
  "v_mfma_f32_16x16x32_bf16 a[80:83], v[136:139], v[184:187], a[80:83]\n\t"
  "ds_read_b64_tr_b16 v[176:177], v244 offset:8192\n\t"
  "ds_read_b64_tr_b16 v[178:179], v244 offset:10240\n\t"
  "v_mfma_f32_16x16x32_bf16 a[84:87], v[136:139], v[188:191], a[84:87]\n\t"
  "v_mfma_f32_16x16x32_bf16 a[88:91], v[140:143], v[184:187], a[88:91]\n\t"
  "ds_read_b64_tr_b16 v[180:181], v245 offset:8192\n\t"
  "ds_read_b64_tr_b16 v[182:183], v245 offset:10240\n\t"
  "v_mfma_f32_16x16x32_bf16 a[92:95], v[140:143], v[188:191], a[92:95]\n\t"
  "ds_read_b64_tr_b16 v[184:185], v244 offset:8704\n\t"
  "ds_read_b64_tr_b16 v[186:187], v244 offset:10752\n\t"
  "s_waitcnt lgkmcnt(12)\n\t"
  "ds_read_b64_tr_b16 v[188:189], v245 offset:8704\n\t"
  "ds_read_b64_tr_b16 v[190:191], v245 offset:10752\n\t"
  "v_mfma_f32_16x16x32_bf16 a[64:67], v[152:155], v[144:147], a[64:67]\n\t"
  "s_waitcnt vmcnt(12)\n\t"
  "s_barrier\n\t"
  "ds_read_b128 v[128:131], v234 offset:16384\n\t"
  "v_mfma_f32_16x16x32_bf16 a[68:71], v[152:155], v[148:151], a[68:71]\n\t"
  "s_add_u32 m0, %[m0k], 98304\n\t"
  "s_nop 0\n\t"
  "buffer_load_dwordx4 v231, %[srdws], 0 offen offset:0 sc1 lds\n\t"
  "v_mfma_f32_16x16x32_bf16 a[72:75], v[156:159], v[144:147], a[72:75]\n\t"
  "ds_read_b128 v[132:135], v234 offset:18432\n\t"
  "v_mfma_f32_16x16x32_bf16 a[76:79], v[156:159], v[148:151], a[76:79]\n\t"
  "buffer_load_dwordx4 v231, %[srdws], 0 offen offset:1024 sc1 lds\n\t"
  "s_waitcnt lgkmcnt(11)\n\t"
  "v_mfma_f32_16x16x32_bf16 a[80:83], v[160:163], v[144:147], a[80:83]\n\t"
  "ds_read_b64_tr_b16 v[136:137], v240 offset:0\n\t"
  "ds_read_b64_tr_b16 v[138:139], v240 offset:2048\n\t"
  "v_mfma_f32_16x16x32_bf16 a[84:87], v[160:163], v[148:151], a[84:87]\n\t"
  "buffer_load_dwordx4 v231, %[srdws], 0 offen offset:2048 sc1 lds\n\t"
  "v_mfma_f32_16x16x32_bf16 a[88:91], v[164:167], v[144:147], a[88:91]\n\t"
  "ds_read_b64_tr_b16 v[140:141], v241 offset:0\n\t"
  "ds_read_b64_tr_b16 v[142:143], v241 offset:2048\n\t"
  "v_mfma_f32_16x16x32_bf16 a[92:95], v[164:167], v[148:151], a[92:95]\n\t"
  "buffer_load_dwordx4 v231, %[srdws], 0 offen offset:3072 sc1 lds\n\t"
  "v_add_u32 v231, 0x4000, v231\n\t"
  "s_add_u32 m0, %[m0w], 114688\n\t"
  "ds_read_b64_tr_b16 v[144:145], v240 offset:512\n\t"
  "ds_read_b64_tr_b16 v[146:147], v240 offset:2560\n\t"
  "buffer_load_dwordx4 v232, %[srdp], 0 offen offset:0 lds\n\t"
  "v_add_u32 v232, 0x80, v232\n\t"
  "s_waitcnt lgkmcnt(12)\n\t"
  "ds_read_b64_tr_b16 v[148:149], v241 offset:512\n\t"
  "ds_read_b64_tr_b16 v[150:151], v241 offset:2560\n\t"
  "buffer_load_dwordx4 v233, %[srdp], %[c31744] offen offset:1024 lds\n\t"
  "v_add_u32 v233, 0x80, v233\n\t"
  "v_mfma_f32_16x16x32_bf16 a[64:67], v[176:179], v[168:171], a[64:67]\n\t"
  "ds_read_b128 v[152:155], v235 offset:16384\n\t"
  "v_mfma_f32_16x16x32_bf16 a[68:71], v[176:179], v[172:175], a[68:71]\n\t"
  "v_mfma_f32_16x16x32_bf16 a[72:75], v[180:183], v[168:171], a[72:75]\n\t"
  "ds_read_b128 v[156:159], v235 offset:18432\n\t"
  "v_mfma_f32_16x16x32_bf16 a[76:79], v[180:183], v[172:175], a[76:79]\n\t"
  "s_waitcnt lgkmcnt(11)\n\t"
  "v_mfma_f32_16x16x32_bf16 a[80:83], v[184:187], v[168:171], a[80:83]\n\t"
  "ds_read_b64_tr_b16 v[160:161], v240 offset:8192\n\t"
  "ds_read_b64_tr_b16 v[162:163], v240 offset:10240\n\t"
  "v_mfma_f32_16x16x32_bf16 a[84:87], v[184:187], v[172:175], a[84:87]\n\t"
  "v_mfma_f32_16x16x32_bf16 a[88:91], v[188:191], v[168:171], a[88:91]\n\t"
  "ds_read_b64_tr_b16 v[164:165], v241 offset:8192\n\t"
  "ds_read_b64_tr_b16 v[166:167], v241 offset:10240\n\t"
  "v_mfma_f32_16x16x32_bf16 a[92:95], v[188:191], v[172:175], a[92:95]\n\t"
  "ds_read_b64_tr_b16 v[168:169], v240 offset:8704\n\t"
  "ds_read_b64_tr_b16 v[170:171], v240 offset:10752\n\t"
  "s_waitcnt lgkmcnt(12)\n\t"
  "ds_read_b64_tr_b16 v[172:173], v241 offset:8704\n\t"
  "ds_read_b64_tr_b16 v[174:175], v241 offset:10752\n\t"
  "v_mfma_f32_16x16x32_bf16 a[64:67], v[136:139], v[128:131], a[64:67]\n\t"
  "s_waitcnt vmcnt(12)\n\t"
  "s_barrier\n\t"
  "ds_read_b128 v[176:179], v234 offset:49152\n\t"
  "v_mfma_f32_16x16x32_bf16 a[68:71], v[136:139], v[132:135], a[68:71]\n\t"
  "s_add_u32 m0, %[m0k], 131072\n\t"
  "s_nop 0\n\t"
  "buffer_load_dwordx4 v231, %[srdws], 0 offen offset:0 sc1 lds\n\t"
  "v_mfma_f32_16x16x32_bf16 a[72:75], v[140:143], v[128:131], a[72:75]\n\t"
  "ds_read_b128 v[180:183], v234 offset:51200\n\t"
  "v_mfma_f32_16x16x32_bf16 a[76:79], v[140:143], v[132:135], a[76:79]\n\t"
  "buffer_load_dwordx4 v231, %[srdws], 0 offen offset:1024 sc1 lds\n\t"
  "s_waitcnt lgkmcnt(11)\n\t"
  "v_mfma_f32_16x16x32_bf16 a[80:83], v[144:147], v[128:131], a[80:83]\n\t"
  "ds_read_b64_tr_b16 v[184:185], v240 offset:32768\n\t"
  "ds_read_b64_tr_b16 v[186:187], v240 offset:34816\n\t"
  "v_mfma_f32_16x16x32_bf16 a[84:87], v[144:147], v[132:135], a[84:87]\n\t"
  "buffer_load_dwordx4 v231, %[srdws], 0 offen offset:2048 sc1 lds\n\t"
  "v_mfma_f32_16x16x32_bf16 a[88:91], v[148:151], v[128:131], a[88:91]\n\t"
  "ds_read_b64_tr_b16 v[188:189], v241 offset:32768\n\t"
  "ds_read_b64_tr_b16 v[190:191], v241 offset:34816\n\t"
  "v_mfma_f32_16x16x32_bf16 a[92:95], v[148:151], v[132:135], a[92:95]\n\t"
  "buffer_load_dwordx4 v231, %[srdws], 0 offen offset:3072 sc1 lds\n\t"
  "v_add_u32 v231, 0x4000, v231\n\t"
  "s_add_u32 m0, %[m0w], 147456\n\t"
  "ds_read_b64_tr_b16 v[128:129], v240 offset:33280\n\t"
  "ds_read_b64_tr_b16 v[130:131], v240 offset:35328\n\t"
  "buffer_load_dwordx4 v232, %[srdp], 0 offen offset:0 lds\n\t"
  "v_add_u32 v232, 0x80, v232\n\t"
  "s_waitcnt lgkmcnt(12)\n\t"
  "ds_read_b64_tr_b16 v[132:133], v241 offset:33280\n\t"
  "ds_read_b64_tr_b16 v[134:135], v241 offset:35328\n\t"
  "buffer_load_dwordx4 v233, %[srdp], %[c31744] offen offset:1024 lds\n\t"
  "v_add_u32 v233, 0x80, v233\n\t"
  "v_mfma_f32_16x16x32_bf16 a[64:67], v[160:163], v[152:155], a[64:67]\n\t"
  "ds_read_b128 v[136:139], v235 offset:49152\n\t"
  "v_mfma_f32_16x16x32_bf16 a[68:71], v[160:163], v[156:159], a[68:71]\n\t"
  "v_mfma_f32_16x16x32_bf16 a[72:75], v[164:167], v[152:155], a[72:75]\n\t"
  "ds_read_b128 v[140:143], v235 offset:51200\n\t"
  "v_mfma_f32_16x16x32_bf16 a[76:79], v[164:167], v[156:159], a[76:79]\n\t"
  "s_waitcnt lgkmcnt(11)\n\t"
  "v_mfma_f32_16x16x32_bf16 a[80:83], v[168:171], v[152:155], a[80:83]\n\t"
  "ds_read_b64_tr_b16 v[144:145], v240 offset:40960\n\t"
  "ds_read_b64_tr_b16 v[146:147], v240 offset:43008\n\t"
  "v_mfma_f32_16x16x32_bf16 a[84:87], v[168:171], v[156:159], a[84:87]\n\t"
  "v_mfma_f32_16x16x32_bf16 a[88:91], v[172:175], v[152:155], a[88:91]\n\t"
  "ds_read_b64_tr_b16 v[148:149], v241 offset:40960\n\t"
  "ds_read_b64_tr_b16 v[150:151], v241 offset:43008\n\t"
  "v_mfma_f32_16x16x32_bf16 a[92:95], v[172:175], v[156:159], a[92:95]\n\t"
  "ds_read_b64_tr_b16 v[152:153], v240 offset:41472\n\t"
  "ds_read_b64_tr_b16 v[154:155], v240 offset:43520\n\t"
  "s_waitcnt lgkmcnt(12)\n\t"
  "ds_read_b64_tr_b16 v[156:157], v241 offset:41472\n\t"
  "ds_read_b64_tr_b16 v[158:159], v241 offset:43520\n\t"
  "v_mfma_f32_16x16x32_bf16 a[64:67], v[184:187], v[176:179], a[64:67]\n\t"
  "s_waitcnt vmcnt(12)\n\t"
  "s_barrier\n\t"
  "ds_read_b128 v[160:163], v236 offset:16384\n\t"
  "v_mfma_f32_16x16x32_bf16 a[68:71], v[184:187], v[180:183], a[68:71]\n\t"
  "s_add_u32 m0, %[m0k], 0\n\t"
  "s_nop 0\n\t"
  "buffer_load_dwordx4 v231, %[srdws], 0 offen offset:0 sc1 lds\n\t"
  "v_mfma_f32_16x16x32_bf16 a[72:75], v[188:191], v[176:179], a[72:75]\n\t"
  "ds_read_b128 v[164:167], v236 offset:18432\n\t"
  "v_mfma_f32_16x16x32_bf16 a[76:79], v[188:191], v[180:183], a[76:79]\n\t"
  "buffer_load_dwordx4 v231, %[srdws], 0 offen offset:1024 sc1 lds\n\t"
  "s_waitcnt lgkmcnt(11)\n\t"
  "v_mfma_f32_16x16x32_bf16 a[80:83], v[128:131], v[176:179], a[80:83]\n\t"
  "ds_read_b64_tr_b16 v[168:169], v242 offset:0\n\t"
  "ds_read_b64_tr_b16 v[170:171], v242 offset:2048\n\t"
  "v_mfma_f32_16x16x32_bf16 a[84:87], v[128:131], v[180:183], a[84:87]\n\t"
  "buffer_load_dwordx4 v231, %[srdws], 0 offen offset:2048 sc1 lds\n\t"
  "v_mfma_f32_16x16x32_bf16 a[88:91], v[132:135], v[176:179], a[88:91]\n\t"
  "ds_read_b64_tr_b16 v[172:173], v243 offset:0\n\t"
  "ds_read_b64_tr_b16 v[174:175], v243 offset:2048\n\t"
  "v_mfma_f32_16x16x32_bf16 a[92:95], v[132:135], v[180:183], a[92:95]\n\t"
  "buffer_load_dwordx4 v231, %[srdws], 0 offen offset:3072 sc1 lds\n\t"
  "v_add_u32 v231, 0x4000, v231\n\t"
  "s_add_u32 m0, %[m0w], 16384\n\t"
  "ds_read_b64_tr_b16 v[176:177], v242 offset:512\n\t"
  "ds_read_b64_tr_b16 v[178:179], v242 offset:2560\n\t"
  "buffer_load_dwordx4 v232, %[srdp], 0 offen offset:0 lds\n\t"
  "v_add_u32 v232, 0x80, v232\n\t"
  "s_waitcnt lgkmcnt(12)\n\t"
  "ds_read_b64_tr_b16 v[180:181], v243 offset:512\n\t"
  "ds_read_b64_tr_b16 v[182:183], v243 offset:2560\n\t"
  "buffer_load_dwordx4 v233, %[srdp], %[c31744] offen offset:1024 lds\n\t"
  "v_add_u32 v233, 0x80, v233\n\t"
  "v_mfma_f32_16x16x32_bf16 a[64:67], v[144:147], v[136:139], a[64:67]\n\t"
  "ds_read_b128 v[184:187], v237 offset:16384\n\t"
  "v_mfma_f32_16x16x32_bf16 a[68:71], v[144:147], v[140:143], a[68:71]\n\t"
  "v_mfma_f32_16x16x32_bf16 a[72:75], v[148:151], v[136:139], a[72:75]\n\t"
  "ds_read_b128 v[188:191], v237 offset:18432\n\t"
  "v_mfma_f32_16x16x32_bf16 a[76:79], v[148:151], v[140:143], a[76:79]\n\t"
  "s_waitcnt lgkmcnt(11)\n\t"
  "v_mfma_f32_16x16x32_bf16 a[80:83], v[152:155], v[136:139], a[80:83]\n\t"
  "ds_read_b64_tr_b16 v[128:129], v242 offset:8192\n\t"
  "ds_read_b64_tr_b16 v[130:131], v242 offset:10240\n\t"
  "v_mfma_f32_16x16x32_bf16 a[84:87], v[152:155], v[140:143], a[84:87]\n\t"
  "v_mfma_f32_16x16x32_bf16 a[88:91], v[156:159], v[136:139], a[88:91]\n\t"
  "ds_read_b64_tr_b16 v[132:133], v243 offset:8192\n\t"
  "ds_read_b64_tr_b16 v[134:135], v243 offset:10240\n\t"
  "v_mfma_f32_16x16x32_bf16 a[92:95], v[156:159], v[140:143], a[92:95]\n\t"
  "ds_read_b64_tr_b16 v[136:137], v242 offset:8704\n\t"
  "ds_read_b64_tr_b16 v[138:139], v242 offset:10752\n\t"
  "s_waitcnt lgkmcnt(12)\n\t"
  "ds_read_b64_tr_b16 v[140:141], v243 offset:8704\n\t"
  "ds_read_b64_tr_b16 v[142:143], v243 offset:10752\n\t"
  "v_mfma_f32_16x16x32_bf16 a[64:67], v[168:171], v[160:163], a[64:67]\n\t"
  "s_waitcnt vmcnt(12)\n\t"
  "s_barrier\n\t"
  "ds_read_b128 v[144:147], v236 offset:49152\n\t"
  "v_mfma_f32_16x16x32_bf16 a[68:71], v[168:171], v[164:167], a[68:71]\n\t"
  "s_add_u32 m0, %[m0k], 32768\n\t"
  "s_nop 0\n\t"
  "buffer_load_dwordx4 v231, %[srdws], 0 offen offset:0 sc1 lds\n\t"
  "v_mfma_f32_16x16x32_bf16 a[72:75], v[172:175], v[160:163], a[72:75]\n\t"
  "ds_read_b128 v[148:151], v236 offset:51200\n\t"
  "v_mfma_f32_16x16x32_bf16 a[76:79], v[172:175], v[164:167], a[76:79]\n\t"
  "buffer_load_dwordx4 v231, %[srdws], 0 offen offset:1024 sc1 lds\n\t"
  "s_waitcnt lgkmcnt(11)\n\t"
  "v_mfma_f32_16x16x32_bf16 a[80:83], v[176:179], v[160:163], a[80:83]\n\t"
  "ds_read_b64_tr_b16 v[152:153], v242 offset:32768\n\t"
  "ds_read_b64_tr_b16 v[154:155], v242 offset:34816\n\t"
  "v_mfma_f32_16x16x32_bf16 a[84:87], v[176:179], v[164:167], a[84:87]\n\t"
  "buffer_load_dwordx4 v231, %[srdws], 0 offen offset:2048 sc1 lds\n\t"
  "v_mfma_f32_16x16x32_bf16 a[88:91], v[180:183], v[160:163], a[88:91]\n\t"
  "ds_read_b64_tr_b16 v[156:157], v243 offset:32768\n\t"
  "ds_read_b64_tr_b16 v[158:159], v243 offset:34816\n\t"
  "v_mfma_f32_16x16x32_bf16 a[92:95], v[180:183], v[164:167], a[92:95]\n\t"
  "buffer_load_dwordx4 v231, %[srdws], 0 offen offset:3072 sc1 lds\n\t"
  "v_add_u32 v231, 0x4000, v231\n\t"
  "s_add_u32 m0, %[m0w], 49152\n\t"
  "ds_read_b64_tr_b16 v[160:161], v242 offset:33280\n\t"
  "ds_read_b64_tr_b16 v[162:163], v242 offset:35328\n\t"
  "buffer_load_dwordx4 v232, %[srdp], 0 offen offset:0 lds\n\t"
  "v_add_u32 v232, 0x80, v232\n\t"
  "s_waitcnt lgkmcnt(12)\n\t"
  "ds_read_b64_tr_b16 v[164:165], v243 offset:33280\n\t"
  "ds_read_b64_tr_b16 v[166:167], v243 offset:35328\n\t"
  "buffer_load_dwordx4 v233, %[srdp], %[c31744] offen offset:1024 lds\n\t"
  "v_add_u32 v233, 0x80, v233\n\t"
  "v_mfma_f32_16x16x32_bf16 a[64:67], v[128:131], v[184:187], a[64:67]\n\t"
  "ds_read_b128 v[168:171], v237 offset:49152\n\t"
  "v_mfma_f32_16x16x32_bf16 a[68:71], v[128:131], v[188:191], a[68:71]\n\t"
  "v_mfma_f32_16x16x32_bf16 a[72:75], v[132:135], v[184:187], a[72:75]\n\t"
  "ds_read_b128 v[172:175], v237 offset:51200\n\t"
  "v_mfma_f32_16x16x32_bf16 a[76:79], v[132:135], v[188:191], a[76:79]\n\t"
  "s_waitcnt lgkmcnt(11)\n\t"
  "v_mfma_f32_16x16x32_bf16 a[80:83], v[136:139], v[184:187], a[80:83]\n\t"
  "ds_read_b64_tr_b16 v[176:177], v242 offset:40960\n\t"
  "ds_read_b64_tr_b16 v[178:179], v242 offset:43008\n\t"
  "v_mfma_f32_16x16x32_bf16 a[84:87], v[136:139], v[188:191], a[84:87]\n\t"
  "v_mfma_f32_16x16x32_bf16 a[88:91], v[140:143], v[184:187], a[88:91]\n\t"
  "ds_read_b64_tr_b16 v[180:181], v243 offset:40960\n\t"
  "ds_read_b64_tr_b16 v[182:183], v243 offset:43008\n\t"
  "v_mfma_f32_16x16x32_bf16 a[92:95], v[140:143], v[188:191], a[92:95]\n\t"
  "ds_read_b64_tr_b16 v[184:185], v242 offset:41472\n\t"
  "ds_read_b64_tr_b16 v[186:187], v242 offset:43520\n\t"
  "s_waitcnt lgkmcnt(12)\n\t"
  "ds_read_b64_tr_b16 v[188:189], v243 offset:41472\n\t"
  "ds_read_b64_tr_b16 v[190:191], v243 offset:43520\n\t"
  "v_mfma_f32_16x16x32_bf16 a[64:67], v[152:155], v[144:147], a[64:67]\n\t"
  "s_waitcnt vmcnt(12)\n\t"
  "s_barrier\n\t"
  "ds_read_b128 v[128:131], v238 offset:16384\n\t"
  "v_mfma_f32_16x16x32_bf16 a[68:71], v[152:155], v[148:151], a[68:71]\n\t"
  "s_add_u32 m0, %[m0k], 65536\n\t"
  "s_nop 0\n\t"
  "buffer_load_dwordx4 v231, %[srdws], 0 offen offset:0 sc1 lds\n\t"
  "v_mfma_f32_16x16x32_bf16 a[72:75], v[156:159], v[144:147], a[72:75]\n\t"
  "ds_read_b128 v[132:135], v238 offset:18432\n\t"
  "v_mfma_f32_16x16x32_bf16 a[76:79], v[156:159], v[148:151], a[76:79]\n\t"
  "buffer_load_dwordx4 v231, %[srdws], 0 offen offset:1024 sc1 lds\n\t"
  "s_waitcnt lgkmcnt(11)\n\t"
  "v_mfma_f32_16x16x32_bf16 a[80:83], v[160:163], v[144:147], a[80:83]\n\t"
  "ds_read_b64_tr_b16 v[136:137], v244 offset:0\n\t"
  "ds_read_b64_tr_b16 v[138:139], v244 offset:2048\n\t"
  "v_mfma_f32_16x16x32_bf16 a[84:87], v[160:163], v[148:151], a[84:87]\n\t"
  "buffer_load_dwordx4 v231, %[srdws], 0 offen offset:2048 sc1 lds\n\t"
  "v_mfma_f32_16x16x32_bf16 a[88:91], v[164:167], v[144:147], a[88:91]\n\t"
  "ds_read_b64_tr_b16 v[140:141], v245 offset:0\n\t"
  "ds_read_b64_tr_b16 v[142:143], v245 offset:2048\n\t"
  "v_mfma_f32_16x16x32_bf16 a[92:95], v[164:167], v[148:151], a[92:95]\n\t"
  "buffer_load_dwordx4 v231, %[srdws], 0 offen offset:3072 sc1 lds\n\t"
  "v_add_u32 v231, 0x4000, v231\n\t"
  "s_add_u32 m0, %[m0w], 81920\n\t"
  "ds_read_b64_tr_b16 v[144:145], v244 offset:512\n\t"
  "ds_read_b64_tr_b16 v[146:147], v244 offset:2560\n\t"
  "buffer_load_dwordx4 v232, %[srdp], 0 offen offset:0 lds\n\t"
  "v_add_u32 v232, 0x80, v232\n\t"
  "s_waitcnt lgkmcnt(12)\n\t"
  "ds_read_b64_tr_b16 v[148:149], v245 offset:512\n\t"
  "ds_read_b64_tr_b16 v[150:151], v245 offset:2560\n\t"
  "buffer_load_dwordx4 v233, %[srdp], %[c31744] offen offset:1024 lds\n\t"
  "v_add_u32 v233, 0x80, v233\n\t"
  "v_mfma_f32_16x16x32_bf16 a[64:67], v[176:179], v[168:171], a[64:67]\n\t"
  "ds_read_b128 v[152:155], v239 offset:16384\n\t"
  "v_mfma_f32_16x16x32_bf16 a[68:71], v[176:179], v[172:175], a[68:71]\n\t"
  "v_mfma_f32_16x16x32_bf16 a[72:75], v[180:183], v[168:171], a[72:75]\n\t"
  "ds_read_b128 v[156:159], v239 offset:18432\n\t"
  "v_mfma_f32_16x16x32_bf16 a[76:79], v[180:183], v[172:175], a[76:79]\n\t"
  "s_waitcnt lgkmcnt(11)\n\t"
  "v_mfma_f32_16x16x32_bf16 a[80:83], v[184:187], v[168:171], a[80:83]\n\t"
  "ds_read_b64_tr_b16 v[160:161], v244 offset:8192\n\t"
  "ds_read_b64_tr_b16 v[162:163], v244 offset:10240\n\t"
  "v_mfma_f32_16x16x32_bf16 a[84:87], v[184:187], v[172:175], a[84:87]\n\t"
  "v_mfma_f32_16x16x32_bf16 a[88:91], v[188:191], v[168:171], a[88:91]\n\t"
  "ds_read_b64_tr_b16 v[164:165], v245 offset:8192\n\t"
  "ds_read_b64_tr_b16 v[166:167], v245 offset:10240\n\t"
  "v_mfma_f32_16x16x32_bf16 a[92:95], v[188:191], v[172:175], a[92:95]\n\t"
  "ds_read_b64_tr_b16 v[168:169], v244 offset:8704\n\t"
  "ds_read_b64_tr_b16 v[170:171], v244 offset:10752\n\t"
  "s_waitcnt lgkmcnt(12)\n\t"
  "ds_read_b64_tr_b16 v[172:173], v245 offset:8704\n\t"
  "ds_read_b64_tr_b16 v[174:175], v245 offset:10752\n\t"
  "v_mfma_f32_16x16x32_bf16 a[64:67], v[136:139], v[128:131], a[64:67]\n\t"
  "s_waitcnt vmcnt(12)\n\t"
  "s_barrier\n\t"
  "ds_read_b128 v[176:179], v234 offset:16384\n\t"
  "v_mfma_f32_16x16x32_bf16 a[68:71], v[136:139], v[132:135], a[68:71]\n\t"
  "buffer_load_dwordx4 v[208:211], v247, %[srdws], 0 offen offset:0 sc1\n\t"
  "v_mfma_f32_16x16x32_bf16 a[72:75], v[140:143], v[128:131], a[72:75]\n\t"
  "ds_read_b128 v[180:183], v234 offset:18432\n\t"
  "v_mfma_f32_16x16x32_bf16 a[76:79], v[140:143], v[132:135], a[76:79]\n\t"
  "buffer_load_dwordx4 v[212:215], v247, %[srdws], 0 offen offset:1024 sc1\n\t"
  "s_waitcnt lgkmcnt(11)\n\t"
  "v_mfma_f32_16x16x32_bf16 a[80:83], v[144:147], v[128:131], a[80:83]\n\t"
  "ds_read_b64_tr_b16 v[184:185], v240 offset:0\n\t"
  "ds_read_b64_tr_b16 v[186:187], v240 offset:2048\n\t"
  "v_mfma_f32_16x16x32_bf16 a[84:87], v[144:147], v[132:135], a[84:87]\n\t"
  "buffer_load_ushort v204, v249, %[srdb], 0 offen offset:0\n\t"
  "v_mfma_f32_16x16x32_bf16 a[88:91], v[148:151], v[128:131], a[88:91]\n\t"
  "ds_read_b64_tr_b16 v[188:189], v241 offset:0\n\t"
  "ds_read_b64_tr_b16 v[190:191], v241 offset:2048\n\t"
  "v_mfma_f32_16x16x32_bf16 a[92:95], v[148:151], v[132:135], a[92:95]\n\t"
  "buffer_load_dwordx4 v[216:219], v247, %[srdws], s57 offen offset:0 sc1\n\t"
  "ds_read_b64_tr_b16 v[128:129], v240 offset:512\n\t"
  "ds_read_b64_tr_b16 v[130:131], v240 offset:2560\n\t"
  "buffer_load_dwordx4 v[220:223], v247, %[srdws], s57 offen offset:1024 sc1\n\t"
  "buffer_load_ushort v205, v249, %[srdb], 0 offen offset:32\n\t"
  "s_waitcnt lgkmcnt(12)\n\t"
  "ds_read_b64_tr_b16 v[132:133], v241 offset:512\n\t"
  "ds_read_b64_tr_b16 v[134:135], v241 offset:2560\n\t"
  "buffer_load_dword v199, v206, %[srdws], %[cntpr] offen sc1\n\t"
  "v_mfma_f32_16x16x32_bf16 a[64:67], v[160:163], v[152:155], a[64:67]\n\t"
  "ds_read_b128 v[136:139], v235 offset:16384\n\t"
  "v_mfma_f32_16x16x32_bf16 a[68:71], v[160:163], v[156:159], a[68:71]\n\t"
  "v_mfma_f32_16x16x32_bf16 a[72:75], v[164:167], v[152:155], a[72:75]\n\t"
  "ds_read_b128 v[140:143], v235 offset:18432\n\t"
  "v_mfma_f32_16x16x32_bf16 a[76:79], v[164:167], v[156:159], a[76:79]\n\t"
  "s_waitcnt lgkmcnt(11)\n\t"
  "v_mfma_f32_16x16x32_bf16 a[80:83], v[168:171], v[152:155], a[80:83]\n\t"
  "ds_read_b64_tr_b16 v[144:145], v240 offset:8192\n\t"
  "ds_read_b64_tr_b16 v[146:147], v240 offset:10240\n\t"
  "v_mfma_f32_16x16x32_bf16 a[84:87], v[168:171], v[156:159], a[84:87]\n\t"
  "v_mfma_f32_16x16x32_bf16 a[88:91], v[172:175], v[152:155], a[88:91]\n\t"
  "ds_read_b64_tr_b16 v[148:149], v241 offset:8192\n\t"
  "ds_read_b64_tr_b16 v[150:151], v241 offset:10240\n\t"
  "v_mfma_f32_16x16x32_bf16 a[92:95], v[172:175], v[156:159], a[92:95]\n\t"
  "ds_read_b64_tr_b16 v[152:153], v240 offset:8704\n\t"
  "ds_read_b64_tr_b16 v[154:155], v240 offset:10752\n\t"
  "s_waitcnt lgkmcnt(12)\n\t"
  "ds_read_b64_tr_b16 v[156:157], v241 offset:8704\n\t"
  "ds_read_b64_tr_b16 v[158:159], v241 offset:10752\n\t"
  "v_mfma_f32_16x16x32_bf16 a[64:67], v[184:187], v[176:179], a[64:67]\n\t"
  "s_waitcnt vmcnt(13)\n\t"
  "s_barrier\n\t"
  "ds_read_b128 v[160:163], v234 offset:49152\n\t"
  "v_mfma_f32_16x16x32_bf16 a[68:71], v[184:187], v[180:183], a[68:71]\n\t"
  "v_mfma_f32_16x16x32_bf16 a[72:75], v[188:191], v[176:179], a[72:75]\n\t"
  "ds_read_b128 v[164:167], v234 offset:51200\n\t"
  "v_mfma_f32_16x16x32_bf16 a[76:79], v[188:191], v[180:183], a[76:79]\n\t"
  "s_waitcnt lgkmcnt(11)\n\t"
  "v_mfma_f32_16x16x32_bf16 a[80:83], v[128:131], v[176:179], a[80:83]\n\t"
  "ds_read_b64_tr_b16 v[168:169], v240 offset:32768\n\t"
  "ds_read_b64_tr_b16 v[170:171], v240 offset:34816\n\t"
  "v_mfma_f32_16x16x32_bf16 a[84:87], v[128:131], v[180:183], a[84:87]\n\t"
  "v_mfma_f32_16x16x32_bf16 a[88:91], v[132:135], v[176:179], a[88:91]\n\t"
  "ds_read_b64_tr_b16 v[172:173], v241 offset:32768\n\t"
  "ds_read_b64_tr_b16 v[174:175], v241 offset:34816\n\t"
  "v_mfma_f32_16x16x32_bf16 a[92:95], v[132:135], v[180:183], a[92:95]\n\t"
  "ds_read_b64_tr_b16 v[176:177], v240 offset:33280\n\t"
  "ds_read_b64_tr_b16 v[178:179], v240 offset:35328\n\t"
  "s_waitcnt lgkmcnt(12)\n\t"
  "ds_read_b64_tr_b16 v[180:181], v241 offset:33280\n\t"
  "ds_read_b64_tr_b16 v[182:183], v241 offset:35328\n\t"
  "v_mfma_f32_16x16x32_bf16 a[64:67], v[144:147], v[136:139], a[64:67]\n\t"
  "ds_read_b128 v[184:187], v235 offset:49152\n\t"
  "v_mfma_f32_16x16x32_bf16 a[68:71], v[144:147], v[140:143], a[68:71]\n\t"
  "v_mfma_f32_16x16x32_bf16 a[72:75], v[148:151], v[136:139], a[72:75]\n\t"
  "ds_read_b128 v[188:191], v235 offset:51200\n\t"
  "v_mfma_f32_16x16x32_bf16 a[76:79], v[148:151], v[140:143], a[76:79]\n\t"
  "s_waitcnt lgkmcnt(11)\n\t"
  "v_mfma_f32_16x16x32_bf16 a[80:83], v[152:155], v[136:139], a[80:83]\n\t"
  "ds_read_b64_tr_b16 v[128:129], v240 offset:40960\n\t"
  "ds_read_b64_tr_b16 v[130:131], v240 offset:43008\n\t"
  "v_mfma_f32_16x16x32_bf16 a[84:87], v[152:155], v[140:143], a[84:87]\n\t"
  "v_mfma_f32_16x16x32_bf16 a[88:91], v[156:159], v[136:139], a[88:91]\n\t"
  "ds_read_b64_tr_b16 v[132:133], v241 offset:40960\n\t"
  "ds_read_b64_tr_b16 v[134:135], v241 offset:43008\n\t"
  "v_mfma_f32_16x16x32_bf16 a[92:95], v[156:159], v[140:143], a[92:95]\n\t"
  "ds_read_b64_tr_b16 v[136:137], v240 offset:41472\n\t"
  "ds_read_b64_tr_b16 v[138:139], v240 offset:43520\n\t"
  "s_waitcnt lgkmcnt(12)\n\t"
  "ds_read_b64_tr_b16 v[140:141], v241 offset:41472\n\t"
  "ds_read_b64_tr_b16 v[142:143], v241 offset:43520\n\t"
  "v_mfma_f32_16x16x32_bf16 a[64:67], v[168:171], v[160:163], a[64:67]\n\t"
  "s_waitcnt vmcnt(7)\n\t"
  "s_barrier\n\t"
  "ds_read_b128 v[144:147], v236 offset:16384\n\t"
  "v_mfma_f32_16x16x32_bf16 a[68:71], v[168:171], v[164:167], a[68:71]\n\t"
  "v_mfma_f32_16x16x32_bf16 a[72:75], v[172:175], v[160:163], a[72:75]\n\t"
  "ds_read_b128 v[148:151], v236 offset:18432\n\t"
  "v_mfma_f32_16x16x32_bf16 a[76:79], v[172:175], v[164:167], a[76:79]\n\t"
  "s_waitcnt lgkmcnt(11)\n\t"
  "v_mfma_f32_16x16x32_bf16 a[80:83], v[176:179], v[160:163], a[80:83]\n\t"
  "ds_read_b64_tr_b16 v[152:153], v242 offset:0\n\t"
  "ds_read_b64_tr_b16 v[154:155], v242 offset:2048\n\t"
  "v_mfma_f32_16x16x32_bf16 a[84:87], v[176:179], v[164:167], a[84:87]\n\t"
  "v_mfma_f32_16x16x32_bf16 a[88:91], v[180:183], v[160:163], a[88:91]\n\t"
  "ds_read_b64_tr_b16 v[156:157], v243 offset:0\n\t"
  "ds_read_b64_tr_b16 v[158:159], v243 offset:2048\n\t"
  "v_mfma_f32_16x16x32_bf16 a[92:95], v[180:183], v[164:167], a[92:95]\n\t"
  "ds_read_b64_tr_b16 v[160:161], v242 offset:512\n\t"
  "ds_read_b64_tr_b16 v[162:163], v242 offset:2560\n\t"
  "s_waitcnt lgkmcnt(12)\n\t"
  "ds_read_b64_tr_b16 v[164:165], v243 offset:512\n\t"
  "ds_read_b64_tr_b16 v[166:167], v243 offset:2560\n\t"
  "v_mfma_f32_16x16x32_bf16 a[64:67], v[128:131], v[184:187], a[64:67]\n\t"
  "ds_read_b128 v[168:171], v237 offset:16384\n\t"
  "v_mfma_f32_16x16x32_bf16 a[68:71], v[128:131], v[188:191], a[68:71]\n\t"
  "v_mfma_f32_16x16x32_bf16 a[72:75], v[132:135], v[184:187], a[72:75]\n\t"
  "ds_read_b128 v[172:175], v237 offset:18432\n\t"
  "v_mfma_f32_16x16x32_bf16 a[76:79], v[132:135], v[188:191], a[76:79]\n\t"
  "s_waitcnt lgkmcnt(11)\n\t"
  "v_mfma_f32_16x16x32_bf16 a[80:83], v[136:139], v[184:187], a[80:83]\n\t"
  "ds_read_b64_tr_b16 v[176:177], v242 offset:8192\n\t"
  "ds_read_b64_tr_b16 v[178:179], v242 offset:10240\n\t"
  "v_mfma_f32_16x16x32_bf16 a[84:87], v[136:139], v[188:191], a[84:87]\n\t"
  "v_mfma_f32_16x16x32_bf16 a[88:91], v[140:143], v[184:187], a[88:91]\n\t"
  "ds_read_b64_tr_b16 v[180:181], v243 offset:8192\n\t"
  "ds_read_b64_tr_b16 v[182:183], v243 offset:10240\n\t"
  "v_mfma_f32_16x16x32_bf16 a[92:95], v[140:143], v[188:191], a[92:95]\n\t"
  "ds_read_b64_tr_b16 v[184:185], v242 offset:8704\n\t"
  "ds_read_b64_tr_b16 v[186:187], v242 offset:10752\n\t"
  "s_waitcnt lgkmcnt(12)\n\t"
  "ds_read_b64_tr_b16 v[188:189], v243 offset:8704\n\t"
  "ds_read_b64_tr_b16 v[190:191], v243 offset:10752\n\t"
  "v_mfma_f32_16x16x32_bf16 a[64:67], v[152:155], v[144:147], a[64:67]\n\t"
  "v_mfma_f32_16x16x32_bf16 a[68:71], v[152:155], v[148:151], a[68:71]\n\t"
  "v_mfma_f32_16x16x32_bf16 a[72:75], v[156:159], v[144:147], a[72:75]\n\t"
  "v_mfma_f32_16x16x32_bf16 a[76:79], v[156:159], v[148:151], a[76:79]\n\t"
  "s_waitcnt lgkmcnt(9)\n\t"
  "v_mfma_f32_16x16x32_bf16 a[80:83], v[160:163], v[144:147], a[80:83]\n\t"
  "v_mfma_f32_16x16x32_bf16 a[84:87], v[160:163], v[148:151], a[84:87]\n\t"
  "v_mfma_f32_16x16x32_bf16 a[88:91], v[164:167], v[144:147], a[88:91]\n\t"
  "v_mfma_f32_16x16x32_bf16 a[92:95], v[164:167], v[148:151], a[92:95]\n\t"
  "s_waitcnt lgkmcnt(4)\n\t"
  "v_mfma_f32_16x16x32_bf16 a[64:67], v[176:179], v[168:171], a[64:67]\n\t"
  "v_mfma_f32_16x16x32_bf16 a[68:71], v[176:179], v[172:175], a[68:71]\n\t"
  "v_mfma_f32_16x16x32_bf16 a[72:75], v[180:183], v[168:171], a[72:75]\n\t"
  "v_mfma_f32_16x16x32_bf16 a[76:79], v[180:183], v[172:175], a[76:79]\n\t"
  "s_waitcnt lgkmcnt(0)\n\t"
  "v_mfma_f32_16x16x32_bf16 a[80:83], v[184:187], v[168:171], a[80:83]\n\t"
  "v_mfma_f32_16x16x32_bf16 a[84:87], v[184:187], v[172:175], a[84:87]\n\t"
  "v_mfma_f32_16x16x32_bf16 a[88:91], v[188:191], v[168:171], a[88:91]\n\t"
  "v_mfma_f32_16x16x32_bf16 a[92:95], v[188:191], v[172:175], a[92:95]\n\t"
  "s_nop 7\n\t"
  "s_waitcnt vmcnt(0)\n\t"
  "v_lshlrev_b32 v204, 16, v204\n\t"
  "v_lshlrev_b32 v205, 16, v205\n\t"
  "v_accvgpr_read_b32 v192, a64\n\t"
  "v_accvgpr_read_b32 v193, a65\n\t"
  "v_accvgpr_read_b32 v194, a66\n\t"
  "v_accvgpr_read_b32 v195, a67\n\t"
  "v_lshlrev_b32 v200, 16, v208\n\t"
  "v_and_b32 v201, 0xffff0000, v208\n\t"
  "v_lshlrev_b32 v202, 16, v209\n\t"
  "v_and_b32 v203, 0xffff0000, v209\n\t"
  "v_add_f32 v192, v192, v204\n\t"
  "v_add_f32 v193, v193, v204\n\t"
  "v_add_f32 v194, v194, v204\n\t"
  "v_add_f32 v195, v195, v204\n\t"
  "v_mul_f32 v200, v200, v192\n\t"
  "v_mul_f32 v201, v201, v193\n\t"
  "v_mul_f32 v202, v202, v194\n\t"
  "v_mul_f32 v203, v203, v195\n\t"
  "v_cvt_pk_bf16_f32 v208, v200, v201\n\t"
  "v_cvt_pk_bf16_f32 v209, v202, v203\n\t"
  "v_accvgpr_read_b32 v192, a72\n\t"
  "v_accvgpr_read_b32 v193, a73\n\t"
  "v_accvgpr_read_b32 v194, a74\n\t"
  "v_accvgpr_read_b32 v195, a75\n\t"
  "v_lshlrev_b32 v200, 16, v210\n\t"
  "v_and_b32 v201, 0xffff0000, v210\n\t"
  "v_lshlrev_b32 v202, 16, v211\n\t"
  "v_and_b32 v203, 0xffff0000, v211\n\t"
  "v_add_f32 v192, v192, v204\n\t"
  "v_add_f32 v193, v193, v204\n\t"
  "v_add_f32 v194, v194, v204\n\t"
  "v_add_f32 v195, v195, v204\n\t"
  "v_mul_f32 v200, v200, v192\n\t"
  "v_mul_f32 v201, v201, v193\n\t"
  "v_mul_f32 v202, v202, v194\n\t"
  "v_mul_f32 v203, v203, v195\n\t"
  "v_cvt_pk_bf16_f32 v210, v200, v201\n\t"
  "v_cvt_pk_bf16_f32 v211, v202, v203\n\t"
  "buffer_store_dwordx4 v[208:211], v248, %[srdws], 0 offen offset:0 sc1\n\t"
  "v_accvgpr_read_b32 v192, a80\n\t"
  "v_accvgpr_read_b32 v193, a81\n\t"
  "v_accvgpr_read_b32 v194, a82\n\t"
  "v_accvgpr_read_b32 v195, a83\n\t"
  "v_lshlrev_b32 v200, 16, v212\n\t"
  "v_and_b32 v201, 0xffff0000, v212\n\t"
  "v_lshlrev_b32 v202, 16, v213\n\t"
  "v_and_b32 v203, 0xffff0000, v213\n\t"
  "v_add_f32 v192, v192, v204\n\t"
  "v_add_f32 v193, v193, v204\n\t"
  "v_add_f32 v194, v194, v204\n\t"
  "v_add_f32 v195, v195, v204\n\t"
  "v_mul_f32 v200, v200, v192\n\t"
  "v_mul_f32 v201, v201, v193\n\t"
  "v_mul_f32 v202, v202, v194\n\t"
  "v_mul_f32 v203, v203, v195\n\t"
  "v_cvt_pk_bf16_f32 v212, v200, v201\n\t"
  "v_cvt_pk_bf16_f32 v213, v202, v203\n\t"
  "v_accvgpr_read_b32 v192, a88\n\t"
  "v_accvgpr_read_b32 v193, a89\n\t"
  "v_accvgpr_read_b32 v194, a90\n\t"
  "v_accvgpr_read_b32 v195, a91\n\t"
  "v_lshlrev_b32 v200, 16, v214\n\t"
  "v_and_b32 v201, 0xffff0000, v214\n\t"
  "v_lshlrev_b32 v202, 16, v215\n\t"
  "v_and_b32 v203, 0xffff0000, v215\n\t"
  "v_add_f32 v192, v192, v204\n\t"
  "v_add_f32 v193, v193, v204\n\t"
  "v_add_f32 v194, v194, v204\n\t"
  "v_add_f32 v195, v195, v204\n\t"
  "v_mul_f32 v200, v200, v192\n\t"
  "v_mul_f32 v201, v201, v193\n\t"
  "v_mul_f32 v202, v202, v194\n\t"
  "v_mul_f32 v203, v203, v195\n\t"
  "v_cvt_pk_bf16_f32 v214, v200, v201\n\t"
  "v_cvt_pk_bf16_f32 v215, v202, v203\n\t"
  "buffer_store_dwordx4 v[212:215], v248, %[srdws], s51 offen offset:0 sc1\n\t"
  "v_accvgpr_read_b32 v192, a68\n\t"
  "v_accvgpr_read_b32 v193, a69\n\t"
  "v_accvgpr_read_b32 v194, a70\n\t"
  "v_accvgpr_read_b32 v195, a71\n\t"
  "v_lshlrev_b32 v200, 16, v216\n\t"
  "v_and_b32 v201, 0xffff0000, v216\n\t"
  "v_lshlrev_b32 v202, 16, v217\n\t"
  "v_and_b32 v203, 0xffff0000, v217\n\t"
  "v_add_f32 v192, v192, v205\n\t"
  "v_add_f32 v193, v193, v205\n\t"
  "v_add_f32 v194, v194, v205\n\t"
  "v_add_f32 v195, v195, v205\n\t"
  "v_mul_f32 v200, v200, v192\n\t"
  "v_mul_f32 v201, v201, v193\n\t"
  "v_mul_f32 v202, v202, v194\n\t"
  "v_mul_f32 v203, v203, v195\n\t"
  "v_cvt_pk_bf16_f32 v216, v200, v201\n\t"
  "v_cvt_pk_bf16_f32 v217, v202, v203\n\t"
  "v_accvgpr_read_b32 v192, a76\n\t"
  "v_accvgpr_read_b32 v193, a77\n\t"
  "v_accvgpr_read_b32 v194, a78\n\t"
  "v_accvgpr_read_b32 v195, a79\n\t"
  "v_lshlrev_b32 v200, 16, v218\n\t"
  "v_and_b32 v201, 0xffff0000, v218\n\t"
  "v_lshlrev_b32 v202, 16, v219\n\t"
  "v_and_b32 v203, 0xffff0000, v219\n\t"
  "v_add_f32 v192, v192, v205\n\t"
  "v_add_f32 v193, v193, v205\n\t"
  "v_add_f32 v194, v194, v205\n\t"
  "v_add_f32 v195, v195, v205\n\t"
  "v_mul_f32 v200, v200, v192\n\t"
  "v_mul_f32 v201, v201, v193\n\t"
  "v_mul_f32 v202, v202, v194\n\t"
  "v_mul_f32 v203, v203, v195\n\t"
  "v_cvt_pk_bf16_f32 v218, v200, v201\n\t"
  "v_cvt_pk_bf16_f32 v219, v202, v203\n\t"
  "buffer_store_dwordx4 v[216:219], v248, %[srdws], 0 offen offset:1024 sc1\n\t"
  "v_accvgpr_read_b32 v192, a84\n\t"
  "v_accvgpr_read_b32 v193, a85\n\t"
  "v_accvgpr_read_b32 v194, a86\n\t"
  "v_accvgpr_read_b32 v195, a87\n\t"
  "v_lshlrev_b32 v200, 16, v220\n\t"
  "v_and_b32 v201, 0xffff0000, v220\n\t"
  "v_lshlrev_b32 v202, 16, v221\n\t"
  "v_and_b32 v203, 0xffff0000, v221\n\t"
  "v_add_f32 v192, v192, v205\n\t"
  "v_add_f32 v193, v193, v205\n\t"
  "v_add_f32 v194, v194, v205\n\t"
  "v_add_f32 v195, v195, v205\n\t"
  "v_mul_f32 v200, v200, v192\n\t"
  "v_mul_f32 v201, v201, v193\n\t"
  "v_mul_f32 v202, v202, v194\n\t"
  "v_mul_f32 v203, v203, v195\n\t"
  "v_cvt_pk_bf16_f32 v220, v200, v201\n\t"
  "v_cvt_pk_bf16_f32 v221, v202, v203\n\t"
  "v_accvgpr_read_b32 v192, a92\n\t"
  "v_accvgpr_read_b32 v193, a93\n\t"
  "v_accvgpr_read_b32 v194, a94\n\t"
  "v_accvgpr_read_b32 v195, a95\n\t"
  "v_lshlrev_b32 v200, 16, v222\n\t"
  "v_and_b32 v201, 0xffff0000, v222\n\t"
  "v_lshlrev_b32 v202, 16, v223\n\t"
  "v_and_b32 v203, 0xffff0000, v223\n\t"
  "v_add_f32 v192, v192, v205\n\t"
  "v_add_f32 v193, v193, v205\n\t"
  "v_add_f32 v194, v194, v205\n\t"
  "v_add_f32 v195, v195, v205\n\t"
  "v_mul_f32 v200, v200, v192\n\t"
  "v_mul_f32 v201, v201, v193\n\t"
  "v_mul_f32 v202, v202, v194\n\t"
  "v_mul_f32 v203, v203, v195\n\t"
  "v_cvt_pk_bf16_f32 v222, v200, v201\n\t"
  "v_cvt_pk_bf16_f32 v223, v202, v203\n\t"
  "buffer_store_dwordx4 v[220:223], v248, %[srdws], s51 offen offset:1024 sc1\n\t"
  "s_barrier\n\t"
  "s_add_u32 m0, %[ldsw], 0\n\t"
  "s_nop 0\n\t"
  "buffer_load_dwordx4 v251, %[srdg], 0 offen offset:0 lds\n\t"
  "buffer_load_dwordx4 v251, %[srdg], %[c6144] offen offset:1024 lds\n\t"
  "buffer_load_dwordx4 v251, %[srdg], %[c12288] offen offset:2048 lds\n\t"
  "buffer_load_dwordx4 v251, %[srdg], %[c18432] offen offset:3072 lds\n\t"
  "v_add_u32 v251, 28672, v251\n\t"
  "s_add_u32 m0, %[ldsw], 4096\n\t"
  "s_nop 0\n\t"
  "buffer_load_dwordx4 v251, %[srdg], 0 offen offset:0 lds\n\t"
  "buffer_load_dwordx4 v251, %[srdg], %[c6144] offen offset:1024 lds\n\t"
  "buffer_load_dwordx4 v251, %[srdg], %[c12288] offen offset:2048 lds\n\t"
  "buffer_load_dwordx4 v251, %[srdg], %[c18432] offen offset:3072 lds\n\t"
  "v_add_u32 v251, 28672, v251\n\t"
  "s_waitcnt vmcnt(8)\n\t"
  "s_mov_b64 exec, 1\n\t"
  "buffer_atomic_add v207, v206, %[srdws], %[cntu] offen\n\t"
  "s_mov_b64 exec, -1\n\t"
  "s_add_u32 m0, %[ldsw], 8192\n\t"
  "s_nop 0\n\t"
  "buffer_load_dwordx4 v251, %[srdg], 0 offen offset:0 lds\n\t"
  "buffer_load_dwordx4 v251, %[srdg], %[c6144] offen offset:1024 lds\n\t"
  "buffer_load_dwordx4 v251, %[srdg], %[c12288] offen offset:2048 lds\n\t"
  "buffer_load_dwordx4 v251, %[srdg], %[c18432] offen offset:3072 lds\n\t"
  "v_add_u32 v251, 28672, v251\n\t"
  "s_add_u32 m0, %[ldsw], 12288\n\t"
  "s_nop 0\n\t"
  "buffer_load_dwordx4 v251, %[srdg], 0 offen offset:0 lds\n\t"
  "buffer_load_dwordx4 v251, %[srdg], %[c6144] offen offset:1024 lds\n\t"
  "buffer_load_dwordx4 v251, %[srdg], %[c12288] offen offset:2048 lds\n\t"
  "buffer_load_dwordx4 v251, %[srdg], %[c18432] offen offset:3072 lds\n\t"
  "v_add_u32 v251, 28672, v251\n\t"
  "s_add_u32 m0, %[ldsw], 16384\n\t"
  "s_nop 0\n\t"
  "buffer_load_dwordx4 v251, %[srdg], 0 offen offset:0 lds\n\t"
  "buffer_load_dwordx4 v251, %[srdg], %[c6144] offen offset:1024 lds\n\t"
  "buffer_load_dwordx4 v251, %[srdg], %[c12288] offen offset:2048 lds\n\t"
  "buffer_load_dwordx4 v251, %[srdg], %[c18432] offen offset:3072 lds\n\t"
  "v_add_u32 v251, 28672, v251\n\t"
  "s_add_u32 m0, %[ldsw], 20480\n\t"
  "s_nop 0\n\t"
  "buffer_load_dwordx4 v251, %[srdg], 0 offen offset:0 lds\n\t"
  "buffer_load_dwordx4 v251, %[srdg], %[c6144] offen offset:1024 lds\n\t"
  "buffer_load_dwordx4 v251, %[srdg], %[c12288] offen offset:2048 lds\n\t"
  "buffer_load_dwordx4 v251, %[srdg], %[c18432] offen offset:3072 lds\n\t"
  "v_add_u32 v251, 28672, v251\n\t"
  "s_add_u32 m0, %[ldsw], 24576\n\t"
  "s_nop 0\n\t"
  "buffer_load_dwordx4 v251, %[srdg], 0 offen offset:0 lds\n\t"
  "buffer_load_dwordx4 v251, %[srdg], %[c6144] offen offset:1024 lds\n\t"
  "buffer_load_dwordx4 v251, %[srdg], %[c12288] offen offset:2048 lds\n\t"
  "buffer_load_dwordx4 v251, %[srdg], %[c18432] offen offset:3072 lds\n\t"
  "v_add_u32 v251, 28672, v251\n\t"
  "v_cmp_ne_u32 vcc, 64, v199\n\t"
  "s_cbranch_vccz .Lppd_%=\n\t"
  "s_mov_b32 s43, 0x40000\n\t"
  ".Lpq_%=:\n\t"
  "buffer_load_dword v199, v206, %[srdws], %[cntpr] offen sc1\n\t"
  "s_waitcnt vmcnt(0)\n\t"
  "v_cmp_ne_u32 vcc, 64, v199\n\t"
  "s_cbranch_vccz .Lpqd_%=\n\t"
  "s_sleep 2\n\t"
  "s_sub_u32 s43, s43, 1\n\t"
  "s_cmp_lg_u32 s43, 0\n\t"
  "s_cbranch_scc1 .Lpq_%=\n\t"
  ".Lpqd_%=:\n\t"
  ".Lppd_%=:\n\t"
  "s_mov_b32 s43, 0\n\t"
  "buffer_load_dwordx4 a[128:131], v252, %[srdws], s43 offen offset:0 sc1\n\t"
  "buffer_load_dwordx4 a[132:135], v252, %[srdws], s43 offen offset:1024 sc1\n\t"
  "s_add_u32 s43, s43, 1835008\n\t"
  "buffer_load_dwordx4 a[136:139], v252, %[srdws], s43 offen offset:0 sc1\n\t"
  "buffer_load_dwordx4 a[140:143], v252, %[srdws], s43 offen offset:1024 sc1\n\t"
  "s_add_u32 s43, s43, 1835008\n\t"
  "buffer_load_dwordx4 a[144:147], v252, %[srdws], s43 offen offset:0 sc1\n\t"
  "buffer_load_dwordx4 a[148:151], v252, %[srdws], s43 offen offset:1024 sc1\n\t"
  "s_add_u32 s43, s43, 1835008\n\t"
  "buffer_load_dwordx4 a[152:155], v252, %[srdws], s43 offen offset:0 sc1\n\t"
  "buffer_load_dwordx4 a[156:159], v252, %[srdws], s43 offen offset:1024 sc1\n\t"
  "s_add_u32 s43, s43, 1835008\n\t"
  "buffer_load_dwordx4 a[160:163], v252, %[srdws], s43 offen offset:0 sc1\n\t"
  "buffer_load_dwordx4 a[164:167], v252, %[srdws], s43 offen offset:1024 sc1\n\t"
  "s_add_u32 s43, s43, 1835008\n\t"
  "buffer_load_dwordx4 a[168:171], v252, %[srdws], s43 offen offset:0 sc1\n\t"
  "buffer_load_dwordx4 a[172:175], v252, %[srdws], s43 offen offset:1024 sc1\n\t"
  "s_add_u32 s43, s43, 1835008\n\t"
  "buffer_load_dwordx4 a[176:179], v252, %[srdws], s43 offen offset:0 sc1\n\t"
  "buffer_load_dwordx4 a[180:183], v252, %[srdws], s43 offen offset:1024 sc1\n\t"
  "s_add_u32 s43, s43, 1835008\n\t"
  "buffer_load_dwordx4 a[184:187], v252, %[srdws], s43 offen offset:0 sc1\n\t"
  "buffer_load_dwordx4 a[188:191], v252, %[srdws], s43 offen offset:1024 sc1\n\t"
  "s_add_u32 s43, s43, 1835008\n\t"
  "s_mov_b32 s47, 0x40000\n\t"
  ".Lpu_%=:\n\t"
  "buffer_load_dword v204, v206, %[srdws], %[cntu] offen sc1\n\t"
  "s_waitcnt vmcnt(0)\n\t"
  "v_cmp_ne_u32 vcc, 28, v204\n\t"
  "s_cbranch_vccz .Lpud_%=\n\t"
  "s_sleep 2\n\t"
  "s_sub_u32 s47, s47, 1\n\t"
  "s_cmp_lg_u32 s47, 0\n\t"
  "s_cbranch_scc1 .Lpu_%=\n\t"
  ".Lpud_%=:\n\t"
  "s_barrier\n\t"
  "buffer_load_dwordx4 v[64:67], v250, %[srdws], 0 offen sc1\n\t"
  "v_add_u32 v250, 0x1000, v250\n\t"
  "buffer_load_dwordx4 v[68:71], v250, %[srdws], 0 offen sc1\n\t"
  "v_add_u32 v250, 0x1000, v250\n\t"
  "buffer_load_dwordx4 v[72:75], v250, %[srdws], 0 offen sc1\n\t"
  "v_add_u32 v250, 0x1000, v250\n\t"
  "buffer_load_dwordx4 v[76:79], v250, %[srdws], 0 offen sc1\n\t"
  "v_add_u32 v250, 0x1000, v250\n\t"
  "buffer_load_dwordx4 v[80:83], v250, %[srdws], 0 offen sc1\n\t"
  "v_add_u32 v250, 0x1000, v250\n\t"
  "buffer_load_dwordx4 v[84:87], v250, %[srdws], 0 offen sc1\n\t"
  "v_add_u32 v250, 0x1000, v250\n\t"
  "buffer_load_dwordx4 v[88:91], v250, %[srdws], 0 offen sc1\n\t"
  "v_add_u32 v250, 0x1000, v250\n\t"
  "buffer_load_dwordx4 v[92:95], v250, %[srdws], 0 offen sc1\n\t"
  "v_add_u32 v250, 0x1000, v250\n\t"
  "buffer_load_dwordx4 v[96:99], v250, %[srdws], 0 offen sc1\n\t"
  "v_add_u32 v250, 0x1000, v250\n\t"
  "buffer_load_dwordx4 v[100:103], v250, %[srdws], 0 offen sc1\n\t"
  "v_add_u32 v250, 0x1000, v250\n\t"
  "buffer_load_dwordx4 v[104:107], v250, %[srdws], 0 offen sc1\n\t"
  "v_add_u32 v250, 0x1000, v250\n\t"
  "buffer_load_dwordx4 v[108:111], v250, %[srdws], 0 offen sc1\n\t"
  "v_add_u32 v250, 0x1000, v250\n\t"
  "buffer_load_dwordx4 v[112:115], v250, %[srdws], 0 offen sc1\n\t"
  "v_add_u32 v250, 0x1000, v250\n\t"
  "buffer_load_dwordx4 v[116:119], v250, %[srdws], 0 offen sc1\n\t"
  "v_add_u32 v250, 0x1000, v250\n\t"
  "buffer_load_dwordx4 v[120:123], v250, %[srdws], 0 offen sc1\n\t"
  "v_add_u32 v250, 0x1000, v250\n\t"
  "buffer_load_dwordx4 v[124:127], v250, %[srdws], 0 offen sc1\n\t"
  "v_add_u32 v250, 0x1000, v250\n\t"
  "buffer_load_dwordx4 v[128:131], v250, %[srdws], 0 offen sc1\n\t"
  "v_add_u32 v250, 0x1000, v250\n\t"
  "buffer_load_dwordx4 v[132:135], v250, %[srdws], 0 offen sc1\n\t"
  "v_add_u32 v250, 0x1000, v250\n\t"
  "buffer_load_dwordx4 v[136:139], v250, %[srdws], 0 offen sc1\n\t"
  "v_add_u32 v250, 0x1000, v250\n\t"
  "buffer_load_dwordx4 v[140:143], v250, %[srdws], 0 offen sc1\n\t"
  "v_add_u32 v250, 0x1000, v250\n\t"
  "buffer_load_dwordx4 v[144:147], v250, %[srdws], 0 offen sc1\n\t"
  "v_add_u32 v250, 0x1000, v250\n\t"
  "buffer_load_dwordx4 v[148:151], v250, %[srdws], 0 offen sc1\n\t"
  "v_add_u32 v250, 0x1000, v250\n\t"
  "buffer_load_dwordx4 v[152:155], v250, %[srdws], 0 offen sc1\n\t"
  "v_add_u32 v250, 0x1000, v250\n\t"
  "buffer_load_dwordx4 v[156:159], v250, %[srdws], 0 offen sc1\n\t"
  "v_add_u32 v250, 0x1000, v250\n\t"
  "buffer_load_dwordx4 v[160:163], v250, %[srdws], 0 offen sc1\n\t"
  "v_add_u32 v250, 0x1000, v250\n\t"
  "buffer_load_dwordx4 v[164:167], v250, %[srdws], 0 offen sc1\n\t"
  "v_add_u32 v250, 0x1000, v250\n\t"
  "buffer_load_dwordx4 v[168:171], v250, %[srdws], 0 offen sc1\n\t"
  "v_add_u32 v250, 0x1000, v250\n\t"
  "buffer_load_dwordx4 v[172:175], v250, %[srdws], 0 offen sc1\n\t"
  "v_add_u32 v250, 0x1000, v250\n\t"
  "buffer_load_dwordx4 a[192:195], v252, %[srdws], s43 offen offset:0 sc1\n\t"
  "buffer_load_dwordx4 a[196:199], v252, %[srdws], s43 offen offset:1024 sc1\n\t"
  "s_add_u32 s43, s43, 1835008\n\t"
  "buffer_load_dwordx4 a[200:203], v252, %[srdws], s43 offen offset:0 sc1\n\t"
  "buffer_load_dwordx4 a[204:207], v252, %[srdws], s43 offen offset:1024 sc1\n\t"
  "s_add_u32 s43, s43, 1835008\n\t"
  "buffer_load_dwordx4 a[208:211], v252, %[srdws], s43 offen offset:0 sc1\n\t"
  "buffer_load_dwordx4 a[212:215], v252, %[srdws], s43 offen offset:1024 sc1\n\t"
  "s_add_u32 s43, s43, 1835008\n\t"
  "buffer_load_dwordx4 a[216:219], v252, %[srdws], s43 offen offset:0 sc1\n\t"
  "buffer_load_dwordx4 a[220:223], v252, %[srdws], s43 offen offset:1024 sc1\n\t"
  "s_add_u32 s43, s43, 1835008\n\t"
  "buffer_load_dwordx4 a[224:227], v252, %[srdws], s43 offen offset:0 sc1\n\t"
  "buffer_load_dwordx4 a[228:231], v252, %[srdws], s43 offen offset:1024 sc1\n\t"
  "s_add_u32 s43, s43, 1835008\n\t"
  "buffer_load_dwordx4 a[232:235], v252, %[srdws], s43 offen offset:0 sc1\n\t"
  "buffer_load_dwordx4 a[236:239], v252, %[srdws], s43 offen offset:1024 sc1\n\t"
  "s_add_u32 s43, s43, 1835008\n\t"
  "buffer_load_dwordx4 a[240:243], v252, %[srdws], s43 offen offset:0 sc1\n\t"
  "buffer_load_dwordx4 a[244:247], v252, %[srdws], s43 offen offset:1024 sc1\n\t"
  "s_add_u32 s43, s43, 1835008\n\t"
  "buffer_load_dwordx4 a[248:251], v252, %[srdws], s43 offen offset:0 sc1\n\t"
  "buffer_load_dwordx4 a[252:255], v252, %[srdws], s43 offen offset:1024 sc1\n\t"
  "s_add_u32 s43, s43, 1835008\n\t"
  "ds_read_b64_tr_b16 v[176:177], v225 offset:0\n\t"
  "ds_read_b64_tr_b16 v[178:179], v225 offset:2048\n\t"
  "ds_read_b64_tr_b16 v[180:181], v226 offset:0\n\t"
  "ds_read_b64_tr_b16 v[182:183], v226 offset:2048\n\t"
  "ds_read_b64_tr_b16 v[184:185], v225 offset:512\n\t"
  "ds_read_b64_tr_b16 v[186:187], v225 offset:2560\n\t"
  "ds_read_b64_tr_b16 v[188:189], v226 offset:512\n\t"
  "ds_read_b64_tr_b16 v[190:191], v226 offset:2560\n\t"
  "ds_read_b64_tr_b16 v[192:193], v225 offset:4096\n\t"
  "ds_read_b64_tr_b16 v[194:195], v225 offset:6144\n\t"
  "ds_read_b64_tr_b16 v[196:197], v226 offset:4096\n\t"
  "ds_read_b64_tr_b16 v[198:199], v226 offset:6144\n\t"
  "ds_read_b64_tr_b16 v[200:201], v225 offset:4608\n\t"
  "ds_read_b64_tr_b16 v[202:203], v225 offset:6656\n\t"
  "ds_read_b64_tr_b16 v[204:205], v226 offset:4608\n\t"
  "ds_read_b64_tr_b16 v[206:207], v226 offset:6656\n\t"
  "v_mfma_f32_16x16x32_bf16 a[16:19], %[sel0], a[128:131], 0\n\t"
  "v_mfma_f32_16x16x32_bf16 a[20:23], %[sel1], a[128:131], 0\n\t"
  "v_mfma_f32_16x16x32_bf16 a[24:27], %[sel0], a[132:135], 0\n\t"
  "v_mfma_f32_16x16x32_bf16 a[28:31], %[sel1], a[132:135], 0\n\t"
  "v_mfma_f32_16x16x32_bf16 a[16:19], %[sel0], a[136:139], a[16:19]\n\t"
  "v_mfma_f32_16x16x32_bf16 a[20:23], %[sel1], a[136:139], a[20:23]\n\t"
  "v_mfma_f32_16x16x32_bf16 a[24:27], %[sel0], a[140:143], a[24:27]\n\t"
  "v_mfma_f32_16x16x32_bf16 a[28:31], %[sel1], a[140:143], a[28:31]\n\t"
  "v_mfma_f32_16x16x32_bf16 a[16:19], %[sel0], a[144:147], a[16:19]\n\t"
  "v_mfma_f32_16x16x32_bf16 a[20:23], %[sel1], a[144:147], a[20:23]\n\t"
  "v_mfma_f32_16x16x32_bf16 a[24:27], %[sel0], a[148:151], a[24:27]\n\t"
  "v_mfma_f32_16x16x32_bf16 a[28:31], %[sel1], a[148:151], a[28:31]\n\t"
  "v_mfma_f32_16x16x32_bf16 a[16:19], %[sel0], a[152:155], a[16:19]\n\t"
  "v_mfma_f32_16x16x32_bf16 a[20:23], %[sel1], a[152:155], a[20:23]\n\t"
  "v_mfma_f32_16x16x32_bf16 a[24:27], %[sel0], a[156:159], a[24:27]\n\t"
  "v_mfma_f32_16x16x32_bf16 a[28:31], %[sel1], a[156:159], a[28:31]\n\t"
  "v_mfma_f32_16x16x32_bf16 a[16:19], %[sel0], a[160:163], a[16:19]\n\t"
  "v_mfma_f32_16x16x32_bf16 a[20:23], %[sel1], a[160:163], a[20:23]\n\t"
  "v_mfma_f32_16x16x32_bf16 a[24:27], %[sel0], a[164:167], a[24:27]\n\t"
  "v_mfma_f32_16x16x32_bf16 a[28:31], %[sel1], a[164:167], a[28:31]\n\t"
  "v_mfma_f32_16x16x32_bf16 a[16:19], %[sel0], a[168:171], a[16:19]\n\t"
  "v_mfma_f32_16x16x32_bf16 a[20:23], %[sel1], a[168:171], a[20:23]\n\t"
  "v_mfma_f32_16x16x32_bf16 a[24:27], %[sel0], a[172:175], a[24:27]\n\t"
  "v_mfma_f32_16x16x32_bf16 a[28:31], %[sel1], a[172:175], a[28:31]\n\t"
  "v_mfma_f32_16x16x32_bf16 a[16:19], %[sel0], a[176:179], a[16:19]\n\t"
  "v_mfma_f32_16x16x32_bf16 a[20:23], %[sel1], a[176:179], a[20:23]\n\t"
  "v_mfma_f32_16x16x32_bf16 a[24:27], %[sel0], a[180:183], a[24:27]\n\t"
  "v_mfma_f32_16x16x32_bf16 a[28:31], %[sel1], a[180:183], a[28:31]\n\t"
  "v_mfma_f32_16x16x32_bf16 a[16:19], %[sel0], a[184:187], a[16:19]\n\t"
  "v_mfma_f32_16x16x32_bf16 a[20:23], %[sel1], a[184:187], a[20:23]\n\t"
  "v_mfma_f32_16x16x32_bf16 a[24:27], %[sel0], a[188:191], a[24:27]\n\t"
  "v_mfma_f32_16x16x32_bf16 a[28:31], %[sel1], a[188:191], a[28:31]\n\t"
  "s_waitcnt vmcnt(43) lgkmcnt(8)\n\t"
  "v_mfma_f32_16x16x32_bf16 a[0:3], v[176:179], v[64:67], 0\n\t"
  "ds_read_b64_tr_b16 v[208:209], v225 offset:8192\n\t"
  "ds_read_b64_tr_b16 v[210:211], v225 offset:10240\n\t"
  "v_mfma_f32_16x16x32_bf16 a[4:7], v[180:183], v[64:67], 0\n\t"
  "ds_read_b64_tr_b16 v[212:213], v226 offset:8192\n\t"
  "ds_read_b64_tr_b16 v[214:215], v226 offset:10240\n\t"
  "v_mfma_f32_16x16x32_bf16 a[8:11], v[184:187], v[64:67], 0\n\t"
  "ds_read_b64_tr_b16 v[216:217], v225 offset:8704\n\t"
  "ds_read_b64_tr_b16 v[218:219], v225 offset:10752\n\t"
  "v_mfma_f32_16x16x32_bf16 a[12:15], v[188:191], v[64:67], 0\n\t"
  "ds_read_b64_tr_b16 v[220:221], v226 offset:8704\n\t"
  "ds_read_b64_tr_b16 v[222:223], v226 offset:10752\n\t"
  "s_waitcnt vmcnt(42) lgkmcnt(8)\n\t"
  "v_mfma_f32_16x16x32_bf16 a[0:3], v[192:195], v[68:71], a[0:3]\n\t"
  "ds_read_b64_tr_b16 v[176:177], v225 offset:12288\n\t"
  "ds_read_b64_tr_b16 v[178:179], v225 offset:14336\n\t"
  "v_mfma_f32_16x16x32_bf16 a[4:7], v[196:199], v[68:71], a[4:7]\n\t"
  "ds_read_b64_tr_b16 v[180:181], v226 offset:12288\n\t"
  "ds_read_b64_tr_b16 v[182:183], v226 offset:14336\n\t"
  "v_mfma_f32_16x16x32_bf16 a[8:11], v[200:203], v[68:71], a[8:11]\n\t"
  "ds_read_b64_tr_b16 v[184:185], v225 offset:12800\n\t"
  "ds_read_b64_tr_b16 v[186:187], v225 offset:14848\n\t"
  "v_mfma_f32_16x16x32_bf16 a[12:15], v[204:207], v[68:71], a[12:15]\n\t"
  "ds_read_b64_tr_b16 v[188:189], v226 offset:12800\n\t"
  "ds_read_b64_tr_b16 v[190:191], v226 offset:14848\n\t"
  "s_waitcnt vmcnt(41) lgkmcnt(8)\n\t"
  "v_mfma_f32_16x16x32_bf16 a[0:3], v[208:211], v[72:75], a[0:3]\n\t"
  "ds_read_b64_tr_b16 v[192:193], v225 offset:16384\n\t"
  "ds_read_b64_tr_b16 v[194:195], v225 offset:18432\n\t"
  "v_mfma_f32_16x16x32_bf16 a[4:7], v[212:215], v[72:75], a[4:7]\n\t"
  "ds_read_b64_tr_b16 v[196:197], v226 offset:16384\n\t"
  "ds_read_b64_tr_b16 v[198:199], v226 offset:18432\n\t"
  "v_mfma_f32_16x16x32_bf16 a[8:11], v[216:219], v[72:75], a[8:11]\n\t"
  "ds_read_b64_tr_b16 v[200:201], v225 offset:16896\n\t"
  "ds_read_b64_tr_b16 v[202:203], v225 offset:18944\n\t"
  "v_mfma_f32_16x16x32_bf16 a[12:15], v[220:223], v[72:75], a[12:15]\n\t"
  "ds_read_b64_tr_b16 v[204:205], v226 offset:16896\n\t"
  "ds_read_b64_tr_b16 v[206:207], v226 offset:18944\n\t"
  "s_waitcnt vmcnt(40) lgkmcnt(8)\n\t"
  "v_mfma_f32_16x16x32_bf16 a[0:3], v[176:179], v[76:79], a[0:3]\n\t"
  "ds_read_b64_tr_b16 v[208:209], v225 offset:20480\n\t"
  "ds_read_b64_tr_b16 v[210:211], v225 offset:22528\n\t"
  "v_mfma_f32_16x16x32_bf16 a[4:7], v[180:183], v[76:79], a[4:7]\n\t"
  "ds_read_b64_tr_b16 v[212:213], v226 offset:20480\n\t"
  "ds_read_b64_tr_b16 v[214:215], v226 offset:22528\n\t"
  "v_mfma_f32_16x16x32_bf16 a[8:11], v[184:187], v[76:79], a[8:11]\n\t"
  "ds_read_b64_tr_b16 v[216:217], v225 offset:20992\n\t"
  "ds_read_b64_tr_b16 v[218:219], v225 offset:23040\n\t"
  "v_mfma_f32_16x16x32_bf16 a[12:15], v[188:191], v[76:79], a[12:15]\n\t"
  "ds_read_b64_tr_b16 v[220:221], v226 offset:20992\n\t"
  "ds_read_b64_tr_b16 v[222:223], v226 offset:23040\n\t"
  "s_waitcnt vmcnt(39) lgkmcnt(8)\n\t"
  "v_mfma_f32_16x16x32_bf16 a[0:3], v[192:195], v[80:83], a[0:3]\n\t"
  "ds_read_b64_tr_b16 v[176:177], v225 offset:24576\n\t"
  "ds_read_b64_tr_b16 v[178:179], v225 offset:26624\n\t"
  "v_mfma_f32_16x16x32_bf16 a[4:7], v[196:199], v[80:83], a[4:7]\n\t"
  "ds_read_b64_tr_b16 v[180:181], v226 offset:24576\n\t"
  "ds_read_b64_tr_b16 v[182:183], v226 offset:26624\n\t"
  "v_mfma_f32_16x16x32_bf16 a[8:11], v[200:203], v[80:83], a[8:11]\n\t"
  "ds_read_b64_tr_b16 v[184:185], v225 offset:25088\n\t"
  "ds_read_b64_tr_b16 v[186:187], v225 offset:27136\n\t"
  "v_mfma_f32_16x16x32_bf16 a[12:15], v[204:207], v[80:83], a[12:15]\n\t"
  "ds_read_b64_tr_b16 v[188:189], v226 offset:25088\n\t"
  "ds_read_b64_tr_b16 v[190:191], v226 offset:27136\n\t"
  "s_waitcnt vmcnt(38) lgkmcnt(8)\n\t"
  "v_mfma_f32_16x16x32_bf16 a[0:3], v[208:211], v[84:87], a[0:3]\n\t"
  "ds_read_b64_tr_b16 v[192:193], v225 offset:28672\n\t"
  "ds_read_b64_tr_b16 v[194:195], v225 offset:30720\n\t"
  "v_mfma_f32_16x16x32_bf16 a[4:7], v[212:215], v[84:87], a[4:7]\n\t"
  "ds_read_b64_tr_b16 v[196:197], v226 offset:28672\n\t"
  "ds_read_b64_tr_b16 v[198:199], v226 offset:30720\n\t"
  "v_mfma_f32_16x16x32_bf16 a[8:11], v[216:219], v[84:87], a[8:11]\n\t"
  "ds_read_b64_tr_b16 v[200:201], v225 offset:29184\n\t"
  "ds_read_b64_tr_b16 v[202:203], v225 offset:31232\n\t"
  "v_mfma_f32_16x16x32_bf16 a[12:15], v[220:223], v[84:87], a[12:15]\n\t"
  "ds_read_b64_tr_b16 v[204:205], v226 offset:29184\n\t"
  "ds_read_b64_tr_b16 v[206:207], v226 offset:31232\n\t"
  "s_waitcnt vmcnt(37) lgkmcnt(8)\n\t"
  "v_mfma_f32_16x16x32_bf16 a[0:3], v[176:179], v[88:91], a[0:3]\n\t"
  "ds_read_b64_tr_b16 v[208:209], v225 offset:32768\n\t"
  "ds_read_b64_tr_b16 v[210:211], v225 offset:34816\n\t"
  "v_mfma_f32_16x16x32_bf16 a[4:7], v[180:183], v[88:91], a[4:7]\n\t"
  "ds_read_b64_tr_b16 v[212:213], v226 offset:32768\n\t"
  "ds_read_b64_tr_b16 v[214:215], v226 offset:34816\n\t"
  "v_mfma_f32_16x16x32_bf16 a[8:11], v[184:187], v[88:91], a[8:11]\n\t"
  "ds_read_b64_tr_b16 v[216:217], v225 offset:33280\n\t"
  "ds_read_b64_tr_b16 v[218:219], v225 offset:35328\n\t"
  "v_mfma_f32_16x16x32_bf16 a[12:15], v[188:191], v[88:91], a[12:15]\n\t"
  "ds_read_b64_tr_b16 v[220:221], v226 offset:33280\n\t"
  "ds_read_b64_tr_b16 v[222:223], v226 offset:35328\n\t"
  "s_waitcnt vmcnt(36) lgkmcnt(8)\n\t"
  "v_mfma_f32_16x16x32_bf16 a[0:3], v[192:195], v[92:95], a[0:3]\n\t"
  "ds_read_b64_tr_b16 v[176:177], v225 offset:36864\n\t"
  "ds_read_b64_tr_b16 v[178:179], v225 offset:38912\n\t"
  "v_mfma_f32_16x16x32_bf16 a[4:7], v[196:199], v[92:95], a[4:7]\n\t"
  "ds_read_b64_tr_b16 v[180:181], v226 offset:36864\n\t"
  "ds_read_b64_tr_b16 v[182:183], v226 offset:38912\n\t"
  "v_mfma_f32_16x16x32_bf16 a[8:11], v[200:203], v[92:95], a[8:11]\n\t"
  "ds_read_b64_tr_b16 v[184:185], v225 offset:37376\n\t"
  "ds_read_b64_tr_b16 v[186:187], v225 offset:39424\n\t"
  "v_mfma_f32_16x16x32_bf16 a[12:15], v[204:207], v[92:95], a[12:15]\n\t"
  "ds_read_b64_tr_b16 v[188:189], v226 offset:37376\n\t"
  "ds_read_b64_tr_b16 v[190:191], v226 offset:39424\n\t"
  "s_waitcnt vmcnt(35) lgkmcnt(8)\n\t"
  "v_mfma_f32_16x16x32_bf16 a[0:3], v[208:211], v[96:99], a[0:3]\n\t"
  "ds_read_b64_tr_b16 v[192:193], v225 offset:40960\n\t"
  "ds_read_b64_tr_b16 v[194:195], v225 offset:43008\n\t"
  "v_mfma_f32_16x16x32_bf16 a[4:7], v[212:215], v[96:99], a[4:7]\n\t"
  "ds_read_b64_tr_b16 v[196:197], v226 offset:40960\n\t"
  "ds_read_b64_tr_b16 v[198:199], v226 offset:43008\n\t"
  "v_mfma_f32_16x16x32_bf16 a[8:11], v[216:219], v[96:99], a[8:11]\n\t"
  "ds_read_b64_tr_b16 v[200:201], v225 offset:41472\n\t"
  "ds_read_b64_tr_b16 v[202:203], v225 offset:43520\n\t"
  "v_mfma_f32_16x16x32_bf16 a[12:15], v[220:223], v[96:99], a[12:15]\n\t"
  "ds_read_b64_tr_b16 v[204:205], v226 offset:41472\n\t"
  "ds_read_b64_tr_b16 v[206:207], v226 offset:43520\n\t"
  "s_waitcnt vmcnt(34) lgkmcnt(8)\n\t"
  "v_mfma_f32_16x16x32_bf16 a[0:3], v[176:179], v[100:103], a[0:3]\n\t"
  "ds_read_b64_tr_b16 v[208:209], v225 offset:45056\n\t"
  "ds_read_b64_tr_b16 v[210:211], v225 offset:47104\n\t"
  "v_mfma_f32_16x16x32_bf16 a[4:7], v[180:183], v[100:103], a[4:7]\n\t"
  "ds_read_b64_tr_b16 v[212:213], v226 offset:45056\n\t"
  "ds_read_b64_tr_b16 v[214:215], v226 offset:47104\n\t"
  "v_mfma_f32_16x16x32_bf16 a[8:11], v[184:187], v[100:103], a[8:11]\n\t"
  "ds_read_b64_tr_b16 v[216:217], v225 offset:45568\n\t"
  "ds_read_b64_tr_b16 v[218:219], v225 offset:47616\n\t"
  "v_mfma_f32_16x16x32_bf16 a[12:15], v[188:191], v[100:103], a[12:15]\n\t"
  "ds_read_b64_tr_b16 v[220:221], v226 offset:45568\n\t"
  "ds_read_b64_tr_b16 v[222:223], v226 offset:47616\n\t"
  "s_waitcnt vmcnt(33) lgkmcnt(8)\n\t"
  "v_mfma_f32_16x16x32_bf16 a[0:3], v[192:195], v[104:107], a[0:3]\n\t"
  "ds_read_b64_tr_b16 v[176:177], v225 offset:49152\n\t"
  "ds_read_b64_tr_b16 v[178:179], v225 offset:51200\n\t"
  "v_mfma_f32_16x16x32_bf16 a[4:7], v[196:199], v[104:107], a[4:7]\n\t"
  "ds_read_b64_tr_b16 v[180:181], v226 offset:49152\n\t"
  "ds_read_b64_tr_b16 v[182:183], v226 offset:51200\n\t"
  "v_mfma_f32_16x16x32_bf16 a[8:11], v[200:203], v[104:107], a[8:11]\n\t"
  "ds_read_b64_tr_b16 v[184:185], v225 offset:49664\n\t"
  "ds_read_b64_tr_b16 v[186:187], v225 offset:51712\n\t"
  "v_mfma_f32_16x16x32_bf16 a[12:15], v[204:207], v[104:107], a[12:15]\n\t"
  "ds_read_b64_tr_b16 v[188:189], v226 offset:49664\n\t"
  "ds_read_b64_tr_b16 v[190:191], v226 offset:51712\n\t"
  "s_waitcnt vmcnt(32) lgkmcnt(8)\n\t"
  "v_mfma_f32_16x16x32_bf16 a[0:3], v[208:211], v[108:111], a[0:3]\n\t"
  "ds_read_b64_tr_b16 v[192:193], v225 offset:53248\n\t"
  "ds_read_b64_tr_b16 v[194:195], v225 offset:55296\n\t"
  "v_mfma_f32_16x16x32_bf16 a[4:7], v[212:215], v[108:111], a[4:7]\n\t"
  "ds_read_b64_tr_b16 v[196:197], v226 offset:53248\n\t"
  "ds_read_b64_tr_b16 v[198:199], v226 offset:55296\n\t"
  "v_mfma_f32_16x16x32_bf16 a[8:11], v[216:219], v[108:111], a[8:11]\n\t"
  "ds_read_b64_tr_b16 v[200:201], v225 offset:53760\n\t"
  "ds_read_b64_tr_b16 v[202:203], v225 offset:55808\n\t"
  "v_mfma_f32_16x16x32_bf16 a[12:15], v[220:223], v[108:111], a[12:15]\n\t"
  "ds_read_b64_tr_b16 v[204:205], v226 offset:53760\n\t"
  "ds_read_b64_tr_b16 v[206:207], v226 offset:55808\n\t"
  "s_waitcnt vmcnt(31) lgkmcnt(8)\n\t"
  "v_mfma_f32_16x16x32_bf16 a[0:3], v[176:179], v[112:115], a[0:3]\n\t"
  "ds_read_b64_tr_b16 v[208:209], v225 offset:57344\n\t"
  "ds_read_b64_tr_b16 v[210:211], v225 offset:59392\n\t"
  "v_mfma_f32_16x16x32_bf16 a[4:7], v[180:183], v[112:115], a[4:7]\n\t"
  "ds_read_b64_tr_b16 v[212:213], v226 offset:57344\n\t"
  "ds_read_b64_tr_b16 v[214:215], v226 offset:59392\n\t"
  "v_mfma_f32_16x16x32_bf16 a[8:11], v[184:187], v[112:115], a[8:11]\n\t"
  "ds_read_b64_tr_b16 v[216:217], v225 offset:57856\n\t"
  "ds_read_b64_tr_b16 v[218:219], v225 offset:59904\n\t"
  "v_mfma_f32_16x16x32_bf16 a[12:15], v[188:191], v[112:115], a[12:15]\n\t"
  "ds_read_b64_tr_b16 v[220:221], v226 offset:57856\n\t"
  "ds_read_b64_tr_b16 v[222:223], v226 offset:59904\n\t"
  "s_waitcnt vmcnt(30) lgkmcnt(8)\n\t"
  "v_mfma_f32_16x16x32_bf16 a[0:3], v[192:195], v[116:119], a[0:3]\n\t"
  "ds_read_b64_tr_b16 v[176:177], v225 offset:61440\n\t"
  "ds_read_b64_tr_b16 v[178:179], v225 offset:63488\n\t"
  "v_mfma_f32_16x16x32_bf16 a[4:7], v[196:199], v[116:119], a[4:7]\n\t"
  "ds_read_b64_tr_b16 v[180:181], v226 offset:61440\n\t"
  "ds_read_b64_tr_b16 v[182:183], v226 offset:63488\n\t"
  "v_mfma_f32_16x16x32_bf16 a[8:11], v[200:203], v[116:119], a[8:11]\n\t"
  "ds_read_b64_tr_b16 v[184:185], v225 offset:61952\n\t"
  "ds_read_b64_tr_b16 v[186:187], v225 offset:64000\n\t"
  "v_mfma_f32_16x16x32_bf16 a[12:15], v[204:207], v[116:119], a[12:15]\n\t"
  "ds_read_b64_tr_b16 v[188:189], v226 offset:61952\n\t"
  "ds_read_b64_tr_b16 v[190:191], v226 offset:64000\n\t"
  "s_waitcnt vmcnt(29) lgkmcnt(8)\n\t"
  "v_mfma_f32_16x16x32_bf16 a[0:3], v[208:211], v[120:123], a[0:3]\n\t"
  "ds_read_b64_tr_b16 v[192:193], v227 offset:0\n\t"
  "ds_read_b64_tr_b16 v[194:195], v227 offset:2048\n\t"
  "v_mfma_f32_16x16x32_bf16 a[4:7], v[212:215], v[120:123], a[4:7]\n\t"
  "ds_read_b64_tr_b16 v[196:197], v228 offset:0\n\t"
  "ds_read_b64_tr_b16 v[198:199], v228 offset:2048\n\t"
  "v_mfma_f32_16x16x32_bf16 a[8:11], v[216:219], v[120:123], a[8:11]\n\t"
  "ds_read_b64_tr_b16 v[200:201], v227 offset:512\n\t"
  "ds_read_b64_tr_b16 v[202:203], v227 offset:2560\n\t"
  "v_mfma_f32_16x16x32_bf16 a[12:15], v[220:223], v[120:123], a[12:15]\n\t"
  "ds_read_b64_tr_b16 v[204:205], v228 offset:512\n\t"
  "ds_read_b64_tr_b16 v[206:207], v228 offset:2560\n\t"
  "s_waitcnt vmcnt(28) lgkmcnt(8)\n\t"
  "v_mfma_f32_16x16x32_bf16 a[0:3], v[176:179], v[124:127], a[0:3]\n\t"
  "ds_read_b64_tr_b16 v[208:209], v227 offset:4096\n\t"
  "ds_read_b64_tr_b16 v[210:211], v227 offset:6144\n\t"
  "v_mfma_f32_16x16x32_bf16 a[4:7], v[180:183], v[124:127], a[4:7]\n\t"
  "ds_read_b64_tr_b16 v[212:213], v228 offset:4096\n\t"
  "ds_read_b64_tr_b16 v[214:215], v228 offset:6144\n\t"
  "v_mfma_f32_16x16x32_bf16 a[8:11], v[184:187], v[124:127], a[8:11]\n\t"
  "ds_read_b64_tr_b16 v[216:217], v227 offset:4608\n\t"
  "ds_read_b64_tr_b16 v[218:219], v227 offset:6656\n\t"
  "v_mfma_f32_16x16x32_bf16 a[12:15], v[188:191], v[124:127], a[12:15]\n\t"
  "ds_read_b64_tr_b16 v[220:221], v228 offset:4608\n\t"
  "ds_read_b64_tr_b16 v[222:223], v228 offset:6656\n\t"
  "s_waitcnt vmcnt(27) lgkmcnt(8)\n\t"
  "v_mfma_f32_16x16x32_bf16 a[0:3], v[192:195], v[128:131], a[0:3]\n\t"
  "ds_read_b64_tr_b16 v[176:177], v227 offset:8192\n\t"
  "ds_read_b64_tr_b16 v[178:179], v227 offset:10240\n\t"
  "v_mfma_f32_16x16x32_bf16 a[4:7], v[196:199], v[128:131], a[4:7]\n\t"
  "ds_read_b64_tr_b16 v[180:181], v228 offset:8192\n\t"
  "ds_read_b64_tr_b16 v[182:183], v228 offset:10240\n\t"
  "v_mfma_f32_16x16x32_bf16 a[8:11], v[200:203], v[128:131], a[8:11]\n\t"
  "ds_read_b64_tr_b16 v[184:185], v227 offset:8704\n\t"
  "ds_read_b64_tr_b16 v[186:187], v227 offset:10752\n\t"
  "v_mfma_f32_16x16x32_bf16 a[12:15], v[204:207], v[128:131], a[12:15]\n\t"
  "ds_read_b64_tr_b16 v[188:189], v228 offset:8704\n\t"
  "ds_read_b64_tr_b16 v[190:191], v228 offset:10752\n\t"
  "s_waitcnt vmcnt(26) lgkmcnt(8)\n\t"
  "v_mfma_f32_16x16x32_bf16 a[0:3], v[208:211], v[132:135], a[0:3]\n\t"
  "ds_read_b64_tr_b16 v[192:193], v227 offset:12288\n\t"
  "ds_read_b64_tr_b16 v[194:195], v227 offset:14336\n\t"
  "v_mfma_f32_16x16x32_bf16 a[4:7], v[212:215], v[132:135], a[4:7]\n\t"
  "ds_read_b64_tr_b16 v[196:197], v228 offset:12288\n\t"
  "ds_read_b64_tr_b16 v[198:199], v228 offset:14336\n\t"
  "v_mfma_f32_16x16x32_bf16 a[8:11], v[216:219], v[132:135], a[8:11]\n\t"
  "ds_read_b64_tr_b16 v[200:201], v227 offset:12800\n\t"
  "ds_read_b64_tr_b16 v[202:203], v227 offset:14848\n\t"
  "v_mfma_f32_16x16x32_bf16 a[12:15], v[220:223], v[132:135], a[12:15]\n\t"
  "ds_read_b64_tr_b16 v[204:205], v228 offset:12800\n\t"
  "ds_read_b64_tr_b16 v[206:207], v228 offset:14848\n\t"
  "s_waitcnt vmcnt(25) lgkmcnt(8)\n\t"
  "v_mfma_f32_16x16x32_bf16 a[0:3], v[176:179], v[136:139], a[0:3]\n\t"
  "ds_read_b64_tr_b16 v[208:209], v227 offset:16384\n\t"
  "ds_read_b64_tr_b16 v[210:211], v227 offset:18432\n\t"
  "v_mfma_f32_16x16x32_bf16 a[4:7], v[180:183], v[136:139], a[4:7]\n\t"
  "ds_read_b64_tr_b16 v[212:213], v228 offset:16384\n\t"
  "ds_read_b64_tr_b16 v[214:215], v228 offset:18432\n\t"
  "v_mfma_f32_16x16x32_bf16 a[8:11], v[184:187], v[136:139], a[8:11]\n\t"
  "ds_read_b64_tr_b16 v[216:217], v227 offset:16896\n\t"
  "ds_read_b64_tr_b16 v[218:219], v227 offset:18944\n\t"
  "v_mfma_f32_16x16x32_bf16 a[12:15], v[188:191], v[136:139], a[12:15]\n\t"
  "ds_read_b64_tr_b16 v[220:221], v228 offset:16896\n\t"
  "ds_read_b64_tr_b16 v[222:223], v228 offset:18944\n\t"
  "s_waitcnt vmcnt(24) lgkmcnt(8)\n\t"
  "v_mfma_f32_16x16x32_bf16 a[0:3], v[192:195], v[140:143], a[0:3]\n\t"
  "ds_read_b64_tr_b16 v[176:177], v227 offset:20480\n\t"
  "ds_read_b64_tr_b16 v[178:179], v227 offset:22528\n\t"
  "v_mfma_f32_16x16x32_bf16 a[4:7], v[196:199], v[140:143], a[4:7]\n\t"
  "ds_read_b64_tr_b16 v[180:181], v228 offset:20480\n\t"
  "ds_read_b64_tr_b16 v[182:183], v228 offset:22528\n\t"
  "v_mfma_f32_16x16x32_bf16 a[8:11], v[200:203], v[140:143], a[8:11]\n\t"
  "ds_read_b64_tr_b16 v[184:185], v227 offset:20992\n\t"
  "ds_read_b64_tr_b16 v[186:187], v227 offset:23040\n\t"
  "v_mfma_f32_16x16x32_bf16 a[12:15], v[204:207], v[140:143], a[12:15]\n\t"
  "ds_read_b64_tr_b16 v[188:189], v228 offset:20992\n\t"
  "ds_read_b64_tr_b16 v[190:191], v228 offset:23040\n\t"
  "s_waitcnt vmcnt(23) lgkmcnt(8)\n\t"
  "v_mfma_f32_16x16x32_bf16 a[0:3], v[208:211], v[144:147], a[0:3]\n\t"
  "ds_read_b64_tr_b16 v[192:193], v227 offset:24576\n\t"
  "ds_read_b64_tr_b16 v[194:195], v227 offset:26624\n\t"
  "v_mfma_f32_16x16x32_bf16 a[4:7], v[212:215], v[144:147], a[4:7]\n\t"
  "ds_read_b64_tr_b16 v[196:197], v228 offset:24576\n\t"
  "ds_read_b64_tr_b16 v[198:199], v228 offset:26624\n\t"
  "v_mfma_f32_16x16x32_bf16 a[8:11], v[216:219], v[144:147], a[8:11]\n\t"
  "ds_read_b64_tr_b16 v[200:201], v227 offset:25088\n\t"
  "ds_read_b64_tr_b16 v[202:203], v227 offset:27136\n\t"
  "v_mfma_f32_16x16x32_bf16 a[12:15], v[220:223], v[144:147], a[12:15]\n\t"
  "ds_read_b64_tr_b16 v[204:205], v228 offset:25088\n\t"
  "ds_read_b64_tr_b16 v[206:207], v228 offset:27136\n\t"
  "s_waitcnt vmcnt(22) lgkmcnt(8)\n\t"
  "v_mfma_f32_16x16x32_bf16 a[0:3], v[176:179], v[148:151], a[0:3]\n\t"
  "ds_read_b64_tr_b16 v[208:209], v227 offset:28672\n\t"
  "ds_read_b64_tr_b16 v[210:211], v227 offset:30720\n\t"
  "v_mfma_f32_16x16x32_bf16 a[4:7], v[180:183], v[148:151], a[4:7]\n\t"
  "ds_read_b64_tr_b16 v[212:213], v228 offset:28672\n\t"
  "ds_read_b64_tr_b16 v[214:215], v228 offset:30720\n\t"
  "v_mfma_f32_16x16x32_bf16 a[8:11], v[184:187], v[148:151], a[8:11]\n\t"
  "ds_read_b64_tr_b16 v[216:217], v227 offset:29184\n\t"
  "ds_read_b64_tr_b16 v[218:219], v227 offset:31232\n\t"
  "v_mfma_f32_16x16x32_bf16 a[12:15], v[188:191], v[148:151], a[12:15]\n\t"
  "ds_read_b64_tr_b16 v[220:221], v228 offset:29184\n\t"
  "ds_read_b64_tr_b16 v[222:223], v228 offset:31232\n\t"
  "s_waitcnt vmcnt(21) lgkmcnt(8)\n\t"
  "v_mfma_f32_16x16x32_bf16 a[0:3], v[192:195], v[152:155], a[0:3]\n\t"
  "ds_read_b64_tr_b16 v[176:177], v227 offset:32768\n\t"
  "ds_read_b64_tr_b16 v[178:179], v227 offset:34816\n\t"
  "v_mfma_f32_16x16x32_bf16 a[4:7], v[196:199], v[152:155], a[4:7]\n\t"
  "ds_read_b64_tr_b16 v[180:181], v228 offset:32768\n\t"
  "ds_read_b64_tr_b16 v[182:183], v228 offset:34816\n\t"
  "v_mfma_f32_16x16x32_bf16 a[8:11], v[200:203], v[152:155], a[8:11]\n\t"
  "ds_read_b64_tr_b16 v[184:185], v227 offset:33280\n\t"
  "ds_read_b64_tr_b16 v[186:187], v227 offset:35328\n\t"
  "v_mfma_f32_16x16x32_bf16 a[12:15], v[204:207], v[152:155], a[12:15]\n\t"
  "ds_read_b64_tr_b16 v[188:189], v228 offset:33280\n\t"
  "ds_read_b64_tr_b16 v[190:191], v228 offset:35328\n\t"
  "s_waitcnt vmcnt(20) lgkmcnt(8)\n\t"
  "v_mfma_f32_16x16x32_bf16 a[0:3], v[208:211], v[156:159], a[0:3]\n\t"
  "ds_read_b64_tr_b16 v[192:193], v227 offset:36864\n\t"
  "ds_read_b64_tr_b16 v[194:195], v227 offset:38912\n\t"
  "v_mfma_f32_16x16x32_bf16 a[4:7], v[212:215], v[156:159], a[4:7]\n\t"
  "ds_read_b64_tr_b16 v[196:197], v228 offset:36864\n\t"
  "ds_read_b64_tr_b16 v[198:199], v228 offset:38912\n\t"
  "v_mfma_f32_16x16x32_bf16 a[8:11], v[216:219], v[156:159], a[8:11]\n\t"
  "ds_read_b64_tr_b16 v[200:201], v227 offset:37376\n\t"
  "ds_read_b64_tr_b16 v[202:203], v227 offset:39424\n\t"
  "v_mfma_f32_16x16x32_bf16 a[12:15], v[220:223], v[156:159], a[12:15]\n\t"
  "ds_read_b64_tr_b16 v[204:205], v228 offset:37376\n\t"
  "ds_read_b64_tr_b16 v[206:207], v228 offset:39424\n\t"
  "s_waitcnt vmcnt(19) lgkmcnt(8)\n\t"
  "v_mfma_f32_16x16x32_bf16 a[0:3], v[176:179], v[160:163], a[0:3]\n\t"
  "ds_read_b64_tr_b16 v[208:209], v227 offset:40960\n\t"
  "ds_read_b64_tr_b16 v[210:211], v227 offset:43008\n\t"
  "v_mfma_f32_16x16x32_bf16 a[4:7], v[180:183], v[160:163], a[4:7]\n\t"
  "ds_read_b64_tr_b16 v[212:213], v228 offset:40960\n\t"
  "ds_read_b64_tr_b16 v[214:215], v228 offset:43008\n\t"
  "v_mfma_f32_16x16x32_bf16 a[8:11], v[184:187], v[160:163], a[8:11]\n\t"
  "ds_read_b64_tr_b16 v[216:217], v227 offset:41472\n\t"
  "ds_read_b64_tr_b16 v[218:219], v227 offset:43520\n\t"
  "v_mfma_f32_16x16x32_bf16 a[12:15], v[188:191], v[160:163], a[12:15]\n\t"
  "ds_read_b64_tr_b16 v[220:221], v228 offset:41472\n\t"
  "ds_read_b64_tr_b16 v[222:223], v228 offset:43520\n\t"
  "s_waitcnt vmcnt(18) lgkmcnt(8)\n\t"
  "v_mfma_f32_16x16x32_bf16 a[0:3], v[192:195], v[164:167], a[0:3]\n\t"
  "ds_read_b64_tr_b16 v[176:177], v227 offset:45056\n\t"
  "ds_read_b64_tr_b16 v[178:179], v227 offset:47104\n\t"
  "v_mfma_f32_16x16x32_bf16 a[4:7], v[196:199], v[164:167], a[4:7]\n\t"
  "ds_read_b64_tr_b16 v[180:181], v228 offset:45056\n\t"
  "ds_read_b64_tr_b16 v[182:183], v228 offset:47104\n\t"
  "v_mfma_f32_16x16x32_bf16 a[8:11], v[200:203], v[164:167], a[8:11]\n\t"
  "ds_read_b64_tr_b16 v[184:185], v227 offset:45568\n\t"
  "ds_read_b64_tr_b16 v[186:187], v227 offset:47616\n\t"
  "v_mfma_f32_16x16x32_bf16 a[12:15], v[204:207], v[164:167], a[12:15]\n\t"
  "ds_read_b64_tr_b16 v[188:189], v228 offset:45568\n\t"
  "ds_read_b64_tr_b16 v[190:191], v228 offset:47616\n\t"
  "s_waitcnt vmcnt(17) lgkmcnt(8)\n\t"
  "v_mfma_f32_16x16x32_bf16 a[0:3], v[208:211], v[168:171], a[0:3]\n\t"
  "v_mfma_f32_16x16x32_bf16 a[4:7], v[212:215], v[168:171], a[4:7]\n\t"
  "v_mfma_f32_16x16x32_bf16 a[8:11], v[216:219], v[168:171], a[8:11]\n\t"
  "v_mfma_f32_16x16x32_bf16 a[12:15], v[220:223], v[168:171], a[12:15]\n\t"
  "s_waitcnt vmcnt(16) lgkmcnt(0)\n\t"
  "v_mfma_f32_16x16x32_bf16 a[0:3], v[176:179], v[172:175], a[0:3]\n\t"
  "v_mfma_f32_16x16x32_bf16 a[4:7], v[180:183], v[172:175], a[4:7]\n\t"
  "v_mfma_f32_16x16x32_bf16 a[8:11], v[184:187], v[172:175], a[8:11]\n\t"
  "v_mfma_f32_16x16x32_bf16 a[12:15], v[188:191], v[172:175], a[12:15]\n\t"
  "s_waitcnt vmcnt(14)\n\t"
  "v_mfma_f32_16x16x32_bf16 a[16:19], %[sel0], a[192:195], a[16:19]\n\t"
  "v_mfma_f32_16x16x32_bf16 a[20:23], %[sel1], a[192:195], a[20:23]\n\t"
  "v_mfma_f32_16x16x32_bf16 a[24:27], %[sel0], a[196:199], a[24:27]\n\t"
  "v_mfma_f32_16x16x32_bf16 a[28:31], %[sel1], a[196:199], a[28:31]\n\t"
  "s_waitcnt vmcnt(12)\n\t"
  "v_mfma_f32_16x16x32_bf16 a[16:19], %[sel0], a[200:203], a[16:19]\n\t"
  "v_mfma_f32_16x16x32_bf16 a[20:23], %[sel1], a[200:203], a[20:23]\n\t"
  "v_mfma_f32_16x16x32_bf16 a[24:27], %[sel0], a[204:207], a[24:27]\n\t"
  "v_mfma_f32_16x16x32_bf16 a[28:31], %[sel1], a[204:207], a[28:31]\n\t"
  "s_waitcnt vmcnt(10)\n\t"
  "v_mfma_f32_16x16x32_bf16 a[16:19], %[sel0], a[208:211], a[16:19]\n\t"
  "v_mfma_f32_16x16x32_bf16 a[20:23], %[sel1], a[208:211], a[20:23]\n\t"
  "v_mfma_f32_16x16x32_bf16 a[24:27], %[sel0], a[212:215], a[24:27]\n\t"
  "v_mfma_f32_16x16x32_bf16 a[28:31], %[sel1], a[212:215], a[28:31]\n\t"
  "s_waitcnt vmcnt(8)\n\t"
  "v_mfma_f32_16x16x32_bf16 a[16:19], %[sel0], a[216:219], a[16:19]\n\t"
  "v_mfma_f32_16x16x32_bf16 a[20:23], %[sel1], a[216:219], a[20:23]\n\t"
  "v_mfma_f32_16x16x32_bf16 a[24:27], %[sel0], a[220:223], a[24:27]\n\t"
  "v_mfma_f32_16x16x32_bf16 a[28:31], %[sel1], a[220:223], a[28:31]\n\t"
  "s_waitcnt vmcnt(6)\n\t"
  "v_mfma_f32_16x16x32_bf16 a[16:19], %[sel0], a[224:227], a[16:19]\n\t"
  "v_mfma_f32_16x16x32_bf16 a[20:23], %[sel1], a[224:227], a[20:23]\n\t"
  "v_mfma_f32_16x16x32_bf16 a[24:27], %[sel0], a[228:231], a[24:27]\n\t"
  "v_mfma_f32_16x16x32_bf16 a[28:31], %[sel1], a[228:231], a[28:31]\n\t"
  "s_waitcnt vmcnt(4)\n\t"
  "v_mfma_f32_16x16x32_bf16 a[16:19], %[sel0], a[232:235], a[16:19]\n\t"
  "v_mfma_f32_16x16x32_bf16 a[20:23], %[sel1], a[232:235], a[20:23]\n\t"
  "v_mfma_f32_16x16x32_bf16 a[24:27], %[sel0], a[236:239], a[24:27]\n\t"
  "v_mfma_f32_16x16x32_bf16 a[28:31], %[sel1], a[236:239], a[28:31]\n\t"
  "s_waitcnt vmcnt(2)\n\t"
  "v_mfma_f32_16x16x32_bf16 a[16:19], %[sel0], a[240:243], a[16:19]\n\t"
  "v_mfma_f32_16x16x32_bf16 a[20:23], %[sel1], a[240:243], a[20:23]\n\t"
  "v_mfma_f32_16x16x32_bf16 a[24:27], %[sel0], a[244:247], a[24:27]\n\t"
  "v_mfma_f32_16x16x32_bf16 a[28:31], %[sel1], a[244:247], a[28:31]\n\t"
  "s_waitcnt vmcnt(0)\n\t"
  "v_mfma_f32_16x16x32_bf16 a[16:19], %[sel0], a[248:251], a[16:19]\n\t"
  "v_mfma_f32_16x16x32_bf16 a[20:23], %[sel1], a[248:251], a[20:23]\n\t"
  "v_mfma_f32_16x16x32_bf16 a[24:27], %[sel0], a[252:255], a[24:27]\n\t"
  "v_mfma_f32_16x16x32_bf16 a[28:31], %[sel1], a[252:255], a[28:31]\n\t"
  "s_nop 7\n\t"
  "s_branch .Lpend_%=\n\t"
  ".Lnos2_%=:\n\t"
  "s_barrier\n\t"
  "ds_read_b64_tr_b16 v[128:129], v229 offset:0\n\t"
  "ds_read_b64_tr_b16 v[130:131], v229 offset:2048\n\t"
  "ds_read_b64_tr_b16 v[132:133], v230 offset:0\n\t"
  "ds_read_b64_tr_b16 v[134:135], v230 offset:2048\n\t"
  "ds_read_b64_tr_b16 v[136:137], v229 offset:512\n\t"
  "ds_read_b64_tr_b16 v[138:139], v229 offset:2560\n\t"
  "ds_read_b64_tr_b16 v[140:141], v230 offset:512\n\t"
  "ds_read_b64_tr_b16 v[142:143], v230 offset:2560\n\t"
  "ds_read_b64_tr_b16 v[144:145], v229 offset:4096\n\t"
  "ds_read_b64_tr_b16 v[146:147], v229 offset:6144\n\t"
  "ds_read_b64_tr_b16 v[148:149], v230 offset:4096\n\t"
  "ds_read_b64_tr_b16 v[150:151], v230 offset:6144\n\t"
  "ds_read_b64_tr_b16 v[152:153], v229 offset:4608\n\t"
  "ds_read_b64_tr_b16 v[154:155], v229 offset:6656\n\t"
  "ds_read_b64_tr_b16 v[156:157], v230 offset:4608\n\t"
  "ds_read_b64_tr_b16 v[158:159], v230 offset:6656\n\t"
  "ds_read_b64_tr_b16 v[160:161], v229 offset:8192\n\t"
  "ds_read_b64_tr_b16 v[162:163], v229 offset:10240\n\t"
  "ds_read_b64_tr_b16 v[164:165], v230 offset:8192\n\t"
  "ds_read_b64_tr_b16 v[166:167], v230 offset:10240\n\t"
  "s_waitcnt lgkmcnt(14)\n\t"
  "v_mfma_f32_16x16x32_bf16 a[0:3], v[128:131], v[64:67], 0\n\t"
  "ds_read_b64_tr_b16 v[168:169], v229 offset:8704\n\t"
  "ds_read_b64_tr_b16 v[170:171], v229 offset:10752\n\t"
  "v_mfma_f32_16x16x32_bf16 a[4:7], v[128:131], v[96:99], 0\n\t"
  "s_add_u32 m0, %[m0o], 65536\n\t"
  "s_nop 0\n\t"
  "buffer_load_dwordx4 v224, %[srdo], 0 offen offset:0 lds\n\t"
  "v_mfma_f32_16x16x32_bf16 a[8:11], v[132:135], v[64:67], 0\n\t"
  "ds_read_b64_tr_b16 v[172:173], v230 offset:8704\n\t"
  "ds_read_b64_tr_b16 v[174:175], v230 offset:10752\n\t"
  "v_mfma_f32_16x16x32_bf16 a[12:15], v[132:135], v[96:99], 0\n\t"
  "buffer_load_dwordx4 v224, %[srdo], %[c6144] offen offset:1024 lds\n\t"
  "v_mfma_f32_16x16x32_bf16 a[16:19], v[136:139], v[64:67], 0\n\t"
  "ds_read_b64_tr_b16 v[176:177], v229 offset:12288\n\t"
  "ds_read_b64_tr_b16 v[178:179], v229 offset:14336\n\t"
  "v_mfma_f32_16x16x32_bf16 a[20:23], v[136:139], v[96:99], 0\n\t"
  "buffer_load_dwordx4 v224, %[srdo], %[c12288] offen offset:2048 lds\n\t"
  "buffer_load_dwordx4 v224, %[srdo], %[c18432] offen offset:3072 lds\n\t"
  "s_add_u32 m0, %[m0o], 69632\n\t"
  "s_waitcnt lgkmcnt(14)\n\t"
  "v_mfma_f32_16x16x32_bf16 a[24:27], v[140:143], v[64:67], 0\n\t"
  "ds_read_b64_tr_b16 v[180:181], v230 offset:12288\n\t"
  "ds_read_b64_tr_b16 v[182:183], v230 offset:14336\n\t"
  "v_mfma_f32_16x16x32_bf16 a[28:31], v[140:143], v[96:99], 0\n\t"
  "buffer_load_dwordx4 v224, %[srdo], %[c28672] offen offset:0 lds\n\t"
  "v_mfma_f32_16x16x32_bf16 a[0:3], v[144:147], v[68:71], a[0:3]\n\t"
  "ds_read_b64_tr_b16 v[184:185], v229 offset:12800\n\t"
  "ds_read_b64_tr_b16 v[186:187], v229 offset:14848\n\t"
  "v_mfma_f32_16x16x32_bf16 a[4:7], v[144:147], v[100:103], a[4:7]\n\t"
  "buffer_load_dwordx4 v224, %[srdo], %[c34816] offen offset:1024 lds\n\t"
  "buffer_load_dwordx4 v224, %[srdo], %[c40960] offen offset:2048 lds\n\t"
  "v_mfma_f32_16x16x32_bf16 a[8:11], v[148:151], v[68:71], a[8:11]\n\t"
  "ds_read_b64_tr_b16 v[188:189], v230 offset:12800\n\t"
  "ds_read_b64_tr_b16 v[190:191], v230 offset:14848\n\t"
  "v_mfma_f32_16x16x32_bf16 a[12:15], v[148:151], v[100:103], a[12:15]\n\t"
  "buffer_load_dwordx4 v224, %[srdo], %[c47104] offen offset:3072 lds\n\t"
  "v_add_u32 v224, 0x80, v224\n\t"
  "s_add_u32 m0, %[m0o], 98304\n\t"
  "s_waitcnt lgkmcnt(14)\n\t"
  "v_mfma_f32_16x16x32_bf16 a[16:19], v[152:155], v[68:71], a[16:19]\n\t"
  "ds_read_b64_tr_b16 v[128:129], v229 offset:16384\n\t"
  "ds_read_b64_tr_b16 v[130:131], v229 offset:18432\n\t"
  "v_mfma_f32_16x16x32_bf16 a[20:23], v[152:155], v[100:103], a[20:23]\n\t"
  "buffer_load_dwordx4 v224, %[srdo], 0 offen offset:0 lds\n\t"
  "buffer_load_dwordx4 v224, %[srdo], %[c6144] offen offset:1024 lds\n\t"
  "v_mfma_f32_16x16x32_bf16 a[24:27], v[156:159], v[68:71], a[24:27]\n\t"
  "ds_read_b64_tr_b16 v[132:133], v230 offset:16384\n\t"
  "ds_read_b64_tr_b16 v[134:135], v230 offset:18432\n\t"
  "v_mfma_f32_16x16x32_bf16 a[28:31], v[156:159], v[100:103], a[28:31]\n\t"
  "buffer_load_dwordx4 v224, %[srdo], %[c12288] offen offset:2048 lds\n\t"
  "v_mfma_f32_16x16x32_bf16 a[0:3], v[160:163], v[72:75], a[0:3]\n\t"
  "ds_read_b64_tr_b16 v[136:137], v229 offset:16896\n\t"
  "ds_read_b64_tr_b16 v[138:139], v229 offset:18944\n\t"
  "v_mfma_f32_16x16x32_bf16 a[4:7], v[160:163], v[104:107], a[4:7]\n\t"
  "buffer_load_dwordx4 v224, %[srdo], %[c18432] offen offset:3072 lds\n\t"
  "s_add_u32 m0, %[m0o], 102400\n\t"
  "s_nop 0\n\t"
  "buffer_load_dwordx4 v224, %[srdo], %[c28672] offen offset:0 lds\n\t"
  "s_waitcnt lgkmcnt(14)\n\t"
  "v_mfma_f32_16x16x32_bf16 a[8:11], v[164:167], v[72:75], a[8:11]\n\t"
  "ds_read_b64_tr_b16 v[140:141], v230 offset:16896\n\t"
  "ds_read_b64_tr_b16 v[142:143], v230 offset:18944\n\t"
  "v_mfma_f32_16x16x32_bf16 a[12:15], v[164:167], v[104:107], a[12:15]\n\t"
  "buffer_load_dwordx4 v224, %[srdo], %[c34816] offen offset:1024 lds\n\t"
  "v_mfma_f32_16x16x32_bf16 a[16:19], v[168:171], v[72:75], a[16:19]\n\t"
  "ds_read_b64_tr_b16 v[144:145], v229 offset:20480\n\t"
  "ds_read_b64_tr_b16 v[146:147], v229 offset:22528\n\t"
  "v_mfma_f32_16x16x32_bf16 a[20:23], v[168:171], v[104:107], a[20:23]\n\t"
  "buffer_load_dwordx4 v224, %[srdo], %[c40960] offen offset:2048 lds\n\t"
  "buffer_load_dwordx4 v224, %[srdo], %[c47104] offen offset:3072 lds\n\t"
  "v_add_u32 v224, 0x80, v224\n\t"
  "v_mfma_f32_16x16x32_bf16 a[24:27], v[172:175], v[72:75], a[24:27]\n\t"
  "ds_read_b64_tr_b16 v[148:149], v230 offset:20480\n\t"
  "ds_read_b64_tr_b16 v[150:151], v230 offset:22528\n\t"
  "v_mfma_f32_16x16x32_bf16 a[28:31], v[172:175], v[104:107], a[28:31]\n\t"
  "s_waitcnt lgkmcnt(14)\n\t"
  "v_mfma_f32_16x16x32_bf16 a[0:3], v[176:179], v[76:79], a[0:3]\n\t"
  "ds_read_b64_tr_b16 v[152:153], v229 offset:20992\n\t"
  "ds_read_b64_tr_b16 v[154:155], v229 offset:23040\n\t"
  "v_mfma_f32_16x16x32_bf16 a[4:7], v[176:179], v[108:111], a[4:7]\n\t"
  "v_mfma_f32_16x16x32_bf16 a[8:11], v[180:183], v[76:79], a[8:11]\n\t"
  "ds_read_b64_tr_b16 v[156:157], v230 offset:20992\n\t"
  "ds_read_b64_tr_b16 v[158:159], v230 offset:23040\n\t"
  "v_mfma_f32_16x16x32_bf16 a[12:15], v[180:183], v[108:111], a[12:15]\n\t"
  "v_mfma_f32_16x16x32_bf16 a[16:19], v[184:187], v[76:79], a[16:19]\n\t"
  "ds_read_b64_tr_b16 v[160:161], v229 offset:24576\n\t"
  "ds_read_b64_tr_b16 v[162:163], v229 offset:26624\n\t"
  "v_mfma_f32_16x16x32_bf16 a[20:23], v[184:187], v[108:111], a[20:23]\n\t"
  "s_waitcnt lgkmcnt(14)\n\t"
  "v_mfma_f32_16x16x32_bf16 a[24:27], v[188:191], v[76:79], a[24:27]\n\t"
  "ds_read_b64_tr_b16 v[164:165], v230 offset:24576\n\t"
  "ds_read_b64_tr_b16 v[166:167], v230 offset:26624\n\t"
  "v_mfma_f32_16x16x32_bf16 a[28:31], v[188:191], v[108:111], a[28:31]\n\t"
  "v_mfma_f32_16x16x32_bf16 a[0:3], v[128:131], v[80:83], a[0:3]\n\t"
  "ds_read_b64_tr_b16 v[168:169], v229 offset:25088\n\t"
  "ds_read_b64_tr_b16 v[170:171], v229 offset:27136\n\t"
  "v_mfma_f32_16x16x32_bf16 a[4:7], v[128:131], v[112:115], a[4:7]\n\t"
  "v_mfma_f32_16x16x32_bf16 a[8:11], v[132:135], v[80:83], a[8:11]\n\t"
  "ds_read_b64_tr_b16 v[172:173], v230 offset:25088\n\t"
  "ds_read_b64_tr_b16 v[174:175], v230 offset:27136\n\t"
  "v_mfma_f32_16x16x32_bf16 a[12:15], v[132:135], v[112:115], a[12:15]\n\t"
  "s_waitcnt lgkmcnt(14)\n\t"
  "v_mfma_f32_16x16x32_bf16 a[16:19], v[136:139], v[80:83], a[16:19]\n\t"
  "ds_read_b64_tr_b16 v[176:177], v229 offset:28672\n\t"
  "ds_read_b64_tr_b16 v[178:179], v229 offset:30720\n\t"
  "v_mfma_f32_16x16x32_bf16 a[20:23], v[136:139], v[112:115], a[20:23]\n\t"
  "v_mfma_f32_16x16x32_bf16 a[24:27], v[140:143], v[80:83], a[24:27]\n\t"
  "ds_read_b64_tr_b16 v[180:181], v230 offset:28672\n\t"
  "ds_read_b64_tr_b16 v[182:183], v230 offset:30720\n\t"
  "v_mfma_f32_16x16x32_bf16 a[28:31], v[140:143], v[112:115], a[28:31]\n\t"
  "v_mfma_f32_16x16x32_bf16 a[0:3], v[144:147], v[84:87], a[0:3]\n\t"
  "ds_read_b64_tr_b16 v[184:185], v229 offset:29184\n\t"
  "ds_read_b64_tr_b16 v[186:187], v229 offset:31232\n\t"
  "v_mfma_f32_16x16x32_bf16 a[4:7], v[144:147], v[116:119], a[4:7]\n\t"
  "s_waitcnt lgkmcnt(14)\n\t"
  "v_mfma_f32_16x16x32_bf16 a[8:11], v[148:151], v[84:87], a[8:11]\n\t"
  "ds_read_b64_tr_b16 v[188:189], v230 offset:29184\n\t"
  "ds_read_b64_tr_b16 v[190:191], v230 offset:31232\n\t"
  "v_mfma_f32_16x16x32_bf16 a[12:15], v[148:151], v[116:119], a[12:15]\n\t"
  "v_mfma_f32_16x16x32_bf16 a[16:19], v[152:155], v[84:87], a[16:19]\n\t"
  "s_barrier\n\t"
  "ds_read_b64_tr_b16 v[128:129], v225 offset:0\n\t"
  "ds_read_b64_tr_b16 v[130:131], v225 offset:2048\n\t"
  "v_mfma_f32_16x16x32_bf16 a[20:23], v[152:155], v[116:119], a[20:23]\n\t"
  "v_mfma_f32_16x16x32_bf16 a[24:27], v[156:159], v[84:87], a[24:27]\n\t"
  "ds_read_b64_tr_b16 v[132:133], v226 offset:0\n\t"
  "ds_read_b64_tr_b16 v[134:135], v226 offset:2048\n\t"
  "v_mfma_f32_16x16x32_bf16 a[28:31], v[156:159], v[116:119], a[28:31]\n\t"
  "s_waitcnt lgkmcnt(14)\n\t"
  "v_mfma_f32_16x16x32_bf16 a[0:3], v[160:163], v[88:91], a[0:3]\n\t"
  "ds_read_b64_tr_b16 v[136:137], v225 offset:512\n\t"
  "ds_read_b64_tr_b16 v[138:139], v225 offset:2560\n\t"
  "v_mfma_f32_16x16x32_bf16 a[4:7], v[160:163], v[120:123], a[4:7]\n\t"
  "v_mfma_f32_16x16x32_bf16 a[8:11], v[164:167], v[88:91], a[8:11]\n\t"
  "ds_read_b64_tr_b16 v[140:141], v226 offset:512\n\t"
  "ds_read_b64_tr_b16 v[142:143], v226 offset:2560\n\t"
  "v_mfma_f32_16x16x32_bf16 a[12:15], v[164:167], v[120:123], a[12:15]\n\t"
  "v_mfma_f32_16x16x32_bf16 a[16:19], v[168:171], v[88:91], a[16:19]\n\t"
  "ds_read_b64_tr_b16 v[144:145], v225 offset:4096\n\t"
  "ds_read_b64_tr_b16 v[146:147], v225 offset:6144\n\t"
  "v_mfma_f32_16x16x32_bf16 a[20:23], v[168:171], v[120:123], a[20:23]\n\t"
  "s_waitcnt lgkmcnt(14)\n\t"
  "v_mfma_f32_16x16x32_bf16 a[24:27], v[172:175], v[88:91], a[24:27]\n\t"
  "ds_read_b64_tr_b16 v[148:149], v226 offset:4096\n\t"
  "ds_read_b64_tr_b16 v[150:151], v226 offset:6144\n\t"
  "v_mfma_f32_16x16x32_bf16 a[28:31], v[172:175], v[120:123], a[28:31]\n\t"
  "v_mfma_f32_16x16x32_bf16 a[0:3], v[176:179], v[92:95], a[0:3]\n\t"
  "ds_read_b64_tr_b16 v[152:153], v225 offset:4608\n\t"
  "ds_read_b64_tr_b16 v[154:155], v225 offset:6656\n\t"
  "v_mfma_f32_16x16x32_bf16 a[4:7], v[176:179], v[124:127], a[4:7]\n\t"
  "v_mfma_f32_16x16x32_bf16 a[8:11], v[180:183], v[92:95], a[8:11]\n\t"
  "ds_read_b64_tr_b16 v[156:157], v226 offset:4608\n\t"
  "ds_read_b64_tr_b16 v[158:159], v226 offset:6656\n\t"
  "v_mfma_f32_16x16x32_bf16 a[12:15], v[180:183], v[124:127], a[12:15]\n\t"
  "s_waitcnt lgkmcnt(14)\n\t"
  "v_mfma_f32_16x16x32_bf16 a[16:19], v[184:187], v[92:95], a[16:19]\n\t"
  "ds_read_b64_tr_b16 v[160:161], v225 offset:8192\n\t"
  "ds_read_b64_tr_b16 v[162:163], v225 offset:10240\n\t"
  "v_mfma_f32_16x16x32_bf16 a[20:23], v[184:187], v[124:127], a[20:23]\n\t"
  "v_mfma_f32_16x16x32_bf16 a[24:27], v[188:191], v[92:95], a[24:27]\n\t"
  "ds_read_b64_tr_b16 v[164:165], v226 offset:8192\n\t"
  "ds_read_b64_tr_b16 v[166:167], v226 offset:10240\n\t"
  "v_mfma_f32_16x16x32_bf16 a[28:31], v[188:191], v[124:127], a[28:31]\n\t"
  "v_mfma_f32_16x16x32_bf16 a[32:35], v[128:131], v[64:67], 0\n\t"
  "ds_read_b64_tr_b16 v[168:169], v225 offset:8704\n\t"
  "ds_read_b64_tr_b16 v[170:171], v225 offset:10752\n\t"
  "v_mfma_f32_16x16x32_bf16 a[36:39], v[128:131], v[96:99], 0\n\t"
  "s_waitcnt lgkmcnt(14)\n\t"
  "v_mfma_f32_16x16x32_bf16 a[40:43], v[132:135], v[64:67], 0\n\t"
  "ds_read_b64_tr_b16 v[172:173], v226 offset:8704\n\t"
  "ds_read_b64_tr_b16 v[174:175], v226 offset:10752\n\t"
  "v_mfma_f32_16x16x32_bf16 a[44:47], v[132:135], v[96:99], 0\n\t"
  "v_mfma_f32_16x16x32_bf16 a[48:51], v[136:139], v[64:67], 0\n\t"
  "ds_read_b64_tr_b16 v[176:177], v225 offset:12288\n\t"
  "ds_read_b64_tr_b16 v[178:179], v225 offset:14336\n\t"
  "v_mfma_f32_16x16x32_bf16 a[52:55], v[136:139], v[96:99], 0\n\t"
  "v_mfma_f32_16x16x32_bf16 a[56:59], v[140:143], v[64:67], 0\n\t"
  "ds_read_b64_tr_b16 v[180:181], v226 offset:12288\n\t"
  "ds_read_b64_tr_b16 v[182:183], v226 offset:14336\n\t"
  "v_mfma_f32_16x16x32_bf16 a[60:63], v[140:143], v[96:99], 0\n\t"
  "s_waitcnt lgkmcnt(14)\n\t"
  "v_mfma_f32_16x16x32_bf16 a[32:35], v[144:147], v[68:71], a[32:35]\n\t"
  "ds_read_b64_tr_b16 v[184:185], v225 offset:12800\n\t"
  "ds_read_b64_tr_b16 v[186:187], v225 offset:14848\n\t"
  "v_mfma_f32_16x16x32_bf16 a[36:39], v[144:147], v[100:103], a[36:39]\n\t"
  "v_accvgpr_read_b32 v200, a0\n\t"
  "v_accvgpr_read_b32 v201, a1\n\t"
  "v_accvgpr_read_b32 v202, a2\n\t"
  "v_mfma_f32_16x16x32_bf16 a[40:43], v[148:151], v[68:71], a[40:43]\n\t"
  "ds_read_b64_tr_b16 v[188:189], v226 offset:12800\n\t"
  "ds_read_b64_tr_b16 v[190:191], v226 offset:14848\n\t"
  "v_mfma_f32_16x16x32_bf16 a[44:47], v[148:151], v[100:103], a[44:47]\n\t"
  "v_accvgpr_read_b32 v203, a3\n\t"
  "v_cvt_pk_bf16_f32 v192, v200, v201\n\t"
  "v_cvt_pk_bf16_f32 v193, v202, v203\n\t"
  "v_mfma_f32_16x16x32_bf16 a[48:51], v[152:155], v[68:71], a[48:51]\n\t"
  "ds_read_b64_tr_b16 v[128:129], v225 offset:16384\n\t"
  "ds_read_b64_tr_b16 v[130:131], v225 offset:18432\n\t"
  "v_mfma_f32_16x16x32_bf16 a[52:55], v[152:155], v[100:103], a[52:55]\n\t"
  "v_accvgpr_read_b32 v200, a8\n\t"
  "v_accvgpr_read_b32 v201, a9\n\t"
  "s_waitcnt lgkmcnt(14)\n\t"
  "v_mfma_f32_16x16x32_bf16 a[56:59], v[156:159], v[68:71], a[56:59]\n\t"
  "ds_read_b64_tr_b16 v[132:133], v226 offset:16384\n\t"
  "ds_read_b64_tr_b16 v[134:135], v226 offset:18432\n\t"
  "v_mfma_f32_16x16x32_bf16 a[60:63], v[156:159], v[100:103], a[60:63]\n\t"
  "v_accvgpr_read_b32 v202, a10\n\t"
  "v_accvgpr_read_b32 v203, a11\n\t"
  "v_cvt_pk_bf16_f32 v194, v200, v201\n\t"
  "v_mfma_f32_16x16x32_bf16 a[32:35], v[160:163], v[72:75], a[32:35]\n\t"
  "ds_read_b64_tr_b16 v[136:137], v225 offset:16896\n\t"
  "ds_read_b64_tr_b16 v[138:139], v225 offset:18944\n\t"
  "v_mfma_f32_16x16x32_bf16 a[36:39], v[160:163], v[104:107], a[36:39]\n\t"
  "v_cvt_pk_bf16_f32 v195, v202, v203\n\t"
  "buffer_store_dwordx4 v[192:195], v246, %[srdws], 0 offen offset:0 sc1\n\t"
  "v_accvgpr_read_b32 v200, a16\n\t"
  "v_mfma_f32_16x16x32_bf16 a[40:43], v[164:167], v[72:75], a[40:43]\n\t"
  "ds_read_b64_tr_b16 v[140:141], v226 offset:16896\n\t"
  "ds_read_b64_tr_b16 v[142:143], v226 offset:18944\n\t"
  "v_mfma_f32_16x16x32_bf16 a[44:47], v[164:167], v[104:107], a[44:47]\n\t"
  "v_accvgpr_read_b32 v201, a17\n\t"
  "v_accvgpr_read_b32 v202, a18\n\t"
  "s_waitcnt lgkmcnt(14)\n\t"
  "v_mfma_f32_16x16x32_bf16 a[48:51], v[168:171], v[72:75], a[48:51]\n\t"
  "ds_read_b64_tr_b16 v[144:145], v225 offset:20480\n\t"
  "ds_read_b64_tr_b16 v[146:147], v225 offset:22528\n\t"
  "v_mfma_f32_16x16x32_bf16 a[52:55], v[168:171], v[104:107], a[52:55]\n\t"
  "v_accvgpr_read_b32 v203, a19\n\t"
  "v_cvt_pk_bf16_f32 v196, v200, v201\n\t"
  "v_cvt_pk_bf16_f32 v197, v202, v203\n\t"
  "v_mfma_f32_16x16x32_bf16 a[56:59], v[172:175], v[72:75], a[56:59]\n\t"
  "ds_read_b64_tr_b16 v[148:149], v226 offset:20480\n\t"
  "ds_read_b64_tr_b16 v[150:151], v226 offset:22528\n\t"
  "v_mfma_f32_16x16x32_bf16 a[60:63], v[172:175], v[104:107], a[60:63]\n\t"
  "v_accvgpr_read_b32 v200, a24\n\t"
  "v_accvgpr_read_b32 v201, a25\n\t"
  "v_accvgpr_read_b32 v202, a26\n\t"
  "v_mfma_f32_16x16x32_bf16 a[32:35], v[176:179], v[76:79], a[32:35]\n\t"
  "ds_read_b64_tr_b16 v[152:153], v225 offset:20992\n\t"
  "ds_read_b64_tr_b16 v[154:155], v225 offset:23040\n\t"
  "v_mfma_f32_16x16x32_bf16 a[36:39], v[176:179], v[108:111], a[36:39]\n\t"
  "v_accvgpr_read_b32 v203, a27\n\t"
  "v_cvt_pk_bf16_f32 v198, v200, v201\n\t"
  "s_waitcnt lgkmcnt(14)\n\t"
  "v_mfma_f32_16x16x32_bf16 a[40:43], v[180:183], v[76:79], a[40:43]\n\t"
  "ds_read_b64_tr_b16 v[156:157], v226 offset:20992\n\t"
  "ds_read_b64_tr_b16 v[158:159], v226 offset:23040\n\t"
  "v_mfma_f32_16x16x32_bf16 a[44:47], v[180:183], v[108:111], a[44:47]\n\t"
  "v_cvt_pk_bf16_f32 v199, v202, v203\n\t"
  "buffer_store_dwordx4 v[196:199], v246, %[srdws], 0 offen offset:1024 sc1\n\t"
  "v_accvgpr_read_b32 v200, a4\n\t"
  "v_mfma_f32_16x16x32_bf16 a[48:51], v[184:187], v[76:79], a[48:51]\n\t"
  "ds_read_b64_tr_b16 v[160:161], v225 offset:24576\n\t"
  "ds_read_b64_tr_b16 v[162:163], v225 offset:26624\n\t"
  "v_mfma_f32_16x16x32_bf16 a[52:55], v[184:187], v[108:111], a[52:55]\n\t"
  "v_accvgpr_read_b32 v201, a5\n\t"
  "v_accvgpr_read_b32 v202, a6\n\t"
  "v_accvgpr_read_b32 v203, a7\n\t"
  "v_mfma_f32_16x16x32_bf16 a[56:59], v[188:191], v[76:79], a[56:59]\n\t"
  "ds_read_b64_tr_b16 v[164:165], v226 offset:24576\n\t"
  "ds_read_b64_tr_b16 v[166:167], v226 offset:26624\n\t"
  "v_mfma_f32_16x16x32_bf16 a[60:63], v[188:191], v[108:111], a[60:63]\n\t"
  "v_cvt_pk_bf16_f32 v192, v200, v201\n\t"
  "v_cvt_pk_bf16_f32 v193, v202, v203\n\t"
  "s_waitcnt lgkmcnt(14)\n\t"
  "v_mfma_f32_16x16x32_bf16 a[32:35], v[128:131], v[80:83], a[32:35]\n\t"
  "ds_read_b64_tr_b16 v[168:169], v225 offset:25088\n\t"
  "ds_read_b64_tr_b16 v[170:171], v225 offset:27136\n\t"
  "v_mfma_f32_16x16x32_bf16 a[36:39], v[128:131], v[112:115], a[36:39]\n\t"
  "v_accvgpr_read_b32 v200, a12\n\t"
  "v_accvgpr_read_b32 v201, a13\n\t"
  "v_accvgpr_read_b32 v202, a14\n\t"
  "v_mfma_f32_16x16x32_bf16 a[40:43], v[132:135], v[80:83], a[40:43]\n\t"
  "ds_read_b64_tr_b16 v[172:173], v226 offset:25088\n\t"
  "ds_read_b64_tr_b16 v[174:175], v226 offset:27136\n\t"
  "v_mfma_f32_16x16x32_bf16 a[44:47], v[132:135], v[112:115], a[44:47]\n\t"
  "v_accvgpr_read_b32 v203, a15\n\t"
  "v_cvt_pk_bf16_f32 v194, v200, v201\n\t"
  "v_cvt_pk_bf16_f32 v195, v202, v203\n\t"
  "v_mfma_f32_16x16x32_bf16 a[48:51], v[136:139], v[80:83], a[48:51]\n\t"
  "ds_read_b64_tr_b16 v[176:177], v225 offset:28672\n\t"
  "ds_read_b64_tr_b16 v[178:179], v225 offset:30720\n\t"
  "v_mfma_f32_16x16x32_bf16 a[52:55], v[136:139], v[112:115], a[52:55]\n\t"
  "buffer_store_dwordx4 v[192:195], v246, %[srdws], s52 offen offset:0 sc1\n\t"
  "v_accvgpr_read_b32 v200, a20\n\t"
  "s_waitcnt lgkmcnt(14)\n\t"
  "v_mfma_f32_16x16x32_bf16 a[56:59], v[140:143], v[80:83], a[56:59]\n\t"
  "ds_read_b64_tr_b16 v[180:181], v226 offset:28672\n\t"
  "ds_read_b64_tr_b16 v[182:183], v226 offset:30720\n\t"
  "v_mfma_f32_16x16x32_bf16 a[60:63], v[140:143], v[112:115], a[60:63]\n\t"
  "v_accvgpr_read_b32 v201, a21\n\t"
  "v_accvgpr_read_b32 v202, a22\n\t"
  "v_accvgpr_read_b32 v203, a23\n\t"
  "v_mfma_f32_16x16x32_bf16 a[32:35], v[144:147], v[84:87], a[32:35]\n\t"
  "ds_read_b64_tr_b16 v[184:185], v225 offset:29184\n\t"
  "ds_read_b64_tr_b16 v[186:187], v225 offset:31232\n\t"
  "v_mfma_f32_16x16x32_bf16 a[36:39], v[144:147], v[116:119], a[36:39]\n\t"
  "v_cvt_pk_bf16_f32 v196, v200, v201\n\t"
  "v_cvt_pk_bf16_f32 v197, v202, v203\n\t"
  "v_accvgpr_read_b32 v200, a28\n\t"
  "v_mfma_f32_16x16x32_bf16 a[40:43], v[148:151], v[84:87], a[40:43]\n\t"
  "ds_read_b64_tr_b16 v[188:189], v226 offset:29184\n\t"
  "ds_read_b64_tr_b16 v[190:191], v226 offset:31232\n\t"
  "v_mfma_f32_16x16x32_bf16 a[44:47], v[148:151], v[116:119], a[44:47]\n\t"
  "v_accvgpr_read_b32 v201, a29\n\t"
  "v_accvgpr_read_b32 v202, a30\n\t"
  "s_waitcnt lgkmcnt(14)\n\t"
  "v_mfma_f32_16x16x32_bf16 a[48:51], v[152:155], v[84:87], a[48:51]\n\t"
  "s_barrier\n\t"
  "ds_read_b64_tr_b16 v[128:129], v225 offset:32768\n\t"
  "ds_read_b64_tr_b16 v[130:131], v225 offset:34816\n\t"
  "v_mfma_f32_16x16x32_bf16 a[52:55], v[152:155], v[116:119], a[52:55]\n\t"
  "s_add_u32 m0, %[m0o], 131072\n\t"
  "s_nop 0\n\t"
  "buffer_load_dwordx4 v224, %[srdo], 0 offen offset:0 lds\n\t"
  "v_accvgpr_read_b32 v203, a31\n\t"
  "v_cvt_pk_bf16_f32 v198, v200, v201\n\t"
  "v_cvt_pk_bf16_f32 v199, v202, v203\n\t"
  "v_mfma_f32_16x16x32_bf16 a[56:59], v[156:159], v[84:87], a[56:59]\n\t"
  "ds_read_b64_tr_b16 v[132:133], v226 offset:32768\n\t"
  "ds_read_b64_tr_b16 v[134:135], v226 offset:34816\n\t"
  "v_mfma_f32_16x16x32_bf16 a[60:63], v[156:159], v[116:119], a[60:63]\n\t"
  "buffer_store_dwordx4 v[196:199], v246, %[srdws], s52 offen offset:1024 sc1\n\t"
  "v_add_u32 v246, 0x800, v246\n\t"
  "v_mfma_f32_16x16x32_bf16 a[32:35], v[160:163], v[88:91], a[32:35]\n\t"
  "ds_read_b64_tr_b16 v[136:137], v225 offset:33280\n\t"
  "ds_read_b64_tr_b16 v[138:139], v225 offset:35328\n\t"
  "v_mfma_f32_16x16x32_bf16 a[36:39], v[160:163], v[120:123], a[36:39]\n\t"
  "buffer_load_dwordx4 v224, %[srdo], %[c6144] offen offset:1024 lds\n\t"
  "s_waitcnt lgkmcnt(14)\n\t"
  "v_mfma_f32_16x16x32_bf16 a[40:43], v[164:167], v[88:91], a[40:43]\n\t"
  "ds_read_b64_tr_b16 v[140:141], v226 offset:33280\n\t"
  "ds_read_b64_tr_b16 v[142:143], v226 offset:35328\n\t"
  "v_mfma_f32_16x16x32_bf16 a[44:47], v[164:167], v[120:123], a[44:47]\n\t"
  "v_mfma_f32_16x16x32_bf16 a[48:51], v[168:171], v[88:91], a[48:51]\n\t"
  "ds_read_b64_tr_b16 v[144:145], v225 offset:36864\n\t"
  "ds_read_b64_tr_b16 v[146:147], v225 offset:38912\n\t"
  "v_mfma_f32_16x16x32_bf16 a[52:55], v[168:171], v[120:123], a[52:55]\n\t"
  "buffer_load_dwordx4 v224, %[srdo], %[c12288] offen offset:2048 lds\n\t"
  "v_mfma_f32_16x16x32_bf16 a[56:59], v[172:175], v[88:91], a[56:59]\n\t"
  "ds_read_b64_tr_b16 v[148:149], v226 offset:36864\n\t"
  "ds_read_b64_tr_b16 v[150:151], v226 offset:38912\n\t"
  "v_mfma_f32_16x16x32_bf16 a[60:63], v[172:175], v[120:123], a[60:63]\n\t"
  "s_waitcnt lgkmcnt(14)\n\t"
  "v_mfma_f32_16x16x32_bf16 a[32:35], v[176:179], v[92:95], a[32:35]\n\t"
  "ds_read_b64_tr_b16 v[152:153], v225 offset:37376\n\t"
  "ds_read_b64_tr_b16 v[154:155], v225 offset:39424\n\t"
  "v_mfma_f32_16x16x32_bf16 a[36:39], v[176:179], v[124:127], a[36:39]\n\t"
  "buffer_load_dwordx4 v224, %[srdo], %[c18432] offen offset:3072 lds\n\t"
  "s_add_u32 m0, %[m0o], 135168\n\t"
  "v_mfma_f32_16x16x32_bf16 a[40:43], v[180:183], v[92:95], a[40:43]\n\t"
  "ds_read_b64_tr_b16 v[156:157], v226 offset:37376\n\t"
  "ds_read_b64_tr_b16 v[158:159], v226 offset:39424\n\t"
  "v_mfma_f32_16x16x32_bf16 a[44:47], v[180:183], v[124:127], a[44:47]\n\t"
  "v_mfma_f32_16x16x32_bf16 a[48:51], v[184:187], v[92:95], a[48:51]\n\t"
  "ds_read_b64_tr_b16 v[160:161], v225 offset:40960\n\t"
  "ds_read_b64_tr_b16 v[162:163], v225 offset:43008\n\t"
  "v_mfma_f32_16x16x32_bf16 a[52:55], v[184:187], v[124:127], a[52:55]\n\t"
  "buffer_load_dwordx4 v224, %[srdo], %[c28672] offen offset:0 lds\n\t"
  "s_waitcnt lgkmcnt(14)\n\t"
  "v_mfma_f32_16x16x32_bf16 a[56:59], v[188:191], v[92:95], a[56:59]\n\t"
  "ds_read_b64_tr_b16 v[164:165], v226 offset:40960\n\t"
  "ds_read_b64_tr_b16 v[166:167], v226 offset:43008\n\t"
  "v_mfma_f32_16x16x32_bf16 a[60:63], v[188:191], v[124:127], a[60:63]\n\t"
  "v_mfma_f32_16x16x32_bf16 a[0:3], v[128:131], v[64:67], 0\n\t"
  "ds_read_b64_tr_b16 v[168:169], v225 offset:41472\n\t"
  "ds_read_b64_tr_b16 v[170:171], v225 offset:43520\n\t"
  "v_mfma_f32_16x16x32_bf16 a[4:7], v[128:131], v[96:99], 0\n\t"
  "buffer_load_dwordx4 v224, %[srdo], %[c34816] offen offset:1024 lds\n\t"
  "v_mfma_f32_16x16x32_bf16 a[8:11], v[132:135], v[64:67], 0\n\t"
  "ds_read_b64_tr_b16 v[172:173], v226 offset:41472\n\t"
  "ds_read_b64_tr_b16 v[174:175], v226 offset:43520\n\t"
  "v_mfma_f32_16x16x32_bf16 a[12:15], v[132:135], v[96:99], 0\n\t"
  "s_waitcnt lgkmcnt(14)\n\t"
  "v_mfma_f32_16x16x32_bf16 a[16:19], v[136:139], v[64:67], 0\n\t"
  "ds_read_b64_tr_b16 v[176:177], v225 offset:45056\n\t"
  "ds_read_b64_tr_b16 v[178:179], v225 offset:47104\n\t"
  "v_mfma_f32_16x16x32_bf16 a[20:23], v[136:139], v[96:99], 0\n\t"
  "buffer_load_dwordx4 v224, %[srdo], %[c40960] offen offset:2048 lds\n\t"
  "v_mfma_f32_16x16x32_bf16 a[24:27], v[140:143], v[64:67], 0\n\t"
  "ds_read_b64_tr_b16 v[180:181], v226 offset:45056\n\t"
  "ds_read_b64_tr_b16 v[182:183], v226 offset:47104\n\t"
  "v_mfma_f32_16x16x32_bf16 a[28:31], v[140:143], v[96:99], 0\n\t"
  "v_mfma_f32_16x16x32_bf16 a[0:3], v[144:147], v[68:71], a[0:3]\n\t"
  "ds_read_b64_tr_b16 v[184:185], v225 offset:45568\n\t"
  "ds_read_b64_tr_b16 v[186:187], v225 offset:47616\n\t"
  "v_mfma_f32_16x16x32_bf16 a[4:7], v[144:147], v[100:103], a[4:7]\n\t"
  "buffer_load_dwordx4 v224, %[srdo], %[c47104] offen offset:3072 lds\n\t"
  "v_add_u32 v224, 0x80, v224\n\t"
  "v_accvgpr_read_b32 v200, a32\n\t"
  "v_accvgpr_read_b32 v201, a33\n\t"
  "v_accvgpr_read_b32 v202, a34\n\t"
  "s_waitcnt lgkmcnt(14)\n\t"
  "v_mfma_f32_16x16x32_bf16 a[8:11], v[148:151], v[68:71], a[8:11]\n\t"
  "ds_read_b64_tr_b16 v[188:189], v226 offset:45568\n\t"
  "ds_read_b64_tr_b16 v[190:191], v226 offset:47616\n\t"
  "v_mfma_f32_16x16x32_bf16 a[12:15], v[148:151], v[100:103], a[12:15]\n\t"
  "v_accvgpr_read_b32 v203, a35\n\t"
  "v_cvt_pk_bf16_f32 v192, v200, v201\n\t"
  "v_cvt_pk_bf16_f32 v193, v202, v203\n\t"
  "v_mfma_f32_16x16x32_bf16 a[16:19], v[152:155], v[68:71], a[16:19]\n\t"
  "ds_read_b64_tr_b16 v[128:129], v225 offset:49152\n\t"
  "ds_read_b64_tr_b16 v[130:131], v225 offset:51200\n\t"
  "v_mfma_f32_16x16x32_bf16 a[20:23], v[152:155], v[100:103], a[20:23]\n\t"
  "v_accvgpr_read_b32 v200, a40\n\t"
  "v_accvgpr_read_b32 v201, a41\n\t"
  "v_mfma_f32_16x16x32_bf16 a[24:27], v[156:159], v[68:71], a[24:27]\n\t"
  "ds_read_b64_tr_b16 v[132:133], v226 offset:49152\n\t"
  "ds_read_b64_tr_b16 v[134:135], v226 offset:51200\n\t"
  "v_mfma_f32_16x16x32_bf16 a[28:31], v[156:159], v[100:103], a[28:31]\n\t"
  "v_accvgpr_read_b32 v202, a42\n\t"
  "v_accvgpr_read_b32 v203, a43\n\t"
  "v_cvt_pk_bf16_f32 v194, v200, v201\n\t"
  "s_waitcnt lgkmcnt(14)\n\t"
  "v_mfma_f32_16x16x32_bf16 a[0:3], v[160:163], v[72:75], a[0:3]\n\t"
  "ds_read_b64_tr_b16 v[136:137], v225 offset:49664\n\t"
  "ds_read_b64_tr_b16 v[138:139], v225 offset:51712\n\t"
  "v_mfma_f32_16x16x32_bf16 a[4:7], v[160:163], v[104:107], a[4:7]\n\t"
  "v_cvt_pk_bf16_f32 v195, v202, v203\n\t"
  "buffer_store_dwordx4 v[192:195], v246, %[srdws], 0 offen offset:0 sc1\n\t"
  "v_accvgpr_read_b32 v200, a48\n\t"
  "v_mfma_f32_16x16x32_bf16 a[8:11], v[164:167], v[72:75], a[8:11]\n\t"
  "ds_read_b64_tr_b16 v[140:141], v226 offset:49664\n\t"
  "ds_read_b64_tr_b16 v[142:143], v226 offset:51712\n\t"
  "v_mfma_f32_16x16x32_bf16 a[12:15], v[164:167], v[104:107], a[12:15]\n\t"
  "v_accvgpr_read_b32 v201, a49\n\t"
  "v_accvgpr_read_b32 v202, a50\n\t"
  "v_mfma_f32_16x16x32_bf16 a[16:19], v[168:171], v[72:75], a[16:19]\n\t"
  "ds_read_b64_tr_b16 v[144:145], v225 offset:53248\n\t"
  "ds_read_b64_tr_b16 v[146:147], v225 offset:55296\n\t"
  "v_mfma_f32_16x16x32_bf16 a[20:23], v[168:171], v[104:107], a[20:23]\n\t"
  "v_accvgpr_read_b32 v203, a51\n\t"
  "v_cvt_pk_bf16_f32 v196, v200, v201\n\t"
  "v_cvt_pk_bf16_f32 v197, v202, v203\n\t"
  "s_waitcnt lgkmcnt(14)\n\t"
  "v_mfma_f32_16x16x32_bf16 a[24:27], v[172:175], v[72:75], a[24:27]\n\t"
  "ds_read_b64_tr_b16 v[148:149], v226 offset:53248\n\t"
  "ds_read_b64_tr_b16 v[150:151], v226 offset:55296\n\t"
  "v_mfma_f32_16x16x32_bf16 a[28:31], v[172:175], v[104:107], a[28:31]\n\t"
  "v_accvgpr_read_b32 v200, a56\n\t"
  "v_accvgpr_read_b32 v201, a57\n\t"
  "v_accvgpr_read_b32 v202, a58\n\t"
  "v_mfma_f32_16x16x32_bf16 a[0:3], v[176:179], v[76:79], a[0:3]\n\t"
  "ds_read_b64_tr_b16 v[152:153], v225 offset:53760\n\t"
  "ds_read_b64_tr_b16 v[154:155], v225 offset:55808\n\t"
  "v_mfma_f32_16x16x32_bf16 a[4:7], v[176:179], v[108:111], a[4:7]\n\t"
  "v_accvgpr_read_b32 v203, a59\n\t"
  "v_cvt_pk_bf16_f32 v198, v200, v201\n\t"
  "v_mfma_f32_16x16x32_bf16 a[8:11], v[180:183], v[76:79], a[8:11]\n\t"
  "ds_read_b64_tr_b16 v[156:157], v226 offset:53760\n\t"
  "ds_read_b64_tr_b16 v[158:159], v226 offset:55808\n\t"
  "v_mfma_f32_16x16x32_bf16 a[12:15], v[180:183], v[108:111], a[12:15]\n\t"
  "v_cvt_pk_bf16_f32 v199, v202, v203\n\t"
  "buffer_store_dwordx4 v[196:199], v246, %[srdws], 0 offen offset:1024 sc1\n\t"
  "v_accvgpr_read_b32 v200, a36\n\t"
  "s_waitcnt lgkmcnt(14)\n\t"
  "v_mfma_f32_16x16x32_bf16 a[16:19], v[184:187], v[76:79], a[16:19]\n\t"
  "ds_read_b64_tr_b16 v[160:161], v225 offset:57344\n\t"
  "ds_read_b64_tr_b16 v[162:163], v225 offset:59392\n\t"
  "v_mfma_f32_16x16x32_bf16 a[20:23], v[184:187], v[108:111], a[20:23]\n\t"
  "v_accvgpr_read_b32 v201, a37\n\t"
  "v_accvgpr_read_b32 v202, a38\n\t"
  "v_accvgpr_read_b32 v203, a39\n\t"
  "v_mfma_f32_16x16x32_bf16 a[24:27], v[188:191], v[76:79], a[24:27]\n\t"
  "ds_read_b64_tr_b16 v[164:165], v226 offset:57344\n\t"
  "ds_read_b64_tr_b16 v[166:167], v226 offset:59392\n\t"
  "v_mfma_f32_16x16x32_bf16 a[28:31], v[188:191], v[108:111], a[28:31]\n\t"
  "v_cvt_pk_bf16_f32 v192, v200, v201\n\t"
  "v_cvt_pk_bf16_f32 v193, v202, v203\n\t"
  "v_mfma_f32_16x16x32_bf16 a[0:3], v[128:131], v[80:83], a[0:3]\n\t"
  "ds_read_b64_tr_b16 v[168:169], v225 offset:57856\n\t"
  "ds_read_b64_tr_b16 v[170:171], v225 offset:59904\n\t"
  "v_mfma_f32_16x16x32_bf16 a[4:7], v[128:131], v[112:115], a[4:7]\n\t"
  "v_accvgpr_read_b32 v200, a44\n\t"
  "v_accvgpr_read_b32 v201, a45\n\t"
  "v_accvgpr_read_b32 v202, a46\n\t"
  "s_waitcnt lgkmcnt(14)\n\t"
  "v_mfma_f32_16x16x32_bf16 a[8:11], v[132:135], v[80:83], a[8:11]\n\t"
  "ds_read_b64_tr_b16 v[172:173], v226 offset:57856\n\t"
  "ds_read_b64_tr_b16 v[174:175], v226 offset:59904\n\t"
  "v_mfma_f32_16x16x32_bf16 a[12:15], v[132:135], v[112:115], a[12:15]\n\t"
  "v_accvgpr_read_b32 v203, a47\n\t"
  "v_cvt_pk_bf16_f32 v194, v200, v201\n\t"
  "v_cvt_pk_bf16_f32 v195, v202, v203\n\t"
  "v_mfma_f32_16x16x32_bf16 a[16:19], v[136:139], v[80:83], a[16:19]\n\t"
  "ds_read_b64_tr_b16 v[176:177], v225 offset:61440\n\t"
  "ds_read_b64_tr_b16 v[178:179], v225 offset:63488\n\t"
  "v_mfma_f32_16x16x32_bf16 a[20:23], v[136:139], v[112:115], a[20:23]\n\t"
  "buffer_store_dwordx4 v[192:195], v246, %[srdws], s52 offen offset:0 sc1\n\t"
  "v_accvgpr_read_b32 v200, a52\n\t"
  "v_mfma_f32_16x16x32_bf16 a[24:27], v[140:143], v[80:83], a[24:27]\n\t"
  "ds_read_b64_tr_b16 v[180:181], v226 offset:61440\n\t"
  "ds_read_b64_tr_b16 v[182:183], v226 offset:63488\n\t"
  "v_mfma_f32_16x16x32_bf16 a[28:31], v[140:143], v[112:115], a[28:31]\n\t"
  "v_accvgpr_read_b32 v201, a53\n\t"
  "v_accvgpr_read_b32 v202, a54\n\t"
  "v_accvgpr_read_b32 v203, a55\n\t"
  "s_waitcnt lgkmcnt(14)\n\t"
  "v_mfma_f32_16x16x32_bf16 a[0:3], v[144:147], v[84:87], a[0:3]\n\t"
  "ds_read_b64_tr_b16 v[184:185], v225 offset:61952\n\t"
  "ds_read_b64_tr_b16 v[186:187], v225 offset:64000\n\t"
  "v_mfma_f32_16x16x32_bf16 a[4:7], v[144:147], v[116:119], a[4:7]\n\t"
  "v_cvt_pk_bf16_f32 v196, v200, v201\n\t"
  "v_cvt_pk_bf16_f32 v197, v202, v203\n\t"
  "v_accvgpr_read_b32 v200, a60\n\t"
  "v_mfma_f32_16x16x32_bf16 a[8:11], v[148:151], v[84:87], a[8:11]\n\t"
  "ds_read_b64_tr_b16 v[188:189], v226 offset:61952\n\t"
  "ds_read_b64_tr_b16 v[190:191], v226 offset:64000\n\t"
  "v_mfma_f32_16x16x32_bf16 a[12:15], v[148:151], v[116:119], a[12:15]\n\t"
  "v_accvgpr_read_b32 v201, a61\n\t"
  "v_accvgpr_read_b32 v202, a62\n\t"
  "v_mfma_f32_16x16x32_bf16 a[16:19], v[152:155], v[84:87], a[16:19]\n\t"
  "s_waitcnt vmcnt(23)\n\t"
  "s_barrier\n\t"
  "ds_read_b64_tr_b16 v[128:129], v227 offset:0\n\t"
  "ds_read_b64_tr_b16 v[130:131], v227 offset:2048\n\t"
  "v_mfma_f32_16x16x32_bf16 a[20:23], v[152:155], v[116:119], a[20:23]\n\t"
  "s_add_u32 m0, %[m0o], 0\n\t"
  "s_nop 0\n\t"
  "buffer_load_dwordx4 v224, %[srdo], 0 offen offset:0 lds\n\t"
  "v_accvgpr_read_b32 v203, a63\n\t"
  "v_cvt_pk_bf16_f32 v198, v200, v201\n\t"
  "v_cvt_pk_bf16_f32 v199, v202, v203\n\t"
  "s_waitcnt lgkmcnt(14)\n\t"
  "v_mfma_f32_16x16x32_bf16 a[24:27], v[156:159], v[84:87], a[24:27]\n\t"
  "ds_read_b64_tr_b16 v[132:133], v228 offset:0\n\t"
  "ds_read_b64_tr_b16 v[134:135], v228 offset:2048\n\t"
  "v_mfma_f32_16x16x32_bf16 a[28:31], v[156:159], v[116:119], a[28:31]\n\t"
  "buffer_store_dwordx4 v[196:199], v246, %[srdws], s52 offen offset:1024 sc1\n\t"
  "v_add_u32 v246, 0x800, v246\n\t"
  "v_mfma_f32_16x16x32_bf16 a[0:3], v[160:163], v[88:91], a[0:3]\n\t"
  "ds_read_b64_tr_b16 v[136:137], v227 offset:512\n\t"
  "ds_read_b64_tr_b16 v[138:139], v227 offset:2560\n\t"
  "v_mfma_f32_16x16x32_bf16 a[4:7], v[160:163], v[120:123], a[4:7]\n\t"
  "buffer_load_dwordx4 v224, %[srdo], %[c6144] offen offset:1024 lds\n\t"
  "v_mfma_f32_16x16x32_bf16 a[8:11], v[164:167], v[88:91], a[8:11]\n\t"
  "ds_read_b64_tr_b16 v[140:141], v228 offset:512\n\t"
  "ds_read_b64_tr_b16 v[142:143], v228 offset:2560\n\t"
  "v_mfma_f32_16x16x32_bf16 a[12:15], v[164:167], v[120:123], a[12:15]\n\t"
  "s_waitcnt lgkmcnt(14)\n\t"
  "v_mfma_f32_16x16x32_bf16 a[16:19], v[168:171], v[88:91], a[16:19]\n\t"
  "ds_read_b64_tr_b16 v[144:145], v227 offset:4096\n\t"
  "ds_read_b64_tr_b16 v[146:147], v227 offset:6144\n\t"
  "v_mfma_f32_16x16x32_bf16 a[20:23], v[168:171], v[120:123], a[20:23]\n\t"
  "buffer_load_dwordx4 v224, %[srdo], %[c12288] offen offset:2048 lds\n\t"
  "v_mfma_f32_16x16x32_bf16 a[24:27], v[172:175], v[88:91], a[24:27]\n\t"
  "ds_read_b64_tr_b16 v[148:149], v228 offset:4096\n\t"
  "ds_read_b64_tr_b16 v[150:151], v228 offset:6144\n\t"
  "v_mfma_f32_16x16x32_bf16 a[28:31], v[172:175], v[120:123], a[28:31]\n\t"
  "v_mfma_f32_16x16x32_bf16 a[0:3], v[176:179], v[92:95], a[0:3]\n\t"
  "ds_read_b64_tr_b16 v[152:153], v227 offset:4608\n\t"
  "ds_read_b64_tr_b16 v[154:155], v227 offset:6656\n\t"
  "v_mfma_f32_16x16x32_bf16 a[4:7], v[176:179], v[124:127], a[4:7]\n\t"
  "buffer_load_dwordx4 v224, %[srdo], %[c18432] offen offset:3072 lds\n\t"
  "s_add_u32 m0, %[m0o], 4096\n\t"
  "s_waitcnt lgkmcnt(14)\n\t"
  "v_mfma_f32_16x16x32_bf16 a[8:11], v[180:183], v[92:95], a[8:11]\n\t"
  "ds_read_b64_tr_b16 v[156:157], v228 offset:4608\n\t"
  "ds_read_b64_tr_b16 v[158:159], v228 offset:6656\n\t"
  "v_mfma_f32_16x16x32_bf16 a[12:15], v[180:183], v[124:127], a[12:15]\n\t"
  "v_mfma_f32_16x16x32_bf16 a[16:19], v[184:187], v[92:95], a[16:19]\n\t"
  "ds_read_b64_tr_b16 v[160:161], v227 offset:8192\n\t"
  "ds_read_b64_tr_b16 v[162:163], v227 offset:10240\n\t"
  "v_mfma_f32_16x16x32_bf16 a[20:23], v[184:187], v[124:127], a[20:23]\n\t"
  "buffer_load_dwordx4 v224, %[srdo], %[c28672] offen offset:0 lds\n\t"
  "v_mfma_f32_16x16x32_bf16 a[24:27], v[188:191], v[92:95], a[24:27]\n\t"
  "ds_read_b64_tr_b16 v[164:165], v228 offset:8192\n\t"
  "ds_read_b64_tr_b16 v[166:167], v228 offset:10240\n\t"
  "v_mfma_f32_16x16x32_bf16 a[28:31], v[188:191], v[124:127], a[28:31]\n\t"
  "s_waitcnt lgkmcnt(14)\n\t"
  "v_mfma_f32_16x16x32_bf16 a[32:35], v[128:131], v[64:67], 0\n\t"
  "ds_read_b64_tr_b16 v[168:169], v227 offset:8704\n\t"
  "ds_read_b64_tr_b16 v[170:171], v227 offset:10752\n\t"
  "v_mfma_f32_16x16x32_bf16 a[36:39], v[128:131], v[96:99], 0\n\t"
  "buffer_load_dwordx4 v224, %[srdo], %[c34816] offen offset:1024 lds\n\t"
  "v_mfma_f32_16x16x32_bf16 a[40:43], v[132:135], v[64:67], 0\n\t"
  "ds_read_b64_tr_b16 v[172:173], v228 offset:8704\n\t"
  "ds_read_b64_tr_b16 v[174:175], v228 offset:10752\n\t"
  "v_mfma_f32_16x16x32_bf16 a[44:47], v[132:135], v[96:99], 0\n\t"
  "v_mfma_f32_16x16x32_bf16 a[48:51], v[136:139], v[64:67], 0\n\t"
  "ds_read_b64_tr_b16 v[176:177], v227 offset:12288\n\t"
  "ds_read_b64_tr_b16 v[178:179], v227 offset:14336\n\t"
  "v_mfma_f32_16x16x32_bf16 a[52:55], v[136:139], v[96:99], 0\n\t"
  "buffer_load_dwordx4 v224, %[srdo], %[c40960] offen offset:2048 lds\n\t"
  "s_waitcnt lgkmcnt(14)\n\t"
  "v_mfma_f32_16x16x32_bf16 a[56:59], v[140:143], v[64:67], 0\n\t"
  "ds_read_b64_tr_b16 v[180:181], v228 offset:12288\n\t"
  "ds_read_b64_tr_b16 v[182:183], v228 offset:14336\n\t"
  "v_mfma_f32_16x16x32_bf16 a[60:63], v[140:143], v[96:99], 0\n\t"
  "v_mfma_f32_16x16x32_bf16 a[32:35], v[144:147], v[68:71], a[32:35]\n\t"
  "ds_read_b64_tr_b16 v[184:185], v227 offset:12800\n\t"
  "ds_read_b64_tr_b16 v[186:187], v227 offset:14848\n\t"
  "v_mfma_f32_16x16x32_bf16 a[36:39], v[144:147], v[100:103], a[36:39]\n\t"
  "buffer_load_dwordx4 v224, %[srdo], %[c47104] offen offset:3072 lds\n\t"
  "v_add_u32 v224, 0x80, v224\n\t"
  "v_accvgpr_read_b32 v200, a0\n\t"
  "v_accvgpr_read_b32 v201, a1\n\t"
  "v_accvgpr_read_b32 v202, a2\n\t"
  "v_mfma_f32_16x16x32_bf16 a[40:43], v[148:151], v[68:71], a[40:43]\n\t"
  "ds_read_b64_tr_b16 v[188:189], v228 offset:12800\n\t"
  "ds_read_b64_tr_b16 v[190:191], v228 offset:14848\n\t"
  "v_mfma_f32_16x16x32_bf16 a[44:47], v[148:151], v[100:103], a[44:47]\n\t"
  "v_accvgpr_read_b32 v203, a3\n\t"
  "v_cvt_pk_bf16_f32 v192, v200, v201\n\t"
  "v_cvt_pk_bf16_f32 v193, v202, v203\n\t"
  "s_waitcnt lgkmcnt(14)\n\t"
  "v_mfma_f32_16x16x32_bf16 a[48:51], v[152:155], v[68:71], a[48:51]\n\t"
  "ds_read_b64_tr_b16 v[128:129], v227 offset:16384\n\t"
  "ds_read_b64_tr_b16 v[130:131], v227 offset:18432\n\t"
  "v_mfma_f32_16x16x32_bf16 a[52:55], v[152:155], v[100:103], a[52:55]\n\t"
  "v_accvgpr_read_b32 v200, a8\n\t"
  "v_accvgpr_read_b32 v201, a9\n\t"
  "v_mfma_f32_16x16x32_bf16 a[56:59], v[156:159], v[68:71], a[56:59]\n\t"
  "ds_read_b64_tr_b16 v[132:133], v228 offset:16384\n\t"
  "ds_read_b64_tr_b16 v[134:135], v228 offset:18432\n\t"
  "v_mfma_f32_16x16x32_bf16 a[60:63], v[156:159], v[100:103], a[60:63]\n\t"
  "v_accvgpr_read_b32 v202, a10\n\t"
  "v_accvgpr_read_b32 v203, a11\n\t"
  "v_cvt_pk_bf16_f32 v194, v200, v201\n\t"
  "v_mfma_f32_16x16x32_bf16 a[32:35], v[160:163], v[72:75], a[32:35]\n\t"
  "ds_read_b64_tr_b16 v[136:137], v227 offset:16896\n\t"
  "ds_read_b64_tr_b16 v[138:139], v227 offset:18944\n\t"
  "v_mfma_f32_16x16x32_bf16 a[36:39], v[160:163], v[104:107], a[36:39]\n\t"
  "v_cvt_pk_bf16_f32 v195, v202, v203\n\t"
  "buffer_store_dwordx4 v[192:195], v246, %[srdws], 0 offen offset:0 sc1\n\t"
  "v_accvgpr_read_b32 v200, a16\n\t"
  "s_waitcnt lgkmcnt(14)\n\t"
  "v_mfma_f32_16x16x32_bf16 a[40:43], v[164:167], v[72:75], a[40:43]\n\t"
  "ds_read_b64_tr_b16 v[140:141], v228 offset:16896\n\t"
  "ds_read_b64_tr_b16 v[142:143], v228 offset:18944\n\t"
  "v_mfma_f32_16x16x32_bf16 a[44:47], v[164:167], v[104:107], a[44:47]\n\t"
  "v_accvgpr_read_b32 v201, a17\n\t"
  "v_accvgpr_read_b32 v202, a18\n\t"
  "v_mfma_f32_16x16x32_bf16 a[48:51], v[168:171], v[72:75], a[48:51]\n\t"
  "ds_read_b64_tr_b16 v[144:145], v227 offset:20480\n\t"
  "ds_read_b64_tr_b16 v[146:147], v227 offset:22528\n\t"
  "v_mfma_f32_16x16x32_bf16 a[52:55], v[168:171], v[104:107], a[52:55]\n\t"
  "v_accvgpr_read_b32 v203, a19\n\t"
  "v_cvt_pk_bf16_f32 v196, v200, v201\n\t"
  "v_cvt_pk_bf16_f32 v197, v202, v203\n\t"
  "v_mfma_f32_16x16x32_bf16 a[56:59], v[172:175], v[72:75], a[56:59]\n\t"
  "ds_read_b64_tr_b16 v[148:149], v228 offset:20480\n\t"
  "ds_read_b64_tr_b16 v[150:151], v228 offset:22528\n\t"
  "v_mfma_f32_16x16x32_bf16 a[60:63], v[172:175], v[104:107], a[60:63]\n\t"
  "v_accvgpr_read_b32 v200, a24\n\t"
  "v_accvgpr_read_b32 v201, a25\n\t"
  "v_accvgpr_read_b32 v202, a26\n\t"
  "s_waitcnt lgkmcnt(14)\n\t"
  "v_mfma_f32_16x16x32_bf16 a[32:35], v[176:179], v[76:79], a[32:35]\n\t"
  "ds_read_b64_tr_b16 v[152:153], v227 offset:20992\n\t"
  "ds_read_b64_tr_b16 v[154:155], v227 offset:23040\n\t"
  "v_mfma_f32_16x16x32_bf16 a[36:39], v[176:179], v[108:111], a[36:39]\n\t"
  "v_accvgpr_read_b32 v203, a27\n\t"
  "v_cvt_pk_bf16_f32 v198, v200, v201\n\t"
  "v_mfma_f32_16x16x32_bf16 a[40:43], v[180:183], v[76:79], a[40:43]\n\t"
  "ds_read_b64_tr_b16 v[156:157], v228 offset:20992\n\t"
  "ds_read_b64_tr_b16 v[158:159], v228 offset:23040\n\t"
  "v_mfma_f32_16x16x32_bf16 a[44:47], v[180:183], v[108:111], a[44:47]\n\t"
  "v_cvt_pk_bf16_f32 v199, v202, v203\n\t"
  "buffer_store_dwordx4 v[196:199], v246, %[srdws], 0 offen offset:1024 sc1\n\t"
  "v_accvgpr_read_b32 v200, a4\n\t"
  "v_mfma_f32_16x16x32_bf16 a[48:51], v[184:187], v[76:79], a[48:51]\n\t"
  "ds_read_b64_tr_b16 v[160:161], v227 offset:24576\n\t"
  "ds_read_b64_tr_b16 v[162:163], v227 offset:26624\n\t"
  "v_mfma_f32_16x16x32_bf16 a[52:55], v[184:187], v[108:111], a[52:55]\n\t"
  "v_accvgpr_read_b32 v201, a5\n\t"
  "v_accvgpr_read_b32 v202, a6\n\t"
  "v_accvgpr_read_b32 v203, a7\n\t"
  "s_waitcnt lgkmcnt(14)\n\t"
  "v_mfma_f32_16x16x32_bf16 a[56:59], v[188:191], v[76:79], a[56:59]\n\t"
  "ds_read_b64_tr_b16 v[164:165], v228 offset:24576\n\t"
  "ds_read_b64_tr_b16 v[166:167], v228 offset:26624\n\t"
  "v_mfma_f32_16x16x32_bf16 a[60:63], v[188:191], v[108:111], a[60:63]\n\t"
  "v_cvt_pk_bf16_f32 v192, v200, v201\n\t"
  "v_cvt_pk_bf16_f32 v193, v202, v203\n\t"
  "v_mfma_f32_16x16x32_bf16 a[32:35], v[128:131], v[80:83], a[32:35]\n\t"
  "ds_read_b64_tr_b16 v[168:169], v227 offset:25088\n\t"
  "ds_read_b64_tr_b16 v[170:171], v227 offset:27136\n\t"
  "v_mfma_f32_16x16x32_bf16 a[36:39], v[128:131], v[112:115], a[36:39]\n\t"
  "v_accvgpr_read_b32 v200, a12\n\t"
  "v_accvgpr_read_b32 v201, a13\n\t"
  "v_accvgpr_read_b32 v202, a14\n\t"
  "v_mfma_f32_16x16x32_bf16 a[40:43], v[132:135], v[80:83], a[40:43]\n\t"
  "ds_read_b64_tr_b16 v[172:173], v228 offset:25088\n\t"
  "ds_read_b64_tr_b16 v[174:175], v228 offset:27136\n\t"
  "v_mfma_f32_16x16x32_bf16 a[44:47], v[132:135], v[112:115], a[44:47]\n\t"
  "v_accvgpr_read_b32 v203, a15\n\t"
  "v_cvt_pk_bf16_f32 v194, v200, v201\n\t"
  "v_cvt_pk_bf16_f32 v195, v202, v203\n\t"
  "s_waitcnt lgkmcnt(14)\n\t"
  "v_mfma_f32_16x16x32_bf16 a[48:51], v[136:139], v[80:83], a[48:51]\n\t"
  "ds_read_b64_tr_b16 v[176:177], v227 offset:28672\n\t"
  "ds_read_b64_tr_b16 v[178:179], v227 offset:30720\n\t"
  "v_mfma_f32_16x16x32_bf16 a[52:55], v[136:139], v[112:115], a[52:55]\n\t"
  "buffer_store_dwordx4 v[192:195], v246, %[srdws], s52 offen offset:0 sc1\n\t"
  "v_accvgpr_read_b32 v200, a20\n\t"
  "v_mfma_f32_16x16x32_bf16 a[56:59], v[140:143], v[80:83], a[56:59]\n\t"
  "ds_read_b64_tr_b16 v[180:181], v228 offset:28672\n\t"
  "ds_read_b64_tr_b16 v[182:183], v228 offset:30720\n\t"
  "v_mfma_f32_16x16x32_bf16 a[60:63], v[140:143], v[112:115], a[60:63]\n\t"
  "v_accvgpr_read_b32 v201, a21\n\t"
  "v_accvgpr_read_b32 v202, a22\n\t"
  "v_accvgpr_read_b32 v203, a23\n\t"
  "v_mfma_f32_16x16x32_bf16 a[32:35], v[144:147], v[84:87], a[32:35]\n\t"
  "ds_read_b64_tr_b16 v[184:185], v227 offset:29184\n\t"
  "ds_read_b64_tr_b16 v[186:187], v227 offset:31232\n\t"
  "v_mfma_f32_16x16x32_bf16 a[36:39], v[144:147], v[116:119], a[36:39]\n\t"
  "v_cvt_pk_bf16_f32 v196, v200, v201\n\t"
  "v_cvt_pk_bf16_f32 v197, v202, v203\n\t"
  "v_accvgpr_read_b32 v200, a28\n\t"
  "s_waitcnt lgkmcnt(14)\n\t"
  "v_mfma_f32_16x16x32_bf16 a[40:43], v[148:151], v[84:87], a[40:43]\n\t"
  "ds_read_b64_tr_b16 v[188:189], v228 offset:29184\n\t"
  "ds_read_b64_tr_b16 v[190:191], v228 offset:31232\n\t"
  "v_mfma_f32_16x16x32_bf16 a[44:47], v[148:151], v[116:119], a[44:47]\n\t"
  "v_accvgpr_read_b32 v201, a29\n\t"
  "v_accvgpr_read_b32 v202, a30\n\t"
  "v_mfma_f32_16x16x32_bf16 a[48:51], v[152:155], v[84:87], a[48:51]\n\t"
  "s_waitcnt vmcnt(27)\n\t"
  "s_barrier\n\t"
  "ds_read_b64_tr_b16 v[128:129], v227 offset:32768\n\t"
  "ds_read_b64_tr_b16 v[130:131], v227 offset:34816\n\t"
  "v_mfma_f32_16x16x32_bf16 a[52:55], v[152:155], v[116:119], a[52:55]\n\t"
  "v_accvgpr_read_b32 v203, a31\n\t"
  "v_cvt_pk_bf16_f32 v198, v200, v201\n\t"
  "v_cvt_pk_bf16_f32 v199, v202, v203\n\t"
  "v_mfma_f32_16x16x32_bf16 a[56:59], v[156:159], v[84:87], a[56:59]\n\t"
  "ds_read_b64_tr_b16 v[132:133], v228 offset:32768\n\t"
  "ds_read_b64_tr_b16 v[134:135], v228 offset:34816\n\t"
  "v_mfma_f32_16x16x32_bf16 a[60:63], v[156:159], v[116:119], a[60:63]\n\t"
  "buffer_store_dwordx4 v[196:199], v246, %[srdws], s52 offen offset:1024 sc1\n\t"
  "v_add_u32 v246, 0x800, v246\n\t"
  "s_waitcnt lgkmcnt(14)\n\t"
  "v_mfma_f32_16x16x32_bf16 a[32:35], v[160:163], v[88:91], a[32:35]\n\t"
  "ds_read_b64_tr_b16 v[136:137], v227 offset:33280\n\t"
  "ds_read_b64_tr_b16 v[138:139], v227 offset:35328\n\t"
  "v_mfma_f32_16x16x32_bf16 a[36:39], v[160:163], v[120:123], a[36:39]\n\t"
  "v_mfma_f32_16x16x32_bf16 a[40:43], v[164:167], v[88:91], a[40:43]\n\t"
  "ds_read_b64_tr_b16 v[140:141], v228 offset:33280\n\t"
  "ds_read_b64_tr_b16 v[142:143], v228 offset:35328\n\t"
  "v_mfma_f32_16x16x32_bf16 a[44:47], v[164:167], v[120:123], a[44:47]\n\t"
  "v_mfma_f32_16x16x32_bf16 a[48:51], v[168:171], v[88:91], a[48:51]\n\t"
  "ds_read_b64_tr_b16 v[144:145], v227 offset:36864\n\t"
  "ds_read_b64_tr_b16 v[146:147], v227 offset:38912\n\t"
  "v_mfma_f32_16x16x32_bf16 a[52:55], v[168:171], v[120:123], a[52:55]\n\t"
  "s_waitcnt lgkmcnt(14)\n\t"
  "v_mfma_f32_16x16x32_bf16 a[56:59], v[172:175], v[88:91], a[56:59]\n\t"
  "ds_read_b64_tr_b16 v[148:149], v228 offset:36864\n\t"
  "ds_read_b64_tr_b16 v[150:151], v228 offset:38912\n\t"
  "v_mfma_f32_16x16x32_bf16 a[60:63], v[172:175], v[120:123], a[60:63]\n\t"
  "v_mfma_f32_16x16x32_bf16 a[32:35], v[176:179], v[92:95], a[32:35]\n\t"
  "ds_read_b64_tr_b16 v[152:153], v227 offset:37376\n\t"
  "ds_read_b64_tr_b16 v[154:155], v227 offset:39424\n\t"
  "v_mfma_f32_16x16x32_bf16 a[36:39], v[176:179], v[124:127], a[36:39]\n\t"
  "v_mfma_f32_16x16x32_bf16 a[40:43], v[180:183], v[92:95], a[40:43]\n\t"
  "ds_read_b64_tr_b16 v[156:157], v228 offset:37376\n\t"
  "ds_read_b64_tr_b16 v[158:159], v228 offset:39424\n\t"
  "v_mfma_f32_16x16x32_bf16 a[44:47], v[180:183], v[124:127], a[44:47]\n\t"
  "s_waitcnt lgkmcnt(14)\n\t"
  "v_mfma_f32_16x16x32_bf16 a[48:51], v[184:187], v[92:95], a[48:51]\n\t"
  "ds_read_b64_tr_b16 v[160:161], v227 offset:40960\n\t"
  "ds_read_b64_tr_b16 v[162:163], v227 offset:43008\n\t"
  "v_mfma_f32_16x16x32_bf16 a[52:55], v[184:187], v[124:127], a[52:55]\n\t"
  "v_mfma_f32_16x16x32_bf16 a[56:59], v[188:191], v[92:95], a[56:59]\n\t"
  "ds_read_b64_tr_b16 v[164:165], v228 offset:40960\n\t"
  "ds_read_b64_tr_b16 v[166:167], v228 offset:43008\n\t"
  "v_mfma_f32_16x16x32_bf16 a[60:63], v[188:191], v[124:127], a[60:63]\n\t"
  "v_mfma_f32_16x16x32_bf16 a[0:3], v[128:131], v[64:67], 0\n\t"
  "ds_read_b64_tr_b16 v[168:169], v227 offset:41472\n\t"
  "ds_read_b64_tr_b16 v[170:171], v227 offset:43520\n\t"
  "v_mfma_f32_16x16x32_bf16 a[4:7], v[128:131], v[96:99], 0\n\t"
  "s_waitcnt lgkmcnt(14)\n\t"
  "v_mfma_f32_16x16x32_bf16 a[8:11], v[132:135], v[64:67], 0\n\t"
  "ds_read_b64_tr_b16 v[172:173], v228 offset:41472\n\t"
  "ds_read_b64_tr_b16 v[174:175], v228 offset:43520\n\t"
  "v_mfma_f32_16x16x32_bf16 a[12:15], v[132:135], v[96:99], 0\n\t"
  "v_mfma_f32_16x16x32_bf16 a[16:19], v[136:139], v[64:67], 0\n\t"
  "ds_read_b64_tr_b16 v[176:177], v227 offset:45056\n\t"
  "ds_read_b64_tr_b16 v[178:179], v227 offset:47104\n\t"
  "v_mfma_f32_16x16x32_bf16 a[20:23], v[136:139], v[96:99], 0\n\t"
  "v_mfma_f32_16x16x32_bf16 a[24:27], v[140:143], v[64:67], 0\n\t"
  "ds_read_b64_tr_b16 v[180:181], v228 offset:45056\n\t"
  "ds_read_b64_tr_b16 v[182:183], v228 offset:47104\n\t"
  "v_mfma_f32_16x16x32_bf16 a[28:31], v[140:143], v[96:99], 0\n\t"
  "s_waitcnt lgkmcnt(14)\n\t"
  "v_mfma_f32_16x16x32_bf16 a[0:3], v[144:147], v[68:71], a[0:3]\n\t"
  "ds_read_b64_tr_b16 v[184:185], v227 offset:45568\n\t"
  "ds_read_b64_tr_b16 v[186:187], v227 offset:47616\n\t"
  "v_mfma_f32_16x16x32_bf16 a[4:7], v[144:147], v[100:103], a[4:7]\n\t"
  "v_accvgpr_read_b32 v200, a32\n\t"
  "v_accvgpr_read_b32 v201, a33\n\t"
  "v_accvgpr_read_b32 v202, a34\n\t"
  "v_mfma_f32_16x16x32_bf16 a[8:11], v[148:151], v[68:71], a[8:11]\n\t"
  "ds_read_b64_tr_b16 v[188:189], v228 offset:45568\n\t"
  "ds_read_b64_tr_b16 v[190:191], v228 offset:47616\n\t"
  "v_mfma_f32_16x16x32_bf16 a[12:15], v[148:151], v[100:103], a[12:15]\n\t"
  "v_accvgpr_read_b32 v203, a35\n\t"
  "v_cvt_pk_bf16_f32 v192, v200, v201\n\t"
  "v_cvt_pk_bf16_f32 v193, v202, v203\n\t"
  "v_mfma_f32_16x16x32_bf16 a[16:19], v[152:155], v[68:71], a[16:19]\n\t"
  "ds_read_b64_tr_b16 v[128:129], v227 offset:49152\n\t"
  "ds_read_b64_tr_b16 v[130:131], v227 offset:51200\n\t"
  "v_mfma_f32_16x16x32_bf16 a[20:23], v[152:155], v[100:103], a[20:23]\n\t"
  "v_accvgpr_read_b32 v200, a40\n\t"
  "v_accvgpr_read_b32 v201, a41\n\t"
  "s_waitcnt lgkmcnt(14)\n\t"
  "v_mfma_f32_16x16x32_bf16 a[24:27], v[156:159], v[68:71], a[24:27]\n\t"
  "ds_read_b64_tr_b16 v[132:133], v228 offset:49152\n\t"
  "ds_read_b64_tr_b16 v[134:135], v228 offset:51200\n\t"
  "v_mfma_f32_16x16x32_bf16 a[28:31], v[156:159], v[100:103], a[28:31]\n\t"
  "v_accvgpr_read_b32 v202, a42\n\t"
  "v_accvgpr_read_b32 v203, a43\n\t"
  "v_cvt_pk_bf16_f32 v194, v200, v201\n\t"
  "v_mfma_f32_16x16x32_bf16 a[0:3], v[160:163], v[72:75], a[0:3]\n\t"
  "ds_read_b64_tr_b16 v[136:137], v227 offset:49664\n\t"
  "ds_read_b64_tr_b16 v[138:139], v227 offset:51712\n\t"
  "v_mfma_f32_16x16x32_bf16 a[4:7], v[160:163], v[104:107], a[4:7]\n\t"
  "v_cvt_pk_bf16_f32 v195, v202, v203\n\t"
  "buffer_store_dwordx4 v[192:195], v246, %[srdws], 0 offen offset:0 sc1\n\t"
  "v_accvgpr_read_b32 v200, a48\n\t"
  "v_mfma_f32_16x16x32_bf16 a[8:11], v[164:167], v[72:75], a[8:11]\n\t"
  "ds_read_b64_tr_b16 v[140:141], v228 offset:49664\n\t"
  "ds_read_b64_tr_b16 v[142:143], v228 offset:51712\n\t"
  "v_mfma_f32_16x16x32_bf16 a[12:15], v[164:167], v[104:107], a[12:15]\n\t"
  "v_accvgpr_read_b32 v201, a49\n\t"
  "v_accvgpr_read_b32 v202, a50\n\t"
  "s_waitcnt lgkmcnt(14)\n\t"
  "v_mfma_f32_16x16x32_bf16 a[16:19], v[168:171], v[72:75], a[16:19]\n\t"
  "ds_read_b64_tr_b16 v[144:145], v227 offset:53248\n\t"
  "ds_read_b64_tr_b16 v[146:147], v227 offset:55296\n\t"
  "v_mfma_f32_16x16x32_bf16 a[20:23], v[168:171], v[104:107], a[20:23]\n\t"
  "v_accvgpr_read_b32 v203, a51\n\t"
  "v_cvt_pk_bf16_f32 v196, v200, v201\n\t"
  "v_cvt_pk_bf16_f32 v197, v202, v203\n\t"
  "v_mfma_f32_16x16x32_bf16 a[24:27], v[172:175], v[72:75], a[24:27]\n\t"
  "ds_read_b64_tr_b16 v[148:149], v228 offset:53248\n\t"
  "ds_read_b64_tr_b16 v[150:151], v228 offset:55296\n\t"
  "v_mfma_f32_16x16x32_bf16 a[28:31], v[172:175], v[104:107], a[28:31]\n\t"
  "v_accvgpr_read_b32 v200, a56\n\t"
  "v_accvgpr_read_b32 v201, a57\n\t"
  "v_accvgpr_read_b32 v202, a58\n\t"
  "v_mfma_f32_16x16x32_bf16 a[0:3], v[176:179], v[76:79], a[0:3]\n\t"
  "ds_read_b64_tr_b16 v[152:153], v227 offset:53760\n\t"
  "ds_read_b64_tr_b16 v[154:155], v227 offset:55808\n\t"
  "v_mfma_f32_16x16x32_bf16 a[4:7], v[176:179], v[108:111], a[4:7]\n\t"
  "v_accvgpr_read_b32 v203, a59\n\t"
  "v_cvt_pk_bf16_f32 v198, v200, v201\n\t"
  "s_waitcnt lgkmcnt(14)\n\t"
  "v_mfma_f32_16x16x32_bf16 a[8:11], v[180:183], v[76:79], a[8:11]\n\t"
  "ds_read_b64_tr_b16 v[156:157], v228 offset:53760\n\t"
  "ds_read_b64_tr_b16 v[158:159], v228 offset:55808\n\t"
  "v_mfma_f32_16x16x32_bf16 a[12:15], v[180:183], v[108:111], a[12:15]\n\t"
  "v_cvt_pk_bf16_f32 v199, v202, v203\n\t"
  "buffer_store_dwordx4 v[196:199], v246, %[srdws], 0 offen offset:1024 sc1\n\t"
  "v_accvgpr_read_b32 v200, a36\n\t"
  "v_mfma_f32_16x16x32_bf16 a[16:19], v[184:187], v[76:79], a[16:19]\n\t"
  "ds_read_b64_tr_b16 v[160:161], v227 offset:57344\n\t"
  "ds_read_b64_tr_b16 v[162:163], v227 offset:59392\n\t"
  "v_mfma_f32_16x16x32_bf16 a[20:23], v[184:187], v[108:111], a[20:23]\n\t"
  "v_accvgpr_read_b32 v201, a37\n\t"
  "v_accvgpr_read_b32 v202, a38\n\t"
  "v_accvgpr_read_b32 v203, a39\n\t"
  "v_mfma_f32_16x16x32_bf16 a[24:27], v[188:191], v[76:79], a[24:27]\n\t"
  "ds_read_b64_tr_b16 v[164:165], v228 offset:57344\n\t"
  "ds_read_b64_tr_b16 v[166:167], v228 offset:59392\n\t"
  "v_mfma_f32_16x16x32_bf16 a[28:31], v[188:191], v[108:111], a[28:31]\n\t"
  "v_cvt_pk_bf16_f32 v192, v200, v201\n\t"
  "v_cvt_pk_bf16_f32 v193, v202, v203\n\t"
  "s_waitcnt lgkmcnt(14)\n\t"
  "v_mfma_f32_16x16x32_bf16 a[0:3], v[128:131], v[80:83], a[0:3]\n\t"
  "ds_read_b64_tr_b16 v[168:169], v227 offset:57856\n\t"
  "ds_read_b64_tr_b16 v[170:171], v227 offset:59904\n\t"
  "v_mfma_f32_16x16x32_bf16 a[4:7], v[128:131], v[112:115], a[4:7]\n\t"
  "v_accvgpr_read_b32 v200, a44\n\t"
  "v_accvgpr_read_b32 v201, a45\n\t"
  "v_accvgpr_read_b32 v202, a46\n\t"
  "v_mfma_f32_16x16x32_bf16 a[8:11], v[132:135], v[80:83], a[8:11]\n\t"
  "ds_read_b64_tr_b16 v[172:173], v228 offset:57856\n\t"
  "ds_read_b64_tr_b16 v[174:175], v228 offset:59904\n\t"
  "v_mfma_f32_16x16x32_bf16 a[12:15], v[132:135], v[112:115], a[12:15]\n\t"
  "v_accvgpr_read_b32 v203, a47\n\t"
  "v_cvt_pk_bf16_f32 v194, v200, v201\n\t"
  "v_cvt_pk_bf16_f32 v195, v202, v203\n\t"
  "v_mfma_f32_16x16x32_bf16 a[16:19], v[136:139], v[80:83], a[16:19]\n\t"
  "ds_read_b64_tr_b16 v[176:177], v227 offset:61440\n\t"
  "ds_read_b64_tr_b16 v[178:179], v227 offset:63488\n\t"
  "v_mfma_f32_16x16x32_bf16 a[20:23], v[136:139], v[112:115], a[20:23]\n\t"
  "buffer_store_dwordx4 v[192:195], v246, %[srdws], s52 offen offset:0 sc1\n\t"
  "v_accvgpr_read_b32 v200, a52\n\t"
  "s_waitcnt lgkmcnt(14)\n\t"
  "v_mfma_f32_16x16x32_bf16 a[24:27], v[140:143], v[80:83], a[24:27]\n\t"
  "ds_read_b64_tr_b16 v[180:181], v228 offset:61440\n\t"
  "ds_read_b64_tr_b16 v[182:183], v228 offset:63488\n\t"
  "v_mfma_f32_16x16x32_bf16 a[28:31], v[140:143], v[112:115], a[28:31]\n\t"
  "v_accvgpr_read_b32 v201, a53\n\t"
  "v_accvgpr_read_b32 v202, a54\n\t"
  "v_accvgpr_read_b32 v203, a55\n\t"
  "v_mfma_f32_16x16x32_bf16 a[0:3], v[144:147], v[84:87], a[0:3]\n\t"
  "ds_read_b64_tr_b16 v[184:185], v227 offset:61952\n\t"
  "ds_read_b64_tr_b16 v[186:187], v227 offset:64000\n\t"
  "v_mfma_f32_16x16x32_bf16 a[4:7], v[144:147], v[116:119], a[4:7]\n\t"
  "v_cvt_pk_bf16_f32 v196, v200, v201\n\t"
  "v_cvt_pk_bf16_f32 v197, v202, v203\n\t"
  "v_accvgpr_read_b32 v200, a60\n\t"
  "v_mfma_f32_16x16x32_bf16 a[8:11], v[148:151], v[84:87], a[8:11]\n\t"
  "ds_read_b64_tr_b16 v[188:189], v228 offset:61952\n\t"
  "ds_read_b64_tr_b16 v[190:191], v228 offset:64000\n\t"
  "v_mfma_f32_16x16x32_bf16 a[12:15], v[148:151], v[116:119], a[12:15]\n\t"
  "v_accvgpr_read_b32 v201, a61\n\t"
  "v_accvgpr_read_b32 v202, a62\n\t"
  "s_waitcnt lgkmcnt(14)\n\t"
  "v_mfma_f32_16x16x32_bf16 a[16:19], v[152:155], v[84:87], a[16:19]\n\t"
  "s_waitcnt vmcnt(19)\n\t"
  "s_barrier\n\t"
  "ds_read_b64_tr_b16 v[128:129], v229 offset:0\n\t"
  "ds_read_b64_tr_b16 v[130:131], v229 offset:2048\n\t"
  "v_mfma_f32_16x16x32_bf16 a[20:23], v[152:155], v[116:119], a[20:23]\n\t"
  "v_accvgpr_read_b32 v203, a63\n\t"
  "v_cvt_pk_bf16_f32 v198, v200, v201\n\t"
  "v_cvt_pk_bf16_f32 v199, v202, v203\n\t"
  "v_mfma_f32_16x16x32_bf16 a[24:27], v[156:159], v[84:87], a[24:27]\n\t"
  "ds_read_b64_tr_b16 v[132:133], v230 offset:0\n\t"
  "ds_read_b64_tr_b16 v[134:135], v230 offset:2048\n\t"
  "v_mfma_f32_16x16x32_bf16 a[28:31], v[156:159], v[116:119], a[28:31]\n\t"
  "buffer_store_dwordx4 v[196:199], v246, %[srdws], s52 offen offset:1024 sc1\n\t"
  "v_add_u32 v246, 0x800, v246\n\t"
  "v_mfma_f32_16x16x32_bf16 a[0:3], v[160:163], v[88:91], a[0:3]\n\t"
  "ds_read_b64_tr_b16 v[136:137], v229 offset:512\n\t"
  "ds_read_b64_tr_b16 v[138:139], v229 offset:2560\n\t"
  "v_mfma_f32_16x16x32_bf16 a[4:7], v[160:163], v[120:123], a[4:7]\n\t"
  "s_waitcnt lgkmcnt(14)\n\t"
  "v_mfma_f32_16x16x32_bf16 a[8:11], v[164:167], v[88:91], a[8:11]\n\t"
  "ds_read_b64_tr_b16 v[140:141], v230 offset:512\n\t"
  "ds_read_b64_tr_b16 v[142:143], v230 offset:2560\n\t"
  "v_mfma_f32_16x16x32_bf16 a[12:15], v[164:167], v[120:123], a[12:15]\n\t"
  "v_mfma_f32_16x16x32_bf16 a[16:19], v[168:171], v[88:91], a[16:19]\n\t"
  "ds_read_b64_tr_b16 v[144:145], v229 offset:4096\n\t"
  "ds_read_b64_tr_b16 v[146:147], v229 offset:6144\n\t"
  "v_mfma_f32_16x16x32_bf16 a[20:23], v[168:171], v[120:123], a[20:23]\n\t"
  "v_mfma_f32_16x16x32_bf16 a[24:27], v[172:175], v[88:91], a[24:27]\n\t"
  "ds_read_b64_tr_b16 v[148:149], v230 offset:4096\n\t"
  "ds_read_b64_tr_b16 v[150:151], v230 offset:6144\n\t"
  "v_mfma_f32_16x16x32_bf16 a[28:31], v[172:175], v[120:123], a[28:31]\n\t"
  "s_waitcnt lgkmcnt(14)\n\t"
  "v_mfma_f32_16x16x32_bf16 a[0:3], v[176:179], v[92:95], a[0:3]\n\t"
  "ds_read_b64_tr_b16 v[152:153], v229 offset:4608\n\t"
  "ds_read_b64_tr_b16 v[154:155], v229 offset:6656\n\t"
  "v_mfma_f32_16x16x32_bf16 a[4:7], v[176:179], v[124:127], a[4:7]\n\t"
  "v_mfma_f32_16x16x32_bf16 a[8:11], v[180:183], v[92:95], a[8:11]\n\t"
  "ds_read_b64_tr_b16 v[156:157], v230 offset:4608\n\t"
  "ds_read_b64_tr_b16 v[158:159], v230 offset:6656\n\t"
  "v_mfma_f32_16x16x32_bf16 a[12:15], v[180:183], v[124:127], a[12:15]\n\t"
  "v_mfma_f32_16x16x32_bf16 a[16:19], v[184:187], v[92:95], a[16:19]\n\t"
  "ds_read_b64_tr_b16 v[160:161], v229 offset:8192\n\t"
  "ds_read_b64_tr_b16 v[162:163], v229 offset:10240\n\t"
  "v_mfma_f32_16x16x32_bf16 a[20:23], v[184:187], v[124:127], a[20:23]\n\t"
  "s_waitcnt lgkmcnt(14)\n\t"
  "v_mfma_f32_16x16x32_bf16 a[24:27], v[188:191], v[92:95], a[24:27]\n\t"
  "ds_read_b64_tr_b16 v[164:165], v230 offset:8192\n\t"
  "ds_read_b64_tr_b16 v[166:167], v230 offset:10240\n\t"
  "v_mfma_f32_16x16x32_bf16 a[28:31], v[188:191], v[124:127], a[28:31]\n\t"
  "v_mfma_f32_16x16x32_bf16 a[32:35], v[128:131], v[64:67], 0\n\t"
  "ds_read_b64_tr_b16 v[168:169], v229 offset:8704\n\t"
  "ds_read_b64_tr_b16 v[170:171], v229 offset:10752\n\t"
  "v_mfma_f32_16x16x32_bf16 a[36:39], v[128:131], v[96:99], 0\n\t"
  "v_mfma_f32_16x16x32_bf16 a[40:43], v[132:135], v[64:67], 0\n\t"
  "ds_read_b64_tr_b16 v[172:173], v230 offset:8704\n\t"
  "ds_read_b64_tr_b16 v[174:175], v230 offset:10752\n\t"
  "v_mfma_f32_16x16x32_bf16 a[44:47], v[132:135], v[96:99], 0\n\t"
  "s_waitcnt lgkmcnt(14)\n\t"
  "v_mfma_f32_16x16x32_bf16 a[48:51], v[136:139], v[64:67], 0\n\t"
  "ds_read_b64_tr_b16 v[176:177], v229 offset:12288\n\t"
  "ds_read_b64_tr_b16 v[178:179], v229 offset:14336\n\t"
  "v_mfma_f32_16x16x32_bf16 a[52:55], v[136:139], v[96:99], 0\n\t"
  "v_mfma_f32_16x16x32_bf16 a[56:59], v[140:143], v[64:67], 0\n\t"
  "ds_read_b64_tr_b16 v[180:181], v230 offset:12288\n\t"
  "ds_read_b64_tr_b16 v[182:183], v230 offset:14336\n\t"
  "v_mfma_f32_16x16x32_bf16 a[60:63], v[140:143], v[96:99], 0\n\t"
  "v_mfma_f32_16x16x32_bf16 a[32:35], v[144:147], v[68:71], a[32:35]\n\t"
  "ds_read_b64_tr_b16 v[184:185], v229 offset:12800\n\t"
  "ds_read_b64_tr_b16 v[186:187], v229 offset:14848\n\t"
  "v_mfma_f32_16x16x32_bf16 a[36:39], v[144:147], v[100:103], a[36:39]\n\t"
  "v_accvgpr_read_b32 v200, a0\n\t"
  "v_accvgpr_read_b32 v201, a1\n\t"
  "v_accvgpr_read_b32 v202, a2\n\t"
  "s_waitcnt lgkmcnt(14)\n\t"
  "v_mfma_f32_16x16x32_bf16 a[40:43], v[148:151], v[68:71], a[40:43]\n\t"
  "ds_read_b64_tr_b16 v[188:189], v230 offset:12800\n\t"
  "ds_read_b64_tr_b16 v[190:191], v230 offset:14848\n\t"
  "v_mfma_f32_16x16x32_bf16 a[44:47], v[148:151], v[100:103], a[44:47]\n\t"
  "v_accvgpr_read_b32 v203, a3\n\t"
  "v_cvt_pk_bf16_f32 v192, v200, v201\n\t"
  "v_cvt_pk_bf16_f32 v193, v202, v203\n\t"
  "v_mfma_f32_16x16x32_bf16 a[48:51], v[152:155], v[68:71], a[48:51]\n\t"
  "ds_read_b64_tr_b16 v[128:129], v229 offset:16384\n\t"
  "ds_read_b64_tr_b16 v[130:131], v229 offset:18432\n\t"
  "v_mfma_f32_16x16x32_bf16 a[52:55], v[152:155], v[100:103], a[52:55]\n\t"
  "v_accvgpr_read_b32 v200, a8\n\t"
  "v_accvgpr_read_b32 v201, a9\n\t"
  "v_mfma_f32_16x16x32_bf16 a[56:59], v[156:159], v[68:71], a[56:59]\n\t"
  "ds_read_b64_tr_b16 v[132:133], v230 offset:16384\n\t"
  "ds_read_b64_tr_b16 v[134:135], v230 offset:18432\n\t"
  "v_mfma_f32_16x16x32_bf16 a[60:63], v[156:159], v[100:103], a[60:63]\n\t"
  "v_accvgpr_read_b32 v202, a10\n\t"
  "v_accvgpr_read_b32 v203, a11\n\t"
  "v_cvt_pk_bf16_f32 v194, v200, v201\n\t"
  "s_waitcnt lgkmcnt(14)\n\t"
  "v_mfma_f32_16x16x32_bf16 a[32:35], v[160:163], v[72:75], a[32:35]\n\t"
  "ds_read_b64_tr_b16 v[136:137], v229 offset:16896\n\t"
  "ds_read_b64_tr_b16 v[138:139], v229 offset:18944\n\t"
  "v_mfma_f32_16x16x32_bf16 a[36:39], v[160:163], v[104:107], a[36:39]\n\t"
  "v_cvt_pk_bf16_f32 v195, v202, v203\n\t"
  "buffer_store_dwordx4 v[192:195], v246, %[srdws], 0 offen offset:0 sc1\n\t"
  "v_accvgpr_read_b32 v200, a16\n\t"
  "v_mfma_f32_16x16x32_bf16 a[40:43], v[164:167], v[72:75], a[40:43]\n\t"
  "ds_read_b64_tr_b16 v[140:141], v230 offset:16896\n\t"
  "ds_read_b64_tr_b16 v[142:143], v230 offset:18944\n\t"
  "v_mfma_f32_16x16x32_bf16 a[44:47], v[164:167], v[104:107], a[44:47]\n\t"
  "v_accvgpr_read_b32 v201, a17\n\t"
  "v_accvgpr_read_b32 v202, a18\n\t"
  "v_mfma_f32_16x16x32_bf16 a[48:51], v[168:171], v[72:75], a[48:51]\n\t"
  "ds_read_b64_tr_b16 v[144:145], v229 offset:20480\n\t"
  "ds_read_b64_tr_b16 v[146:147], v229 offset:22528\n\t"
  "v_mfma_f32_16x16x32_bf16 a[52:55], v[168:171], v[104:107], a[52:55]\n\t"
  "v_accvgpr_read_b32 v203, a19\n\t"
  "v_cvt_pk_bf16_f32 v196, v200, v201\n\t"
  "v_cvt_pk_bf16_f32 v197, v202, v203\n\t"
  "s_waitcnt lgkmcnt(14)\n\t"
  "v_mfma_f32_16x16x32_bf16 a[56:59], v[172:175], v[72:75], a[56:59]\n\t"
  "ds_read_b64_tr_b16 v[148:149], v230 offset:20480\n\t"
  "ds_read_b64_tr_b16 v[150:151], v230 offset:22528\n\t"
  "v_mfma_f32_16x16x32_bf16 a[60:63], v[172:175], v[104:107], a[60:63]\n\t"
  "v_accvgpr_read_b32 v200, a24\n\t"
  "v_accvgpr_read_b32 v201, a25\n\t"
  "v_accvgpr_read_b32 v202, a26\n\t"
  "v_mfma_f32_16x16x32_bf16 a[32:35], v[176:179], v[76:79], a[32:35]\n\t"
  "ds_read_b64_tr_b16 v[152:153], v229 offset:20992\n\t"
  "ds_read_b64_tr_b16 v[154:155], v229 offset:23040\n\t"
  "v_mfma_f32_16x16x32_bf16 a[36:39], v[176:179], v[108:111], a[36:39]\n\t"
  "v_accvgpr_read_b32 v203, a27\n\t"
  "v_cvt_pk_bf16_f32 v198, v200, v201\n\t"
  "v_mfma_f32_16x16x32_bf16 a[40:43], v[180:183], v[76:79], a[40:43]\n\t"
  "ds_read_b64_tr_b16 v[156:157], v230 offset:20992\n\t"
  "ds_read_b64_tr_b16 v[158:159], v230 offset:23040\n\t"
  "v_mfma_f32_16x16x32_bf16 a[44:47], v[180:183], v[108:111], a[44:47]\n\t"
  "v_cvt_pk_bf16_f32 v199, v202, v203\n\t"
  "buffer_store_dwordx4 v[196:199], v246, %[srdws], 0 offen offset:1024 sc1\n\t"
  "v_accvgpr_read_b32 v200, a4\n\t"
  "s_waitcnt lgkmcnt(14)\n\t"
  "v_mfma_f32_16x16x32_bf16 a[48:51], v[184:187], v[76:79], a[48:51]\n\t"
  "ds_read_b64_tr_b16 v[160:161], v229 offset:24576\n\t"
  "ds_read_b64_tr_b16 v[162:163], v229 offset:26624\n\t"
  "v_mfma_f32_16x16x32_bf16 a[52:55], v[184:187], v[108:111], a[52:55]\n\t"
  "v_accvgpr_read_b32 v201, a5\n\t"
  "v_accvgpr_read_b32 v202, a6\n\t"
  "v_accvgpr_read_b32 v203, a7\n\t"
  "v_mfma_f32_16x16x32_bf16 a[56:59], v[188:191], v[76:79], a[56:59]\n\t"
  "ds_read_b64_tr_b16 v[164:165], v230 offset:24576\n\t"
  "ds_read_b64_tr_b16 v[166:167], v230 offset:26624\n\t"
  "v_mfma_f32_16x16x32_bf16 a[60:63], v[188:191], v[108:111], a[60:63]\n\t"
  "v_cvt_pk_bf16_f32 v192, v200, v201\n\t"
  "v_cvt_pk_bf16_f32 v193, v202, v203\n\t"
  "v_mfma_f32_16x16x32_bf16 a[32:35], v[128:131], v[80:83], a[32:35]\n\t"
  "ds_read_b64_tr_b16 v[168:169], v229 offset:25088\n\t"
  "ds_read_b64_tr_b16 v[170:171], v229 offset:27136\n\t"
  "v_mfma_f32_16x16x32_bf16 a[36:39], v[128:131], v[112:115], a[36:39]\n\t"
  "v_accvgpr_read_b32 v200, a12\n\t"
  "v_accvgpr_read_b32 v201, a13\n\t"
  "v_accvgpr_read_b32 v202, a14\n\t"
  "s_waitcnt lgkmcnt(14)\n\t"
  "v_mfma_f32_16x16x32_bf16 a[40:43], v[132:135], v[80:83], a[40:43]\n\t"
  "ds_read_b64_tr_b16 v[172:173], v230 offset:25088\n\t"
  "ds_read_b64_tr_b16 v[174:175], v230 offset:27136\n\t"
  "v_mfma_f32_16x16x32_bf16 a[44:47], v[132:135], v[112:115], a[44:47]\n\t"
  "v_accvgpr_read_b32 v203, a15\n\t"
  "v_cvt_pk_bf16_f32 v194, v200, v201\n\t"
  "v_cvt_pk_bf16_f32 v195, v202, v203\n\t"
  "v_mfma_f32_16x16x32_bf16 a[48:51], v[136:139], v[80:83], a[48:51]\n\t"
  "ds_read_b64_tr_b16 v[176:177], v229 offset:28672\n\t"
  "ds_read_b64_tr_b16 v[178:179], v229 offset:30720\n\t"
  "v_mfma_f32_16x16x32_bf16 a[52:55], v[136:139], v[112:115], a[52:55]\n\t"
  "buffer_store_dwordx4 v[192:195], v246, %[srdws], s52 offen offset:0 sc1\n\t"
  "v_accvgpr_read_b32 v200, a20\n\t"
  "v_mfma_f32_16x16x32_bf16 a[56:59], v[140:143], v[80:83], a[56:59]\n\t"
  "ds_read_b64_tr_b16 v[180:181], v230 offset:28672\n\t"
  "ds_read_b64_tr_b16 v[182:183], v230 offset:30720\n\t"
  "v_mfma_f32_16x16x32_bf16 a[60:63], v[140:143], v[112:115], a[60:63]\n\t"
  "v_accvgpr_read_b32 v201, a21\n\t"
  "v_accvgpr_read_b32 v202, a22\n\t"
  "v_accvgpr_read_b32 v203, a23\n\t"
  "s_waitcnt lgkmcnt(14)\n\t"
  "v_mfma_f32_16x16x32_bf16 a[32:35], v[144:147], v[84:87], a[32:35]\n\t"
  "ds_read_b64_tr_b16 v[184:185], v229 offset:29184\n\t"
  "ds_read_b64_tr_b16 v[186:187], v229 offset:31232\n\t"
  "v_mfma_f32_16x16x32_bf16 a[36:39], v[144:147], v[116:119], a[36:39]\n\t"
  "v_cvt_pk_bf16_f32 v196, v200, v201\n\t"
  "v_cvt_pk_bf16_f32 v197, v202, v203\n\t"
  "v_accvgpr_read_b32 v200, a28\n\t"
  "v_mfma_f32_16x16x32_bf16 a[40:43], v[148:151], v[84:87], a[40:43]\n\t"
  "ds_read_b64_tr_b16 v[188:189], v230 offset:29184\n\t"
  "ds_read_b64_tr_b16 v[190:191], v230 offset:31232\n\t"
  "v_mfma_f32_16x16x32_bf16 a[44:47], v[148:151], v[116:119], a[44:47]\n\t"
  "v_accvgpr_read_b32 v201, a29\n\t"
  "v_accvgpr_read_b32 v202, a30\n\t"
  "v_mfma_f32_16x16x32_bf16 a[48:51], v[152:155], v[84:87], a[48:51]\n\t"
  "s_waitcnt vmcnt(11)\n\t"
  "s_barrier\n\t"
  "ds_read_b64_tr_b16 v[128:129], v225 offset:0\n\t"
  "ds_read_b64_tr_b16 v[130:131], v225 offset:2048\n\t"
  "v_mfma_f32_16x16x32_bf16 a[52:55], v[152:155], v[116:119], a[52:55]\n\t"
  "v_accvgpr_read_b32 v203, a31\n\t"
  "v_cvt_pk_bf16_f32 v198, v200, v201\n\t"
  "v_cvt_pk_bf16_f32 v199, v202, v203\n\t"
  "s_waitcnt lgkmcnt(14)\n\t"
  "v_mfma_f32_16x16x32_bf16 a[56:59], v[156:159], v[84:87], a[56:59]\n\t"
  "ds_read_b64_tr_b16 v[132:133], v226 offset:0\n\t"
  "ds_read_b64_tr_b16 v[134:135], v226 offset:2048\n\t"
  "v_mfma_f32_16x16x32_bf16 a[60:63], v[156:159], v[116:119], a[60:63]\n\t"
  "buffer_store_dwordx4 v[196:199], v246, %[srdws], s52 offen offset:1024 sc1\n\t"
  "v_add_u32 v246, 0x800, v246\n\t"
  "v_mfma_f32_16x16x32_bf16 a[32:35], v[160:163], v[88:91], a[32:35]\n\t"
  "ds_read_b64_tr_b16 v[136:137], v225 offset:512\n\t"
  "ds_read_b64_tr_b16 v[138:139], v225 offset:2560\n\t"
  "v_mfma_f32_16x16x32_bf16 a[36:39], v[160:163], v[120:123], a[36:39]\n\t"
  "v_mfma_f32_16x16x32_bf16 a[40:43], v[164:167], v[88:91], a[40:43]\n\t"
  "ds_read_b64_tr_b16 v[140:141], v226 offset:512\n\t"
  "ds_read_b64_tr_b16 v[142:143], v226 offset:2560\n\t"
  "v_mfma_f32_16x16x32_bf16 a[44:47], v[164:167], v[120:123], a[44:47]\n\t"
  "s_waitcnt lgkmcnt(14)\n\t"
  "v_mfma_f32_16x16x32_bf16 a[48:51], v[168:171], v[88:91], a[48:51]\n\t"
  "ds_read_b64_tr_b16 v[144:145], v225 offset:4096\n\t"
  "ds_read_b64_tr_b16 v[146:147], v225 offset:6144\n\t"
  "v_mfma_f32_16x16x32_bf16 a[52:55], v[168:171], v[120:123], a[52:55]\n\t"
  "v_mfma_f32_16x16x32_bf16 a[56:59], v[172:175], v[88:91], a[56:59]\n\t"
  "ds_read_b64_tr_b16 v[148:149], v226 offset:4096\n\t"
  "ds_read_b64_tr_b16 v[150:151], v226 offset:6144\n\t"
  "v_mfma_f32_16x16x32_bf16 a[60:63], v[172:175], v[120:123], a[60:63]\n\t"
  "v_mfma_f32_16x16x32_bf16 a[32:35], v[176:179], v[92:95], a[32:35]\n\t"
  "ds_read_b64_tr_b16 v[152:153], v225 offset:4608\n\t"
  "ds_read_b64_tr_b16 v[154:155], v225 offset:6656\n\t"
  "v_mfma_f32_16x16x32_bf16 a[36:39], v[176:179], v[124:127], a[36:39]\n\t"
  "s_waitcnt lgkmcnt(14)\n\t"
  "v_mfma_f32_16x16x32_bf16 a[40:43], v[180:183], v[92:95], a[40:43]\n\t"
  "ds_read_b64_tr_b16 v[156:157], v226 offset:4608\n\t"
  "ds_read_b64_tr_b16 v[158:159], v226 offset:6656\n\t"
  "v_mfma_f32_16x16x32_bf16 a[44:47], v[180:183], v[124:127], a[44:47]\n\t"
  "v_mfma_f32_16x16x32_bf16 a[48:51], v[184:187], v[92:95], a[48:51]\n\t"
  "ds_read_b64_tr_b16 v[160:161], v225 offset:8192\n\t"
  "ds_read_b64_tr_b16 v[162:163], v225 offset:10240\n\t"
  "v_mfma_f32_16x16x32_bf16 a[52:55], v[184:187], v[124:127], a[52:55]\n\t"
  "v_mfma_f32_16x16x32_bf16 a[56:59], v[188:191], v[92:95], a[56:59]\n\t"
  "ds_read_b64_tr_b16 v[164:165], v226 offset:8192\n\t"
  "ds_read_b64_tr_b16 v[166:167], v226 offset:10240\n\t"
  "v_mfma_f32_16x16x32_bf16 a[60:63], v[188:191], v[124:127], a[60:63]\n\t"
  "s_waitcnt lgkmcnt(14)\n\t"
  "v_mfma_f32_16x16x32_bf16 a[0:3], v[128:131], v[64:67], 0\n\t"
  "ds_read_b64_tr_b16 v[168:169], v225 offset:8704\n\t"
  "ds_read_b64_tr_b16 v[170:171], v225 offset:10752\n\t"
  "v_mfma_f32_16x16x32_bf16 a[4:7], v[128:131], v[96:99], 0\n\t"
  "v_mfma_f32_16x16x32_bf16 a[8:11], v[132:135], v[64:67], 0\n\t"
  "ds_read_b64_tr_b16 v[172:173], v226 offset:8704\n\t"
  "ds_read_b64_tr_b16 v[174:175], v226 offset:10752\n\t"
  "v_mfma_f32_16x16x32_bf16 a[12:15], v[132:135], v[96:99], 0\n\t"
  "v_mfma_f32_16x16x32_bf16 a[16:19], v[136:139], v[64:67], 0\n\t"
  "ds_read_b64_tr_b16 v[176:177], v225 offset:12288\n\t"
  "ds_read_b64_tr_b16 v[178:179], v225 offset:14336\n\t"
  "v_mfma_f32_16x16x32_bf16 a[20:23], v[136:139], v[96:99], 0\n\t"
  "s_waitcnt lgkmcnt(14)\n\t"
  "v_mfma_f32_16x16x32_bf16 a[24:27], v[140:143], v[64:67], 0\n\t"
  "ds_read_b64_tr_b16 v[180:181], v226 offset:12288\n\t"
  "ds_read_b64_tr_b16 v[182:183], v226 offset:14336\n\t"
  "v_mfma_f32_16x16x32_bf16 a[28:31], v[140:143], v[96:99], 0\n\t"
  "v_mfma_f32_16x16x32_bf16 a[0:3], v[144:147], v[68:71], a[0:3]\n\t"
  "ds_read_b64_tr_b16 v[184:185], v225 offset:12800\n\t"
  "ds_read_b64_tr_b16 v[186:187], v225 offset:14848\n\t"
  "v_mfma_f32_16x16x32_bf16 a[4:7], v[144:147], v[100:103], a[4:7]\n\t"
  "v_accvgpr_read_b32 v200, a32\n\t"
  "v_accvgpr_read_b32 v201, a33\n\t"
  "v_accvgpr_read_b32 v202, a34\n\t"
  "v_mfma_f32_16x16x32_bf16 a[8:11], v[148:151], v[68:71], a[8:11]\n\t"
  "ds_read_b64_tr_b16 v[188:189], v226 offset:12800\n\t"
  "ds_read_b64_tr_b16 v[190:191], v226 offset:14848\n\t"
  "v_mfma_f32_16x16x32_bf16 a[12:15], v[148:151], v[100:103], a[12:15]\n\t"
  "v_accvgpr_read_b32 v203, a35\n\t"
  "v_cvt_pk_bf16_f32 v192, v200, v201\n\t"
  "v_cvt_pk_bf16_f32 v193, v202, v203\n\t"
  "s_waitcnt lgkmcnt(14)\n\t"
  "v_mfma_f32_16x16x32_bf16 a[16:19], v[152:155], v[68:71], a[16:19]\n\t"
  "ds_read_b64_tr_b16 v[128:129], v225 offset:16384\n\t"
  "ds_read_b64_tr_b16 v[130:131], v225 offset:18432\n\t"
  "v_mfma_f32_16x16x32_bf16 a[20:23], v[152:155], v[100:103], a[20:23]\n\t"
  "v_accvgpr_read_b32 v200, a40\n\t"
  "v_accvgpr_read_b32 v201, a41\n\t"
  "v_mfma_f32_16x16x32_bf16 a[24:27], v[156:159], v[68:71], a[24:27]\n\t"
  "ds_read_b64_tr_b16 v[132:133], v226 offset:16384\n\t"
  "ds_read_b64_tr_b16 v[134:135], v226 offset:18432\n\t"
  "v_mfma_f32_16x16x32_bf16 a[28:31], v[156:159], v[100:103], a[28:31]\n\t"
  "v_accvgpr_read_b32 v202, a42\n\t"
  "v_accvgpr_read_b32 v203, a43\n\t"
  "v_cvt_pk_bf16_f32 v194, v200, v201\n\t"
  "v_mfma_f32_16x16x32_bf16 a[0:3], v[160:163], v[72:75], a[0:3]\n\t"
  "ds_read_b64_tr_b16 v[136:137], v225 offset:16896\n\t"
  "ds_read_b64_tr_b16 v[138:139], v225 offset:18944\n\t"
  "v_mfma_f32_16x16x32_bf16 a[4:7], v[160:163], v[104:107], a[4:7]\n\t"
  "v_cvt_pk_bf16_f32 v195, v202, v203\n\t"
  "buffer_store_dwordx4 v[192:195], v246, %[srdws], 0 offen offset:0 sc1\n\t"
  "v_accvgpr_read_b32 v200, a48\n\t"
  "s_waitcnt lgkmcnt(14)\n\t"
  "v_mfma_f32_16x16x32_bf16 a[8:11], v[164:167], v[72:75], a[8:11]\n\t"
  "ds_read_b64_tr_b16 v[140:141], v226 offset:16896\n\t"
  "ds_read_b64_tr_b16 v[142:143], v226 offset:18944\n\t"
  "v_mfma_f32_16x16x32_bf16 a[12:15], v[164:167], v[104:107], a[12:15]\n\t"
  "v_accvgpr_read_b32 v201, a49\n\t"
  "v_accvgpr_read_b32 v202, a50\n\t"
  "v_mfma_f32_16x16x32_bf16 a[16:19], v[168:171], v[72:75], a[16:19]\n\t"
  "ds_read_b64_tr_b16 v[144:145], v225 offset:20480\n\t"
  "ds_read_b64_tr_b16 v[146:147], v225 offset:22528\n\t"
  "v_mfma_f32_16x16x32_bf16 a[20:23], v[168:171], v[104:107], a[20:23]\n\t"
  "v_accvgpr_read_b32 v203, a51\n\t"
  "v_cvt_pk_bf16_f32 v196, v200, v201\n\t"
  "v_cvt_pk_bf16_f32 v197, v202, v203\n\t"
  "v_mfma_f32_16x16x32_bf16 a[24:27], v[172:175], v[72:75], a[24:27]\n\t"
  "ds_read_b64_tr_b16 v[148:149], v226 offset:20480\n\t"
  "ds_read_b64_tr_b16 v[150:151], v226 offset:22528\n\t"
  "v_mfma_f32_16x16x32_bf16 a[28:31], v[172:175], v[104:107], a[28:31]\n\t"
  "v_accvgpr_read_b32 v200, a56\n\t"
  "v_accvgpr_read_b32 v201, a57\n\t"
  "v_accvgpr_read_b32 v202, a58\n\t"
  "s_waitcnt lgkmcnt(14)\n\t"
  "v_mfma_f32_16x16x32_bf16 a[0:3], v[176:179], v[76:79], a[0:3]\n\t"
  "ds_read_b64_tr_b16 v[152:153], v225 offset:20992\n\t"
  "ds_read_b64_tr_b16 v[154:155], v225 offset:23040\n\t"
  "v_mfma_f32_16x16x32_bf16 a[4:7], v[176:179], v[108:111], a[4:7]\n\t"
  "v_accvgpr_read_b32 v203, a59\n\t"
  "v_cvt_pk_bf16_f32 v198, v200, v201\n\t"
  "v_mfma_f32_16x16x32_bf16 a[8:11], v[180:183], v[76:79], a[8:11]\n\t"
  "ds_read_b64_tr_b16 v[156:157], v226 offset:20992\n\t"
  "ds_read_b64_tr_b16 v[158:159], v226 offset:23040\n\t"
  "v_mfma_f32_16x16x32_bf16 a[12:15], v[180:183], v[108:111], a[12:15]\n\t"
  "v_cvt_pk_bf16_f32 v199, v202, v203\n\t"
  "buffer_store_dwordx4 v[196:199], v246, %[srdws], 0 offen offset:1024 sc1\n\t"
  "v_accvgpr_read_b32 v200, a36\n\t"
  "v_mfma_f32_16x16x32_bf16 a[16:19], v[184:187], v[76:79], a[16:19]\n\t"
  "ds_read_b64_tr_b16 v[160:161], v225 offset:24576\n\t"
  "ds_read_b64_tr_b16 v[162:163], v225 offset:26624\n\t"
  "v_mfma_f32_16x16x32_bf16 a[20:23], v[184:187], v[108:111], a[20:23]\n\t"
  "v_accvgpr_read_b32 v201, a37\n\t"
  "v_accvgpr_read_b32 v202, a38\n\t"
  "v_accvgpr_read_b32 v203, a39\n\t"
  "s_waitcnt lgkmcnt(14)\n\t"
  "v_mfma_f32_16x16x32_bf16 a[24:27], v[188:191], v[76:79], a[24:27]\n\t"
  "ds_read_b64_tr_b16 v[164:165], v226 offset:24576\n\t"
  "ds_read_b64_tr_b16 v[166:167], v226 offset:26624\n\t"
  "v_mfma_f32_16x16x32_bf16 a[28:31], v[188:191], v[108:111], a[28:31]\n\t"
  "v_cvt_pk_bf16_f32 v192, v200, v201\n\t"
  "v_cvt_pk_bf16_f32 v193, v202, v203\n\t"
  "v_mfma_f32_16x16x32_bf16 a[0:3], v[128:131], v[80:83], a[0:3]\n\t"
  "ds_read_b64_tr_b16 v[168:169], v225 offset:25088\n\t"
  "ds_read_b64_tr_b16 v[170:171], v225 offset:27136\n\t"
  "v_mfma_f32_16x16x32_bf16 a[4:7], v[128:131], v[112:115], a[4:7]\n\t"
  "v_accvgpr_read_b32 v200, a44\n\t"
  "v_accvgpr_read_b32 v201, a45\n\t"
  "v_accvgpr_read_b32 v202, a46\n\t"
  "v_mfma_f32_16x16x32_bf16 a[8:11], v[132:135], v[80:83], a[8:11]\n\t"
  "ds_read_b64_tr_b16 v[172:173], v226 offset:25088\n\t"
  "ds_read_b64_tr_b16 v[174:175], v226 offset:27136\n\t"
  "v_mfma_f32_16x16x32_bf16 a[12:15], v[132:135], v[112:115], a[12:15]\n\t"
  "v_accvgpr_read_b32 v203, a47\n\t"
  "v_cvt_pk_bf16_f32 v194, v200, v201\n\t"
  "v_cvt_pk_bf16_f32 v195, v202, v203\n\t"
  "s_waitcnt lgkmcnt(14)\n\t"
  "v_mfma_f32_16x16x32_bf16 a[16:19], v[136:139], v[80:83], a[16:19]\n\t"
  "ds_read_b64_tr_b16 v[176:177], v225 offset:28672\n\t"
  "ds_read_b64_tr_b16 v[178:179], v225 offset:30720\n\t"
  "v_mfma_f32_16x16x32_bf16 a[20:23], v[136:139], v[112:115], a[20:23]\n\t"
  "buffer_store_dwordx4 v[192:195], v246, %[srdws], s52 offen offset:0 sc1\n\t"
  "v_accvgpr_read_b32 v200, a52\n\t"
  "v_mfma_f32_16x16x32_bf16 a[24:27], v[140:143], v[80:83], a[24:27]\n\t"
  "ds_read_b64_tr_b16 v[180:181], v226 offset:28672\n\t"
  "ds_read_b64_tr_b16 v[182:183], v226 offset:30720\n\t"
  "v_mfma_f32_16x16x32_bf16 a[28:31], v[140:143], v[112:115], a[28:31]\n\t"
  "v_accvgpr_read_b32 v201, a53\n\t"
  "v_accvgpr_read_b32 v202, a54\n\t"
  "v_accvgpr_read_b32 v203, a55\n\t"
  "v_mfma_f32_16x16x32_bf16 a[0:3], v[144:147], v[84:87], a[0:3]\n\t"
  "ds_read_b64_tr_b16 v[184:185], v225 offset:29184\n\t"
  "ds_read_b64_tr_b16 v[186:187], v225 offset:31232\n\t"
  "v_mfma_f32_16x16x32_bf16 a[4:7], v[144:147], v[116:119], a[4:7]\n\t"
  "v_cvt_pk_bf16_f32 v196, v200, v201\n\t"
  "v_cvt_pk_bf16_f32 v197, v202, v203\n\t"
  "v_accvgpr_read_b32 v200, a60\n\t"
  "s_waitcnt lgkmcnt(14)\n\t"
  "v_mfma_f32_16x16x32_bf16 a[8:11], v[148:151], v[84:87], a[8:11]\n\t"
  "ds_read_b64_tr_b16 v[188:189], v226 offset:29184\n\t"
  "ds_read_b64_tr_b16 v[190:191], v226 offset:31232\n\t"
  "v_mfma_f32_16x16x32_bf16 a[12:15], v[148:151], v[116:119], a[12:15]\n\t"
  "v_accvgpr_read_b32 v201, a61\n\t"
  "v_accvgpr_read_b32 v202, a62\n\t"
  "v_mfma_f32_16x16x32_bf16 a[16:19], v[152:155], v[84:87], a[16:19]\n\t"
  "v_mfma_f32_16x16x32_bf16 a[20:23], v[152:155], v[116:119], a[20:23]\n\t"
  "v_accvgpr_read_b32 v203, a63\n\t"
  "v_cvt_pk_bf16_f32 v198, v200, v201\n\t"
  "v_cvt_pk_bf16_f32 v199, v202, v203\n\t"
  "v_mfma_f32_16x16x32_bf16 a[24:27], v[156:159], v[84:87], a[24:27]\n\t"
  "v_mfma_f32_16x16x32_bf16 a[28:31], v[156:159], v[116:119], a[28:31]\n\t"
  "buffer_store_dwordx4 v[196:199], v246, %[srdws], s52 offen offset:1024 sc1\n\t"
  "v_add_u32 v246, 0x800, v246\n\t"
  "s_waitcnt lgkmcnt(10)\n\t"
  "v_mfma_f32_16x16x32_bf16 a[0:3], v[160:163], v[88:91], a[0:3]\n\t"
  "v_mfma_f32_16x16x32_bf16 a[4:7], v[160:163], v[120:123], a[4:7]\n\t"
  "v_mfma_f32_16x16x32_bf16 a[8:11], v[164:167], v[88:91], a[8:11]\n\t"
  "v_mfma_f32_16x16x32_bf16 a[12:15], v[164:167], v[120:123], a[12:15]\n\t"
  "v_mfma_f32_16x16x32_bf16 a[16:19], v[168:171], v[88:91], a[16:19]\n\t"
  "v_mfma_f32_16x16x32_bf16 a[20:23], v[168:171], v[120:123], a[20:23]\n\t"
  "s_waitcnt lgkmcnt(4)\n\t"
  "v_mfma_f32_16x16x32_bf16 a[24:27], v[172:175], v[88:91], a[24:27]\n\t"
  "v_mfma_f32_16x16x32_bf16 a[28:31], v[172:175], v[120:123], a[28:31]\n\t"
  "v_mfma_f32_16x16x32_bf16 a[0:3], v[176:179], v[92:95], a[0:3]\n\t"
  "v_mfma_f32_16x16x32_bf16 a[4:7], v[176:179], v[124:127], a[4:7]\n\t"
  "v_mfma_f32_16x16x32_bf16 a[8:11], v[180:183], v[92:95], a[8:11]\n\t"
  "v_mfma_f32_16x16x32_bf16 a[12:15], v[180:183], v[124:127], a[12:15]\n\t"
  "s_waitcnt lgkmcnt(0)\n\t"
  "v_mfma_f32_16x16x32_bf16 a[16:19], v[184:187], v[92:95], a[16:19]\n\t"
  "v_mfma_f32_16x16x32_bf16 a[20:23], v[184:187], v[124:127], a[20:23]\n\t"
  "v_mfma_f32_16x16x32_bf16 a[24:27], v[188:191], v[92:95], a[24:27]\n\t"
  "v_mfma_f32_16x16x32_bf16 a[28:31], v[188:191], v[124:127], a[28:31]\n\t"
  "s_nop 7\n\t"
  "v_accvgpr_read_b32 v200, a0\n\t"
  "v_accvgpr_read_b32 v201, a1\n\t"
  "v_accvgpr_read_b32 v202, a2\n\t"
  "v_accvgpr_read_b32 v203, a3\n\t"
  "v_cvt_pk_bf16_f32 v192, v200, v201\n\t"
  "v_cvt_pk_bf16_f32 v193, v202, v203\n\t"
  "v_accvgpr_read_b32 v200, a8\n\t"
  "v_accvgpr_read_b32 v201, a9\n\t"
  "v_accvgpr_read_b32 v202, a10\n\t"
  "v_accvgpr_read_b32 v203, a11\n\t"
  "v_cvt_pk_bf16_f32 v194, v200, v201\n\t"
  "v_cvt_pk_bf16_f32 v195, v202, v203\n\t"
  "buffer_store_dwordx4 v[192:195], v246, %[srdws], 0 offen offset:0 sc1\n\t"
  "v_accvgpr_read_b32 v200, a16\n\t"
  "v_accvgpr_read_b32 v201, a17\n\t"
  "v_accvgpr_read_b32 v202, a18\n\t"
  "v_accvgpr_read_b32 v203, a19\n\t"
  "v_cvt_pk_bf16_f32 v196, v200, v201\n\t"
  "v_cvt_pk_bf16_f32 v197, v202, v203\n\t"
  "v_accvgpr_read_b32 v200, a24\n\t"
  "v_accvgpr_read_b32 v201, a25\n\t"
  "v_accvgpr_read_b32 v202, a26\n\t"
  "v_accvgpr_read_b32 v203, a27\n\t"
  "v_cvt_pk_bf16_f32 v198, v200, v201\n\t"
  "v_cvt_pk_bf16_f32 v199, v202, v203\n\t"
  "buffer_store_dwordx4 v[196:199], v246, %[srdws], 0 offen offset:1024 sc1\n\t"
  "v_accvgpr_read_b32 v200, a4\n\t"
  "v_accvgpr_read_b32 v201, a5\n\t"
  "v_accvgpr_read_b32 v202, a6\n\t"
  "v_accvgpr_read_b32 v203, a7\n\t"
  "v_cvt_pk_bf16_f32 v192, v200, v201\n\t"
  "v_cvt_pk_bf16_f32 v193, v202, v203\n\t"
  "v_accvgpr_read_b32 v200, a12\n\t"
  "v_accvgpr_read_b32 v201, a13\n\t"
  "v_accvgpr_read_b32 v202, a14\n\t"
  "v_accvgpr_read_b32 v203, a15\n\t"
  "v_cvt_pk_bf16_f32 v194, v200, v201\n\t"
  "v_cvt_pk_bf16_f32 v195, v202, v203\n\t"
  "buffer_store_dwordx4 v[192:195], v246, %[srdws], s52 offen offset:0 sc1\n\t"
  "v_accvgpr_read_b32 v200, a20\n\t"
  "v_accvgpr_read_b32 v201, a21\n\t"
  "v_accvgpr_read_b32 v202, a22\n\t"
  "v_accvgpr_read_b32 v203, a23\n\t"
  "v_cvt_pk_bf16_f32 v196, v200, v201\n\t"
  "v_cvt_pk_bf16_f32 v197, v202, v203\n\t"
  "v_accvgpr_read_b32 v200, a28\n\t"
  "v_accvgpr_read_b32 v201, a29\n\t"
  "v_accvgpr_read_b32 v202, a30\n\t"
  "v_accvgpr_read_b32 v203, a31\n\t"
  "v_cvt_pk_bf16_f32 v198, v200, v201\n\t"
  "v_cvt_pk_bf16_f32 v199, v202, v203\n\t"
  "buffer_store_dwordx4 v[196:199], v246, %[srdws], s52 offen offset:1024 sc1\n\t"
  "v_add_u32 v246, 0x800, v246\n\t"
  "s_waitcnt vmcnt(0)\n\t"
  "s_mov_b64 exec, 1\n\t"
  "buffer_atomic_add v207, v206, %[srdws], %[cntp] offen\n\t"
  "s_mov_b64 exec, -1\n\t"
  ".Lpend_%=:\n\t"
    ".Lkm_code_end:\n\t"
    : "={a[0:15]}"(acc[0]), "={a[16:31]}"(acc[1]), "+{v[64:79]}"(of[0]), "+{v[80:95]}"(of[1]), "+{v[96:111]}"(of[2]), "+{v[112:127]}"(of[3]), "+{v[224:239]}"(pa), "+{v[240:255]}"(pb)
    : [srdo] "s"(srdo), [srdws] "s"(srdws), [srdp] "s"(srdp), [srdg] "s"(srdg), [srdb] "s"(srdb), [m0o] "s"(m0o), [m0k] "s"(m0k), [m0w] "s"(m0w),
      [ldsw] "s"(ldsw), [hs2] "s"(hs2), [cntp] "s"(cntp), [cntpr] "s"(cntpr), [cntu] "s"(cntu), [sel0] "v"(sel0), [sel1] "v"(sel1), [c4096] "s"(4096u), [c6144] "s"(6144u), [c12288] "s"(12288u), [c18432] "s"(18432u), [c28672] "s"(28672u), [c31744] "s"(31744u), [c34816] "s"(34816u), [c40960] "s"(40960u), [c47104] "s"(47104u)
    : "v128", "v129", "v130", "v131", "v132", "v133", "v134", "v135", "v136", "v137", "v138", "v139", "v140", "v141", "v142", "v143", "v144", "v145", "v146", "v147", "v148", "v149", "v150", "v151", "v152", "v153", "v154", "v155", "v156", "v157", "v158", "v159", "v160", "v161", "v162", "v163", "v164", "v165", "v166", "v167", "v168", "v169", "v170", "v171", "v172", "v173", "v174", "v175", "v176", "v177", "v178", "v179", "v180", "v181", "v182", "v183", "v184", "v185", "v186", "v187", "v188", "v189", "v190", "v191", "v192", "v193", "v194", "v195", "v196", "v197", "v198", "v199", "v200", "v201", "v202", "v203", "v204", "v205", "v206", "v207", "v208", "v209", "v210", "v211", "v212", "v213", "v214", "v215", "v216", "v217", "v218", "v219", "v220", "v221", "v222", "v223", "a32", "a33", "a34", "a35", "a36", "a37", "a38", "a39", "a40", "a41", "a42", "a43", "a44", "a45", "a46", "a47", "a48", "a49", "a50", "a51", "a52", "a53", "a54", "a55", "a56", "a57", "a58", "a59", "a60", "a61", "a62", "a63", "a64", "a65", "a66", "a67", "a68", "a69", "a70", "a71", "a72", "a73", "a74", "a75", "a76", "a77", "a78", "a79", "a80", "a81", "a82", "a83", "a84", "a85", "a86", "a87", "a88", "a89", "a90", "a91", "a92", "a93", "a94", "a95", "a96", "a97", "a98", "a99", "a100", "a101", "a102", "a103", "a104", "a105", "a106", "a107", "a108", "a109", "a110", "a111", "a112", "a113", "a114", "a115", "a116", "a117", "a118", "a119", "a120", "a121", "a122", "a123", "a124", "a125", "a126", "a127", "a128", "a129", "a130", "a131", "a132", "a133", "a134", "a135", "a136", "a137", "a138", "a139", "a140", "a141", "a142", "a143", "a144", "a145", "a146", "a147", "a148", "a149", "a150", "a151", "a152", "a153", "a154", "a155", "a156", "a157", "a158", "a159", "a160", "a161", "a162", "a163", "a164", "a165", "a166", "a167", "a168", "a169", "a170", "a171", "a172", "a173", "a174", "a175", "a176", "a177", "a178", "a179", "a180", "a181", "a182", "a183", "a184", "a185", "a186", "a187", "a188", "a189", "a190", "a191", "a192", "a193", "a194", "a195", "a196", "a197", "a198", "a199", "a200", "a201", "a202", "a203", "a204", "a205", "a206", "a207", "a208", "a209", "a210", "a211", "a212", "a213", "a214", "a215", "a216", "a217", "a218", "a219", "a220", "a221", "a222", "a223", "a224", "a225", "a226", "a227", "a228", "a229", "a230", "a231", "a232", "a233", "a234", "a235", "a236", "a237", "a238", "a239", "a240", "a241", "a242", "a243", "a244", "a245", "a246", "a247", "a248", "a249", "a250", "a251", "a252", "a253", "a254", "a255", "s43", "s47", "s51", "s52", "s57", "vcc", "scc", "memory");
  TS(1, 7);
  if (!has_s2) return;
  #pragma unroll
  for (int nt = 0; nt < 4; ++nt) {
    float r[4];
    #pragma unroll
    for (int e = 0; e < 4; ++e) {
      const float gt = acc[0][nt * 4 + e];
      const float sg = gt * __builtin_amdgcn_rcpf(1.0f + __builtin_amdgcn_exp2f(-1.4426950408889634f * gt));
      const float xx = (e & 1) ? bfhi(xv[nt][e >> 1]) : bflo(xv[nt][e >> 1]);
      r[e] = xx + acc[1][nt * 4 + e] * sg;
    }
    u32x2 w = {cvtpk(r[0], r[1]), cvtpk(r[2], r[3])};
    *(u32x2*)(out + ooff + nt * 16u) = w;
  }
  TS(1, 8);
}

extern "C" void kernel_launch(void* const* d_in, const int* in_sizes, int n_in, void* d_out, int out_size, void* d_ws, size_t ws_size,
                              hipStream_t stream) {
  (void)hipFuncSetAttribute((const void*)k_main, hipFuncAttributeMaxDynamicSharedMemorySize, 163840);
  char* ws = (char*)d_ws;
  k_pre<<<256, 512, 0, stream>>>((const u16*)d_in[0], ws, (const u16*)d_in[3], (const u16*)d_in[4], (const u16*)d_in[1], (const u16*)d_in[2], (const u16*)d_in[6]);
  k_main<<<256, 256, 163840, stream>>>((const u16*)d_in[1], (const u16*)d_in[2], (const u16*)d_in[3], (const u16*)d_in[4], (const u16*)d_in[7], (const u16*)d_in[8],
                                       (const u16*)d_in[6], (const u16*)d_in[9], (const u16*)d_in[0], ws, (u16*)d_out);
}
```
